```python
import torch

def _softmax(x, axis=-1):
    return torch.softmax(x, dim=axis)
def _gelu(x):
    return torch.nn.functional.gelu(x, approximate="tanh")
def _silu(x):
    return torch.nn.functional.silu(x)
def _sigmoid(x):
    return torch.sigmoid(x)
def _layernorm(x, axis=-1, eps=1e-5):
    m = x.mean(dim=axis, keepdim=True)
    v = x.var(dim=axis, keepdim=True, unbiased=False)
    return (x - m) / torch.sqrt(v + eps)
def _scatter_add(tgt, idx, src, axis=0):
    return tgt.clone().index_add_(axis, idx, src)
def _segment_sum(src, seg, n):
    out = torch.zeros((n,) + src.shape[1:], dtype=src.dtype)
    return out.index_add_(0, seg, src)
def _pad(x, pw):
    flat = tuple(v for pair in reversed(pw) for v in pair)
    return torch.nn.functional.pad(x, flat)

kernel_name = 'concat_heads__mha_block_cached__compressed_kv_cla__decode'

def setup_inputs():

    _w = (lambda a: (lambda t: t.to(torch.bfloat16) if t.is_floating_point() else t)(torch.as_tensor(a)))
    def _A_setup():
        B, Sq, D = 4, 1, 576
        H, Sk, Dh = 32, 4096, 128
        return {
            "x": _w((lambda *s: torch.randn(*s, dtype=torch.bfloat16) * 1.6)(B, Sq, D)),
            "k_cache": _w((lambda *s: torch.randn(*s, dtype=torch.bfloat16) * 1.6)(B, H, Sk, Dh)),
            "v_cache": _w((lambda *s: torch.randn(*s, dtype=torch.bfloat16) * 0.05)(B, H, Sk, Dh)),
            "wq": _w((lambda *s: torch.randn(*s, dtype=torch.bfloat16) * 1.6)(D, H * Dh) * (D ** -0.5)),
            "wo": _w((lambda *s: torch.randn(*s, dtype=torch.bfloat16) * 0.05)(H * Dh, D) * ((H * Dh) ** -0.5)),
        }
    _A_in = _A_setup()

    def _B_setup():
        B, H, Sq, Sk, D, M = 4, 32, 1, 4096, 128, 576
        Dc = 4 * D
        return {
            "x": _w((lambda *s: torch.randn(*s, dtype=torch.bfloat16) * 3.2)(B, Sq, M)),
            "c_kv": _w((lambda *s: torch.randn(*s, dtype=torch.bfloat16) * 0.1)(B, Sk, Dc)),
            "w_q": _w((lambda *s: torch.randn(*s, dtype=torch.bfloat16) * 6.4)(M, H, D) * (M ** -0.5)),
            "w_uk": _w((lambda *s: torch.randn(*s, dtype=torch.bfloat16) * 6.4)(Dc, H, D) * (Dc ** -0.5)),
            "w_uv": _w((lambda *s: torch.randn(*s, dtype=torch.bfloat16) * 0.1)(Dc, H, D) * (Dc ** -0.5)),
            "w_o": _w((lambda *s: torch.randn(*s, dtype=torch.bfloat16) * 0.1)(H, D, M) * ((H * D) ** -0.5)),
        }
    _B_in = _B_setup()

    return {
        "A_x": _A_in["x"],
        "A_k_cache": _A_in["k_cache"],
        "A_v_cache": _A_in["v_cache"],
        "A_wq": _A_in["wq"],
        "A_wo": _A_in["wo"],
        "B_x": _B_in["x"],
        "B_c_kv": _B_in["c_kv"],
        "B_w_q": _B_in["w_q"],
        "B_w_uk": _B_in["w_uk"],
        "B_w_uv": _B_in["w_uv"],
        "B_w_o": _B_in["w_o"],
    }

def reference(A_x, A_k_cache, A_v_cache, A_wq, A_wo, B_x, B_c_kv, B_w_q, B_w_uk, B_w_uv, B_w_o):
    def _A_ref(x, k_cache, v_cache, wq, wo):
        B, Sq, D = x.shape
        H, Dh = 32, 128
        q = (lambda x, *d: x.permute(*d))((x @ wq).reshape(B, Sq, H, Dh), 0, 2, 1, 3)
        scores = torch.einsum("bhqd,bhkd->bhqk", q, k_cache) * (Dh ** -0.5)
        p = _softmax(scores, axis=-1)
        o = torch.einsum("bhqk,bhkd->bhqd", p, v_cache)
        return (lambda x, *d: x.permute(*d))(o, 0, 2, 1, 3).reshape(B, Sq, H * Dh) @ wo

    def _B_ref(x, c_kv, w_q, w_uk, w_uv, w_o):
        B, Sq, M = x.shape[0], x.shape[1], x.shape[2]
        Sk = c_kv.shape[1]
        H, D = w_q.shape[1], w_q.shape[2]
        q = torch.einsum("bqm,mhd->bhqd", x, w_q)
        k = torch.einsum("bkc,chd->bhkd", c_kv, w_uk)
        v = torch.einsum("bkc,chd->bhkd", c_kv, w_uv)
        scores = torch.einsum("bhqd,bhkd->bhqk", q, k) * (D ** -0.5)
        off = Sk - Sq
        qi = torch.arange(Sq)[:, None]
        kj = torch.arange(Sk)[None, :]
        cmask = kj <= (qi + off)
        scores = torch.where(cmask[None, None], scores, -1e9)
        p = _softmax(scores, axis=-1)
        ctx = torch.einsum("bhqk,bhkd->bhqd", p, v)
        return torch.einsum("bhqd,hdm->bqm", ctx, w_o)

    a = _A_ref(A_x, A_k_cache, A_v_cache, A_wq, A_wo)
    b = _B_ref(B_x, B_c_kv, B_w_q, B_w_uk, B_w_uv, B_w_o)
    return torch.concatenate([a, b], axis=-1)
```

```cpp
#define STAMP
#define DIAG 0
#include <hip/hip_runtime.h>
#include <stdint.h>
#include <utility>

typedef unsigned short u16;
typedef unsigned u32;
typedef unsigned long long u64;
typedef u32 u32x4 __attribute__((ext_vector_type(4)));
typedef u32 u32x2 __attribute__((ext_vector_type(2)));
typedef float f32x4 __attribute__((ext_vector_type(4)));
typedef float f32x2 __attribute__((ext_vector_type(2)));
typedef short s16x4 __attribute__((ext_vector_type(4)));
typedef __bf16 bf16x8 __attribute__((ext_vector_type(8)));
typedef __bf16 bf16x2 __attribute__((ext_vector_type(2)));
#define DI __device__ __forceinline__
#define RLX_AGENT __ATOMIC_RELAXED, __HIP_MEMORY_SCOPE_AGENT
extern "C" __device__ u64 hw_dispatch_id() __asm("llvm.amdgcn.dispatch.id");

template <int... Is, class F> DI void static_for_impl(std::integer_sequence<int, Is...>, F&& f) { (f(std::integral_constant<int, Is>{}), ...); }
template <int N, class F> DI void static_for(F&& f) { static_for_impl(std::make_integer_sequence<int, N>{}, f); }

constexpr int H = 32, SK = 4096, DM = 576, DC = 512;
constexpr int ACU = 7;
constexpr int NBW = 32;
constexpr int NAW = H * ACU;
constexpr int NSP = 8;
constexpr float C2 = 0.08838834764831845f * 1.4426950408889634f;

constexpr size_t WS_F1 = 0, WS_F2 = 512;
constexpr size_t WS_QABS = 4096;
constexpr size_t WS_CTXP = WS_QABS + 4 * 32 * 512 * 2;
constexpr size_t WS_MLB = WS_CTXP + (size_t)4 * NSP * 32 * 512 * 2;
constexpr size_t WS_Y = WS_MLB + 4 * NSP * 32 * 2 * 4;
constexpr size_t WS_MLA = WS_Y + (size_t)72 * 256 * 128;
constexpr size_t WS_END = WS_MLA + 256 * 32;

constexpr int CRS = 1056;
constexpr int CBUF = 64 * CRS;
constexpr int L_BIG = 0;
constexpr int L_AM = 2 * CBUF;
constexpr int L_QS = L_AM + 16 * 272;
constexpr int L_ST = L_QS + 1024;
constexpr int L_PF = L_ST + 4096;
constexpr int L_TOTAL = L_PF + 4096;
constexpr int AM_LD = 136;
constexpr int AM2_LD = 520;
constexpr int L_AM2 = 69632, L_CP = 88064, L_W8 = 106496;

#define ST(i) do { if (threadIdx.x == 0) reinterpret_cast<u64*>(a.ws + (16u << 20))[blockIdx.x * 16 + (i)] = __builtin_amdgcn_s_memrealtime(); } while (0)
struct Args {
  const u16 *xA, *K, *V, *wq, *wo, *xB, *C, *wqB, *wuk, *wuv, *woB;
  unsigned char* ws;
  u16* out;
};

DI float bf_lo(u32 x) { return __uint_as_float(x << 16); }
DI float bf_hi(u32 x) { return __uint_as_float(x & 0xffff0000u); }
DI u32 pk_bf16(float lo, float hi) { bf16x2 v = {(__bf16)lo, (__bf16)hi}; return __builtin_bit_cast(u32, v); }
DI u16 to_bf16(float x) { return __builtin_bit_cast(u16, (__bf16)x); }
DI float bf2f(u16 x) { return __uint_as_float(((u32)x) << 16); }
DI float dot2(u32 a, u32 b, float c) { return __builtin_amdgcn_fdot2_f32_bf16(__builtin_bit_cast(bf16x2, a), __builtin_bit_cast(bf16x2, b), c, false); }
template <int CTRL> DI float dpp(float x) { return __builtin_bit_cast(float, __builtin_amdgcn_update_dpp(0, __builtin_bit_cast(int, x), CTRL, 0xf, 0xf, true)); }
DI float row16_sum(float x) { x += dpp<0x128>(x); x += dpp<0x124>(x); x += dpp<0x122>(x); x += dpp<0x121>(x); return x; }
DI float row16_max(float x) { x = fmaxf(x, dpp<0x128>(x)); x = fmaxf(x, dpp<0x124>(x)); x = fmaxf(x, dpp<0x122>(x)); x = fmaxf(x, dpp<0x121>(x)); return x; }
DI float xg_max(float x) {
  auto s = __builtin_amdgcn_permlane16_swap(__float_as_uint(x), __float_as_uint(x), false, false);
  x = fmaxf(__uint_as_float(s[0]), __uint_as_float(s[1]));
  auto t = __builtin_amdgcn_permlane32_swap(__float_as_uint(x), __float_as_uint(x), false, false);
  return fmaxf(__uint_as_float(t[0]), __uint_as_float(t[1]));
}
DI float xg_sum(float x) {
  auto s = __builtin_amdgcn_permlane16_swap(__float_as_uint(x), __float_as_uint(x), false, false);
  x = __uint_as_float(s[0]) + __uint_as_float(s[1]);
  auto t = __builtin_amdgcn_permlane32_swap(__float_as_uint(x), __float_as_uint(x), false, false);
  return __uint_as_float(t[0]) + __uint_as_float(t[1]);
}
DI f32x4 mfma16(u32x4 a, u32x4 b, f32x4 c) { return __builtin_amdgcn_mfma_f32_16x16x32_bf16(__builtin_bit_cast(bf16x8, a), __builtin_bit_cast(bf16x8, b), c, 0, 0, 0); }
DI u32x4 ld16(const u16* p) { return __builtin_nontemporal_load(reinterpret_cast<const u32x4*>(p)); }
DI u32x2 ld8(const u16* p) { return __builtin_nontemporal_load(reinterpret_cast<const u32x2*>(p)); }
DI u32x2 trread(const unsigned char* p) {
  return __builtin_bit_cast(u32x2, __builtin_amdgcn_ds_read_tr16_b64_v4i16((__attribute__((address_space(3))) s16x4*)(p)));
}
DI u32 prm(u32 hi, u32 lo, int odd) { return __builtin_amdgcn_perm(hi, lo, odd ? 0x07060302u : 0x05040100u); }

DI u32x4 tcol16(const u32x4 (&R)[8], int t) {
  u32x4 T;
#pragma unroll
  for (int q = 0; q < 4; ++q) T[q] = prm(R[2 * q + 1][t >> 1], R[2 * q][t >> 1], t & 1);
  return T;
}
DI u32x4 tcol8(const u32x2 (&R)[8], int t) {
  u32x4 T;
#pragma unroll
  for (int q = 0; q < 4; ++q) T[q] = prm(R[2 * q + 1][t >> 1], R[2 * q][t >> 1], t & 1);
  return T;
}

#define SPINLOG 22
#define PSL 2
#define YAUX 16
constexpr u32 SPIN_CAP = 1u << SPINLOG;
DI void wave_wait(const u64* f, bool active, u64 tag) {
  for (u32 spins = 0; spins < SPIN_CAP; ++spins) {
    const u64 v = active ? __hip_atomic_load(f, RLX_AGENT) : tag;
    if (__all(v == tag)) break;
    __builtin_amdgcn_s_sleep(PSL);
  }
  __builtin_amdgcn_fence(__ATOMIC_ACQUIRE, "agent");
}
DI void wg_wait64(const unsigned char* flags, u64 tag, int pw = 0) {
  if ((int)(threadIdx.x >> 6) == pw) {
    wave_wait(reinterpret_cast<const u64*>(flags) + (threadIdx.x & (NBW - 1)), true, tag);
    asm volatile("s_waitcnt vmcnt(0)" ::: "memory");
  }
  __syncthreads();
}
DI void st16_sc1(__amdgpu_buffer_rsrc_t rs, size_t off, u32x4 v) { __builtin_amdgcn_raw_buffer_store_b128(v, rs, (int)off, 0, 16); }
DI void st8_sc1(unsigned char* p, f32x2 v) { __hip_atomic_store(reinterpret_cast<u64*>(p), __builtin_bit_cast(u64, v), RLX_AGENT); }

struct QPre { u32x4 R0[8]; u32x4 R1; u32x2 xa, xb; u32 x1; };
DI void qproj_load(QPre& Q, const u16* x, const u16* Wh, int w, int lane) {
  const int j = lane & 15, g = lane >> 4;
  Q.xa = u32x2{0u, 0u}; Q.xb = u32x2{0u, 0u}; Q.x1 = 0u;
  if (j < 4) { Q.xa = ld8(x + j * DM + 36 * w + 8 * g); Q.xb = ld8(x + j * DM + 36 * w + 8 * g + 4); Q.x1 = x[j * DM + 36 * w + 32 + g]; }
#pragma unroll
  for (int jj = 0; jj < 8; ++jj) Q.R0[jj] = ld16(Wh + (size_t)(36 * w + 8 * g + jj) * 4096 + 8 * j);
  Q.R1 = ld16(Wh + (size_t)(36 * w + 32 + g) * 4096 + 8 * j);
}
DI void qproj_mfma(const QPre& Q, unsigned char* lds, int w, int lane) {
  const int j = lane & 15, g = lane >> 4;
  f32x4 acc[8];
  {
    const u32x4 a = {Q.xa[0], Q.xa[1], Q.xb[0], Q.xb[1]};
#pragma unroll
    for (int t = 0; t < 8; ++t) acc[t] = mfma16(a, tcol16(Q.R0, t), f32x4{0.f, 0.f, 0.f, 0.f});
    const u32x4 a1 = {Q.x1, 0u, 0u, 0u};
#pragma unroll
    for (int t = 0; t < 8; ++t) { const u32x4 bb = {(t & 1) ? (Q.R1[t >> 1] >> 16) : (Q.R1[t >> 1] & 0xffffu), 0u, 0u, 0u}; acc[t] = mfma16(a1, bb, acc[t]); }
  }
  float* qpart = reinterpret_cast<float*>(lds + L_BIG);
  if (g == 0) {
#pragma unroll
    for (int r = 0; r < 4; ++r) {
      float* p = qpart + (w * 4 + r) * 128 + 8 * j;
      *reinterpret_cast<f32x4*>(p) = f32x4{acc[0][r], acc[1][r], acc[2][r], acc[3][r]};
      *reinterpret_cast<f32x4*>(p + 4) = f32x4{acc[4][r], acc[5][r], acc[6][r], acc[7][r]};
    }
  }
  __builtin_amdgcn_sched_barrier(0);
}
DI void qproj_finish(unsigned char* lds) {
  const float* qpart = reinterpret_cast<const float*>(lds + L_BIG);
  __syncthreads();
  if (threadIdx.x < 512) {
    float s = 0.f;
#pragma unroll
    for (int ww = 0; ww < 16; ++ww) s += qpart[ww * 512 + threadIdx.x];
    reinterpret_cast<u16*>(lds + L_QS)[threadIdx.x] = to_bf16(s);
  }
  __syncthreads();
}

DI void wo_load(const u16* Wo, u32x2 (&R)[4][8], int nb, int lane, int s0, int s1) {
  const int j = lane & 15, g = lane >> 4;
#pragma unroll
  for (int s = 0; s < 4; ++s) if (s >= s0 && s < s1)
#pragma unroll
    for (int jj = 0; jj < 8; ++jj) R[s][jj] = ld8(Wo + (size_t)(32 * s + 8 * g + jj) * DM + 64 * nb + 4 * j);
}
DI void wo_mfma(const u32x2 (&R)[4][8], const unsigned char* lds, int lane, f32x4 (&acc)[4]) {
  const int j = lane & 15, g = lane >> 4;
#pragma unroll
  for (int t = 0; t < 4; ++t) acc[t] = f32x4{0.f, 0.f, 0.f, 0.f};
#pragma unroll
  for (int s = 0; s < 4; ++s) {
    const u32x4 a = *reinterpret_cast<const u32x4*>(lds + L_AM + j * 272 + (32 * s + 8 * g) * 2);
#pragma unroll
    for (int t = 0; t < 4; ++t) acc[t] = mfma16(a, tcol8(R[s], t), acc[t]);
  }
}
DI u32x4 yrow(const f32x4 (&acc)[4]) {
  const f32x4 o = {acc[0][0] + acc[0][1], acc[1][0] + acc[1][1], acc[2][0] + acc[2][1], acc[3][0] + acc[3][1]};
  return __builtin_bit_cast(u32x4, o);
}
DI void am_zero(unsigned char* lds, int tid) {
  if (tid < 136) { const int pr = tid / 34; *reinterpret_cast<u32x4*>(lds + L_AM + (4 * pr + 2) * 272 + (tid - 34 * pr) * 16) = u32x4{0u, 0u, 0u, 0u}; }
}
DI void st16_y(__amdgpu_buffer_rsrc_t rs, int off, u32x4 v) { __builtin_amdgcn_raw_buffer_store_b128(v, rs, off, 0, YAUX); }
DI float rdlane(float x, int l) { return __builtin_bit_cast(float, __builtin_amdgcn_readlane(__builtin_bit_cast(int, x), l)); }

constexpr int NS = 10;
constexpr int NSEQ = 8 * NS;
constexpr int DEPTH = 2, RING = 12, RAMP = 2;
constexpr int issued(int i) {
  int d = RING - (i + RAMP - 1) / RAMP; if (d < DEPTH) d = DEPTH;
  int e = i + d; if (e < RING) e = RING; return e > NSEQ ? NSEQ : e;
}
DI void part_a(const Args& a, unsigned char* lds, int idx, u64 tag) {
  const int tid = threadIdx.x, lane = tid & 63, w = __builtin_amdgcn_readfirstlane(tid >> 6);
  const int j = lane & 15, g = lane >> 4;
  const int h = (idx & 7) * 4 + ((idx >> 3) & 3), ci = idx >> 5;
  const int U0 = (ci * 1024) / ACU, NUN = ((ci + 1) * 1024) / ACU - U0;
  const int rot = (h + ci) % NS;
  const int bad_sl = (16 * (NS - 1) + w < NUN) ? -1 : NS - 1 - rot;
  const __amdgpu_buffer_rsrc_t rs = __builtin_amdgcn_make_buffer_rsrc(a.ws, 0, (int)WS_END, 0x00020000);
  const u16* Wq = a.wq + h * 128;
  QPre Q;
  ST(0);
  qproj_load(Q, a.xA, Wq, w, lane);
  const size_t base = ((size_t)h * SK + 4 * U0) * 128;
  const int voff = lane * 16;
  u32x4 R[RING];
#define A_ISSUE(i) R[(i) % RING] = __builtin_amdgcn_raw_buffer_load_b128( \
    __builtin_amdgcn_make_buffer_rsrc(const_cast<u16*>(((((i) / NS) & 1) ? a.V : a.K) + base + (size_t)((i) / (2 * NS)) * H * SK * 128), 0, NUN * 1024, 0x00020000), \
    voff, (((i) % NS) + rot >= NS ? ((i) % NS) + rot - NS : ((i) % NS) + rot) * 16384 + w * 1024, 2)
  constexpr int PRE = RING / 2;
#pragma unroll
  for (int t = 0; t < PRE; ++t) A_ISSUE(t);
  __builtin_amdgcn_sched_barrier(0);
  am_zero(lds, tid);
  qproj_mfma(Q, lds, w, lane);
#pragma unroll
  for (int t = PRE; t < RING; ++t) A_ISSUE(t);
  __builtin_amdgcn_sched_barrier(0);
  qproj_finish(lds);
  ST(1);
  float* pacc = reinterpret_cast<float*>(lds + L_BIG);
  float* pml = reinterpret_cast<float*>(lds + L_ST);
  u32x4 q;
  float m, l, acc[8], s[NS];
  u32x2 RW[4][8];
  const u16* Wo = a.wo + (size_t)h * 128 * DM;
  static_for<NSEQ>([&](auto ic) __attribute__((always_inline)) {
    constexpr int i = ic.value, bb = i / (2 * NS), pass = (i / NS) & 1, sl = i % NS;
    const u32x4 r = R[i % RING];
    if constexpr (pass == 0) {
      if constexpr (sl == 0) { q = *reinterpret_cast<const u32x4*>(lds + L_QS + (bb * 128 + 8 * j) * 2); m = -INFINITY; }
      float d = dot2(r[0], q[0], 0.f); d = dot2(r[1], q[1], d); d = dot2(r[2], q[2], d); d = dot2(r[3], q[3], d);
      asm volatile("" : "+v"(d));
      if constexpr (issued(i + 1) > issued(i)) A_ISSUE(issued(i));
      d = row16_sum(d) * C2;
      d = (sl == bad_sl) ? -INFINITY : d;
      s[sl] = d; m = fmaxf(m, d);
      if constexpr (sl == NS - 1) {
        m = xg_max(m); l = 0.f;
#pragma unroll
        for (int t = 0; t < NS; ++t) { s[t] = __builtin_amdgcn_exp2f(s[t] - m); l += s[t]; }
        l = xg_sum(l);
        if (lane == 0) *reinterpret_cast<f32x2*>(pml + (bb * 16 + w) * 2) = f32x2{m, l};
#pragma unroll
        for (int k = 0; k < 8; ++k) acc[k] = 0.f;
      }
    } else {
      const float p = s[sl];
#pragma unroll
      for (int k = 0; k < 4; ++k) { acc[2 * k] = fmaf(p, bf_lo(r[k]), acc[2 * k]); acc[2 * k + 1] = fmaf(p, bf_hi(r[k]), acc[2 * k + 1]); }
      asm volatile("" : "+v"(acc[0]), "+v"(acc[1]), "+v"(acc[2]), "+v"(acc[3]), "+v"(acc[4]), "+v"(acc[5]), "+v"(acc[6]), "+v"(acc[7]));
      if constexpr (issued(i + 1) > issued(i)) A_ISSUE(issued(i));
      if (sl == 4) { if (w < 9) wo_load(Wo, RW, w, lane, bb, bb + 1); }
      if constexpr (sl == NS - 1) {
#pragma unroll
        for (int k = 0; k < 8; ++k) acc[k] = xg_sum(acc[k]);
        if (g == 0) {
          float* pp = pacc + (bb * 16 + w) * 128 + 8 * j;
          *reinterpret_cast<f32x4*>(pp) = f32x4{acc[0], acc[1], acc[2], acc[3]};
          *reinterpret_cast<f32x4*>(pp + 4) = f32x4{acc[4], acc[5], acc[6], acc[7]};
        }
      }
    }
    __builtin_amdgcn_sched_barrier(0);
  });
#undef A_ISSUE
  if (lane == 0) reinterpret_cast<u64*>(a.ws + (17u << 20))[blockIdx.x * 16 + w] = __builtin_amdgcn_s_memrealtime();
  ST(2);
  __syncthreads();
  ST(3);
  if (w < 8) {
    const int bb = w >> 1, d = tid & 127;
    u16* Am = reinterpret_cast<u16*>(lds + L_AM);
    const f32x2 ml = *reinterpret_cast<const f32x2*>(pml + (bb * 16 + j) * 2);
    float pv[16];
#pragma unroll
    for (int i = 0; i < 16; ++i) pv[i] = pacc[(bb * 16 + i) * 128 + d];
    const float wt = __builtin_amdgcn_exp2f(ml[0] - row16_max(ml[0]));
    float o0 = 0.f, o1 = 0.f;
#pragma unroll
    for (int i = 0; i < 16; i += 2) { o0 = fmaf(rdlane(wt, i), pv[i], o0); o1 = fmaf(rdlane(wt, i + 1), pv[i + 1], o1); }
    const float o = o0 + o1;
    const u16 hi = to_bf16(o); const u16 lo = to_bf16(o - bf2f(hi));
    Am[(4 * bb) * AM_LD + d] = hi; Am[(4 * bb + 1) * AM_LD + d] = lo;
  } else if (w == 9) {
    const f32x2 ml = *reinterpret_cast<const f32x2*>(pml + lane * 2);
    const float M = row16_max(ml[0]);
    const float L = row16_sum(__builtin_amdgcn_exp2f(ml[0] - M) * ml[1]);
    if (j == 0) __builtin_amdgcn_raw_buffer_store_b64(__builtin_bit_cast(u32x2, f32x2{M, L}), rs, (int)WS_MLA + (((g >> 1) * 32 + h) * 2 + (ci >> 2)) * 64 + (g & 1) * 32 + (ci & 3) * 8, 0, YAUX);
  }
  __syncthreads();
  if (w < 9) {
    f32x4 ya[4];
    wo_mfma(RW, lds, lane, ya);
    st16_y(rs, (int)WS_Y + (((8 * w + (j >> 1)) * 2 + (g >> 1)) * 256 + h * 8 + 2 * (ci & 3) + (ci >> 2)) * 64 + (g & 1) * 32 + (j & 1) * 16, yrow(ya));
  }
  ST(4);
}

template <bool LD> DI void part_b(const Args& a, unsigned char* lds, int wg, u64 tag) {
  const int tid = threadIdx.x, lane = tid & 63, w = __builtin_amdgcn_readfirstlane(tid >> 6);
  const int j = lane & 15, g = lane >> 4;
  const __amdgpu_buffer_rsrc_t rs = __builtin_amdgcn_make_buffer_rsrc(a.ws, 0, (int)WS_END, 0x00020000);
  const int h = wg;
  const int h3 = wg ^ 2;
  const int b2 = wg >> 3, sp = wg & 7;
  const u16* cl = a.C + ((size_t)b2 * SK + sp * 512 + 8 * (w - 8)) * DC + lane * 8;
  u32x4 X[16];
  u32x2 RW[4][8];
  u32x4 W8[2];
  u32* cnt = reinterpret_cast<u32*>(lds + L_ST + 4064);
#define C_ISSUE(T) _Pragma("unroll") for (int r = 0; r < 8; ++r) X[8 * ((T) & 1) + r] = ld16(cl + (size_t)(64 * (T) + r) * DC)
  u32 sb0 = L_BIG + 8 * (w - 8) * CRS + lane * 16, sb1 = sb0 + CBUF;
  asm volatile("" : "+v"(sb0), "+v"(sb1));
#define C_STAGE(T) _Pragma("unroll") for (int r = 0; r < 8; ++r) *reinterpret_cast<u32x4*>(lds + (((T) & 1) ? sb1 : sb0) + r * CRS) = X[8 * ((T) & 1) + r]
  {
    const u16* Wq = a.wqB + h * 128;
    u32x4 U[8];
    QPre Q;
    if constexpr (!LD) { ST(0); }
    qproj_load(Q, a.xB, Wq, w, lane);
#pragma unroll
    for (int i = 0; i < 8; ++i) U[i] = ld16(a.wuk + ((size_t)(32 * w + 4 * i + g) * H + h) * 128 + 8 * j);
    __builtin_amdgcn_sched_barrier(0);
    if constexpr (!LD) am_zero(lds, tid);
    qproj_mfma(Q, lds, w, lane);
    if constexpr (LD) { C_ISSUE(0); }
    __builtin_amdgcn_sched_barrier(0);
    qproj_finish(lds);
    u32x4 qv[4];
#pragma unroll
    for (int bb = 0; bb < 4; ++bb) qv[bb] = *reinterpret_cast<const u32x4*>(lds + L_QS + (bb * 128 + 8 * j) * 2);
    u16* qa = reinterpret_cast<u16*>(lds + L_PF);
#pragma unroll
    for (int i = 0; i < 8; ++i) {
      float v = 0.f;
#pragma unroll
      for (int bb = 0; bb < 4; ++bb) {
        float d = dot2(U[i][0], qv[bb][0], 0.f); d = dot2(U[i][1], qv[bb][1], d); d = dot2(U[i][2], qv[bb][2], d); d = dot2(U[i][3], qv[bb][3], d);
        d = row16_sum(d);
        v = (j == bb) ? d : v;
      }
      if (j < 4) qa[j * 512 + 32 * w + 4 * i + g] = to_bf16(v);
    }
    __builtin_amdgcn_sched_barrier(0);
    if constexpr (LD) { asm volatile("s_waitcnt vmcnt(0)" ::: "memory"); C_ISSUE(1); }
    __syncthreads();
    if constexpr (!LD) {
      if (w == 0) {
#pragma unroll
        for (int bb = 0; bb < 4; ++bb) st16_sc1(rs, WS_QABS + ((bb * H + h) * 512 + lane * 8) * 2, *reinterpret_cast<const u32x4*>(qa + bb * 512 + lane * 8));
        asm volatile("s_waitcnt vmcnt(0)" ::: "memory");
        if (lane == 0) { __hip_atomic_store(reinterpret_cast<u64*>(a.ws + WS_F1 + wg * 8), tag, RLX_AGENT); *cnt = 0u; }
      }
      __builtin_amdgcn_sched_barrier(0);
      __builtin_amdgcn_s_barrier();
    } else {
      C_STAGE(0);
      __builtin_amdgcn_sched_barrier(0);
      __builtin_amdgcn_s_barrier();
      C_ISSUE(2);
    }
    if constexpr (!LD) { ST(1); }
  }
  {
    const int b = b2;
    if constexpr (!LD) wg_wait64(a.ws + WS_F1, tag); else __syncthreads();
    if constexpr (!LD) { ST(2); }
    const int kt = w & 3, hh = (w >> 2) & 1;
    float* stmax = reinterpret_cast<float*>(lds + L_ST);
    float* stsum = stmax + 128;
    float* salpha = stmax + 256;
    unsigned char* Pf = lds + L_PF;
    float M = -INFINITY, lsum = 0.f;
    f32x4 acc[2][2];
#pragma unroll
    for (int ct = 0; ct < 2; ++ct) { acc[ct][0] = f32x4{0.f, 0.f, 0.f, 0.f}; acc[ct][1] = f32x4{0.f, 0.f, 0.f, 0.f}; }
    const int troff = (4 * g + (j >> 2)) * CRS + (32 * w + 4 * (j & 3)) * 2;
    auto pv_step = [&](const unsigned char* cb) __attribute__((always_inline)) {
      const f32x4 al0 = *reinterpret_cast<const f32x4*>(salpha + 4 * g), al1 = *reinterpret_cast<const f32x4*>(salpha + 16 + 4 * g);
#pragma unroll
      for (int ct = 0; ct < 2; ++ct) { acc[ct][0] *= al0; acc[ct][1] *= al1; }
#pragma unroll
      for (int blk = 0; blk < 2; ++blk) {
        const u32x4 p0 = *reinterpret_cast<const u32x4*>(Pf + ((blk * 2 + 0) * 64 + lane) * 16);
        const u32x4 p1 = *reinterpret_cast<const u32x4*>(Pf + ((blk * 2 + 1) * 64 + lane) * 16);
#pragma unroll
        for (int ct = 0; ct < 2; ++ct) {
          const u32x2 lo = trread(cb + troff + (32 * blk) * CRS + 32 * ct);
          const u32x2 hi = trread(cb + troff + (32 * blk + 16) * CRS + 32 * ct);
          const u32x4 bf = {lo[0], lo[1], hi[0], hi[1]};
          acc[ct][0] = mfma16(p0, bf, acc[ct][0]); acc[ct][1] = mfma16(p1, bf, acc[ct][1]);
        }
      }
    };
    unsigned char* Qf = lds + L_BIG + CBUF;
    if constexpr (!LD) {
      u32x4 qv[4];
#pragma unroll
      for (int k = 0; k < 4; ++k) qv[k] = *reinterpret_cast<const u32x4*>(a.ws + WS_QABS + ((size_t)b * H * 512 + (tid + 512 * k) * 8) * 2);
#pragma unroll
      for (int k = 0; k < 4; ++k) { const int e = tid + 512 * k, head = e >> 6, ch = e & 63;
        *reinterpret_cast<u32x4*>(Qf + ((head >> 4) * 16 + (ch >> 2)) * 1040 + ((ch & 3) * 16 + (head & 15)) * 16) = qv[k]; }
    }
    __syncthreads();
    if constexpr (!LD) {
      u32x4 QF[16];
#pragma unroll
      for (int s = 0; s < 16; ++s) QF[s] = *reinterpret_cast<const u32x4*>(Qf + (hh * 16 + s) * 1040 + lane * 16);
      auto wuv_load = [&]() __attribute__((always_inline)) {
#pragma unroll
        for (int k2 = 0; k2 < 2; ++k2)
#pragma unroll
          for (int jj = 0; jj < 8; ++jj) {
            const u32x4 v = ld16(a.wuv + ((size_t)(64 * w + 32 * k2 + 8 * g + jj) * H + h3) * 128 + 8 * j);
            RW[2 * k2][jj] = u32x2{v[0], v[1]}; RW[2 * k2 + 1][jj] = u32x2{v[2], v[3]};
          }
      };
      auto s_tile = [&](const unsigned char* cb, bool last) __attribute__((always_inline)) {
        __syncthreads();
        f32x4 s0 = {0.f, 0.f, 0.f, 0.f}, s1 = {0.f, 0.f, 0.f, 0.f};
#pragma unroll
        for (int s = 0; s < 16; s += 2) {
          s0 = mfma16(*reinterpret_cast<const u32x4*>(cb + (16 * kt + j) * CRS + (32 * s + 8 * g) * 2), QF[s], s0);
          s1 = mfma16(*reinterpret_cast<const u32x4*>(cb + (16 * kt + j) * CRS + (32 * s + 32 + 8 * g) * 2), QF[s + 1], s1);
        }
        const f32x4 sv = (s0 + s1) * C2;
        const float v = xg_max(fmaxf(fmaxf(sv[0], sv[1]), fmaxf(sv[2], sv[3])));
        if (g == 0) stmax[kt * 32 + 16 * hh + j] = v;
        __syncthreads();
        const float* sm = stmax + 16 * hh + j;
        const float Mn = fmaxf(fmaxf(M, fmaxf(sm[0], sm[32])), fmaxf(sm[64], sm[96]));
        const float alpha = __builtin_amdgcn_exp2f(M - Mn);
        M = Mn;
        u32x2 pk;
        pk[0] = pk_bf16(__builtin_amdgcn_exp2f(sv[0] - Mn), __builtin_amdgcn_exp2f(sv[1] - Mn));
        pk[1] = pk_bf16(__builtin_amdgcn_exp2f(sv[2] - Mn), __builtin_amdgcn_exp2f(sv[3] - Mn));
        lsum = lsum * alpha + ((bf_lo(pk[0]) + bf_hi(pk[0])) + (bf_lo(pk[1]) + bf_hi(pk[1])));
        *reinterpret_cast<u32x2*>(Pf + (((kt >> 1) * 2 + hh) * 64 + lane) * 16 + (kt & 1) * 8) = pk;
        if (kt == 0 && g == 0) salpha[16 * hh + j] = alpha;
        __syncthreads();
        if (last) { __builtin_amdgcn_sched_barrier(0); wuv_load(); __builtin_amdgcn_sched_barrier(0); }
        pv_step(cb);
      };
#pragma unroll 1
      for (int T = 0; T < 6; T += 2) { s_tile(lds + L_BIG, false); s_tile(lds + L_BIG + CBUF, false); }
      s_tile(lds + L_BIG, false); s_tile(lds + L_BIG + CBUF, true);
    } else {
#pragma unroll
      for (int T = 0; T < 8; ++T) {
        __syncthreads();
        if (T + 1 < 8) { C_STAGE(T + 1); if (T + 3 < 8) { C_ISSUE(T + 3); } }
        if (T == 5) wo_load(a.woB + (size_t)h3 * 128 * DM, RW, w - 8, lane, 0, 2);
        if (T == 6) wo_load(a.woB + (size_t)h3 * 128 * DM, RW, w - 8, lane, 2, 4);
        if (T == 6) {
#pragma unroll
          for (int k = 0; k < 2; ++k) W8[k] = ld16(a.woB + ((size_t)h3 * 128 + 16 * (w - 8) + 8 * k + (lane >> 3)) * DM + 512 + (lane & 7) * 8);
        }
        __syncthreads();
        __syncthreads();
        pv_step(lds + L_BIG + (T & 1) * CBUF);
      }
    }
#undef C_ISSUE
#undef C_STAGE
    if constexpr (!LD) { ST(3); }
    __syncthreads();
    if constexpr (!LD) { lsum = xg_sum(lsum); if (g == 0) stsum[kt * 32 + 16 * hh + j] = lsum; if (kt == 0 && g == 0) salpha[32 + 16 * hh + j] = M; }
    float* tb = reinterpret_cast<float*>(lds + L_BIG);
#pragma unroll
    for (int ct = 0; ct < 2; ++ct)
#pragma unroll
      for (int h2 = 0; h2 < 2; ++h2)
#pragma unroll
        for (int r = 0; r < 4; ++r) tb[(16 * h2 + 4 * g + r) * 516 + 32 * w + 16 * ct + j] = acc[ct][h2][r];
    if constexpr (!LD) { for (int i = tid; i < 8 * AM2_LD * 2 / 16; i += 512) reinterpret_cast<u32x4*>(lds + L_AM2 + 8 * AM2_LD * 2)[i] = u32x4{0u, 0u, 0u, 0u}; }
    __syncthreads();
    if constexpr (!LD) { ST(4); }
    if constexpr (LD) {
      const int t2 = tid - 512;
#pragma unroll
      for (int k = 0; k < 4; ++k) {
        const int e = t2 + 512 * k, head = e >> 6, c8 = e & 63;
        const f32x4 v0 = *reinterpret_cast<const f32x4*>(tb + head * 516 + 8 * c8), v1 = *reinterpret_cast<const f32x4*>(tb + head * 516 + 8 * c8 + 4);
        st16_sc1(rs, WS_CTXP + ((size_t)((b * NSP + sp) * 32 + head) * 512 + 8 * c8) * 2, u32x4{pk_bf16(v0[0], v0[1]), pk_bf16(v0[2], v0[3]), pk_bf16(v1[0], v1[1]), pk_bf16(v1[2], v1[3])});
      }
      if (t2 < 32) {
        const float L = (stsum[t2] + stsum[32 + t2]) + (stsum[64 + t2] + stsum[96 + t2]);
        st8_sc1(a.ws + WS_MLB + ((size_t)(b * NSP + sp) * 32 + t2) * 8, f32x2{salpha[32 + t2], L});
      }
      asm volatile("s_waitcnt vmcnt(0)" ::: "memory");
      if (lane == 0) {
        if (__hip_atomic_fetch_add(cnt, 1u, __ATOMIC_RELAXED, __HIP_MEMORY_SCOPE_WORKGROUP) == 7u) {
          __hip_atomic_store(reinterpret_cast<u64*>(a.ws + WS_F2 + wg * 8), tag, RLX_AGENT);
          reinterpret_cast<u64*>(a.ws + (16u << 20))[blockIdx.x * 16 + 5] = __builtin_amdgcn_s_memrealtime();
        }
      }
#pragma unroll
      for (int k = 0; k < 2; ++k) *reinterpret_cast<u32x4*>(lds + L_W8 + (16 * (w - 8) + 8 * k + (lane >> 3)) * 144 + (lane & 7) * 16) = W8[k];
    }
  }
  {
    int tid = threadIdx.x; asm volatile("" : "+v"(tid));
    const int lane = tid & 63, j = lane & 15, g = lane >> 4;
    u16* Am2 = reinterpret_cast<u16*>(lds + L_AM2);
    float* cpart = reinterpret_cast<float*>(lds + L_CP);
    __builtin_amdgcn_sched_barrier(0);
    if constexpr (LD) {
      wg_wait64(a.ws + WS_F2, tag, 8);
      const int b = (tid >> 7) & 3, c4 = tid & 127;
      u32x2 pv[NSP];
      const f32x2 ml = *reinterpret_cast<const f32x2*>(a.ws + WS_MLB + ((size_t)(b * NSP + (j & 7)) * 32 + h3) * 8);
#pragma unroll
      for (int s = 0; s < NSP; ++s) pv[s] = *reinterpret_cast<const u32x2*>(a.ws + WS_CTXP + (((size_t)(b * NSP + s) * 32 + h3) * 512 + 4 * c4) * 2);
      const float M = row16_max(ml[0]);
      float wt = __builtin_amdgcn_exp2f(ml[0] - M);
      wt *= 2.f / row16_sum(wt * ml[1]);
      float c0 = 0.f, c1 = 0.f, c2 = 0.f, c3 = 0.f;
#pragma unroll
      for (int s = 0; s < NSP; ++s) { const float ws_ = rdlane(wt, s); c0 = fmaf(ws_, bf_lo(pv[s][0]), c0); c1 = fmaf(ws_, bf_hi(pv[s][0]), c1); c2 = fmaf(ws_, bf_lo(pv[s][1]), c2); c3 = fmaf(ws_, bf_hi(pv[s][1]), c3); }
      const u32 h0 = pk_bf16(c0, c1), h1 = pk_bf16(c2, c3);
      *reinterpret_cast<u32x2*>(Am2 + (2 * b) * AM2_LD + 4 * c4) = u32x2{h0, h1};
      *reinterpret_cast<u32x2*>(Am2 + (2 * b + 1) * AM2_LD + 4 * c4) = u32x2{pk_bf16(c0 - bf_lo(h0), c1 - bf_hi(h0)), pk_bf16(c2 - bf_lo(h1), c3 - bf_hi(h1))};
      __syncthreads();
      __syncthreads();
      const int t2 = tid - 512;
      float s = 0.f;
#pragma unroll
      for (int ww = 0; ww < 8; ++ww) s += cpart[ww * 512 + t2];
      u16* Am = reinterpret_cast<u16*>(lds + L_AM);
      const int bb = t2 >> 7, d = t2 & 127;
      const u16 hi = to_bf16(s);
      Am[(4 * bb) * AM_LD + d] = hi; Am[(4 * bb + 1) * AM_LD + d] = to_bf16(s - bf2f(hi));
    } else {
      __builtin_amdgcn_s_barrier();
      if constexpr (!LD) { ST(6); }
      __syncthreads();
      f32x4 acc[8];
#pragma unroll
      for (int t = 0; t < 8; ++t) acc[t] = f32x4{0.f, 0.f, 0.f, 0.f};
#pragma unroll
      for (int k2 = 0; k2 < 2; ++k2) {
        const u32x4 af = *reinterpret_cast<const u32x4*>(Am2 + j * AM2_LD + 64 * w + 32 * k2 + 8 * g);
#pragma unroll
        for (int t = 0; t < 8; ++t) acc[t] = mfma16(af, tcol8(RW[2 * k2 + (t >> 2)], t & 3), acc[t]);
      }
      if (g < 2) {
#pragma unroll
        for (int bb = 0; bb < 2; ++bb) {
          float* p = cpart + (w * 4 + 2 * g + bb) * 128 + 8 * j;
          *reinterpret_cast<f32x4*>(p) = f32x4{acc[0][2 * bb] + acc[0][2 * bb + 1], acc[1][2 * bb] + acc[1][2 * bb + 1], acc[2][2 * bb] + acc[2][2 * bb + 1], acc[3][2 * bb] + acc[3][2 * bb + 1]};
          *reinterpret_cast<f32x4*>(p + 4) = f32x4{acc[4][2 * bb] + acc[4][2 * bb + 1], acc[5][2 * bb] + acc[5][2 * bb + 1], acc[6][2 * bb] + acc[6][2 * bb + 1], acc[7][2 * bb] + acc[7][2 * bb + 1]};
        }
      }
      __builtin_amdgcn_sched_barrier(0);
      __syncthreads();
      if (w == 0) {
#pragma unroll
        for (int s = 0; s < 4; ++s)
#pragma unroll
          for (int jj = 0; jj < 8; ++jj) RW[s][jj] = *reinterpret_cast<const u32x2*>(lds + L_W8 + (32 * s + 8 * g + jj) * 144 + 8 * j);
      }
    }
    __syncthreads();
    if (LD || w == 0) {
      f32x4 ya[4];
      wo_mfma(RW, lds, lane, ya);
      st16_y(rs, (int)WS_Y + (((8 * (LD ? w - 8 : 8) + (j >> 1)) * 2 + (g >> 1)) * 256 + h3 * 8 + 7) * 64 + (g & 1) * 32 + (j & 1) * 16, yrow(ya));
    }
    if constexpr (!LD) { ST(7); }
  }
}

DI float red4(f32x4 x) {
  auto s0 = __builtin_amdgcn_permlane32_swap(__float_as_uint(x[0]), __float_as_uint(x[1]), false, false);
  auto s1 = __builtin_amdgcn_permlane32_swap(__float_as_uint(x[2]), __float_as_uint(x[3]), false, false);
  const float r0 = __uint_as_float(s0[0]) + __uint_as_float(s0[1]), r1 = __uint_as_float(s1[0]) + __uint_as_float(s1[1]);
  auto s2 = __builtin_amdgcn_permlane16_swap(__float_as_uint(r0), __float_as_uint(r1), false, false);
  float r = __uint_as_float(s2[0]) + __uint_as_float(s2[1]);
  r += dpp<0x124>(r); r += dpp<0x128>(r);
  return r;
}
extern "C" __global__ void __launch_bounds__(256) k_final(unsigned char* ws, u16* out) {
  __shared__ float red[4][32];
  const int tid = threadIdx.x, lane = tid & 63, wv = tid >> 6, id = blockIdx.x;
  if (tid == 0) reinterpret_cast<u64*>(ws + (19u << 20))[id * 2] = __builtin_amdgcn_s_memrealtime();
  const int c8 = id >> 1, bp = id & 1, half = (lane >> 3) & 1, hd = 8 * wv + 2 * (lane >> 4) + ((lane >> 2) & 1);
  u32 yo = (u32)WS_Y + id * 16384 + hd * 512 + half * 64 + (lane & 3) * 16, mo = (u32)WS_MLA + bp * 4096 + hd * 128 + half * 64 + (lane & 2) * 16;
  asm volatile("" : "+v"(yo), "+v"(mo));
  f32x4 v[4], ml[2];
#pragma unroll
  for (int i = 0; i < 2; ++i) ml[i] = *reinterpret_cast<const f32x4*>(ws + mo + i * 16);
#pragma unroll
  for (int i = 0; i < 4; ++i) v[i] = *reinterpret_cast<const f32x4*>(ws + yo + i * 128);
  __builtin_amdgcn_sched_barrier(0);
  if (id == 0 && tid < 128) reinterpret_cast<u64*>(ws)[tid] = 0ull;
  const float m3 = half ? -INFINITY : ml[1][2], l3 = half ? 0.f : ml[1][3];
  float M = fmaxf(fmaxf(ml[0][0], ml[0][2]), fmaxf(ml[1][0], m3));
  M = fmaxf(M, dpp<0x128>(M));
  const float e0 = __builtin_amdgcn_exp2f(ml[0][0] - M), e1 = __builtin_amdgcn_exp2f(ml[0][2] - M), e2 = __builtin_amdgcn_exp2f(ml[1][0] - M), e3 = __builtin_amdgcn_exp2f(m3 - M);
  float L = fmaf(e0, ml[0][1], e1 * ml[0][3]) + fmaf(e2, ml[1][1], e3 * l3);
  L += dpp<0x128>(L);
  const f32x4 z = {0.f, 0.f, 0.f, 0.f};
  const f32x4 ya = ((v[0] * e0 + v[1] * e1) + (v[2] * e2 + (half ? z : v[3]) * e3)) * __builtin_amdgcn_rcpf(L);
  const float ra = red4(ya), rb = red4(half ? v[3] : z);
  if ((lane & 8) == 0) red[wv][(lane & 4) * 4 + (lane & 3) * 4 + (((lane >> 4) & 1) * 2 + (lane >> 5))] = (lane & 4) ? rb : ra;
  __syncthreads();
  if (tid < 32) out[(2 * bp + ((tid >> 3) & 1)) * 1152 + (tid >> 4) * 576 + c8 * 8 + (tid & 7)] = to_bf16((red[0][tid] + red[1][tid]) + (red[2][tid] + red[3][tid]));
  if (tid == 0) reinterpret_cast<u64*>(ws + (19u << 20))[id * 2 + 1] = __builtin_amdgcn_s_memrealtime();

  if (id == 0) {
    const u64 t_end = __builtin_amdgcn_s_memrealtime();
    __shared__ u64 sh[256]; __shared__ u64 sx[256];
    const u64* st = reinterpret_cast<const u64*>(ws + (16u << 20)) + tid * 16;
    sh[tid] = st[15];
    __syncthreads();
    u64 t0 = ~0ull; for (int i = 0; i < 256; ++i) t0 = sh[i] < t0 ? sh[i] : t0;
    const bool isB = tid < NBW;
    u64 x = 0; int mode = 0;
    if (DIAG == 1) x = t_end - t0;
    if (DIAG == 2) x = st[isB ? 7 : 4] - t0;
    if (DIAG == 3) { x = isB ? 0 : st[1] - t0; mode = 1; }
    if (DIAG == 4) { x = isB ? 0 : st[4] - t0; mode = 1; }
    if (DIAG == 5) x = isB ? st[1] - t0 : 0;
    if (DIAG == 6) x = isB ? st[3] - t0 : 0;
    if (DIAG == 7) x = st[15] - t0;
    if (DIAG == 8) x = isB ? 0 : st[4] - t0;
    if (DIAG == 9) x = isB ? st[7] - t0 : 0;
    if (DIAG == 10) { x = isB ? 0 : st[0] - t0; mode = 1; }
    if (DIAG == 11) { x = isB ? st[2] - t0 : 0; }
    sx[tid] = x;
    __syncthreads();
    u64 X = 0;
    if (mode == 0) { for (int i = 0; i < 256; ++i) X = sx[i] > X ? sx[i] : X; }
    else { for (int i = 0; i < 256; ++i) X += sx[i]; X /= (mode == 1 ? 224 : 32); }
    if (X > 20000) X = 20000;
    const u64 tgt = t0 + 10000 + X;
    for (int i = 0; i < 2000000; ++i) { if (__builtin_amdgcn_s_memrealtime() >= tgt) break; __builtin_amdgcn_s_sleep(1); }
  }
}

__global__ void __launch_bounds__(1024, 4) k_main(Args a) {
  __shared__ __attribute__((aligned(16))) unsigned char lds[L_TOTAL];
  u64 t_entry; asm volatile("s_memrealtime %0\n s_waitcnt lgkmcnt(0)" : "=s"(t_entry) :: "memory");
  u32 pf;
  { u64 pc; u32 len;
    asm volatile("s_getpc_b64 %0\n.Lpf%=:\n\ts_mov_b32 %1, k_tail_mark-.Lpf%=" : "=s"(pc), "=s"(len));
    pf = __builtin_amdgcn_raw_buffer_load_b32(__builtin_amdgcn_make_buffer_rsrc(reinterpret_cast<void*>(pc), 0, len < 131072u ? len : 0u, 0x00020000), threadIdx.x * 128, 0, 0);
    __builtin_amdgcn_sched_barrier(0); }
  u64 tag = (hw_dispatch_id() + 1ull) * 0x9E3779B97F4A7C15ull;
  tag ^= ((u64)__builtin_amdgcn_dispatch_ptr() >> 6) * 0xC2B2AE3D27D4EB4Full;
  tag |= 1ull << 63;
  if (__builtin_expect(blockIdx.x >= NBW, 1)) part_a(a, lds, blockIdx.x - NBW, tag);
  else if (__builtin_amdgcn_readfirstlane(threadIdx.x >> 6) < 8) part_b<false>(a, lds, blockIdx.x, tag);
  else part_b<true>(a, lds, blockIdx.x, tag);
  asm volatile("" :: "v"(pf));
  if (threadIdx.x == 0) reinterpret_cast<u64*>(a.ws + (16u << 20))[blockIdx.x * 16 + 15] = t_entry;
}

extern "C" __global__ void k_tail_mark() {}

extern "C" void kernel_launch(void* const* d_in, const int* in_sizes, int n_in, void* d_out, int out_size, void* d_ws, size_t ws_size,
                              hipStream_t stream) {
  Args a;
  a.xA = (const u16*)d_in[0]; a.K = (const u16*)d_in[1]; a.V = (const u16*)d_in[2]; a.wq = (const u16*)d_in[3]; a.wo = (const u16*)d_in[4];
  a.xB = (const u16*)d_in[5]; a.C = (const u16*)d_in[6]; a.wqB = (const u16*)d_in[7]; a.wuk = (const u16*)d_in[8]; a.wuv = (const u16*)d_in[9]; a.woB = (const u16*)d_in[10];
  a.ws = (unsigned char*)d_ws;
  a.out = (u16*)d_out;
  k_main<<<NBW + NAW, 1024, 0, stream>>>(a);
  k_final<<<144, 256, 0, stream>>>((unsigned char*)d_ws, (u16*)d_out);
}
```

```cpp
#define STAMP
#define DIAG 0
#include <hip/hip_runtime.h>
#include <stdint.h>
#include <utility>

typedef unsigned short u16;
typedef unsigned u32;
typedef unsigned long long u64;
typedef u32 u32x4 __attribute__((ext_vector_type(4)));
typedef u32 u32x2 __attribute__((ext_vector_type(2)));
typedef float f32x4 __attribute__((ext_vector_type(4)));
typedef float f32x2 __attribute__((ext_vector_type(2)));
typedef short s16x4 __attribute__((ext_vector_type(4)));
typedef __bf16 bf16x8 __attribute__((ext_vector_type(8)));
typedef __bf16 bf16x2 __attribute__((ext_vector_type(2)));
#define DI __device__ __forceinline__
#define RLX_AGENT __ATOMIC_RELAXED, __HIP_MEMORY_SCOPE_AGENT
extern "C" __device__ u64 hw_dispatch_id() __asm("llvm.amdgcn.dispatch.id");

template <int... Is, class F> DI void static_for_impl(std::integer_sequence<int, Is...>, F&& f) { (f(std::integral_constant<int, Is>{}), ...); }
template <int N, class F> DI void static_for(F&& f) { static_for_impl(std::make_integer_sequence<int, N>{}, f); }

constexpr int H = 32, SK = 4096, DM = 576, DC = 512;
constexpr int ACU = 7;
constexpr int NBW = 32;
constexpr int NAW = H * ACU;
constexpr int NSP = 8;
constexpr float C2 = 0.08838834764831845f * 1.4426950408889634f;

constexpr size_t WS_F1 = 0, WS_F2 = 512;
constexpr size_t WS_QABS = 4096;
constexpr size_t WS_CTXP = WS_QABS + 4 * 32 * 512 * 2;
constexpr size_t WS_MLB = WS_CTXP + (size_t)4 * NSP * 32 * 512 * 2;
constexpr size_t WS_Y = WS_MLB + 4 * NSP * 32 * 2 * 4;
constexpr size_t WS_MLA = WS_Y + (size_t)72 * 256 * 128;
constexpr size_t WS_END = WS_MLA + 256 * 32;

constexpr int CRS = 1056;
constexpr int CBUF = 64 * CRS;
constexpr int L_BIG = 0;
constexpr int L_AM = 2 * CBUF;
constexpr int L_QS = L_AM + 16 * 272;
constexpr int L_ST = L_QS + 1024;
constexpr int L_PF = L_ST + 4096;
constexpr int L_TOTAL = L_PF + 4096;
constexpr int AM_LD = 136;
constexpr int AM2_LD = 520;
constexpr int L_AM2 = 69632, L_CP = 88064, L_W8 = 106496;

#define ST(i) do { if (threadIdx.x == 0) reinterpret_cast<u64*>(a.ws + (16u << 20))[blockIdx.x * 16 + (i)] = __builtin_amdgcn_s_memrealtime(); } while (0)
struct Args {
  const u16 *xA, *K, *V, *wq, *wo, *xB, *C, *wqB, *wuk, *wuv, *woB;
  unsigned char* ws;
  u16* out;
};

DI float bf_lo(u32 x) { return __uint_as_float(x << 16); }
DI float bf_hi(u32 x) { return __uint_as_float(x & 0xffff0000u); }
DI u32 pk_bf16(float lo, float hi) { bf16x2 v = {(__bf16)lo, (__bf16)hi}; return __builtin_bit_cast(u32, v); }
DI u16 to_bf16(float x) { return __builtin_bit_cast(u16, (__bf16)x); }
DI float bf2f(u16 x) { return __uint_as_float(((u32)x) << 16); }
DI float dot2(u32 a, u32 b, float c) { return __builtin_amdgcn_fdot2_f32_bf16(__builtin_bit_cast(bf16x2, a), __builtin_bit_cast(bf16x2, b), c, false); }
template <int CTRL> DI float dpp(float x) { return __builtin_bit_cast(float, __builtin_amdgcn_update_dpp(0, __builtin_bit_cast(int, x), CTRL, 0xf, 0xf, true)); }
DI float row16_sum(float x) { x += dpp<0x128>(x); x += dpp<0x124>(x); x += dpp<0x122>(x); x += dpp<0x121>(x); return x; }
DI float row16_max(float x) { x = fmaxf(x, dpp<0x128>(x)); x = fmaxf(x, dpp<0x124>(x)); x = fmaxf(x, dpp<0x122>(x)); x = fmaxf(x, dpp<0x121>(x)); return x; }
DI float xg_max(float x) {
  auto s = __builtin_amdgcn_permlane16_swap(__float_as_uint(x), __float_as_uint(x), false, false);
  x = fmaxf(__uint_as_float(s[0]), __uint_as_float(s[1]));
  auto t = __builtin_amdgcn_permlane32_swap(__float_as_uint(x), __float_as_uint(x), false, false);
  return fmaxf(__uint_as_float(t[0]), __uint_as_float(t[1]));
}
DI float xg_sum(float x) {
  auto s = __builtin_amdgcn_permlane16_swap(__float_as_uint(x), __float_as_uint(x), false, false);
  x = __uint_as_float(s[0]) + __uint_as_float(s[1]);
  auto t = __builtin_amdgcn_permlane32_swap(__float_as_uint(x), __float_as_uint(x), false, false);
  return __uint_as_float(t[0]) + __uint_as_float(t[1]);
}
DI f32x4 mfma16(u32x4 a, u32x4 b, f32x4 c) { return __builtin_amdgcn_mfma_f32_16x16x32_bf16(__builtin_bit_cast(bf16x8, a), __builtin_bit_cast(bf16x8, b), c, 0, 0, 0); }
DI u32x4 ld16(const u16* p) { return __builtin_nontemporal_load(reinterpret_cast<const u32x4*>(p)); }
DI u32x2 ld8(const u16* p) { return __builtin_nontemporal_load(reinterpret_cast<const u32x2*>(p)); }
DI u32x2 trread(const unsigned char* p) {
  return __builtin_bit_cast(u32x2, __builtin_amdgcn_ds_read_tr16_b64_v4i16((__attribute__((address_space(3))) s16x4*)(p)));
}
DI u32 prm(u32 hi, u32 lo, int odd) { return __builtin_amdgcn_perm(hi, lo, odd ? 0x07060302u : 0x05040100u); }

DI u32x4 tcol16(const u32x4 (&R)[8], int t) {
  u32x4 T;
#pragma unroll
  for (int q = 0; q < 4; ++q) T[q] = prm(R[2 * q + 1][t >> 1], R[2 * q][t >> 1], t & 1);
  return T;
}
DI u32x4 tcol8(const u32x2 (&R)[8], int t) {
  u32x4 T;
#pragma unroll
  for (int q = 0; q < 4; ++q) T[q] = prm(R[2 * q + 1][t >> 1], R[2 * q][t >> 1], t & 1);
  return T;
}

#define SPINLOG 22
#define PSL 2
#define YAUX 16
constexpr u32 SPIN_CAP = 1u << SPINLOG;
DI void wave_wait(const u64* f, bool active, u64 tag) {
  for (u32 spins = 0; spins < SPIN_CAP; ++spins) {
    const u64 v = active ? __hip_atomic_load(f, RLX_AGENT) : tag;
    if (__all(v == tag)) break;
    __builtin_amdgcn_s_sleep(PSL);
  }
  __builtin_amdgcn_fence(__ATOMIC_ACQUIRE, "agent");
}
DI void wg_wait64(const unsigned char* flags, u64 tag, int pw = 0) {
  if ((int)(threadIdx.x >> 6) == pw) {
    wave_wait(reinterpret_cast<const u64*>(flags) + (threadIdx.x & (NBW - 1)), true, tag);
    asm volatile("s_waitcnt vmcnt(0)" ::: "memory");
  }
  __syncthreads();
}
DI void st16_sc1(__amdgpu_buffer_rsrc_t rs, size_t off, u32x4 v) { __builtin_amdgcn_raw_buffer_store_b128(v, rs, (int)off, 0, 16); }
DI void st8_sc1(unsigned char* p, f32x2 v) { __hip_atomic_store(reinterpret_cast<u64*>(p), __builtin_bit_cast(u64, v), RLX_AGENT); }

struct QPre { u32x4 R0[8]; u32x4 R1; u32x2 xa, xb; u32 x1; };
DI void qproj_load(QPre& Q, const u16* x, const u16* Wh, int w, int lane) {
  const int j = lane & 15, g = lane >> 4;
  Q.xa = u32x2{0u, 0u}; Q.xb = u32x2{0u, 0u}; Q.x1 = 0u;
  if (j < 4) { Q.xa = ld8(x + j * DM + 36 * w + 8 * g); Q.xb = ld8(x + j * DM + 36 * w + 8 * g + 4); Q.x1 = x[j * DM + 36 * w + 32 + g]; }
#pragma unroll
  for (int jj = 0; jj < 8; ++jj) Q.R0[jj] = ld16(Wh + (size_t)(36 * w + 8 * g + jj) * 4096 + 8 * j);
  Q.R1 = ld16(Wh + (size_t)(36 * w + 32 + g) * 4096 + 8 * j);
}
DI void qproj_mfma(const QPre& Q, unsigned char* lds, int w, int lane) {
  const int j = lane & 15, g = lane >> 4;
  f32x4 acc[8];
  {
    const u32x4 a = {Q.xa[0], Q.xa[1], Q.xb[0], Q.xb[1]};
#pragma unroll
    for (int t = 0; t < 8; ++t) acc[t] = mfma16(a, tcol16(Q.R0, t), f32x4{0.f, 0.f, 0.f, 0.f});
    const u32x4 a1 = {Q.x1, 0u, 0u, 0u};
#pragma unroll
    for (int t = 0; t < 8; ++t) { const u32x4 bb = {(t & 1) ? (Q.R1[t >> 1] >> 16) : (Q.R1[t >> 1] & 0xffffu), 0u, 0u, 0u}; acc[t] = mfma16(a1, bb, acc[t]); }
  }
  float* qpart = reinterpret_cast<float*>(lds + L_BIG);
  if (g == 0) {
#pragma unroll
    for (int r = 0; r < 4; ++r) {
      float* p = qpart + (w * 4 + r) * 128 + 8 * j;
      *reinterpret_cast<f32x4*>(p) = f32x4{acc[0][r], acc[1][r], acc[2][r], acc[3][r]};
      *reinterpret_cast<f32x4*>(p + 4) = f32x4{acc[4][r], acc[5][r], acc[6][r], acc[7][r]};
    }
  }
  __builtin_amdgcn_sched_barrier(0);
}
DI void qproj_finish(unsigned char* lds) {
  const float* qpart = reinterpret_cast<const float*>(lds + L_BIG);
  __syncthreads();
  if (threadIdx.x < 512) {
    float s = 0.f;
#pragma unroll
    for (int ww = 0; ww < 16; ++ww) s += qpart[ww * 512 + threadIdx.x];
    reinterpret_cast<u16*>(lds + L_QS)[threadIdx.x] = to_bf16(s);
  }
  __syncthreads();
}

DI void wo_load(const u16* Wo, u32x2 (&R)[4][8], int nb, int lane, int s0, int s1) {
  const int j = lane & 15, g = lane >> 4;
#pragma unroll
  for (int s = 0; s < 4; ++s) if (s >= s0 && s < s1)
#pragma unroll
    for (int jj = 0; jj < 8; ++jj) R[s][jj] = ld8(Wo + (size_t)(32 * s + 8 * g + jj) * DM + 64 * nb + 4 * j);
}
DI void wo_mfma(const u32x2 (&R)[4][8], const unsigned char* lds, int lane, f32x4 (&acc)[4]) {
  const int j = lane & 15, g = lane >> 4;
#pragma unroll
  for (int t = 0; t < 4; ++t) acc[t] = f32x4{0.f, 0.f, 0.f, 0.f};
#pragma unroll
  for (int s = 0; s < 4; ++s) {
    const u32x4 a = *reinterpret_cast<const u32x4*>(lds + L_AM + j * 272 + (32 * s + 8 * g) * 2);
#pragma unroll
    for (int t = 0; t < 4; ++t) acc[t] = mfma16(a, tcol8(R[s], t), acc[t]);
  }
}
DI u32x4 yrow(const f32x4 (&acc)[4]) {
  const f32x4 o = {acc[0][0] + acc[0][1], acc[1][0] + acc[1][1], acc[2][0] + acc[2][1], acc[3][0] + acc[3][1]};
  return __builtin_bit_cast(u32x4, o);
}
DI void am_zero(unsigned char* lds, int tid) {
  if (tid < 136) { const int pr = tid / 34; *reinterpret_cast<u32x4*>(lds + L_AM + (4 * pr + 2) * 272 + (tid - 34 * pr) * 16) = u32x4{0u, 0u, 0u, 0u}; }
}
DI void st16_y(__amdgpu_buffer_rsrc_t rs, int off, u32x4 v) { __builtin_amdgcn_raw_buffer_store_b128(v, rs, off, 0, YAUX); }
DI float rdlane(float x, int l) { return __builtin_bit_cast(float, __builtin_amdgcn_readlane(__builtin_bit_cast(int, x), l)); }

constexpr int NS = 10;
constexpr int NSEQ = 8 * NS;
constexpr int DEPTH = 2, RING = 12, RAMP = 2;
constexpr int issued(int i) {
  int d = RING - (i + RAMP - 1) / RAMP; if (d < DEPTH) d = DEPTH;
  int e = i + d; if (e < RING) e = RING; return e > NSEQ ? NSEQ : e;
}
DI void part_a(const Args& a, unsigned char* lds, int idx, u64 tag) {
  const int tid = threadIdx.x, lane = tid & 63, w = __builtin_amdgcn_readfirstlane(tid >> 6);
  const int j = lane & 15, g = lane >> 4;
  const int h = (idx & 7) * 4 + ((idx >> 3) & 3), ci = idx >> 5;
  const int U0 = (ci * 1024) / ACU, NUN = ((ci + 1) * 1024) / ACU - U0;
  const int rot = (h + ci) % NS;
  const int bad_sl = (16 * (NS - 1) + w < NUN) ? -1 : NS - 1 - rot;
  const __amdgpu_buffer_rsrc_t rs = __builtin_amdgcn_make_buffer_rsrc(a.ws, 0, (int)WS_END, 0x00020000);
  const u16* Wq = a.wq + h * 128;
  QPre Q;
  ST(0);
  qproj_load(Q, a.xA, Wq, w, lane);
  const size_t base = ((size_t)h * SK + 4 * U0) * 128;
  const int voff = lane * 16;
  u32x4 R[RING];
#define A_ISSUE(i) R[(i) % RING] = __builtin_amdgcn_raw_buffer_load_b128( \
    __builtin_amdgcn_make_buffer_rsrc(const_cast<u16*>(((((i) / NS) & 1) ? a.V : a.K) + base + (size_t)((i) / (2 * NS)) * H * SK * 128), 0, NUN * 1024, 0x00020000), \
    voff, (((i) % NS) + rot >= NS ? ((i) % NS) + rot - NS : ((i) % NS) + rot) * 16384 + w * 1024, 2)
  constexpr int PRE = RING / 2;
#pragma unroll
  for (int t = 0; t < PRE; ++t) A_ISSUE(t);
  __builtin_amdgcn_sched_barrier(0);
  am_zero(lds, tid);
  qproj_mfma(Q, lds, w, lane);
#pragma unroll
  for (int t = PRE; t < RING; ++t) A_ISSUE(t);
  __builtin_amdgcn_sched_barrier(0);
  qproj_finish(lds);
  ST(1);
  float* pacc = reinterpret_cast<float*>(lds + L_BIG);
  float* pml = reinterpret_cast<float*>(lds + L_ST);
  u32x4 q;
  float m, l, acc[8], s[NS];
  u32x2 RW[4][8];
  const u16* Wo = a.wo + (size_t)h * 128 * DM;
  static_for<NSEQ>([&](auto ic) __attribute__((always_inline)) {
    constexpr int i = ic.value, bb = i / (2 * NS), pass = (i / NS) & 1, sl = i % NS;
    const u32x4 r = R[i % RING];
    if constexpr (pass == 0) {
      if constexpr (sl == 0) { q = *reinterpret_cast<const u32x4*>(lds + L_QS + (bb * 128 + 8 * j) * 2); m = -INFINITY; }
      float d = dot2(r[0], q[0], 0.f); d = dot2(r[1], q[1], d); d = dot2(r[2], q[2], d); d = dot2(r[3], q[3], d);
      asm volatile("" : "+v"(d));
      if constexpr (issued(i + 1) > issued(i)) A_ISSUE(issued(i));
      d = row16_sum(d) * C2;
      d = (sl == bad_sl) ? -INFINITY : d;
      s[sl] = d; m = fmaxf(m, d);
      if constexpr (sl == NS - 1) {
        m = xg_max(m); l = 0.f;
#pragma unroll
        for (int t = 0; t < NS; ++t) { s[t] = __builtin_amdgcn_exp2f(s[t] - m); l += s[t]; }
        l = xg_sum(l);
        if (lane == 0) *reinterpret_cast<f32x2*>(pml + (bb * 16 + w) * 2) = f32x2{m, l};
#pragma unroll
        for (int k = 0; k < 8; ++k) acc[k] = 0.f;
      }
    } else {
      const float p = s[sl];
#pragma unroll
      for (int k = 0; k < 4; ++k) { acc[2 * k] = fmaf(p, bf_lo(r[k]), acc[2 * k]); acc[2 * k + 1] = fmaf(p, bf_hi(r[k]), acc[2 * k + 1]); }
      asm volatile("" : "+v"(acc[0]), "+v"(acc[1]), "+v"(acc[2]), "+v"(acc[3]), "+v"(acc[4]), "+v"(acc[5]), "+v"(acc[6]), "+v"(acc[7]));
      if constexpr (issued(i + 1) > issued(i)) A_ISSUE(issued(i));
      if (sl == 4) { if (w < 9) wo_load(Wo, RW, w, lane, bb, bb + 1); }
      if constexpr (sl == NS - 1) {
#pragma unroll
        for (int k = 0; k < 8; ++k) acc[k] = xg_sum(acc[k]);
        if (g == 0) {
          float* pp = pacc + (bb * 16 + w) * 128 + 8 * j;
          *reinterpret_cast<f32x4*>(pp) = f32x4{acc[0], acc[1], acc[2], acc[3]};
          *reinterpret_cast<f32x4*>(pp + 4) = f32x4{acc[4], acc[5], acc[6], acc[7]};
        }
      }
    }
    __builtin_amdgcn_sched_barrier(0);
  });
#undef A_ISSUE
  if (lane == 0) reinterpret_cast<u64*>(a.ws + (17u << 20))[blockIdx.x * 16 + w] = __builtin_amdgcn_s_memrealtime();
  ST(2);
  __syncthreads();
  ST(3);
  if (w < 8) {
    const int bb = w >> 1, d = tid & 127;
    u16* Am = reinterpret_cast<u16*>(lds + L_AM);
    const f32x2 ml = *reinterpret_cast<const f32x2*>(pml + (bb * 16 + j) * 2);
    float pv[16];
#pragma unroll
    for (int i = 0; i < 16; ++i) pv[i] = pacc[(bb * 16 + i) * 128 + d];
    const float wt = __builtin_amdgcn_exp2f(ml[0] - row16_max(ml[0]));
    float o0 = 0.f, o1 = 0.f;
#pragma unroll
    for (int i = 0; i < 16; i += 2) { o0 = fmaf(rdlane(wt, i), pv[i], o0); o1 = fmaf(rdlane(wt, i + 1), pv[i + 1], o1); }
    const float o = o0 + o1;
    const u16 hi = to_bf16(o); const u16 lo = to_bf16(o - bf2f(hi));
    Am[(4 * bb) * AM_LD + d] = hi; Am[(4 * bb + 1) * AM_LD + d] = lo;
  } else if (w == 9) {
    const f32x2 ml = *reinterpret_cast<const f32x2*>(pml + lane * 2);
    const float M = row16_max(ml[0]);
    const float L = row16_sum(__builtin_amdgcn_exp2f(ml[0] - M) * ml[1]);
    if (j == 0) __builtin_amdgcn_raw_buffer_store_b64(__builtin_bit_cast(u32x2, f32x2{M, L}), rs, (int)WS_MLA + (((g >> 1) * 32 + h) * 2 + (ci >> 2)) * 64 + (g & 1) * 32 + (ci & 3) * 8, 0, YAUX);
  }
  __syncthreads();
  if (w < 9) {
    f32x4 ya[4];
    wo_mfma(RW, lds, lane, ya);
    st16_y(rs, (int)WS_Y + (((8 * w + (j >> 1)) * 2 + (g >> 1)) * 256 + h * 8 + 2 * (ci & 3) + (ci >> 2)) * 64 + (g & 1) * 32 + (j & 1) * 16, yrow(ya));
  }
  ST(4);
}

template <bool LD> DI void part_b(const Args& a, unsigned char* lds, int wg, u64 tag) {
  const int tid = threadIdx.x, lane = tid & 63, w = __builtin_amdgcn_readfirstlane(tid >> 6);
  const int j = lane & 15, g = lane >> 4;
  const __amdgpu_buffer_rsrc_t rs = __builtin_amdgcn_make_buffer_rsrc(a.ws, 0, (int)WS_END, 0x00020000);
  const int h = wg;
  const int h3 = wg ^ 2;
  const int b2 = wg >> 3, sp = wg & 7;
  const u16* cl = a.C + ((size_t)b2 * SK + sp * 512 + 8 * (w - 8)) * DC + lane * 8;
  u32x4 X[16];
  u32x2 RW[4][8];
  u32x4 W8[2];
  u32* cnt = reinterpret_cast<u32*>(lds + L_ST + 4064);
#define C_ISSUE(T) _Pragma("unroll") for (int r = 0; r < 8; ++r) X[8 * ((T) & 1) + r] = ld16(cl + (size_t)(64 * (T) + r) * DC)
  u32 sb0 = L_BIG + 8 * (w - 8) * CRS + lane * 16, sb1 = sb0 + CBUF;
  asm volatile("" : "+v"(sb0), "+v"(sb1));
#define C_STAGE(T) _Pragma("unroll") for (int r = 0; r < 8; ++r) *reinterpret_cast<u32x4*>(lds + (((T) & 1) ? sb1 : sb0) + r * CRS) = X[8 * ((T) & 1) + r]
  {
    const u16* Wq = a.wqB + h * 128;
    u32x4 U[8];
    QPre Q;
    if constexpr (!LD) { ST(0); }
    qproj_load(Q, a.xB, Wq, w, lane);
#pragma unroll
    for (int i = 0; i < 8; ++i) U[i] = ld16(a.wuk + ((size_t)(32 * w + 4 * i + g) * H + h) * 128 + 8 * j);
    __builtin_amdgcn_sched_barrier(0);
    if constexpr (!LD) am_zero(lds, tid);
    qproj_mfma(Q, lds, w, lane);
    if constexpr (LD) { C_ISSUE(0); }
    __builtin_amdgcn_sched_barrier(0);
    qproj_finish(lds);
    u32x4 qv[4];
#pragma unroll
    for (int bb = 0; bb < 4; ++bb) qv[bb] = *reinterpret_cast<const u32x4*>(lds + L_QS + (bb * 128 + 8 * j) * 2);
    u16* qa = reinterpret_cast<u16*>(lds + L_PF);
#pragma unroll
    for (int i = 0; i < 8; ++i) {
      float v = 0.f;
#pragma unroll
      for (int bb = 0; bb < 4; ++bb) {
        float d = dot2(U[i][0], qv[bb][0], 0.f); d = dot2(U[i][1], qv[bb][1], d); d = dot2(U[i][2], qv[bb][2], d); d = dot2(U[i][3], qv[bb][3], d);
        d = row16_sum(d);
        v = (j == bb) ? d : v;
      }
      if (j < 4) qa[j * 512 + 32 * w + 4 * i + g] = to_bf16(v);
    }
    __builtin_amdgcn_sched_barrier(0);
    if constexpr (LD) { asm volatile("s_waitcnt vmcnt(0)" ::: "memory"); C_ISSUE(1); }
    __syncthreads();
    if constexpr (!LD) {
      if (w == 0) {
#pragma unroll
        for (int bb = 0; bb < 4; ++bb) st16_sc1(rs, WS_QABS + ((bb * H + h) * 512 + lane * 8) * 2, *reinterpret_cast<const u32x4*>(qa + bb * 512 + lane * 8));
        asm volatile("s_waitcnt vmcnt(0)" ::: "memory");
        if (lane == 0) { __hip_atomic_store(reinterpret_cast<u64*>(a.ws + WS_F1 + wg * 8), tag, RLX_AGENT); *cnt = 0u; }
      }
      __builtin_amdgcn_sched_barrier(0);
      __builtin_amdgcn_s_barrier();
    } else {
      C_STAGE(0);
      __builtin_amdgcn_sched_barrier(0);
      __builtin_amdgcn_s_barrier();
      C_ISSUE(2);
    }
    if constexpr (!LD) { ST(1); }
  }
  {
    const int b = b2;
    if constexpr (!LD) wg_wait64(a.ws + WS_F1, tag); else __syncthreads();
    if constexpr (!LD) { ST(2); }
    const int kt = w & 3, hh = (w >> 2) & 1;
    float* stmax = reinterpret_cast<float*>(lds + L_ST);
    float* stsum = stmax + 128;
    float* salpha = stmax + 256;
    unsigned char* Pf = lds + L_PF;
    float M = -INFINITY, lsum = 0.f;
    f32x4 acc[2][2];
#pragma unroll
    for (int ct = 0; ct < 2; ++ct) { acc[ct][0] = f32x4{0.f, 0.f, 0.f, 0.f}; acc[ct][1] = f32x4{0.f, 0.f, 0.f, 0.f}; }
    const int troff = (4 * g + (j >> 2)) * CRS + (32 * w + 4 * (j & 3)) * 2;
    auto pv_step = [&](const unsigned char* cb) __attribute__((always_inline)) {
      const f32x4 al0 = *reinterpret_cast<const f32x4*>(salpha + 4 * g), al1 = *reinterpret_cast<const f32x4*>(salpha + 16 + 4 * g);
#pragma unroll
      for (int ct = 0; ct < 2; ++ct) { acc[ct][0] *= al0; acc[ct][1] *= al1; }
#pragma unroll
      for (int blk = 0; blk < 2; ++blk) {
        const u32x4 p0 = *reinterpret_cast<const u32x4*>(Pf + ((blk * 2 + 0) * 64 + lane) * 16);
        const u32x4 p1 = *reinterpret_cast<const u32x4*>(Pf + ((blk * 2 + 1) * 64 + lane) * 16);
#pragma unroll
        for (int ct = 0; ct < 2; ++ct) {
          const u32x2 lo = trread(cb + troff + (32 * blk) * CRS + 32 * ct);
          const u32x2 hi = trread(cb + troff + (32 * blk + 16) * CRS + 32 * ct);
          const u32x4 bf = {lo[0], lo[1], hi[0], hi[1]};
          acc[ct][0] = mfma16(p0, bf, acc[ct][0]); acc[ct][1] = mfma16(p1, bf, acc[ct][1]);
        }
      }
    };
    unsigned char* Qf = lds + L_BIG + CBUF;
    if constexpr (!LD) {
      u32x4 qv[4];
#pragma unroll
      for (int k = 0; k < 4; ++k) qv[k] = *reinterpret_cast<const u32x4*>(a.ws + WS_QABS + ((size_t)b * H * 512 + (tid + 512 * k) * 8) * 2);
#pragma unroll
      for (int k = 0; k < 4; ++k) { const int e = tid + 512 * k, head = e >> 6, ch = e & 63;
        *reinterpret_cast<u32x4*>(Qf + ((head >> 4) * 16 + (ch >> 2)) * 1040 + ((ch & 3) * 16 + (head & 15)) * 16) = qv[k]; }
    }
    __syncthreads();
    if constexpr (!LD) {
      u32x4 QF[16];
#pragma unroll
      for (int s = 0; s < 16; ++s) QF[s] = *reinterpret_cast<const u32x4*>(Qf + (hh * 16 + s) * 1040 + lane * 16);
      auto wuv_load = [&]() __attribute__((always_inline)) {
#pragma unroll
        for (int k2 = 0; k2 < 2; ++k2)
#pragma unroll
          for (int jj = 0; jj < 8; ++jj) {
            const u32x4 v = ld16(a.wuv + ((size_t)(64 * w + 32 * k2 + 8 * g + jj) * H + h3) * 128 + 8 * j);
            RW[2 * k2][jj] = u32x2{v[0], v[1]}; RW[2 * k2 + 1][jj] = u32x2{v[2], v[3]};
          }
      };
      auto s_tile = [&](const unsigned char* cb, bool last) __attribute__((always_inline)) {
        __syncthreads();
        f32x4 s0 = {0.f, 0.f, 0.f, 0.f}, s1 = {0.f, 0.f, 0.f, 0.f};
#pragma unroll
        for (int s = 0; s < 16; s += 2) {
          s0 = mfma16(*reinterpret_cast<const u32x4*>(cb + (16 * kt + j) * CRS + (32 * s + 8 * g) * 2), QF[s], s0);
          s1 = mfma16(*reinterpret_cast<const u32x4*>(cb + (16 * kt + j) * CRS + (32 * s + 32 + 8 * g) * 2), QF[s + 1], s1);
        }
        const f32x4 sv = (s0 + s1) * C2;
        const float v = xg_max(fmaxf(fmaxf(sv[0], sv[1]), fmaxf(sv[2], sv[3])));
        if (g == 0) stmax[kt * 32 + 16 * hh + j] = v;
        __syncthreads();
        const float* sm = stmax + 16 * hh + j;
        const float Mn = fmaxf(fmaxf(M, fmaxf(sm[0], sm[32])), fmaxf(sm[64], sm[96]));
        const float alpha = __builtin_amdgcn_exp2f(M - Mn);
        M = Mn;
        u32x2 pk;
        pk[0] = pk_bf16(__builtin_amdgcn_exp2f(sv[0] - Mn), __builtin_amdgcn_exp2f(sv[1] - Mn));
        pk[1] = pk_bf16(__builtin_amdgcn_exp2f(sv[2] - Mn), __builtin_amdgcn_exp2f(sv[3] - Mn));
        lsum = lsum * alpha + ((bf_lo(pk[0]) + bf_hi(pk[0])) + (bf_lo(pk[1]) + bf_hi(pk[1])));
        *reinterpret_cast<u32x2*>(Pf + (((kt >> 1) * 2 + hh) * 64 + lane) * 16 + (kt & 1) * 8) = pk;
        if (kt == 0 && g == 0) salpha[16 * hh + j] = alpha;
        __syncthreads();
        if (last) { __builtin_amdgcn_sched_barrier(0); wuv_load(); __builtin_amdgcn_sched_barrier(0); }
        pv_step(cb);
      };
#pragma unroll 1
      for (int T = 0; T < 6; T += 2) { s_tile(lds + L_BIG, false); s_tile(lds + L_BIG + CBUF, false); }
      s_tile(lds + L_BIG, false); s_tile(lds + L_BIG + CBUF, true);
    } else {
#pragma unroll
      for (int T = 0; T < 8; ++T) {
        __syncthreads();
        if (T + 1 < 8) { C_STAGE(T + 1); if (T + 3 < 8) { C_ISSUE(T + 3); } }
        if (T == 5) wo_load(a.woB + (size_t)h3 * 128 * DM, RW, w - 8, lane, 0, 2);
        if (T == 6) wo_load(a.woB + (size_t)h3 * 128 * DM, RW, w - 8, lane, 2, 4);
        if (T == 6) {
#pragma unroll
          for (int k = 0; k < 2; ++k) W8[k] = ld16(a.woB + ((size_t)h3 * 128 + 16 * (w - 8) + 8 * k + (lane >> 3)) * DM + 512 + (lane & 7) * 8);
        }
        __syncthreads();
        __syncthreads();
        pv_step(lds + L_BIG + (T & 1) * CBUF);
      }
    }
#undef C_ISSUE
#undef C_STAGE
    if constexpr (!LD) { ST(3); }
    __syncthreads();
    if constexpr (!LD) { lsum = xg_sum(lsum); if (g == 0) stsum[kt * 32 + 16 * hh + j] = lsum; if (kt == 0 && g == 0) salpha[32 + 16 * hh + j] = M; }
    float* tb = reinterpret_cast<float*>(lds + L_BIG);
#pragma unroll
    for (int ct = 0; ct < 2; ++ct)
#pragma unroll
      for (int h2 = 0; h2 < 2; ++h2)
#pragma unroll
        for (int r = 0; r < 4; ++r) tb[(16 * h2 + 4 * g + r) * 516 + 32 * w + 16 * ct + j] = acc[ct][h2][r];
    if constexpr (!LD) { for (int i = tid; i < 8 * AM2_LD * 2 / 16; i += 512) reinterpret_cast<u32x4*>(lds + L_AM2 + 8 * AM2_LD * 2)[i] = u32x4{0u, 0u, 0u, 0u}; }
    __syncthreads();
    if constexpr (!LD) { ST(4); }
    if constexpr (LD) {
      const int t2 = tid - 512;
#pragma unroll
      for (int k = 0; k < 4; ++k) {
        const int e = t2 + 512 * k, head = e >> 6, c8 = e & 63;
        const f32x4 v0 = *reinterpret_cast<const f32x4*>(tb + head * 516 + 8 * c8), v1 = *reinterpret_cast<const f32x4*>(tb + head * 516 + 8 * c8 + 4);
        st16_sc1(rs, WS_CTXP + ((size_t)((b * NSP + sp) * 32 + head) * 512 + 8 * c8) * 2, u32x4{pk_bf16(v0[0], v0[1]), pk_bf16(v0[2], v0[3]), pk_bf16(v1[0], v1[1]), pk_bf16(v1[2], v1[3])});
      }
      if (t2 < 32) {
        const float L = (stsum[t2] + stsum[32 + t2]) + (stsum[64 + t2] + stsum[96 + t2]);
        st8_sc1(a.ws + WS_MLB + ((size_t)(b * NSP + sp) * 32 + t2) * 8, f32x2{salpha[32 + t2], L});
      }
      asm volatile("s_waitcnt vmcnt(0)" ::: "memory");
      if (lane == 0) {
        if (__hip_atomic_fetch_add(cnt, 1u, __ATOMIC_RELAXED, __HIP_MEMORY_SCOPE_WORKGROUP) == 7u) {
          __hip_atomic_store(reinterpret_cast<u64*>(a.ws + WS_F2 + wg * 8), tag, RLX_AGENT);
          reinterpret_cast<u64*>(a.ws + (16u << 20))[blockIdx.x * 16 + 5] = __builtin_amdgcn_s_memrealtime();
        }
      }
#pragma unroll
      for (int k = 0; k < 2; ++k) *reinterpret_cast<u32x4*>(lds + L_W8 + (16 * (w - 8) + 8 * k + (lane >> 3)) * 144 + (lane & 7) * 16) = W8[k];
    }
  }
  {
    int tid = threadIdx.x; asm volatile("" : "+v"(tid));
    const int lane = tid & 63, j = lane & 15, g = lane >> 4;
    u16* Am2 = reinterpret_cast<u16*>(lds + L_AM2);
    float* cpart = reinterpret_cast<float*>(lds + L_CP);
    __builtin_amdgcn_sched_barrier(0);
    if constexpr (LD) {
      wg_wait64(a.ws + WS_F2, tag, 8);
      const int b = (tid >> 7) & 3, c4 = tid & 127;
      u32x2 pv[NSP];
      const f32x2 ml = *reinterpret_cast<const f32x2*>(a.ws + WS_MLB + ((size_t)(b * NSP + (j & 7)) * 32 + h3) * 8);
#pragma unroll
      for (int s = 0; s < NSP; ++s) pv[s] = *reinterpret_cast<const u32x2*>(a.ws + WS_CTXP + (((size_t)(b * NSP + s) * 32 + h3) * 512 + 4 * c4) * 2);
      const float M = row16_max(ml[0]);
      float wt = __builtin_amdgcn_exp2f(ml[0] - M);
      wt *= 2.f / row16_sum(wt * ml[1]);
      float c0 = 0.f, c1 = 0.f, c2 = 0.f, c3 = 0.f;
#pragma unroll
      for (int s = 0; s < NSP; ++s) { const float ws_ = rdlane(wt, s); c0 = fmaf(ws_, bf_lo(pv[s][0]), c0); c1 = fmaf(ws_, bf_hi(pv[s][0]), c1); c2 = fmaf(ws_, bf_lo(pv[s][1]), c2); c3 = fmaf(ws_, bf_hi(pv[s][1]), c3); }
      const u32 h0 = pk_bf16(c0, c1), h1 = pk_bf16(c2, c3);
      *reinterpret_cast<u32x2*>(Am2 + (2 * b) * AM2_LD + 4 * c4) = u32x2{h0, h1};
      *reinterpret_cast<u32x2*>(Am2 + (2 * b + 1) * AM2_LD + 4 * c4) = u32x2{pk_bf16(c0 - bf_lo(h0), c1 - bf_hi(h0)), pk_bf16(c2 - bf_lo(h1), c3 - bf_hi(h1))};
      __syncthreads();
      __syncthreads();
      const int t2 = tid - 512;
      float s = 0.f;
#pragma unroll
      for (int ww = 0; ww < 8; ++ww) s += cpart[ww * 512 + t2];
      u16* Am = reinterpret_cast<u16*>(lds + L_AM);
      const int bb = t2 >> 7, d = t2 & 127;
      const u16 hi = to_bf16(s);
      Am[(4 * bb) * AM_LD + d] = hi; Am[(4 * bb + 1) * AM_LD + d] = to_bf16(s - bf2f(hi));
    } else {
      __builtin_amdgcn_s_barrier();
      if constexpr (!LD) { ST(6); }
      __syncthreads();
      f32x4 acc[8];
#pragma unroll
      for (int t = 0; t < 8; ++t) acc[t] = f32x4{0.f, 0.f, 0.f, 0.f};
#pragma unroll
      for (int k2 = 0; k2 < 2; ++k2) {
        const u32x4 af = *reinterpret_cast<const u32x4*>(Am2 + j * AM2_LD + 64 * w + 32 * k2 + 8 * g);
#pragma unroll
        for (int t = 0; t < 8; ++t) acc[t] = mfma16(af, tcol8(RW[2 * k2 + (t >> 2)], t & 3), acc[t]);
      }
      if (g < 2) {
#pragma unroll
        for (int bb = 0; bb < 2; ++bb) {
          float* p = cpart + (w * 4 + 2 * g + bb) * 128 + 8 * j;
          *reinterpret_cast<f32x4*>(p) = f32x4{acc[0][2 * bb] + acc[0][2 * bb + 1], acc[1][2 * bb] + acc[1][2 * bb + 1], acc[2][2 * bb] + acc[2][2 * bb + 1], acc[3][2 * bb] + acc[3][2 * bb + 1]};
          *reinterpret_cast<f32x4*>(p + 4) = f32x4{acc[4][2 * bb] + acc[4][2 * bb + 1], acc[5][2 * bb] + acc[5][2 * bb + 1], acc[6][2 * bb] + acc[6][2 * bb + 1], acc[7][2 * bb] + acc[7][2 * bb + 1]};
        }
      }
      __builtin_amdgcn_sched_barrier(0);
      __syncthreads();
      if (w == 0) {
#pragma unroll
        for (int s = 0; s < 4; ++s)
#pragma unroll
          for (int jj = 0; jj < 8; ++jj) RW[s][jj] = *reinterpret_cast<const u32x2*>(lds + L_W8 + (32 * s + 8 * g + jj) * 144 + 8 * j);
      }
    }
    __syncthreads();
    if (LD || w == 0) {
      f32x4 ya[4];
      wo_mfma(RW, lds, lane, ya);
      st16_y(rs, (int)WS_Y + (((8 * (LD ? w - 8 : 8) + (j >> 1)) * 2 + (g >> 1)) * 256 + h3 * 8 + 7) * 64 + (g & 1) * 32 + (j & 1) * 16, yrow(ya));
    }
    if constexpr (!LD) { ST(7); }
  }
}

DI float red4(f32x4 x) {
  auto s0 = __builtin_amdgcn_permlane32_swap(__float_as_uint(x[0]), __float_as_uint(x[1]), false, false);
  auto s1 = __builtin_amdgcn_permlane32_swap(__float_as_uint(x[2]), __float_as_uint(x[3]), false, false);
  const float r0 = __uint_as_float(s0[0]) + __uint_as_float(s0[1]), r1 = __uint_as_float(s1[0]) + __uint_as_float(s1[1]);
  auto s2 = __builtin_amdgcn_permlane16_swap(__float_as_uint(r0), __float_as_uint(r1), false, false);
  float r = __uint_as_float(s2[0]) + __uint_as_float(s2[1]);
  r += dpp<0x124>(r); r += dpp<0x128>(r);
  return r;
}
extern "C" __global__ void __launch_bounds__(256) k_final(unsigned char* ws, u16* out) {
  __shared__ float red[4][32];
  u64 t_fentry; asm volatile("s_memrealtime %0\n s_waitcnt lgkmcnt(0)" : "=s"(t_fentry) :: "memory");
  const int tid = threadIdx.x, lane = tid & 63, wv = tid >> 6, id = blockIdx.x;
  const int c8 = id >> 1, bp = id & 1, half = (lane >> 3) & 1, hd = 8 * wv + 2 * (lane >> 4) + ((lane >> 2) & 1);
  u32 yo = (u32)WS_Y + id * 16384 + hd * 512 + half * 64 + (lane & 3) * 16, mo = (u32)WS_MLA + bp * 4096 + hd * 128 + half * 64 + (lane & 2) * 16;
  asm volatile("" : "+v"(yo), "+v"(mo));
  f32x4 v[4], ml[2];
#pragma unroll
  for (int i = 0; i < 2; ++i) ml[i] = *reinterpret_cast<const f32x4*>(ws + mo + i * 16);
#pragma unroll
  for (int i = 0; i < 4; ++i) v[i] = *reinterpret_cast<const f32x4*>(ws + yo + i * 128);
  __builtin_amdgcn_sched_barrier(0);
  if (id == 0 && tid < 128) reinterpret_cast<u64*>(ws)[tid] = 0ull;
  const float m3 = half ? -INFINITY : ml[1][2], l3 = half ? 0.f : ml[1][3];
  float M = fmaxf(fmaxf(ml[0][0], ml[0][2]), fmaxf(ml[1][0], m3));
  M = fmaxf(M, dpp<0x128>(M));
  const float e0 = __builtin_amdgcn_exp2f(ml[0][0] - M), e1 = __builtin_amdgcn_exp2f(ml[0][2] - M), e2 = __builtin_amdgcn_exp2f(ml[1][0] - M), e3 = __builtin_amdgcn_exp2f(m3 - M);
  float L = fmaf(e0, ml[0][1], e1 * ml[0][3]) + fmaf(e2, ml[1][1], e3 * l3);
  L += dpp<0x128>(L);
  const f32x4 z = {0.f, 0.f, 0.f, 0.f};
  const f32x4 ya = ((v[0] * e0 + v[1] * e1) + (v[2] * e2 + (half ? z : v[3]) * e3)) * __builtin_amdgcn_rcpf(L);
  const float ra = red4(ya), rb = red4(half ? v[3] : z);
  if ((lane & 8) == 0) red[wv][(lane & 4) * 4 + (lane & 3) * 4 + (((lane >> 4) & 1) * 2 + (lane >> 5))] = (lane & 4) ? rb : ra;
  __syncthreads();
  if (tid < 32) out[(2 * bp + ((tid >> 3) & 1)) * 1152 + (tid >> 4) * 576 + c8 * 8 + (tid & 7)] = to_bf16((red[0][tid] + red[1][tid]) + (red[2][tid] + red[3][tid]));
  if (tid == 0) reinterpret_cast<u64*>(ws + (19u << 20))[id * 2 + 1] = __builtin_amdgcn_s_memrealtime();

  if (id == 0) {
    const u64 t_end = __builtin_amdgcn_s_memrealtime();
    __shared__ u64 sh[256]; __shared__ u64 sx[256];
    const u64* st = reinterpret_cast<const u64*>(ws + (16u << 20)) + tid * 16;
    sh[tid] = st[15];
    __syncthreads();
    u64 t0 = ~0ull; for (int i = 0; i < 256; ++i) t0 = sh[i] < t0 ? sh[i] : t0;
    const bool isB = tid < NBW;
    u64 x = 0; int mode = 0;
    if (DIAG == 1) x = t_end - t0;
    if (DIAG == 2) x = st[isB ? 7 : 4] - t0;
    if (DIAG == 3) { x = isB ? 0 : st[1] - t0; mode = 1; }
    if (DIAG == 4) { x = isB ? 0 : st[4] - t0; mode = 1; }
    if (DIAG == 5) x = isB ? st[1] - t0 : 0;
    if (DIAG == 6) x = isB ? st[3] - t0 : 0;
    if (DIAG == 7) x = st[15] - t0;
    if (DIAG == 8) x = isB ? 0 : st[4] - t0;
    if (DIAG == 9) x = isB ? st[7] - t0 : 0;
    if (DIAG == 10) { x = isB ? 0 : st[0] - t0; mode = 1; }
    if (DIAG == 11) { x = isB ? st[2] - t0 : 0; }
    if (DIAG == 12) x = 10 * (t_end - t_fentry);
    if (DIAG == 13) { x = st[isB ? 7 : 4]; mode = 3; }
    sx[tid] = x;
    __syncthreads();
    u64 X = 0;
    if (mode == 0) { for (int i = 0; i < 256; ++i) X = sx[i] > X ? sx[i] : X; }
    else { for (int i = 0; i < 256; ++i) X += sx[i]; X /= (mode == 1 ? 224 : 32); }
    if (mode == 3) { X = 0; for (int i = 0; i < 256; ++i) X = sx[i] > X ? sx[i] : X; X = 10 * (t_fentry - X); }
    if (X > 20000) X = 20000;
    const u64 tgt = t0 + 10000 + X;
    for (int i = 0; i < 2000000; ++i) { if (__builtin_amdgcn_s_memrealtime() >= tgt) break; __builtin_amdgcn_s_sleep(1); }
  }
}

__global__ void __launch_bounds__(1024, 4) k_main(Args a) {
  __shared__ __attribute__((aligned(16))) unsigned char lds[L_TOTAL];
  u64 t_entry; asm volatile("s_memrealtime %0\n s_waitcnt lgkmcnt(0)" : "=s"(t_entry) :: "memory");
  u32 pf;
  { u64 pc; u32 len;
    asm volatile("s_getpc_b64 %0\n.Lpf%=:\n\ts_mov_b32 %1, k_tail_mark-.Lpf%=" : "=s"(pc), "=s"(len));
    pf = __builtin_amdgcn_raw_buffer_load_b32(__builtin_amdgcn_make_buffer_rsrc(reinterpret_cast<void*>(pc), 0, len < 131072u ? len : 0u, 0x00020000), threadIdx.x * 128, 0, 0);
    __builtin_amdgcn_sched_barrier(0); }
  u64 tag = (hw_dispatch_id() + 1ull) * 0x9E3779B97F4A7C15ull;
  tag ^= ((u64)__builtin_amdgcn_dispatch_ptr() >> 6) * 0xC2B2AE3D27D4EB4Full;
  tag |= 1ull << 63;
  if (__builtin_expect(blockIdx.x >= NBW, 1)) part_a(a, lds, blockIdx.x - NBW, tag);
  else if (__builtin_amdgcn_readfirstlane(threadIdx.x >> 6) < 8) part_b<false>(a, lds, blockIdx.x, tag);
  else part_b<true>(a, lds, blockIdx.x, tag);
  asm volatile("" :: "v"(pf));
  if (threadIdx.x == 0) reinterpret_cast<u64*>(a.ws + (16u << 20))[blockIdx.x * 16 + 15] = t_entry;
}

extern "C" __global__ void k_tail_mark() {}

extern "C" void kernel_launch(void* const* d_in, const int* in_sizes, int n_in, void* d_out, int out_size, void* d_ws, size_t ws_size,
                              hipStream_t stream) {
  Args a;
  a.xA = (const u16*)d_in[0]; a.K = (const u16*)d_in[1]; a.V = (const u16*)d_in[2]; a.wq = (const u16*)d_in[3]; a.wo = (const u16*)d_in[4];
  a.xB = (const u16*)d_in[5]; a.C = (const u16*)d_in[6]; a.wqB = (const u16*)d_in[7]; a.wuk = (const u16*)d_in[8]; a.wuv = (const u16*)d_in[9]; a.woB = (const u16*)d_in[10];
  a.ws = (unsigned char*)d_ws;
  a.out = (u16*)d_out;
  k_main<<<NBW + NAW, 1024, 0, stream>>>(a);
  k_final<<<144, 256, 0, stream>>>((unsigned char*)d_ws, (u16*)d_out);
}
```

```cpp
#include <hip/hip_runtime.h>
#include <stdint.h>
#include <utility>

typedef unsigned short u16;
typedef unsigned u32;
typedef unsigned long long u64;
typedef u32 u32x4 __attribute__((ext_vector_type(4)));
typedef u32 u32x2 __attribute__((ext_vector_type(2)));
typedef float f32x4 __attribute__((ext_vector_type(4)));
typedef float f32x2 __attribute__((ext_vector_type(2)));
typedef short s16x4 __attribute__((ext_vector_type(4)));
typedef __bf16 bf16x8 __attribute__((ext_vector_type(8)));
typedef __bf16 bf16x2 __attribute__((ext_vector_type(2)));
#define DI __device__ __forceinline__
#define RLX_AGENT __ATOMIC_RELAXED, __HIP_MEMORY_SCOPE_AGENT
extern "C" __device__ u64 hw_dispatch_id() __asm("llvm.amdgcn.dispatch.id");

template <int... Is, class F> DI void static_for_impl(std::integer_sequence<int, Is...>, F&& f) { (f(std::integral_constant<int, Is>{}), ...); }
template <int N, class F> DI void static_for(F&& f) { static_for_impl(std::make_integer_sequence<int, N>{}, f); }

constexpr int H = 32, SK = 4096, DM = 576, DC = 512;
constexpr int ACU = 7;
constexpr int NBW = 32;
constexpr int NAW = H * ACU;
constexpr int NSP = 8;
constexpr float C2 = 0.08838834764831845f * 1.4426950408889634f;

constexpr size_t WS_T0 = 2048, WS_ACC = 2560;
constexpr size_t WS_F1 = 0, WS_F2 = 512;
constexpr size_t WS_QABS = 4096;
constexpr size_t WS_CTXP = WS_QABS + 4 * 32 * 512 * 2;
constexpr size_t WS_MLB = WS_CTXP + (size_t)4 * NSP * 32 * 512 * 2;
constexpr size_t WS_Y = WS_MLB + 4 * NSP * 32 * 2 * 4;
constexpr size_t WS_MLA = WS_Y + (size_t)72 * 256 * 128;
constexpr size_t WS_RPT = WS_MLA + 256 * 32;
constexpr size_t WS_END = WS_RPT + 64 * 4480;

constexpr int CRS = 1056;
constexpr int CBUF = 64 * CRS;
constexpr int L_BIG = 0;
constexpr int L_AM = 2 * CBUF;
constexpr int L_QS = L_AM + 16 * 272;
constexpr int L_ST = L_QS + 1024;
constexpr int L_PF = L_ST + 4096;
constexpr int L_TOTAL = L_PF + 4096;
constexpr int AM_LD = 136;
constexpr int AM2_LD = 520;
constexpr int L_AM2 = 69632, L_CP = 88064, L_W8 = 106496;

#define ST(i) do {} while (0)
struct Args {
  const u16 *xA, *K, *V, *wq, *wo, *xB, *C, *wqB, *wuk, *wuv, *woB;
  unsigned char* ws;
  u16* out;
};

DI float bf_lo(u32 x) { return __uint_as_float(x << 16); }
DI float bf_hi(u32 x) { return __uint_as_float(x & 0xffff0000u); }
DI u32 pk_bf16(float lo, float hi) { bf16x2 v = {(__bf16)lo, (__bf16)hi}; return __builtin_bit_cast(u32, v); }
DI u16 to_bf16(float x) { return __builtin_bit_cast(u16, (__bf16)x); }
DI float bf2f(u16 x) { return __uint_as_float(((u32)x) << 16); }
DI float dot2(u32 a, u32 b, float c) { return __builtin_amdgcn_fdot2_f32_bf16(__builtin_bit_cast(bf16x2, a), __builtin_bit_cast(bf16x2, b), c, false); }
template <int CTRL> DI float dpp(float x) { return __builtin_bit_cast(float, __builtin_amdgcn_update_dpp(0, __builtin_bit_cast(int, x), CTRL, 0xf, 0xf, true)); }
DI float row16_sum(float x) { x += dpp<0x128>(x); x += dpp<0x124>(x); x += dpp<0x122>(x); x += dpp<0x121>(x); return x; }
DI float row16_max(float x) { x = fmaxf(x, dpp<0x128>(x)); x = fmaxf(x, dpp<0x124>(x)); x = fmaxf(x, dpp<0x122>(x)); x = fmaxf(x, dpp<0x121>(x)); return x; }
DI float xg_max(float x) {
  auto s = __builtin_amdgcn_permlane16_swap(__float_as_uint(x), __float_as_uint(x), false, false);
  x = fmaxf(__uint_as_float(s[0]), __uint_as_float(s[1]));
  auto t = __builtin_amdgcn_permlane32_swap(__float_as_uint(x), __float_as_uint(x), false, false);
  return fmaxf(__uint_as_float(t[0]), __uint_as_float(t[1]));
}
DI float xg_sum(float x) {
  auto s = __builtin_amdgcn_permlane16_swap(__float_as_uint(x), __float_as_uint(x), false, false);
  x = __uint_as_float(s[0]) + __uint_as_float(s[1]);
  auto t = __builtin_amdgcn_permlane32_swap(__float_as_uint(x), __float_as_uint(x), false, false);
  return __uint_as_float(t[0]) + __uint_as_float(t[1]);
}
DI f32x4 mfma16(u32x4 a, u32x4 b, f32x4 c) { return __builtin_amdgcn_mfma_f32_16x16x32_bf16(__builtin_bit_cast(bf16x8, a), __builtin_bit_cast(bf16x8, b), c, 0, 0, 0); }
DI u32x4 ld16(const u16* p) { return __builtin_nontemporal_load(reinterpret_cast<const u32x4*>(p)); }
DI u32x2 ld8(const u16* p) { return __builtin_nontemporal_load(reinterpret_cast<const u32x2*>(p)); }
DI u32x2 trread(const unsigned char* p) {
  return __builtin_bit_cast(u32x2, __builtin_amdgcn_ds_read_tr16_b64_v4i16((__attribute__((address_space(3))) s16x4*)(p)));
}
DI u32 prm(u32 hi, u32 lo, int odd) { return __builtin_amdgcn_perm(hi, lo, odd ? 0x07060302u : 0x05040100u); }

DI u32x4 tcol16(const u32x4 (&R)[8], int t) {
  u32x4 T;
#pragma unroll
  for (int q = 0; q < 4; ++q) T[q] = prm(R[2 * q + 1][t >> 1], R[2 * q][t >> 1], t & 1);
  return T;
}
DI u32x4 tcol8(const u32x2 (&R)[8], int t) {
  u32x4 T;
#pragma unroll
  for (int q = 0; q < 4; ++q) T[q] = prm(R[2 * q + 1][t >> 1], R[2 * q][t >> 1], t & 1);
  return T;
}

#define SPINLOG 22
#define PSL 2
#define YAUX 16
#define ACQ 0
constexpr u32 SPIN_CAP = 1u << SPINLOG;
DI void wave_wait(const u64* f, bool active, u64 tag) {
  for (u32 spins = 0; spins < SPIN_CAP; ++spins) {
    const u64 v = active ? __hip_atomic_load(f, RLX_AGENT) : tag;
    if (__all(v == tag)) break;
    __builtin_amdgcn_s_sleep(PSL);
  }
}
DI void wg_wait64(const unsigned char* flags, u64 tag, int pw = 0) {
  if ((int)(threadIdx.x >> 6) == pw) {
    wave_wait(reinterpret_cast<const u64*>(flags) + (threadIdx.x & (NBW - 1)), true, tag);
    asm volatile("s_waitcnt vmcnt(0)" ::: "memory");
  }
  __syncthreads();
}
DI void st16_sc1(__amdgpu_buffer_rsrc_t rs, size_t off, u32x4 v) { __builtin_amdgcn_raw_buffer_store_b128(v, rs, (int)off, 0, 16); }
DI void st8_sc1(unsigned char* p, f32x2 v) { __hip_atomic_store(reinterpret_cast<u64*>(p), __builtin_bit_cast(u64, v), RLX_AGENT); }

struct QPre { u32x4 R0[8]; u32x4 R1; u32x2 xa, xb; u32 x1; };
DI void qproj_load(QPre& Q, const u16* x, const u16* Wh, int w, int lane) {
  const int j = lane & 15, g = lane >> 4;
  Q.xa = u32x2{0u, 0u}; Q.xb = u32x2{0u, 0u}; Q.x1 = 0u;
  if (j < 4) { Q.xa = ld8(x + j * DM + 36 * w + 8 * g); Q.xb = ld8(x + j * DM + 36 * w + 8 * g + 4); Q.x1 = x[j * DM + 36 * w + 32 + g]; }
#pragma unroll
  for (int jj = 0; jj < 8; ++jj) Q.R0[jj] = ld16(Wh + (size_t)(36 * w + 8 * g + jj) * 4096 + 8 * j);
  Q.R1 = ld16(Wh + (size_t)(36 * w + 32 + g) * 4096 + 8 * j);
}
DI void qproj_mfma(const QPre& Q, unsigned char* lds, int w, int lane) {
  const int j = lane & 15, g = lane >> 4;
  float* qpart = reinterpret_cast<float*>(lds + L_BIG);
  const u32x4 a = {Q.xa[0], Q.xa[1], Q.xb[0], Q.xb[1]}, a1 = {Q.x1, 0u, 0u, 0u};
#pragma unroll
  for (int hf = 0; hf < 2; ++hf) {
    f32x4 acc[4];
#pragma unroll
    for (int t = 0; t < 4; ++t) acc[t] = mfma16(a, tcol16(Q.R0, 4 * hf + t), f32x4{0.f, 0.f, 0.f, 0.f});
#pragma unroll
    for (int t = 0; t < 4; ++t) { const u32x4 bb = {(t & 1) ? (Q.R1[2 * hf + (t >> 1)] >> 16) : (Q.R1[2 * hf + (t >> 1)] & 0xffffu), 0u, 0u, 0u}; acc[t] = mfma16(a1, bb, acc[t]); }
    if (g == 0) {
#pragma unroll
      for (int r = 0; r < 4; ++r) *reinterpret_cast<f32x4*>(qpart + (w * 4 + r) * 128 + 8 * j + 4 * hf) = f32x4{acc[0][r], acc[1][r], acc[2][r], acc[3][r]};
    }
    __builtin_amdgcn_sched_barrier(0);
  }
}
DI void qproj_finish(unsigned char* lds) {
  const float* qpart = reinterpret_cast<const float*>(lds + L_BIG);
  __syncthreads();
  if (threadIdx.x < 512) {
    float s = 0.f;
#pragma unroll
    for (int ww = 0; ww < 16; ++ww) s += qpart[ww * 512 + threadIdx.x];
    reinterpret_cast<u16*>(lds + L_QS)[threadIdx.x] = to_bf16(s);
  }
  __syncthreads();
}

DI void wo_load(const u16* Wo, u32x2 (&R)[4][8], int nb, int lane, int s0, int s1) {
  const int j = lane & 15, g = lane >> 4;
#pragma unroll
  for (int s = 0; s < 4; ++s) if (s >= s0 && s < s1)
#pragma unroll
    for (int jj = 0; jj < 8; ++jj) R[s][jj] = ld8(Wo + (size_t)(32 * s + 8 * g + jj) * DM + 64 * nb + 4 * j);
}
DI void wo_mfma(const u32x2 (&R)[4][8], const unsigned char* lds, int lane, f32x4 (&acc)[4]) {
  const int j = lane & 15, g = lane >> 4;
#pragma unroll
  for (int t = 0; t < 4; ++t) acc[t] = f32x4{0.f, 0.f, 0.f, 0.f};
#pragma unroll
  for (int s = 0; s < 4; ++s) {
    const u32x4 a = *reinterpret_cast<const u32x4*>(lds + L_AM + j * 272 + (32 * s + 8 * g) * 2);
#pragma unroll
    for (int t = 0; t < 4; ++t) acc[t] = mfma16(a, tcol8(R[s], t), acc[t]);
  }
}
DI u32x4 yrow(const f32x4 (&acc)[4]) {
  const f32x4 o = {acc[0][0] + acc[0][1], acc[1][0] + acc[1][1], acc[2][0] + acc[2][1], acc[3][0] + acc[3][1]};
  return __builtin_bit_cast(u32x4, o);
}
DI void am_zero(unsigned char* lds, int tid) {
  if (tid < 136) { const int pr = tid / 34; *reinterpret_cast<u32x4*>(lds + L_AM + (4 * pr + 2) * 272 + (tid - 34 * pr) * 16) = u32x4{0u, 0u, 0u, 0u}; }
}
DI void st16_y(__amdgpu_buffer_rsrc_t rs, int off, u32x4 v) { __builtin_amdgcn_raw_buffer_store_b128(v, rs, off, 0, YAUX); }
DI float rdlane(float x, int l) { return __builtin_bit_cast(float, __builtin_amdgcn_readlane(__builtin_bit_cast(int, x), l)); }

constexpr int NS = 10;
constexpr int NSEQ = 8 * NS;
#define ORDN 0
#define ORD "KWWKWWKWWKWWKWKK"
constexpr char ord[] = ORD;
constexpr int ord_len() { int n = 0; while (ord[n]) ++n; return n; }
constexpr int ord_cnt(int n, char c) { int k = 0; for (int i = 0; i < n; ++i) k += ord[i] == c; return k; }
#define POST 0
#define DEP 2
constexpr int DEPTH = DEP, PRE = ord_cnt(ord_len(), 'K'), RING = PRE + POST, RAMP = 2;
static_assert(ord_cnt(ord_len(), 'W') == 9, "");
constexpr int issued(int i) {
  int d = RING - (i + RAMP - 1) / RAMP; if (d < DEPTH) d = DEPTH;
  int e = i + d; if (e < RING) e = RING; return e > NSEQ ? NSEQ : e;
}
#define PACE 1
#define P20 1350
#define KN 46
#define PK 2780
#define PE 4300
#define THK 0
#define THA 14
#define THB 56
#define PFROM 0
#define YDEF 4400
#define WO0 20
#define WON 32
#define PAT 1
#define TA1 535
#define TA2 450
#define NK 68
#define ORG0 100
#define LEAD 5
#define RN0 14
#define RP 13
#define BETA 10
#define SL0 1
#define SLMAX 128
#define MAXGAP 70
constexpr int cmin(int a, int b) { return a < b ? a : b; }
#define RSH 1
#define RD 3
constexpr int due_at(int n) { return (n <= NK ? n * TA1 : NK * TA1 + (n - NK) * TA2) / 10; }
constexpr int NRND = (NSEQ - 1 - RD - 2 - RN0) / RP + 1;
constexpr int rnd_at(int n) { return (n >= RN0 && (n - RN0) % RP == 0 && (n - RN0) / RP < NRND) ? (n - RN0) / RP : -1; }
constexpr int wo_step(int k) { return WO0 + k * WON / 32; }
#define STEPST 0
#define CPFM 1
#define GT0 0
#define STAG 16
DI u32 rt32() { return (u32)__builtin_amdgcn_s_memrealtime(); }
DI void part_a(const Args& a, unsigned char* lds, int idx, u64 tag) {
  const int tid = threadIdx.x, lane = tid & 63, w = __builtin_amdgcn_readfirstlane(tid >> 6);
  u32 T0 = rt32();
  const u32 toff = (u32)(w * TA2) / (STAG * 10);
  u32* lorg = reinterpret_cast<u32*>(lds + L_ST + 2048);
  if (tid == 0) { lorg[0] = ORG0; lorg[1] = 0u; lorg[2] = T0; lorg[3] = ORG0; }
  u32 last = T0;
  u32x2 rv = {0u, 0u}; u32 rt = 0;
  const int j = lane & 15, g = lane >> 4;
  const int h = (idx & 7) * 4 + ((idx >> 3) & 3), ci = idx >> 5;
  const int U0 = 0, NUN = 1024;
  const int rot = h % NS;
  const int l0 = (ci * 16) / ACU, ln = ((ci + 1) * 16) / ACU - l0;
  const int bad_sl = (w < ln) ? -1 : NS - 1 - rot;
  const int sstr = 7 * 16384, s0 = ci * 16384 + w * 1024, s9 = (w < ln) ? (1008 + l0 + w) * 1024 : 1 << 20;
  const __amdgpu_buffer_rsrc_t rs = __builtin_amdgcn_make_buffer_rsrc(a.ws, 0, (int)WS_END, 0x00020000);
  const u16* Wq = a.wq + h * 128;
  QPre Q;
  ST(0);
  Q.xa = u32x2{0u, 0u}; Q.xb = u32x2{0u, 0u}; Q.x1 = 0u;
  if (j < 4) { Q.xa = ld8(a.xA + j * DM + 36 * w + 8 * g); Q.xb = ld8(a.xA + j * DM + 36 * w + 8 * g + 4); Q.x1 = a.xA[j * DM + 36 * w + 32 + g]; }
  const size_t base = ((size_t)h * SK + 4 * U0) * 128;
  const int voff = lane * 16;
  u32x4 R[RING];
#define A_ISSUE(i) R[(i) % RING] = __builtin_amdgcn_raw_buffer_load_b128( \
    __builtin_amdgcn_make_buffer_rsrc(const_cast<u16*>((((i) >= 4 * NS) ? a.V : a.K) + base + (size_t)(((i) / NS) & 3) * H * SK * 128), 0, NUN * 1024, 0x00020000), \
    voff, ((i) % NS) + rot == NS - 1 ? s9 : (((i) % NS) + rot >= NS ? ((i) % NS) + rot - NS : ((i) % NS) + rot) * sstr + s0, 2)
#define GATE(n) do { if (PACE) { u32 due = T0 + toff + org + ((osl * cmin(n, NK)) >> 4) + (u32)due_at(n); if (MAXGAP && (int)(due - (last + MAXGAP)) > 0) due = last + MAXGAP; \
    while ((int)(now - due) < 0) { __builtin_amdgcn_s_sleep(1); now = rt32(); } last = now; } } while (0)
#define CTRL(n) do { if constexpr (PACE) { \
    if constexpr (rnd_at((n) - RD - 2) >= 0) { constexpr int k = rnd_at((n) - RD - 2); \
      const u32 dt = (rv[1] == (u32)tag + k) ? rv[0] - T0 : rt; \
      const float sm = row16_sum((float)(int)dt); \
      const int o = (int)((RSH ? rdlane(sm, 0) * (1.f / 16.f) : (rdlane(sm, 0) + rdlane(sm, 16)) * (1.f / 32.f))) - due_at(RN0 + k * RP) - LEAD; \
      if (w == ((5 * k + 11) & 15) && lane == 0 && o > 0 && o < 2000) { \
        const int po = (int)lorg[3], on = o > po ? o : po; \
        int sl_ = k >= SL0 ? ((on - po) * BETA) / RP : 0; sl_ = sl_ > SLMAX ? SLMAX : sl_; \
        lorg[3] = (u32)on; __hip_atomic_store(reinterpret_cast<u64*>(lorg), __builtin_bit_cast(u64, u32x2{(u32)(on - ((sl_ * cmin(RN0 + k * RP, NK)) >> 4)), (u32)sl_}), __ATOMIC_RELAXED, __HIP_MEMORY_SCOPE_WORKGROUP); } } \
    if constexpr (rnd_at(n) >= 0) { constexpr int k = rnd_at(n); \
      __builtin_amdgcn_raw_buffer_store_b64(u32x2{rt32() - toff, (u32)tag + k}, rs, (ci == k % ACU && (h & ((1 << RSH) - 1)) == (k & ((1 << RSH) - 1)) && w == ((5 * k + 3) & 15) && lane == 0) ? (int)WS_RPT + (h >> RSH) * 4352 : 0x7ffff000, 128 * k * 35, 16); } \
    if constexpr (rnd_at((n) - RD) >= 0) { constexpr int k = rnd_at((n) - RD); \
      rv = __builtin_amdgcn_raw_buffer_load_b64(rs, (w == ((5 * k + 11) & 15) && lane < (32 >> RSH)) ? (int)WS_RPT + lane * 4352 : 0x7ffff000, 128 * k * 35, 16); rt = rt32() - T0; } \
  } } while (0)
  static_for<ord_len()>([&](auto oc) __attribute__((always_inline)) {
    constexpr int o = oc.value, kk = ord_cnt(o, 'K'), wk = ord_cnt(o, 'W');
    if constexpr (ord[o] == 'K') A_ISSUE(kk);
    else if constexpr (wk < 8) Q.R0[wk] = ld16(Wq + (size_t)(36 * w + 8 * g + wk) * 4096 + 8 * j);
    else Q.R1 = ld16(Wq + (size_t)(36 * w + 32 + g) * 4096 + 8 * j);
    __builtin_amdgcn_sched_barrier(0);
  });
  am_zero(lds, tid);
  qproj_mfma(Q, lds, w, lane);
  u64 g0 = 0; if (GT0) g0 = __hip_atomic_load(reinterpret_cast<const u64*>(a.ws + WS_T0), RLX_AGENT);
  __builtin_amdgcn_sched_barrier(0);
#pragma unroll
  for (int t = PRE; t < RING; ++t) A_ISSUE(t);
  __builtin_amdgcn_sched_barrier(0);
  qproj_finish(lds);
  T0 = __builtin_amdgcn_readfirstlane(lorg[2]);
  ST(1);
  float* pacc = reinterpret_cast<float*>(lds + L_BIG);
  float* pml = reinterpret_cast<float*>(lds + L_ST);
  u32x4 q;
  float acc[8], S[4];
  u32x2 RW[4][8];
  const __amdgpu_buffer_rsrc_t rwo = __builtin_amdgcn_make_buffer_rsrc(const_cast<u16*>(a.wo + (size_t)h * 128 * DM), 0, w < 9 ? 128 * DM * 2 : 0, 0x00020000);
  const int wvo = (8 * g * DM + 64 * w + 4 * j) * 2;
#define WO_ISSUE() static_for<32>([&](auto kc) __attribute__((always_inline)) { constexpr int k = kc.value; \
    if constexpr (wo_step(k) == i) RW[k >> 3][k & 7] = __builtin_amdgcn_raw_buffer_load_b64(rwo, wvo, (32 * (k >> 3) + (k & 7)) * DM * 2, 2); })
  static_for<NSEQ>([&](auto ic) __attribute__((always_inline)) {
    constexpr int i = ic.value, bb = (i / NS) & 3, pass = i / (4 * NS), sl = i % NS;
    u32 now = 0; if (PACE || STEPST) now = rt32();
    u32 org = 0, osl = 0; if (PACE) { const u32x2 ov = __builtin_bit_cast(u32x2, __hip_atomic_load(reinterpret_cast<u64*>(lorg), __ATOMIC_RELAXED, __HIP_MEMORY_SCOPE_WORKGROUP)); org = __builtin_amdgcn_readfirstlane(ov[0]); osl = __builtin_amdgcn_readfirstlane(ov[1]); }
    if (STEPST && lane == 0) reinterpret_cast<u32*>(lds + 65536)[w * 96 + i] = now;
    if constexpr (GT0 && i == PRE + 1) {
      const u32 lo = __builtin_amdgcn_readfirstlane((u32)g0), hi = __builtin_amdgcn_readfirstlane((u32)(g0 >> 32));
      if (hi == (u32)(tag >> 32) && T0 - lo + 400u < 800u) T0 = lo;
    }
    __builtin_amdgcn_sched_barrier(0);
    const u32x4 r = R[i % RING];
    if constexpr (pass == 0) {
      if constexpr (sl == 0) { q = *reinterpret_cast<const u32x4*>(lds + L_QS + (bb * 128 + 8 * j) * 2); S[bb] = -INFINITY; }
      float d = dot2(r[0], q[0], 0.f); d = dot2(r[1], q[1], d); d = dot2(r[2], q[2], d); d = dot2(r[3], q[3], d);
      asm volatile("" : "+v"(d));
      if constexpr (issued(i + 1) > issued(i)) { GATE(issued(i)); WO_ISSUE(); A_ISSUE(issued(i)); CTRL(issued(i)); } else { WO_ISSUE(); }
      d = row16_sum(d);
      S[bb] = (j == sl) ? d : S[bb];
      if constexpr (sl == NS - 1) {
        const float sc = (j == bad_sl) ? -INFINITY : S[bb] * C2;
        const float m = xg_max(row16_max(sc));
        S[bb] = __builtin_amdgcn_exp2f(sc - m);
        const float l = xg_sum(row16_sum(S[bb]));
        if (lane == 0) *reinterpret_cast<f32x2*>(pml + (bb * 16 + w) * 2) = f32x2{m, l};
      }
    } else {
      const float p = dpp<0x150 + sl>(S[bb]);
      if constexpr (sl == 0) {
#pragma unroll
        for (int k = 0; k < 8; ++k) acc[k] = 0.f;
      }
#pragma unroll
      for (int k = 0; k < 4; ++k) { acc[2 * k] = fmaf(p, bf_lo(r[k]), acc[2 * k]); acc[2 * k + 1] = fmaf(p, bf_hi(r[k]), acc[2 * k + 1]); }
      asm volatile("" : "+v"(acc[0]), "+v"(acc[1]), "+v"(acc[2]), "+v"(acc[3]), "+v"(acc[4]), "+v"(acc[5]), "+v"(acc[6]), "+v"(acc[7]));
      if constexpr (issued(i + 1) > issued(i)) { GATE(issued(i)); WO_ISSUE(); A_ISSUE(issued(i)); CTRL(issued(i)); } else { WO_ISSUE(); }
      if constexpr (sl == NS - 1) {
#pragma unroll
        for (int k = 0; k < 8; ++k) acc[k] = xg_sum(acc[k]);
        if (g == 0) {
          float* pp = pacc + (bb * 16 + w) * 128 + 8 * j;
          *reinterpret_cast<f32x4*>(pp) = f32x4{acc[0], acc[1], acc[2], acc[3]};
          *reinterpret_cast<f32x4*>(pp + 4) = f32x4{acc[4], acc[5], acc[6], acc[7]};
        }
      }
    }
    __builtin_amdgcn_sched_barrier(0);
  });
#undef A_ISSUE
  if (STEPST) { if (lane == 0) { reinterpret_cast<u32*>(lds + 65536)[w * 96 + NSEQ] = rt32(); reinterpret_cast<u32*>(lds + 65536)[w * 96 + NSEQ + 1] = lorg[3]; }
    for (int k = lane; k < 96; k += 64) reinterpret_cast<u32*>(a.ws + (20u << 20))[(blockIdx.x * 16 + w) * 96 + k] = reinterpret_cast<u32*>(lds + 65536)[w * 96 + k]; }
  ST(2);
  __syncthreads();
  ST(3);
  if (w < 8) {
    const int bb = w >> 1, d = tid & 127;
    u16* Am = reinterpret_cast<u16*>(lds + L_AM);
    const f32x2 ml = *reinterpret_cast<const f32x2*>(pml + (bb * 16 + j) * 2);
    float pv[16];
#pragma unroll
    for (int i = 0; i < 16; ++i) pv[i] = pacc[(bb * 16 + i) * 128 + d];
    const float wt = __builtin_amdgcn_exp2f(ml[0] - row16_max(ml[0]));
    float o0 = 0.f, o1 = 0.f;
#pragma unroll
    for (int i = 0; i < 16; i += 2) { o0 = fmaf(rdlane(wt, i), pv[i], o0); o1 = fmaf(rdlane(wt, i + 1), pv[i + 1], o1); }
    const float o = o0 + o1;
    const u16 hi = to_bf16(o); const u16 lo = to_bf16(o - bf2f(hi));
    Am[(4 * bb) * AM_LD + d] = hi; Am[(4 * bb + 1) * AM_LD + d] = lo;
  } else if (w == 9) {
    const f32x2 ml = *reinterpret_cast<const f32x2*>(pml + lane * 2);
    const float M = row16_max(ml[0]);
    const float L = row16_sum(__builtin_amdgcn_exp2f(ml[0] - M) * ml[1]);
    if (j == 0) __builtin_amdgcn_raw_buffer_store_b64(__builtin_bit_cast(u32x2, f32x2{M, L}), rs, (int)WS_MLA + (((g >> 1) * 32 + h) * 2 + (ci >> 2)) * 64 + (g & 1) * 32 + (ci & 3) * 8, 0, YAUX);
  }
  __syncthreads();
  if (w < 9) {
    f32x4 ya[4];
    wo_mfma(RW, lds, lane, ya);
    st16_y(rs, (int)WS_Y + (((8 * w + (j >> 1)) * 2 + (g >> 1)) * 256 + h * 8 + 2 * (ci & 3) + (ci >> 2)) * 64 + (g & 1) * 32 + (j & 1) * 16, yrow(ya));
  }
  ST(4);
}

#define BCUT 0
#define CAUX 16
#define WPD 4
template <bool LD> DI void part_b(const Args& a, unsigned char* lds, int wg, u64 tag) {
  const int tid = threadIdx.x, lane = tid & 63, w = __builtin_amdgcn_readfirstlane(tid >> 6);
  const int j = lane & 15, g = lane >> 4;
  const __amdgpu_buffer_rsrc_t rs = __builtin_amdgcn_make_buffer_rsrc(a.ws, 0, (int)WS_END, 0x00020000);
  const u32 tb0 = rt32();
  const int h = wg;
  const int h3 = wg ^ 2;
  const int b2 = wg >> 3, sp = wg & 7;
  const u16* cl = a.C + ((size_t)b2 * SK + sp * 512 + 8 * (w - 8)) * DC + lane * 8;
  u32x4 X[16];
  u32x2 RW[4][8];
  u32x4 W8[2];
  u32 tv = 0;
#define BST(n) do { if (STEPST && LD) { __builtin_amdgcn_sched_barrier(0); const u32 t_ = rt32(); tv = (lane == (n)) ? t_ : tv; __builtin_amdgcn_sched_barrier(0); } } while (0)
  u32* cnt = reinterpret_cast<u32*>(lds + L_ST + 4064);
#define C_ISSUE(T) _Pragma("unroll") for (int r = 0; r < 8; ++r) X[8 * ((T) & 1) + r] = ld16(cl + (size_t)(64 * (T) + r) * DC)
#define C_HALF(T, hf) _Pragma("unroll") for (int r = 4 * (hf); r < 4 * (hf) + 4; ++r) X[8 * ((T) & 1) + r] = ld16(cl + (size_t)(64 * (T) + r) * DC)
#define C_WIN(T, r0, r1) _Pragma("unroll") for (int r = r0; r < r1; ++r) { asm volatile("s_waitcnt vmcnt(%0)" :: "n"(WPD - 1) : "memory"); X[8 * ((T) & 1) + r] = ld16(cl + (size_t)(64 * (T) + r) * DC); __builtin_amdgcn_sched_barrier(0); }
  u32 sb0 = L_BIG + 8 * (w - 8) * CRS + lane * 16, sb1 = sb0 + CBUF;
  asm volatile("" : "+v"(sb0), "+v"(sb1));
#define C_STAGE(T) _Pragma("unroll") for (int r = 0; r < 8; ++r) *reinterpret_cast<u32x4*>(lds + (((T) & 1) ? sb1 : sb0) + r * CRS) = X[8 * ((T) & 1) + r]
  {
    const u16* Wq = a.wqB + h * 128;
    u32x4 U[8];
    QPre Q;
    if constexpr (!LD) { ST(0); }
    qproj_load(Q, a.xB, Wq, w, lane);
#pragma unroll
    for (int i = 0; i < 8; ++i) U[i] = ld16(a.wuk + ((size_t)(32 * w + 4 * i + (j & 3)) * H + h) * 128 + 8 * ((j & 12) + g));
    __builtin_amdgcn_sched_barrier(0);
    BST(0);
    if constexpr (!LD) am_zero(lds, tid);
    qproj_mfma(Q, lds, w, lane);
    BST(1);
    __syncthreads();
    BST(2);
    if constexpr (LD) { C_HALF(0, 0); }
    else {
      const float* qpart = reinterpret_cast<const float*>(lds + L_BIG);
      float s = 0.f;
#pragma unroll
      for (int ww = 0; ww < 16; ++ww) s += qpart[ww * 512 + tid];
      reinterpret_cast<u16*>(lds + L_QS)[tid] = to_bf16(s);
    }
    __builtin_amdgcn_sched_barrier(0);
    __syncthreads();
    BST(3);
    const u32x4 af = *reinterpret_cast<const u32x4*>(lds + L_QS + ((j >> 2) * 128 + ((j & 3) * 4 + g) * 8) * 2);
    u16* qa = reinterpret_cast<u16*>(lds + L_PF);
    float v2[2] = {0.f, 0.f};
#pragma unroll
    for (int i = 0; i < 8; ++i) {
      const f32x4 d4 = mfma16(af, U[i], f32x4{0.f, 0.f, 0.f, 0.f});
      float d = (j & 8) ? ((j & 4) ? d4[3] : d4[2]) : ((j & 4) ? d4[1] : d4[0]);
      d += dpp<0x124>(d); d += dpp<0x128>(d);
      v2[i >> 2] = ((j >> 2) == (i & 3)) ? d : v2[i >> 2];
    }
    qa[g * 512 + 32 * w + j] = to_bf16(v2[0]); qa[g * 512 + 32 * w + 16 + j] = to_bf16(v2[1]);
    __builtin_amdgcn_sched_barrier(0);
    BST(4);
    __syncthreads();
    if constexpr (!LD) {
      if (w == 0) {
#pragma unroll
        for (int bb = 0; bb < 4; ++bb) st16_sc1(rs, WS_QABS + ((bb * H + h) * 512 + lane * 8) * 2, *reinterpret_cast<const u32x4*>(qa + bb * 512 + lane * 8));
      }
      __builtin_amdgcn_sched_barrier(0);
      __builtin_amdgcn_s_barrier();
      if (w == 0) {
        asm volatile("s_waitcnt vmcnt(0)" ::: "memory");
        if (lane == 0) { __hip_atomic_store(reinterpret_cast<u64*>(a.ws + WS_F1 + wg * 8), tag, RLX_AGENT); cnt[0] = 0u; cnt[1] = 0u; }
      }
    } else {
      __builtin_amdgcn_s_barrier();
      BST(5);
      C_WIN(0, 4, 8); C_WIN(1, 0, 8);
      BST(6);
      C_STAGE(0);
      __builtin_amdgcn_sched_barrier(0);
      BST(7);
      C_WIN(2, 0, 8);
      BST(8);
    }
    if constexpr (!LD) { ST(1); }
  }
  {
    const int b = b2;
    if constexpr (!LD) wg_wait64(a.ws + WS_F1, tag); else __syncthreads();
    if constexpr (!LD) { ST(2); }
    const int kt = w & 3, hh = (w >> 2) & 1;
    float* stmax = reinterpret_cast<float*>(lds + L_ST);
    float* stsum = stmax + 128;
    float* salpha = stmax + 256;
    unsigned char* Pf = lds + L_PF;
    float M = -INFINITY, lsum = 0.f;
    f32x4 acc[2][2];
#pragma unroll
    for (int ct = 0; ct < 2; ++ct) { acc[ct][0] = f32x4{0.f, 0.f, 0.f, 0.f}; acc[ct][1] = f32x4{0.f, 0.f, 0.f, 0.f}; }
    const int troff = (4 * g + (j >> 2)) * CRS + (32 * w + 4 * (j & 3)) * 2;
    auto pv_step = [&](const unsigned char* cb) __attribute__((always_inline)) {
      const f32x4 al0 = *reinterpret_cast<const f32x4*>(salpha + 4 * g), al1 = *reinterpret_cast<const f32x4*>(salpha + 16 + 4 * g);
#pragma unroll
      for (int ct = 0; ct < 2; ++ct) { acc[ct][0] *= al0; acc[ct][1] *= al1; }
#pragma unroll
      for (int blk = 0; blk < 2; ++blk) {
        const u32x4 p0 = *reinterpret_cast<const u32x4*>(Pf + ((blk * 2 + 0) * 64 + lane) * 16);
        const u32x4 p1 = *reinterpret_cast<const u32x4*>(Pf + ((blk * 2 + 1) * 64 + lane) * 16);
#pragma unroll
        for (int ct = 0; ct < 2; ++ct) {
          const u32x2 lo = trread(cb + troff + (32 * blk) * CRS + 32 * ct);
          const u32x2 hi = trread(cb + troff + (32 * blk + 16) * CRS + 32 * ct);
          const u32x4 bf = {lo[0], lo[1], hi[0], hi[1]};
          acc[ct][0] = mfma16(p0, bf, acc[ct][0]); acc[ct][1] = mfma16(p1, bf, acc[ct][1]);
        }
      }
    };
    unsigned char* Qf = lds + L_BIG + CBUF;
    if constexpr (!LD) {
      u32x4 qv[4];
#pragma unroll
      for (int k = 0; k < 4; ++k) qv[k] = __builtin_amdgcn_raw_buffer_load_b128(rs, (int)WS_QABS + (b * H * 512 + (tid + 512 * k) * 8) * 2, 0, 16);
#pragma unroll
      for (int k = 0; k < 4; ++k) { const int e = tid + 512 * k, head = e >> 6, ch = e & 63;
        *reinterpret_cast<u32x4*>(Qf + ((head >> 4) * 16 + (ch >> 2)) * 1040 + ((ch & 3) * 16 + (head & 15)) * 16) = qv[k]; }
    }
    __syncthreads();
    if constexpr (!LD) {
      u32x4 QF[16];
#pragma unroll
      for (int s = 0; s < 16; ++s) QF[s] = *reinterpret_cast<const u32x4*>(Qf + (hh * 16 + s) * 1040 + lane * 16);
      auto s_tile = [&](const unsigned char* cb) __attribute__((always_inline)) {
        __syncthreads();
        f32x4 s0 = {0.f, 0.f, 0.f, 0.f}, s1 = {0.f, 0.f, 0.f, 0.f};
#pragma unroll
        for (int s = 0; s < 16; s += 2) {
          s0 = mfma16(*reinterpret_cast<const u32x4*>(cb + (16 * kt + j) * CRS + (32 * s + 8 * g) * 2), QF[s], s0);
          s1 = mfma16(*reinterpret_cast<const u32x4*>(cb + (16 * kt + j) * CRS + (32 * s + 32 + 8 * g) * 2), QF[s + 1], s1);
        }
        const f32x4 sv = (s0 + s1) * C2;
        const float v = xg_max(fmaxf(fmaxf(sv[0], sv[1]), fmaxf(sv[2], sv[3])));
        if (g == 0) stmax[kt * 32 + 16 * hh + j] = v;
        __syncthreads();
        const float* sm = stmax + 16 * hh + j;
        const float Mn = fmaxf(fmaxf(M, fmaxf(sm[0], sm[32])), fmaxf(sm[64], sm[96]));
        const float alpha = __builtin_amdgcn_exp2f(M - Mn);
        M = Mn;
        u32x2 pk;
        pk[0] = pk_bf16(__builtin_amdgcn_exp2f(sv[0] - Mn), __builtin_amdgcn_exp2f(sv[1] - Mn));
        pk[1] = pk_bf16(__builtin_amdgcn_exp2f(sv[2] - Mn), __builtin_amdgcn_exp2f(sv[3] - Mn));
        lsum = lsum * alpha + ((bf_lo(pk[0]) + bf_hi(pk[0])) + (bf_lo(pk[1]) + bf_hi(pk[1])));
        *reinterpret_cast<u32x2*>(Pf + (((kt >> 1) * 2 + hh) * 64 + lane) * 16 + (kt & 1) * 8) = pk;
        if (kt == 0 && g == 0) salpha[16 * hh + j] = alpha;
        __syncthreads();
        pv_step(cb);
      };
#pragma unroll 1
      for (int T = 0; T < 8; T += 2) { s_tile(lds + L_BIG); s_tile(lds + L_BIG + CBUF); }
    } else {
#pragma unroll
      for (int T = 0; T < 8; ++T) {
        __syncthreads();
        BST(10 + 4 * T);
        if (T + 1 < 8) { C_STAGE(T + 1); }
        BST(11 + 4 * T);
        if (T + 3 < 8) { C_HALF(T + 3, 0); }
        if (T == 5 || T == 6) wo_load(a.woB + (size_t)h3 * 128 * DM, RW, w - 8, lane, 2 * (T - 5), 2 * (T - 5) + 1);
        if (T == 7) {
#pragma unroll
          for (int k = 0; k < 2; ++k) W8[k] = ld16(a.woB + ((size_t)h3 * 128 + 16 * (w - 8) + 8 * k + (lane >> 3)) * DM + 512 + (lane & 7) * 8);
        }
        BST(12 + 4 * T);
        __syncthreads();
        __syncthreads();
        BST(13 + 4 * T);
        if (T + 3 < 8) { C_HALF(T + 3, 1); }
        if (T == 5 || T == 6) wo_load(a.woB + (size_t)h3 * 128 * DM, RW, w - 8, lane, 2 * (T - 5) + 1, 2 * (T - 5) + 2);
        __builtin_amdgcn_sched_barrier(0);
        pv_step(lds + L_BIG + (T & 1) * CBUF);
      }
      BST(42);
      if (STEPST) reinterpret_cast<u32*>(a.ws + (20u << 20))[(blockIdx.x * 16 + w) * 96 + lane] = tv;
    }
#undef C_ISSUE
#undef C_STAGE
    if constexpr (!LD) { ST(3); }
    if (BCUT == 1) { asm volatile("" :: "v"(acc[0][0]), "v"(acc[0][1]), "v"(acc[1][0]), "v"(acc[1][1]), "v"(lsum)); if constexpr (LD) { asm volatile("" :: "v"(RW[0][0]), "v"(RW[1][7]), "v"(RW[2][7]), "v"(RW[3][7]), "v"(W8[1])); } ST(7); ST(5); return; }
    __syncthreads();
    if constexpr (!LD) { lsum = xg_sum(lsum); if (g == 0) stsum[kt * 32 + 16 * hh + j] = lsum; if (kt == 0 && g == 0) salpha[32 + 16 * hh + j] = M; }
    float* tb = reinterpret_cast<float*>(lds + L_BIG);
#pragma unroll
    for (int ct = 0; ct < 2; ++ct)
#pragma unroll
      for (int h2 = 0; h2 < 2; ++h2)
#pragma unroll
        for (int r = 0; r < 4; ++r) tb[(16 * h2 + 4 * g + r) * 516 + 32 * w + 16 * ct + j] = acc[ct][h2][r];
    if constexpr (!LD) { for (int i = tid; i < 8 * AM2_LD * 2 / 16; i += 512) reinterpret_cast<u32x4*>(lds + L_AM2 + 8 * AM2_LD * 2)[i] = u32x4{0u, 0u, 0u, 0u}; }
    __syncthreads();
    if constexpr (!LD) { ST(4); }
    if constexpr (LD) {
      const int t2 = tid - 512;
#pragma unroll
      for (int k = 0; k < 4; ++k) {
        const int e = t2 + 512 * k, head = e >> 6, c8 = e & 63;
        const f32x4 v0 = *reinterpret_cast<const f32x4*>(tb + head * 516 + 8 * c8), v1 = *reinterpret_cast<const f32x4*>(tb + head * 516 + 8 * c8 + 4);
        __builtin_amdgcn_raw_buffer_store_b128(u32x4{pk_bf16(v0[0], v0[1]), pk_bf16(v0[2], v0[3]), pk_bf16(v1[0], v1[1]), pk_bf16(v1[2], v1[3])}, rs, (int)WS_CTXP + (((b * NSP + sp) * 32 + head) * 512 + 8 * c8) * 2, 0, CAUX);
      }
      if (t2 < 32) {
        const float L = (stsum[t2] + stsum[32 + t2]) + (stsum[64 + t2] + stsum[96 + t2]);
        st8_sc1(a.ws + WS_MLB + ((size_t)(b * NSP + sp) * 32 + t2) * 8, f32x2{salpha[32 + t2], L});
      }
      __builtin_amdgcn_sched_barrier(0);
      __builtin_amdgcn_s_barrier();
      asm volatile("s_waitcnt vmcnt(0)" ::: "memory");
      if (lane == 0) {
        if (__hip_atomic_fetch_add(cnt, 1u, __ATOMIC_RELAXED, __HIP_MEMORY_SCOPE_WORKGROUP) == 7u) {
          __hip_atomic_store(reinterpret_cast<u64*>(a.ws + WS_F2 + wg * 8), tag, RLX_AGENT);
        }
      }
#pragma unroll
      for (int k = 0; k < 2; ++k) *reinterpret_cast<u32x4*>(lds + L_W8 + (16 * (w - 8) + 8 * k + (lane >> 3)) * 144 + (lane & 7) * 16) = W8[k];
    } else {
      __builtin_amdgcn_s_barrier();
      if (BCUT == 2) { ST(7); return; }
      static_for<16>([&](auto ic) __attribute__((always_inline)) {
        constexpr int k2 = ic.value >> 3, jj = ic.value & 7;
        if constexpr (ic.value >= WPD) asm volatile("s_waitcnt vmcnt(%0)" :: "n"(WPD - 1) : "memory");
        const u32x4 v = ld16(a.wuv + ((size_t)(64 * w + 32 * k2 + 8 * g + jj) * H + h3) * 128 + 8 * j);
        RW[2 * k2][jj] = u32x2{v[0], v[1]}; RW[2 * k2 + 1][jj] = u32x2{v[2], v[3]};
        __builtin_amdgcn_sched_barrier(0);
      });
    }
  }
#define KEEPRW() _Pragma("unroll") for (int s_ = 0; s_ < 4; ++s_) _Pragma("unroll") for (int k_ = 0; k_ < 8; ++k_) asm volatile("" :: "v"(RW[s_][k_]))
  if (BCUT == 2 || BCUT == 3 || ((BCUT == 4 || BCUT == 5) && !LD)) { KEEPRW(); ST(7); return; }
  {
    int tid = threadIdx.x; asm volatile("" : "+v"(tid));
    const int lane = tid & 63, j = lane & 15, g = lane >> 4;
    u16* Am2 = reinterpret_cast<u16*>(lds + L_AM2);
    float* cpart = reinterpret_cast<float*>(lds + L_CP);
    __builtin_amdgcn_sched_barrier(0);
    if constexpr (LD) {
      if (w == 8) {
        wave_wait(reinterpret_cast<const u64*>(a.ws + WS_F2) + (lane & (NBW - 1)), true, tag);
        asm volatile("s_waitcnt vmcnt(0)" ::: "memory");
        if (lane == 0) __hip_atomic_store(cnt + 1, 1u, __ATOMIC_RELEASE, __HIP_MEMORY_SCOPE_WORKGROUP);
      } else {
        for (u32 spins = 0; spins < 64u * SPIN_CAP; ++spins) { if (__hip_atomic_load(cnt + 1, __ATOMIC_ACQUIRE, __HIP_MEMORY_SCOPE_WORKGROUP) != 0u) break; __builtin_amdgcn_s_sleep(1); }
      }
      __builtin_amdgcn_sched_barrier(0);
      if (BCUT == 4) { KEEPRW(); return; }
      const int b = (tid >> 7) & 3, c4 = tid & 127;
      u32x2 pv[NSP];
      const f32x2 ml = __builtin_bit_cast(f32x2, __builtin_amdgcn_raw_buffer_load_b64(rs, (int)WS_MLB + ((b * NSP + (j & 7)) * 32 + h3) * 8, 0, 16));
#pragma unroll
      for (int s = 0; s < NSP; ++s) pv[s] = __builtin_amdgcn_raw_buffer_load_b64(rs, (int)WS_CTXP + (((b * NSP + s) * 32 + h3) * 512 + 4 * c4) * 2, 0, 16);
      const float M = row16_max(ml[0]);
      float wt = __builtin_amdgcn_exp2f(ml[0] - M);
      wt *= 2.f / row16_sum(wt * ml[1]);
      float c0 = 0.f, c1 = 0.f, c2 = 0.f, c3 = 0.f;
#pragma unroll
      for (int s = 0; s < NSP; ++s) { const float ws_ = rdlane(wt, s); c0 = fmaf(ws_, bf_lo(pv[s][0]), c0); c1 = fmaf(ws_, bf_hi(pv[s][0]), c1); c2 = fmaf(ws_, bf_lo(pv[s][1]), c2); c3 = fmaf(ws_, bf_hi(pv[s][1]), c3); }
      const u32 h0 = pk_bf16(c0, c1), h1 = pk_bf16(c2, c3);
      if (BCUT == 5) { KEEPRW(); asm volatile("" :: "v"(h0), "v"(h1)); return; }
      *reinterpret_cast<u32x2*>(Am2 + (2 * b) * AM2_LD + 4 * c4) = u32x2{h0, h1};
      *reinterpret_cast<u32x2*>(Am2 + (2 * b + 1) * AM2_LD + 4 * c4) = u32x2{pk_bf16(c0 - bf_lo(h0), c1 - bf_hi(h0)), pk_bf16(c2 - bf_lo(h1), c3 - bf_hi(h1))};
      __syncthreads();
      __syncthreads();
      const int t2 = tid - 512;
      float s = 0.f;
#pragma unroll
      for (int ww = 0; ww < 8; ++ww) s += cpart[ww * 512 + t2];
      u16* Am = reinterpret_cast<u16*>(lds + L_AM);
      const int bb = t2 >> 7, d = t2 & 127;
      const u16 hi = to_bf16(s);
      Am[(4 * bb) * AM_LD + d] = hi; Am[(4 * bb + 1) * AM_LD + d] = to_bf16(s - bf2f(hi));
    } else {
      __syncthreads();
      f32x4 acc[8];
#pragma unroll
      for (int t = 0; t < 8; ++t) acc[t] = f32x4{0.f, 0.f, 0.f, 0.f};
#pragma unroll
      for (int k2 = 0; k2 < 2; ++k2) {
        const u32x4 af = *reinterpret_cast<const u32x4*>(Am2 + j * AM2_LD + 64 * w + 32 * k2 + 8 * g);
#pragma unroll
        for (int t = 0; t < 8; ++t) acc[t] = mfma16(af, tcol8(RW[2 * k2 + (t >> 2)], t & 3), acc[t]);
      }
      if (g < 2) {
#pragma unroll
        for (int bb = 0; bb < 2; ++bb) {
          float* p = cpart + (w * 4 + 2 * g + bb) * 128 + 8 * j;
          *reinterpret_cast<f32x4*>(p) = f32x4{acc[0][2 * bb] + acc[0][2 * bb + 1], acc[1][2 * bb] + acc[1][2 * bb + 1], acc[2][2 * bb] + acc[2][2 * bb + 1], acc[3][2 * bb] + acc[3][2 * bb + 1]};
          *reinterpret_cast<f32x4*>(p + 4) = f32x4{acc[4][2 * bb] + acc[4][2 * bb + 1], acc[5][2 * bb] + acc[5][2 * bb + 1], acc[6][2 * bb] + acc[6][2 * bb + 1], acc[7][2 * bb] + acc[7][2 * bb + 1]};
        }
      }
      __builtin_amdgcn_sched_barrier(0);
      __syncthreads();
      if (w == 0) {
#pragma unroll
        for (int s = 0; s < 4; ++s)
#pragma unroll
          for (int jj = 0; jj < 8; ++jj) RW[s][jj] = *reinterpret_cast<const u32x2*>(lds + L_W8 + (32 * s + 8 * g + jj) * 144 + 8 * j);
      }
    }
    __syncthreads();
    if (LD || w == 0) {
      f32x4 ya[4];
      wo_mfma(RW, lds, lane, ya);
      if (YDEF) { while ((int)(rt32() - (tb0 + YDEF)) < 0) __builtin_amdgcn_s_sleep(4); }
      st16_y(rs, (int)WS_Y + (((8 * (LD ? w - 8 : 8) + (j >> 1)) * 2 + (g >> 1)) * 256 + h3 * 8 + 7) * 64 + (g & 1) * 32 + (j & 1) * 16, yrow(ya));
    }
    if constexpr (!LD) { ST(7); }
  }
}

DI float red4(f32x4 x) {
  auto s0 = __builtin_amdgcn_permlane32_swap(__float_as_uint(x[0]), __float_as_uint(x[1]), false, false);
  auto s1 = __builtin_amdgcn_permlane32_swap(__float_as_uint(x[2]), __float_as_uint(x[3]), false, false);
  const float r0 = __uint_as_float(s0[0]) + __uint_as_float(s0[1]), r1 = __uint_as_float(s1[0]) + __uint_as_float(s1[1]);
  auto s2 = __builtin_amdgcn_permlane16_swap(__float_as_uint(r0), __float_as_uint(r1), false, false);
  float r = __uint_as_float(s2[0]) + __uint_as_float(s2[1]);
  r += dpp<0x124>(r); r += dpp<0x128>(r);
  return r;
}
extern "C" __global__ void __launch_bounds__(256) k_final(unsigned char* ws, u16* out) {
  __shared__ float red[4][32];
  const int tid = threadIdx.x, lane = tid & 63, wv = tid >> 6, id = blockIdx.x;
  const int c8 = id >> 1, bp = id & 1, half = (lane >> 3) & 1, hd = 8 * wv + 2 * (lane >> 4) + ((lane >> 2) & 1);
  u32 yo = (u32)WS_Y + id * 16384 + hd * 512 + half * 64 + (lane & 3) * 16, mo = (u32)WS_MLA + bp * 4096 + hd * 128 + half * 64 + (lane & 2) * 16;
  asm volatile("" : "+v"(yo), "+v"(mo));
  f32x4 v[4], ml[2];
#pragma unroll
  for (int i = 0; i < 2; ++i) ml[i] = *reinterpret_cast<const f32x4*>(ws + mo + i * 16);
#pragma unroll
  for (int i = 0; i < 4; ++i) v[i] = *reinterpret_cast<const f32x4*>(ws + yo + i * 128);
  __builtin_amdgcn_sched_barrier(0);
  if (id == 0 && tid < 128) reinterpret_cast<u64*>(ws)[tid] = 0ull;
  const float m3 = half ? -INFINITY : ml[1][2], l3 = half ? 0.f : ml[1][3];
  float M = fmaxf(fmaxf(ml[0][0], ml[0][2]), fmaxf(ml[1][0], m3));
  M = fmaxf(M, dpp<0x128>(M));
  const float e0 = __builtin_amdgcn_exp2f(ml[0][0] - M), e1 = __builtin_amdgcn_exp2f(ml[0][2] - M), e2 = __builtin_amdgcn_exp2f(ml[1][0] - M), e3 = __builtin_amdgcn_exp2f(m3 - M);
  float L = fmaf(e0, ml[0][1], e1 * ml[0][3]) + fmaf(e2, ml[1][1], e3 * l3);
  L += dpp<0x128>(L);
  const f32x4 z = {0.f, 0.f, 0.f, 0.f};
  const f32x4 ya = ((v[0] * e0 + v[1] * e1) + (v[2] * e2 + (half ? z : v[3]) * e3)) * __builtin_amdgcn_rcpf(L);
  const float ra = red4(ya), rb = red4(half ? v[3] : z);
  if ((lane & 8) == 0) red[wv][(lane & 4) * 4 + (lane & 3) * 4 + (((lane >> 4) & 1) * 2 + (lane >> 5))] = (lane & 4) ? rb : ra;
  __syncthreads();
  if (tid < 32) out[(2 * bp + ((tid >> 3) & 1)) * 1152 + (tid >> 4) * 576 + c8 * 8 + (tid & 7)] = to_bf16((red[0][tid] + red[1][tid]) + (red[2][tid] + red[3][tid]));
}

__global__ void __launch_bounds__(1024, 4) k_main(Args a) {
  __shared__ __attribute__((aligned(16))) unsigned char lds[L_TOTAL];
  u32 pf;
  { u64 pc; u32 len;
    asm volatile("s_getpc_b64 %0\n.Lpf%=:\n\ts_mov_b32 %1, k_tail_mark-.Lpf%=" : "=s"(pc), "=s"(len));
    pf = __builtin_amdgcn_raw_buffer_load_b32(__builtin_amdgcn_make_buffer_rsrc(reinterpret_cast<void*>(pc), 0, len < 131072u ? len : 0u, 0x00020000), CPFM ? (threadIdx.x < 16 ? ((blockIdx.x >> 3) * 16 + threadIdx.x) * 128 : 0x7ffff000) : threadIdx.x * 128, 0, 0);
    __builtin_amdgcn_sched_barrier(0); }
  u64 tag = (hw_dispatch_id() + 1ull) * 0x9E3779B97F4A7C15ull;
  tag ^= ((u64)__builtin_amdgcn_dispatch_ptr() >> 6) * 0xC2B2AE3D27D4EB4Full;
  tag |= 1ull << 63;
  if (GT0 && blockIdx.x == 0 && (threadIdx.x >> 6) == 7) {
    const u32 t = rt32();
    __hip_atomic_store(reinterpret_cast<u64*>(a.ws + WS_ACC) + (threadIdx.x & 63), 0ull, RLX_AGENT);
    asm volatile("s_waitcnt vmcnt(0)" ::: "memory");
    if ((threadIdx.x & 63) == 0) __hip_atomic_store(reinterpret_cast<u64*>(a.ws + WS_T0), (tag & 0xffffffff00000000ull) | t, RLX_AGENT);
  }
  if (__builtin_expect(blockIdx.x >= NBW, 1)) part_a(a, lds, blockIdx.x - NBW, tag);
  else if (__builtin_amdgcn_readfirstlane(threadIdx.x >> 6) < 8) part_b<false>(a, lds, blockIdx.x, tag);
  else part_b<true>(a, lds, blockIdx.x, tag);
  asm volatile("" :: "v"(pf));
}

extern "C" __global__ void k_tail_mark() {}

static void* g_ws;
extern "C" void kernel_launch(void* const* d_in, const int* in_sizes, int n_in, void* d_out, int out_size, void* d_ws, size_t ws_size,
                              hipStream_t stream) {
  Args a;
  a.xA = (const u16*)d_in[0]; a.K = (const u16*)d_in[1]; a.V = (const u16*)d_in[2]; a.wq = (const u16*)d_in[3]; a.wo = (const u16*)d_in[4];
  a.xB = (const u16*)d_in[5]; a.C = (const u16*)d_in[6]; a.wqB = (const u16*)d_in[7]; a.wuk = (const u16*)d_in[8]; a.wuv = (const u16*)d_in[9]; a.woB = (const u16*)d_in[10];
#define WSTOP 32
  if (WSTOP && ws_size >= ((size_t)WSTOP << 20)) d_ws = (unsigned char*)d_ws + ((ws_size - ((size_t)WSTOP << 20)) & ~(size_t)4095);
  g_ws = d_ws;
  a.ws = (unsigned char*)d_ws;
  a.out = (u16*)d_out;
  k_main<<<NBW + NAW, 1024, 0, stream>>>(a);
  k_final<<<144, 256, 0, stream>>>((unsigned char*)d_ws, (u16*)d_out);
}
```

```cpp
#define STAMP
#define DIAG 0
#include <hip/hip_runtime.h>
#include <stdint.h>
#include <utility>

typedef unsigned short u16;
typedef unsigned u32;
typedef unsigned long long u64;
typedef u32 u32x4 __attribute__((ext_vector_type(4)));
typedef u32 u32x2 __attribute__((ext_vector_type(2)));
typedef float f32x4 __attribute__((ext_vector_type(4)));
typedef float f32x2 __attribute__((ext_vector_type(2)));
typedef short s16x4 __attribute__((ext_vector_type(4)));
typedef __bf16 bf16x8 __attribute__((ext_vector_type(8)));
typedef __bf16 bf16x2 __attribute__((ext_vector_type(2)));
#define DI __device__ __forceinline__
#define RLX_AGENT __ATOMIC_RELAXED, __HIP_MEMORY_SCOPE_AGENT
extern "C" __device__ u64 hw_dispatch_id() __asm("llvm.amdgcn.dispatch.id");

template <int... Is, class F> DI void static_for_impl(std::integer_sequence<int, Is...>, F&& f) { (f(std::integral_constant<int, Is>{}), ...); }
template <int N, class F> DI void static_for(F&& f) { static_for_impl(std::make_integer_sequence<int, N>{}, f); }

constexpr int H = 32, SK = 4096, DM = 576, DC = 512;
constexpr int ACU = 7;
constexpr int NBW = 32;
constexpr int NAW = H * ACU;
constexpr int NSP = 8;
constexpr float C2 = 0.08838834764831845f * 1.4426950408889634f;

constexpr size_t WS_T0 = 2048, WS_ACC = 2560;
constexpr size_t WS_F1 = 0, WS_F2 = 512;
constexpr size_t WS_QABS = 4096;
constexpr size_t WS_CTXP = WS_QABS + 4 * 32 * 512 * 2;
constexpr size_t WS_MLB = WS_CTXP + (size_t)4 * NSP * 32 * 512 * 2;
constexpr size_t WS_Y = WS_MLB + 4 * NSP * 32 * 2 * 4;
constexpr size_t WS_MLA = WS_Y + (size_t)72 * 256 * 128;
constexpr size_t WS_RPT = WS_MLA + 256 * 32;
constexpr size_t WS_END = WS_RPT + 64 * 4480;

constexpr int CRS = 1056;
constexpr int CBUF = 64 * CRS;
constexpr int L_BIG = 0;
constexpr int L_AM = 2 * CBUF;
constexpr int L_QS = L_AM + 16 * 272;
constexpr int L_ST = L_QS + 1024;
constexpr int L_PF = L_ST + 4096;
constexpr int L_TOTAL = L_PF + 4096;
constexpr int AM_LD = 136;
constexpr int AM2_LD = 520;
constexpr int L_AM2 = 69632, L_CP = 88064, L_W8 = 106496;

#define ST(i) do { if (threadIdx.x == 0) reinterpret_cast<u64*>(a.ws + (16u << 20))[blockIdx.x * 16 + (i)] = __builtin_amdgcn_s_memrealtime(); } while (0)
struct Args {
  const u16 *xA, *K, *V, *wq, *wo, *xB, *C, *wqB, *wuk, *wuv, *woB;
  unsigned char* ws;
  u16* out;
};

DI float bf_lo(u32 x) { return __uint_as_float(x << 16); }
DI float bf_hi(u32 x) { return __uint_as_float(x & 0xffff0000u); }
DI u32 pk_bf16(float lo, float hi) { bf16x2 v = {(__bf16)lo, (__bf16)hi}; return __builtin_bit_cast(u32, v); }
DI u16 to_bf16(float x) { return __builtin_bit_cast(u16, (__bf16)x); }
DI float bf2f(u16 x) { return __uint_as_float(((u32)x) << 16); }
DI float dot2(u32 a, u32 b, float c) { return __builtin_amdgcn_fdot2_f32_bf16(__builtin_bit_cast(bf16x2, a), __builtin_bit_cast(bf16x2, b), c, false); }
template <int CTRL> DI float dpp(float x) { return __builtin_bit_cast(float, __builtin_amdgcn_update_dpp(0, __builtin_bit_cast(int, x), CTRL, 0xf, 0xf, true)); }
DI float row16_sum(float x) { x += dpp<0x128>(x); x += dpp<0x124>(x); x += dpp<0x122>(x); x += dpp<0x121>(x); return x; }
DI float row16_max(float x) { x = fmaxf(x, dpp<0x128>(x)); x = fmaxf(x, dpp<0x124>(x)); x = fmaxf(x, dpp<0x122>(x)); x = fmaxf(x, dpp<0x121>(x)); return x; }
DI float xg_max(float x) {
  auto s = __builtin_amdgcn_permlane16_swap(__float_as_uint(x), __float_as_uint(x), false, false);
  x = fmaxf(__uint_as_float(s[0]), __uint_as_float(s[1]));
  auto t = __builtin_amdgcn_permlane32_swap(__float_as_uint(x), __float_as_uint(x), false, false);
  return fmaxf(__uint_as_float(t[0]), __uint_as_float(t[1]));
}
DI float xg_sum(float x) {
  auto s = __builtin_amdgcn_permlane16_swap(__float_as_uint(x), __float_as_uint(x), false, false);
  x = __uint_as_float(s[0]) + __uint_as_float(s[1]);
  auto t = __builtin_amdgcn_permlane32_swap(__float_as_uint(x), __float_as_uint(x), false, false);
  return __uint_as_float(t[0]) + __uint_as_float(t[1]);
}
DI f32x4 mfma16(u32x4 a, u32x4 b, f32x4 c) { return __builtin_amdgcn_mfma_f32_16x16x32_bf16(__builtin_bit_cast(bf16x8, a), __builtin_bit_cast(bf16x8, b), c, 0, 0, 0); }
DI u32x4 ld16(const u16* p) { return __builtin_nontemporal_load(reinterpret_cast<const u32x4*>(p)); }
DI u32x2 ld8(const u16* p) { return __builtin_nontemporal_load(reinterpret_cast<const u32x2*>(p)); }
DI u32x2 trread(const unsigned char* p) {
  return __builtin_bit_cast(u32x2, __builtin_amdgcn_ds_read_tr16_b64_v4i16((__attribute__((address_space(3))) s16x4*)(p)));
}
DI u32 prm(u32 hi, u32 lo, int odd) { return __builtin_amdgcn_perm(hi, lo, odd ? 0x07060302u : 0x05040100u); }

DI u32x4 tcol16(const u32x4 (&R)[8], int t) {
  u32x4 T;
#pragma unroll
  for (int q = 0; q < 4; ++q) T[q] = prm(R[2 * q + 1][t >> 1], R[2 * q][t >> 1], t & 1);
  return T;
}
DI u32x4 tcol8(const u32x2 (&R)[8], int t) {
  u32x4 T;
#pragma unroll
  for (int q = 0; q < 4; ++q) T[q] = prm(R[2 * q + 1][t >> 1], R[2 * q][t >> 1], t & 1);
  return T;
}

#define SPINLOG 22
#define PSL 2
#define YAUX 16
#define ACQ 0
constexpr u32 SPIN_CAP = 1u << SPINLOG;
DI void wave_wait(const u64* f, bool active, u64 tag) {
  for (u32 spins = 0; spins < SPIN_CAP; ++spins) {
    const u64 v = active ? __hip_atomic_load(f, RLX_AGENT) : tag;
    if (__all(v == tag)) break;
    __builtin_amdgcn_s_sleep(PSL);
  }
}
DI void wg_wait64(const unsigned char* flags, u64 tag, int pw = 0) {
  if ((int)(threadIdx.x >> 6) == pw) {
    wave_wait(reinterpret_cast<const u64*>(flags) + (threadIdx.x & (NBW - 1)), true, tag);
    asm volatile("s_waitcnt vmcnt(0)" ::: "memory");
  }
  __syncthreads();
}
DI void st16_sc1(__amdgpu_buffer_rsrc_t rs, size_t off, u32x4 v) { __builtin_amdgcn_raw_buffer_store_b128(v, rs, (int)off, 0, 16); }
DI void st8_sc1(unsigned char* p, f32x2 v) { __hip_atomic_store(reinterpret_cast<u64*>(p), __builtin_bit_cast(u64, v), RLX_AGENT); }

struct QPre { u32x4 R0[8]; u32x4 R1; u32x2 xa, xb; u32 x1; };
DI void qproj_load(QPre& Q, const u16* x, const u16* Wh, int w, int lane) {
  const int j = lane & 15, g = lane >> 4;
  Q.xa = u32x2{0u, 0u}; Q.xb = u32x2{0u, 0u}; Q.x1 = 0u;
  if (j < 4) { Q.xa = ld8(x + j * DM + 36 * w + 8 * g); Q.xb = ld8(x + j * DM + 36 * w + 8 * g + 4); Q.x1 = x[j * DM + 36 * w + 32 + g]; }
#pragma unroll
  for (int jj = 0; jj < 8; ++jj) Q.R0[jj] = ld16(Wh + (size_t)(36 * w + 8 * g + jj) * 4096 + 8 * j);
  Q.R1 = ld16(Wh + (size_t)(36 * w + 32 + g) * 4096 + 8 * j);
}
DI void qproj_mfma(const QPre& Q, unsigned char* lds, int w, int lane) {
  const int j = lane & 15, g = lane >> 4;
  float* qpart = reinterpret_cast<float*>(lds + L_BIG);
  const u32x4 a = {Q.xa[0], Q.xa[1], Q.xb[0], Q.xb[1]}, a1 = {Q.x1, 0u, 0u, 0u};
#pragma unroll
  for (int hf = 0; hf < 2; ++hf) {
    f32x4 acc[4];
#pragma unroll
    for (int t = 0; t < 4; ++t) acc[t] = mfma16(a, tcol16(Q.R0, 4 * hf + t), f32x4{0.f, 0.f, 0.f, 0.f});
#pragma unroll
    for (int t = 0; t < 4; ++t) { const u32x4 bb = {(t & 1) ? (Q.R1[2 * hf + (t >> 1)] >> 16) : (Q.R1[2 * hf + (t >> 1)] & 0xffffu), 0u, 0u, 0u}; acc[t] = mfma16(a1, bb, acc[t]); }
    if (g == 0) {
#pragma unroll
      for (int r = 0; r < 4; ++r) *reinterpret_cast<f32x4*>(qpart + (w * 4 + r) * 128 + 8 * j + 4 * hf) = f32x4{acc[0][r], acc[1][r], acc[2][r], acc[3][r]};
    }
    __builtin_amdgcn_sched_barrier(0);
  }
}
DI void qproj_finish(unsigned char* lds) {
  const float* qpart = reinterpret_cast<const float*>(lds + L_BIG);
  __syncthreads();
  if (threadIdx.x < 512) {
    float s = 0.f;
#pragma unroll
    for (int ww = 0; ww < 16; ++ww) s += qpart[ww * 512 + threadIdx.x];
    reinterpret_cast<u16*>(lds + L_QS)[threadIdx.x] = to_bf16(s);
  }
  __syncthreads();
}

DI void wo_load(const u16* Wo, u32x2 (&R)[4][8], int nb, int lane, int s0, int s1) {
  const int j = lane & 15, g = lane >> 4;
#pragma unroll
  for (int s = 0; s < 4; ++s) if (s >= s0 && s < s1)
#pragma unroll
    for (int jj = 0; jj < 8; ++jj) R[s][jj] = ld8(Wo + (size_t)(32 * s + 8 * g + jj) * DM + 64 * nb + 4 * j);
}
DI void wo_mfma(const u32x2 (&R)[4][8], const unsigned char* lds, int lane, f32x4 (&acc)[4]) {
  const int j = lane & 15, g = lane >> 4;
#pragma unroll
  for (int t = 0; t < 4; ++t) acc[t] = f32x4{0.f, 0.f, 0.f, 0.f};
#pragma unroll
  for (int s = 0; s < 4; ++s) {
    const u32x4 a = *reinterpret_cast<const u32x4*>(lds + L_AM + j * 272 + (32 * s + 8 * g) * 2);
#pragma unroll
    for (int t = 0; t < 4; ++t) acc[t] = mfma16(a, tcol8(R[s], t), acc[t]);
  }
}
DI u32x4 yrow(const f32x4 (&acc)[4]) {
  const f32x4 o = {acc[0][0] + acc[0][1], acc[1][0] + acc[1][1], acc[2][0] + acc[2][1], acc[3][0] + acc[3][1]};
  return __builtin_bit_cast(u32x4, o);
}
DI void am_zero(unsigned char* lds, int tid) {
  if (tid < 136) { const int pr = tid / 34; *reinterpret_cast<u32x4*>(lds + L_AM + (4 * pr + 2) * 272 + (tid - 34 * pr) * 16) = u32x4{0u, 0u, 0u, 0u}; }
}
DI void st16_y(__amdgpu_buffer_rsrc_t rs, int off, u32x4 v) { __builtin_amdgcn_raw_buffer_store_b128(v, rs, off, 0, YAUX); }
DI float rdlane(float x, int l) { return __builtin_bit_cast(float, __builtin_amdgcn_readlane(__builtin_bit_cast(int, x), l)); }

constexpr int NS = 10;
constexpr int NSEQ = 8 * NS;
#define ORDN 0
#define ORD "KWWKWWKWWKWWKWKK"
constexpr char ord[] = ORD;
constexpr int ord_len() { int n = 0; while (ord[n]) ++n; return n; }
constexpr int ord_cnt(int n, char c) { int k = 0; for (int i = 0; i < n; ++i) k += ord[i] == c; return k; }
#define POST 0
#define DEP 2
constexpr int DEPTH = DEP, PRE = ord_cnt(ord_len(), 'K'), RING = PRE + POST, RAMP = 2;
static_assert(ord_cnt(ord_len(), 'W') == 9, "");
constexpr int issued(int i) {
  int d = RING - (i + RAMP - 1) / RAMP; if (d < DEPTH) d = DEPTH;
  int e = i + d; if (e < RING) e = RING; return e > NSEQ ? NSEQ : e;
}
#define PACE 1
#define P20 1350
#define KN 46
#define PK 2780
#define PE 4300
#define THK 0
#define THA 14
#define THB 56
#define PFROM 0
#define YDEF 4400
#define WO0 20
#define WON 32
#define PAT 1
#define TA1 535
#define TA2 450
#define NK 68
#define ORG0 100
#define LEAD 5
#define RN0 14
#define RP 13
#define BETA 10
#define SL0 1
#define SLMAX 128
#define MAXGAP 70
constexpr int cmin(int a, int b) { return a < b ? a : b; }
#define RSH 1
#define RD 3
constexpr int due_at(int n) { return (n <= NK ? n * TA1 : NK * TA1 + (n - NK) * TA2) / 10; }
constexpr int NRND = (NSEQ - 1 - RD - 2 - RN0) / RP + 1;
constexpr int rnd_at(int n) { return (n >= RN0 && (n - RN0) % RP == 0 && (n - RN0) / RP < NRND) ? (n - RN0) / RP : -1; }
constexpr int wo_step(int k) { return WO0 + k * WON / 32; }
#define STEPST 0
#define CPFM 1
#define GT0 0
#define STAG 16
DI u32 rt32() { return (u32)__builtin_amdgcn_s_memrealtime(); }
DI void part_a(const Args& a, unsigned char* lds, int idx, u64 tag) {
  const int tid = threadIdx.x, lane = tid & 63, w = __builtin_amdgcn_readfirstlane(tid >> 6);
  u32 T0 = rt32();
  const u32 toff = (u32)(w * TA2) / (STAG * 10);
  u32* lorg = reinterpret_cast<u32*>(lds + L_ST + 2048);
  if (tid == 0) { lorg[0] = ORG0; lorg[1] = 0u; lorg[2] = T0; lorg[3] = ORG0; }
  u32 last = T0;
  u32x2 rv = {0u, 0u}; u32 rt = 0;
  const int j = lane & 15, g = lane >> 4;
  const int h = (idx & 7) * 4 + ((idx >> 3) & 3), ci = idx >> 5;
  const int U0 = 0, NUN = 1024;
  const int rot = h % NS;
  const int l0 = (ci * 16) / ACU, ln = ((ci + 1) * 16) / ACU - l0;
  const int bad_sl = (w < ln) ? -1 : NS - 1 - rot;
  const int sstr = 7 * 16384, s0 = ci * 16384 + w * 1024, s9 = (w < ln) ? (1008 + l0 + w) * 1024 : 1 << 20;
  const __amdgpu_buffer_rsrc_t rs = __builtin_amdgcn_make_buffer_rsrc(a.ws, 0, (int)WS_END, 0x00020000);
  const u16* Wq = a.wq + h * 128;
  QPre Q;
  ST(0);
  Q.xa = u32x2{0u, 0u}; Q.xb = u32x2{0u, 0u}; Q.x1 = 0u;
  if (j < 4) { Q.xa = ld8(a.xA + j * DM + 36 * w + 8 * g); Q.xb = ld8(a.xA + j * DM + 36 * w + 8 * g + 4); Q.x1 = a.xA[j * DM + 36 * w + 32 + g]; }
  const size_t base = ((size_t)h * SK + 4 * U0) * 128;
  const int voff = lane * 16;
  u32x4 R[RING];
#define A_ISSUE(i) R[(i) % RING] = __builtin_amdgcn_raw_buffer_load_b128( \
    __builtin_amdgcn_make_buffer_rsrc(const_cast<u16*>((((i) >= 4 * NS) ? a.V : a.K) + base + (size_t)(((i) / NS) & 3) * H * SK * 128), 0, NUN * 1024, 0x00020000), \
    voff, ((i) % NS) + rot == NS - 1 ? s9 : (((i) % NS) + rot >= NS ? ((i) % NS) + rot - NS : ((i) % NS) + rot) * sstr + s0, 2)
#define GATE(n) do { if (PACE) { u32 due = T0 + toff + org + ((osl * cmin(n, NK)) >> 4) + (u32)due_at(n); if (MAXGAP && (int)(due - (last + MAXGAP)) > 0) due = last + MAXGAP; \
    while ((int)(now - due) < 0) { __builtin_amdgcn_s_sleep(1); now = rt32(); } last = now; } } while (0)
#define CTRL(n) do { if constexpr (PACE) { \
    if constexpr (rnd_at((n) - RD - 2) >= 0) { constexpr int k = rnd_at((n) - RD - 2); \
      const u32 dt = (rv[1] == (u32)tag + k) ? rv[0] - T0 : rt; \
      const float sm = row16_sum((float)(int)dt); \
      const int o = (int)((RSH ? rdlane(sm, 0) * (1.f / 16.f) : (rdlane(sm, 0) + rdlane(sm, 16)) * (1.f / 32.f))) - due_at(RN0 + k * RP) - LEAD; \
      if (w == ((5 * k + 11) & 15) && lane == 0 && o > 0 && o < 2000) { \
        const int po = (int)lorg[3], on = o > po ? o : po; \
        int sl_ = k >= SL0 ? ((on - po) * BETA) / RP : 0; sl_ = sl_ > SLMAX ? SLMAX : sl_; \
        lorg[3] = (u32)on; __hip_atomic_store(reinterpret_cast<u64*>(lorg), __builtin_bit_cast(u64, u32x2{(u32)(on - ((sl_ * cmin(RN0 + k * RP, NK)) >> 4)), (u32)sl_}), __ATOMIC_RELAXED, __HIP_MEMORY_SCOPE_WORKGROUP); } } \
    if constexpr (rnd_at(n) >= 0) { constexpr int k = rnd_at(n); \
      __builtin_amdgcn_raw_buffer_store_b64(u32x2{rt32() - toff, (u32)tag + k}, rs, (ci == k % ACU && (h & ((1 << RSH) - 1)) == (k & ((1 << RSH) - 1)) && w == ((5 * k + 3) & 15) && lane == 0) ? (int)WS_RPT + (h >> RSH) * 4352 : 0x7ffff000, 128 * k * 35, 16); } \
    if constexpr (rnd_at((n) - RD) >= 0) { constexpr int k = rnd_at((n) - RD); \
      rv = __builtin_amdgcn_raw_buffer_load_b64(rs, (w == ((5 * k + 11) & 15) && lane < (32 >> RSH)) ? (int)WS_RPT + lane * 4352 : 0x7ffff000, 128 * k * 35, 16); rt = rt32() - T0; } \
  } } while (0)
  static_for<ord_len()>([&](auto oc) __attribute__((always_inline)) {
    constexpr int o = oc.value, kk = ord_cnt(o, 'K'), wk = ord_cnt(o, 'W');
    if constexpr (ord[o] == 'K') A_ISSUE(kk);
    else if constexpr (wk < 8) Q.R0[wk] = ld16(Wq + (size_t)(36 * w + 8 * g + wk) * 4096 + 8 * j);
    else Q.R1 = ld16(Wq + (size_t)(36 * w + 32 + g) * 4096 + 8 * j);
    __builtin_amdgcn_sched_barrier(0);
  });
  am_zero(lds, tid);
  qproj_mfma(Q, lds, w, lane);
  u64 g0 = 0; if (GT0) g0 = __hip_atomic_load(reinterpret_cast<const u64*>(a.ws + WS_T0), RLX_AGENT);
  __builtin_amdgcn_sched_barrier(0);
#pragma unroll
  for (int t = PRE; t < RING; ++t) A_ISSUE(t);
  __builtin_amdgcn_sched_barrier(0);
  qproj_finish(lds);
  T0 = __builtin_amdgcn_readfirstlane(lorg[2]);
  ST(1);
  float* pacc = reinterpret_cast<float*>(lds + L_BIG);
  float* pml = reinterpret_cast<float*>(lds + L_ST);
  u32x4 q;
  float acc[8], S[4];
  u32x2 RW[4][8];
  const __amdgpu_buffer_rsrc_t rwo = __builtin_amdgcn_make_buffer_rsrc(const_cast<u16*>(a.wo + (size_t)h * 128 * DM), 0, w < 9 ? 128 * DM * 2 : 0, 0x00020000);
  const int wvo = (8 * g * DM + 64 * w + 4 * j) * 2;
#define WO_ISSUE() static_for<32>([&](auto kc) __attribute__((always_inline)) { constexpr int k = kc.value; \
    if constexpr (wo_step(k) == i) RW[k >> 3][k & 7] = __builtin_amdgcn_raw_buffer_load_b64(rwo, wvo, (32 * (k >> 3) + (k & 7)) * DM * 2, 2); })
  static_for<NSEQ>([&](auto ic) __attribute__((always_inline)) {
    constexpr int i = ic.value, bb = (i / NS) & 3, pass = i / (4 * NS), sl = i % NS;
    u32 now = 0; if (PACE || STEPST) now = rt32();
    u32 org = 0, osl = 0; if (PACE) { const u32x2 ov = __builtin_bit_cast(u32x2, __hip_atomic_load(reinterpret_cast<u64*>(lorg), __ATOMIC_RELAXED, __HIP_MEMORY_SCOPE_WORKGROUP)); org = __builtin_amdgcn_readfirstlane(ov[0]); osl = __builtin_amdgcn_readfirstlane(ov[1]); }
    if (STEPST && lane == 0) reinterpret_cast<u32*>(lds + 65536)[w * 96 + i] = now;
    if constexpr (GT0 && i == PRE + 1) {
      const u32 lo = __builtin_amdgcn_readfirstlane((u32)g0), hi = __builtin_amdgcn_readfirstlane((u32)(g0 >> 32));
      if (hi == (u32)(tag >> 32) && T0 - lo + 400u < 800u) T0 = lo;
    }
    __builtin_amdgcn_sched_barrier(0);
    const u32x4 r = R[i % RING];
    if constexpr (pass == 0) {
      if constexpr (sl == 0) { q = *reinterpret_cast<const u32x4*>(lds + L_QS + (bb * 128 + 8 * j) * 2); S[bb] = -INFINITY; }
      float d = dot2(r[0], q[0], 0.f); d = dot2(r[1], q[1], d); d = dot2(r[2], q[2], d); d = dot2(r[3], q[3], d);
      asm volatile("" : "+v"(d));
      if constexpr (issued(i + 1) > issued(i)) { GATE(issued(i)); WO_ISSUE(); A_ISSUE(issued(i)); CTRL(issued(i)); } else { WO_ISSUE(); }
      d = row16_sum(d);
      S[bb] = (j == sl) ? d : S[bb];
      if constexpr (sl == NS - 1) {
        const float sc = (j == bad_sl) ? -INFINITY : S[bb] * C2;
        const float m = xg_max(row16_max(sc));
        S[bb] = __builtin_amdgcn_exp2f(sc - m);
        const float l = xg_sum(row16_sum(S[bb]));
        if (lane == 0) *reinterpret_cast<f32x2*>(pml + (bb * 16 + w) * 2) = f32x2{m, l};
      }
    } else {
      const float p = dpp<0x150 + sl>(S[bb]);
      if constexpr (sl == 0) {
#pragma unroll
        for (int k = 0; k < 8; ++k) acc[k] = 0.f;
      }
#pragma unroll
      for (int k = 0; k < 4; ++k) { acc[2 * k] = fmaf(p, bf_lo(r[k]), acc[2 * k]); acc[2 * k + 1] = fmaf(p, bf_hi(r[k]), acc[2 * k + 1]); }
      asm volatile("" : "+v"(acc[0]), "+v"(acc[1]), "+v"(acc[2]), "+v"(acc[3]), "+v"(acc[4]), "+v"(acc[5]), "+v"(acc[6]), "+v"(acc[7]));
      if constexpr (issued(i + 1) > issued(i)) { GATE(issued(i)); WO_ISSUE(); A_ISSUE(issued(i)); CTRL(issued(i)); } else { WO_ISSUE(); }
      if constexpr (sl == NS - 1) {
#pragma unroll
        for (int k = 0; k < 8; ++k) acc[k] = xg_sum(acc[k]);
        if (g == 0) {
          float* pp = pacc + (bb * 16 + w) * 128 + 8 * j;
          *reinterpret_cast<f32x4*>(pp) = f32x4{acc[0], acc[1], acc[2], acc[3]};
          *reinterpret_cast<f32x4*>(pp + 4) = f32x4{acc[4], acc[5], acc[6], acc[7]};
        }
      }
    }
    __builtin_amdgcn_sched_barrier(0);
  });
#undef A_ISSUE
  if (STEPST) { if (lane == 0) { reinterpret_cast<u32*>(lds + 65536)[w * 96 + NSEQ] = rt32(); reinterpret_cast<u32*>(lds + 65536)[w * 96 + NSEQ + 1] = lorg[3]; }
    for (int k = lane; k < 96; k += 64) reinterpret_cast<u32*>(a.ws + (20u << 20))[(blockIdx.x * 16 + w) * 96 + k] = reinterpret_cast<u32*>(lds + 65536)[w * 96 + k]; }
  if (lane == 0) reinterpret_cast<u64*>(a.ws + (17u << 20))[blockIdx.x * 16 + w] = __builtin_amdgcn_s_memrealtime();
  ST(2);
  __syncthreads();
  ST(3);
  if (w < 8) {
    const int bb = w >> 1, d = tid & 127;
    u16* Am = reinterpret_cast<u16*>(lds + L_AM);
    const f32x2 ml = *reinterpret_cast<const f32x2*>(pml + (bb * 16 + j) * 2);
    float pv[16];
#pragma unroll
    for (int i = 0; i < 16; ++i) pv[i] = pacc[(bb * 16 + i) * 128 + d];
    const float wt = __builtin_amdgcn_exp2f(ml[0] - row16_max(ml[0]));
    float o0 = 0.f, o1 = 0.f;
#pragma unroll
    for (int i = 0; i < 16; i += 2) { o0 = fmaf(rdlane(wt, i), pv[i], o0); o1 = fmaf(rdlane(wt, i + 1), pv[i + 1], o1); }
    const float o = o0 + o1;
    const u16 hi = to_bf16(o); const u16 lo = to_bf16(o - bf2f(hi));
    Am[(4 * bb) * AM_LD + d] = hi; Am[(4 * bb + 1) * AM_LD + d] = lo;
  } else if (w == 9) {
    const f32x2 ml = *reinterpret_cast<const f32x2*>(pml + lane * 2);
    const float M = row16_max(ml[0]);
    const float L = row16_sum(__builtin_amdgcn_exp2f(ml[0] - M) * ml[1]);
    if (j == 0) __builtin_amdgcn_raw_buffer_store_b64(__builtin_bit_cast(u32x2, f32x2{M, L}), rs, (int)WS_MLA + (((g >> 1) * 32 + h) * 2 + (ci >> 2)) * 64 + (g & 1) * 32 + (ci & 3) * 8, 0, YAUX);
  }
  __syncthreads();
  if (w < 9) {
    f32x4 ya[4];
    wo_mfma(RW, lds, lane, ya);
    st16_y(rs, (int)WS_Y + (((8 * w + (j >> 1)) * 2 + (g >> 1)) * 256 + h * 8 + 2 * (ci & 3) + (ci >> 2)) * 64 + (g & 1) * 32 + (j & 1) * 16, yrow(ya));
  }
  ST(4);
}

#define BCUT 0
#define CAUX 16
#define WPD 4
template <bool LD> DI void part_b(const Args& a, unsigned char* lds, int wg, u64 tag) {
  const int tid = threadIdx.x, lane = tid & 63, w = __builtin_amdgcn_readfirstlane(tid >> 6);
  const int j = lane & 15, g = lane >> 4;
  const __amdgpu_buffer_rsrc_t rs = __builtin_amdgcn_make_buffer_rsrc(a.ws, 0, (int)WS_END, 0x00020000);
  const u32 tb0 = rt32();
  const int h = wg;
  const int h3 = wg ^ 2;
  const int b2 = wg >> 3, sp = wg & 7;
  const u16* cl = a.C + ((size_t)b2 * SK + sp * 512 + 8 * (w - 8)) * DC + lane * 8;
  u32x4 X[16];
  u32x2 RW[4][8];
  u32x4 W8[2];
  u32 tv = 0;
#define BST(n) do { if (STEPST && LD) { __builtin_amdgcn_sched_barrier(0); const u32 t_ = rt32(); tv = (lane == (n)) ? t_ : tv; __builtin_amdgcn_sched_barrier(0); } } while (0)
  u32* cnt = reinterpret_cast<u32*>(lds + L_ST + 4064);
#define C_ISSUE(T) _Pragma("unroll") for (int r = 0; r < 8; ++r) X[8 * ((T) & 1) + r] = ld16(cl + (size_t)(64 * (T) + r) * DC)
#define C_HALF(T, hf) _Pragma("unroll") for (int r = 4 * (hf); r < 4 * (hf) + 4; ++r) X[8 * ((T) & 1) + r] = ld16(cl + (size_t)(64 * (T) + r) * DC)
#define C_WIN(T, r0, r1) _Pragma("unroll") for (int r = r0; r < r1; ++r) { asm volatile("s_waitcnt vmcnt(%0)" :: "n"(WPD - 1) : "memory"); X[8 * ((T) & 1) + r] = ld16(cl + (size_t)(64 * (T) + r) * DC); __builtin_amdgcn_sched_barrier(0); }
  u32 sb0 = L_BIG + 8 * (w - 8) * CRS + lane * 16, sb1 = sb0 + CBUF;
  asm volatile("" : "+v"(sb0), "+v"(sb1));
#define C_STAGE(T) _Pragma("unroll") for (int r = 0; r < 8; ++r) *reinterpret_cast<u32x4*>(lds + (((T) & 1) ? sb1 : sb0) + r * CRS) = X[8 * ((T) & 1) + r]
  {
    const u16* Wq = a.wqB + h * 128;
    u32x4 U[8];
    QPre Q;
    if constexpr (!LD) { ST(0); }
    qproj_load(Q, a.xB, Wq, w, lane);
#pragma unroll
    for (int i = 0; i < 8; ++i) U[i] = ld16(a.wuk + ((size_t)(32 * w + 4 * i + (j & 3)) * H + h) * 128 + 8 * ((j & 12) + g));
    __builtin_amdgcn_sched_barrier(0);
    BST(0);
    if constexpr (!LD) am_zero(lds, tid);
    qproj_mfma(Q, lds, w, lane);
    BST(1);
    __syncthreads();
    BST(2);
    if constexpr (LD) { C_HALF(0, 0); }
    else {
      const float* qpart = reinterpret_cast<const float*>(lds + L_BIG);
      float s = 0.f;
#pragma unroll
      for (int ww = 0; ww < 16; ++ww) s += qpart[ww * 512 + tid];
      reinterpret_cast<u16*>(lds + L_QS)[tid] = to_bf16(s);
    }
    __builtin_amdgcn_sched_barrier(0);
    __syncthreads();
    BST(3);
    const u32x4 af = *reinterpret_cast<const u32x4*>(lds + L_QS + ((j >> 2) * 128 + ((j & 3) * 4 + g) * 8) * 2);
    u16* qa = reinterpret_cast<u16*>(lds + L_PF);
    float v2[2] = {0.f, 0.f};
#pragma unroll
    for (int i = 0; i < 8; ++i) {
      const f32x4 d4 = mfma16(af, U[i], f32x4{0.f, 0.f, 0.f, 0.f});
      float d = (j & 8) ? ((j & 4) ? d4[3] : d4[2]) : ((j & 4) ? d4[1] : d4[0]);
      d += dpp<0x124>(d); d += dpp<0x128>(d);
      v2[i >> 2] = ((j >> 2) == (i & 3)) ? d : v2[i >> 2];
    }
    qa[g * 512 + 32 * w + j] = to_bf16(v2[0]); qa[g * 512 + 32 * w + 16 + j] = to_bf16(v2[1]);
    __builtin_amdgcn_sched_barrier(0);
    BST(4);
    __syncthreads();
    if constexpr (!LD) {
      if (w == 0) {
#pragma unroll
        for (int bb = 0; bb < 4; ++bb) st16_sc1(rs, WS_QABS + ((bb * H + h) * 512 + lane * 8) * 2, *reinterpret_cast<const u32x4*>(qa + bb * 512 + lane * 8));
      }
      __builtin_amdgcn_sched_barrier(0);
      __builtin_amdgcn_s_barrier();
      if (w == 0) {
        asm volatile("s_waitcnt vmcnt(0)" ::: "memory");
        if (lane == 0) { __hip_atomic_store(reinterpret_cast<u64*>(a.ws + WS_F1 + wg * 8), tag, RLX_AGENT); cnt[0] = 0u; cnt[1] = 0u; }
      }
    } else {
      __builtin_amdgcn_s_barrier();
      BST(5);
      C_WIN(0, 4, 8); C_WIN(1, 0, 8);
      BST(6);
      C_STAGE(0);
      __builtin_amdgcn_sched_barrier(0);
      BST(7);
      C_WIN(2, 0, 8);
      BST(8);
    }
    if constexpr (!LD) { ST(1); }
  }
  {
    const int b = b2;
    if constexpr (!LD) wg_wait64(a.ws + WS_F1, tag); else __syncthreads();
    if constexpr (!LD) { ST(2); }
    const int kt = w & 3, hh = (w >> 2) & 1;
    float* stmax = reinterpret_cast<float*>(lds + L_ST);
    float* stsum = stmax + 128;
    float* salpha = stmax + 256;
    unsigned char* Pf = lds + L_PF;
    float M = -INFINITY, lsum = 0.f;
    f32x4 acc[2][2];
#pragma unroll
    for (int ct = 0; ct < 2; ++ct) { acc[ct][0] = f32x4{0.f, 0.f, 0.f, 0.f}; acc[ct][1] = f32x4{0.f, 0.f, 0.f, 0.f}; }
    const int troff = (4 * g + (j >> 2)) * CRS + (32 * w + 4 * (j & 3)) * 2;
    auto pv_step = [&](const unsigned char* cb) __attribute__((always_inline)) {
      const f32x4 al0 = *reinterpret_cast<const f32x4*>(salpha + 4 * g), al1 = *reinterpret_cast<const f32x4*>(salpha + 16 + 4 * g);
#pragma unroll
      for (int ct = 0; ct < 2; ++ct) { acc[ct][0] *= al0; acc[ct][1] *= al1; }
#pragma unroll
      for (int blk = 0; blk < 2; ++blk) {
        const u32x4 p0 = *reinterpret_cast<const u32x4*>(Pf + ((blk * 2 + 0) * 64 + lane) * 16);
        const u32x4 p1 = *reinterpret_cast<const u32x4*>(Pf + ((blk * 2 + 1) * 64 + lane) * 16);
#pragma unroll
        for (int ct = 0; ct < 2; ++ct) {
          const u32x2 lo = trread(cb + troff + (32 * blk) * CRS + 32 * ct);
          const u32x2 hi = trread(cb + troff + (32 * blk + 16) * CRS + 32 * ct);
          const u32x4 bf = {lo[0], lo[1], hi[0], hi[1]};
          acc[ct][0] = mfma16(p0, bf, acc[ct][0]); acc[ct][1] = mfma16(p1, bf, acc[ct][1]);
        }
      }
    };
    unsigned char* Qf = lds + L_BIG + CBUF;
    if constexpr (!LD) {
      u32x4 qv[4];
#pragma unroll
      for (int k = 0; k < 4; ++k) qv[k] = __builtin_amdgcn_raw_buffer_load_b128(rs, (int)WS_QABS + (b * H * 512 + (tid + 512 * k) * 8) * 2, 0, 16);
#pragma unroll
      for (int k = 0; k < 4; ++k) { const int e = tid + 512 * k, head = e >> 6, ch = e & 63;
        *reinterpret_cast<u32x4*>(Qf + ((head >> 4) * 16 + (ch >> 2)) * 1040 + ((ch & 3) * 16 + (head & 15)) * 16) = qv[k]; }
    }
    __syncthreads();
    if constexpr (!LD) {
      u32x4 QF[16];
#pragma unroll
      for (int s = 0; s < 16; ++s) QF[s] = *reinterpret_cast<const u32x4*>(Qf + (hh * 16 + s) * 1040 + lane * 16);
      auto s_tile = [&](const unsigned char* cb) __attribute__((always_inline)) {
        __syncthreads();
        f32x4 s0 = {0.f, 0.f, 0.f, 0.f}, s1 = {0.f, 0.f, 0.f, 0.f};
#pragma unroll
        for (int s = 0; s < 16; s += 2) {
          s0 = mfma16(*reinterpret_cast<const u32x4*>(cb + (16 * kt + j) * CRS + (32 * s + 8 * g) * 2), QF[s], s0);
          s1 = mfma16(*reinterpret_cast<const u32x4*>(cb + (16 * kt + j) * CRS + (32 * s + 32 + 8 * g) * 2), QF[s + 1], s1);
        }
        const f32x4 sv = (s0 + s1) * C2;
        const float v = xg_max(fmaxf(fmaxf(sv[0], sv[1]), fmaxf(sv[2], sv[3])));
        if (g == 0) stmax[kt * 32 + 16 * hh + j] = v;
        __syncthreads();
        const float* sm = stmax + 16 * hh + j;
        const float Mn = fmaxf(fmaxf(M, fmaxf(sm[0], sm[32])), fmaxf(sm[64], sm[96]));
        const float alpha = __builtin_amdgcn_exp2f(M - Mn);
        M = Mn;
        u32x2 pk;
        pk[0] = pk_bf16(__builtin_amdgcn_exp2f(sv[0] - Mn), __builtin_amdgcn_exp2f(sv[1] - Mn));
        pk[1] = pk_bf16(__builtin_amdgcn_exp2f(sv[2] - Mn), __builtin_amdgcn_exp2f(sv[3] - Mn));
        lsum = lsum * alpha + ((bf_lo(pk[0]) + bf_hi(pk[0])) + (bf_lo(pk[1]) + bf_hi(pk[1])));
        *reinterpret_cast<u32x2*>(Pf + (((kt >> 1) * 2 + hh) * 64 + lane) * 16 + (kt & 1) * 8) = pk;
        if (kt == 0 && g == 0) salpha[16 * hh + j] = alpha;
        __syncthreads();
        pv_step(cb);
      };
#pragma unroll 1
      for (int T = 0; T < 8; T += 2) { s_tile(lds + L_BIG); s_tile(lds + L_BIG + CBUF); }
    } else {
#pragma unroll
      for (int T = 0; T < 8; ++T) {
        __syncthreads();
        BST(10 + 4 * T);
        if (T + 1 < 8) { C_STAGE(T + 1); }
        BST(11 + 4 * T);
        if (T + 3 < 8) { C_HALF(T + 3, 0); }
        if (T == 5 || T == 6) wo_load(a.woB + (size_t)h3 * 128 * DM, RW, w - 8, lane, 2 * (T - 5), 2 * (T - 5) + 1);
        if (T == 7) {
#pragma unroll
          for (int k = 0; k < 2; ++k) W8[k] = ld16(a.woB + ((size_t)h3 * 128 + 16 * (w - 8) + 8 * k + (lane >> 3)) * DM + 512 + (lane & 7) * 8);
        }
        BST(12 + 4 * T);
        __syncthreads();
        __syncthreads();
        BST(13 + 4 * T);
        if (T + 3 < 8) { C_HALF(T + 3, 1); }
        if (T == 5 || T == 6) wo_load(a.woB + (size_t)h3 * 128 * DM, RW, w - 8, lane, 2 * (T - 5) + 1, 2 * (T - 5) + 2);
        __builtin_amdgcn_sched_barrier(0);
        pv_step(lds + L_BIG + (T & 1) * CBUF);
      }
      BST(42);
      if (STEPST) reinterpret_cast<u32*>(a.ws + (20u << 20))[(blockIdx.x * 16 + w) * 96 + lane] = tv;
    }
#undef C_ISSUE
#undef C_STAGE
    if constexpr (!LD) { ST(3); }
    if (BCUT == 1) { asm volatile("" :: "v"(acc[0][0]), "v"(acc[0][1]), "v"(acc[1][0]), "v"(acc[1][1]), "v"(lsum)); if constexpr (LD) { asm volatile("" :: "v"(RW[0][0]), "v"(RW[1][7]), "v"(RW[2][7]), "v"(RW[3][7]), "v"(W8[1])); } ST(7); ST(5); return; }
    __syncthreads();
    if constexpr (!LD) { lsum = xg_sum(lsum); if (g == 0) stsum[kt * 32 + 16 * hh + j] = lsum; if (kt == 0 && g == 0) salpha[32 + 16 * hh + j] = M; }
    float* tb = reinterpret_cast<float*>(lds + L_BIG);
#pragma unroll
    for (int ct = 0; ct < 2; ++ct)
#pragma unroll
      for (int h2 = 0; h2 < 2; ++h2)
#pragma unroll
        for (int r = 0; r < 4; ++r) tb[(16 * h2 + 4 * g + r) * 516 + 32 * w + 16 * ct + j] = acc[ct][h2][r];
    if constexpr (!LD) { for (int i = tid; i < 8 * AM2_LD * 2 / 16; i += 512) reinterpret_cast<u32x4*>(lds + L_AM2 + 8 * AM2_LD * 2)[i] = u32x4{0u, 0u, 0u, 0u}; }
    __syncthreads();
    if constexpr (!LD) { ST(4); }
    if constexpr (LD) {
      const int t2 = tid - 512;
#pragma unroll
      for (int k = 0; k < 4; ++k) {
        const int e = t2 + 512 * k, head = e >> 6, c8 = e & 63;
        const f32x4 v0 = *reinterpret_cast<const f32x4*>(tb + head * 516 + 8 * c8), v1 = *reinterpret_cast<const f32x4*>(tb + head * 516 + 8 * c8 + 4);
        __builtin_amdgcn_raw_buffer_store_b128(u32x4{pk_bf16(v0[0], v0[1]), pk_bf16(v0[2], v0[3]), pk_bf16(v1[0], v1[1]), pk_bf16(v1[2], v1[3])}, rs, (int)WS_CTXP + (((b * NSP + sp) * 32 + head) * 512 + 8 * c8) * 2, 0, CAUX);
      }
      if (t2 < 32) {
        const float L = (stsum[t2] + stsum[32 + t2]) + (stsum[64 + t2] + stsum[96 + t2]);
        st8_sc1(a.ws + WS_MLB + ((size_t)(b * NSP + sp) * 32 + t2) * 8, f32x2{salpha[32 + t2], L});
      }
      __builtin_amdgcn_sched_barrier(0);
      __builtin_amdgcn_s_barrier();
      asm volatile("s_waitcnt vmcnt(0)" ::: "memory");
      if (lane == 0) {
        if (__hip_atomic_fetch_add(cnt, 1u, __ATOMIC_RELAXED, __HIP_MEMORY_SCOPE_WORKGROUP) == 7u) {
          __hip_atomic_store(reinterpret_cast<u64*>(a.ws + WS_F2 + wg * 8), tag, RLX_AGENT);
          reinterpret_cast<u64*>(a.ws + (16u << 20))[blockIdx.x * 16 + 5] = __builtin_amdgcn_s_memrealtime();
        }
      }
#pragma unroll
      for (int k = 0; k < 2; ++k) *reinterpret_cast<u32x4*>(lds + L_W8 + (16 * (w - 8) + 8 * k + (lane >> 3)) * 144 + (lane & 7) * 16) = W8[k];
    } else {
      __builtin_amdgcn_s_barrier();
      if (BCUT == 2) { ST(7); return; }
      static_for<16>([&](auto ic) __attribute__((always_inline)) {
        constexpr int k2 = ic.value >> 3, jj = ic.value & 7;
        if constexpr (ic.value >= WPD) asm volatile("s_waitcnt vmcnt(%0)" :: "n"(WPD - 1) : "memory");
        const u32x4 v = ld16(a.wuv + ((size_t)(64 * w + 32 * k2 + 8 * g + jj) * H + h3) * 128 + 8 * j);
        RW[2 * k2][jj] = u32x2{v[0], v[1]}; RW[2 * k2 + 1][jj] = u32x2{v[2], v[3]};
        __builtin_amdgcn_sched_barrier(0);
      });
    }
  }
#define KEEPRW() _Pragma("unroll") for (int s_ = 0; s_ < 4; ++s_) _Pragma("unroll") for (int k_ = 0; k_ < 8; ++k_) asm volatile("" :: "v"(RW[s_][k_]))
  if (BCUT == 2 || BCUT == 3 || ((BCUT == 4 || BCUT == 5) && !LD)) { KEEPRW(); ST(7); return; }
  {
    int tid = threadIdx.x; asm volatile("" : "+v"(tid));
    const int lane = tid & 63, j = lane & 15, g = lane >> 4;
    u16* Am2 = reinterpret_cast<u16*>(lds + L_AM2);
    float* cpart = reinterpret_cast<float*>(lds + L_CP);
    __builtin_amdgcn_sched_barrier(0);
    if constexpr (LD) {
      if (w == 8) {
        wave_wait(reinterpret_cast<const u64*>(a.ws + WS_F2) + (lane & (NBW - 1)), true, tag);
        asm volatile("s_waitcnt vmcnt(0)" ::: "memory");
        if (lane == 0) __hip_atomic_store(cnt + 1, 1u, __ATOMIC_RELEASE, __HIP_MEMORY_SCOPE_WORKGROUP);
        if (lane == 0) reinterpret_cast<u64*>(a.ws + (16u << 20))[blockIdx.x * 16 + 6] = __builtin_amdgcn_s_memrealtime();
      } else {
        for (u32 spins = 0; spins < 64u * SPIN_CAP; ++spins) { if (__hip_atomic_load(cnt + 1, __ATOMIC_ACQUIRE, __HIP_MEMORY_SCOPE_WORKGROUP) != 0u) break; __builtin_amdgcn_s_sleep(1); }
      }
      __builtin_amdgcn_sched_barrier(0);
      if (BCUT == 4) { KEEPRW(); return; }
      const int b = (tid >> 7) & 3, c4 = tid & 127;
      u32x2 pv[NSP];
      const f32x2 ml = __builtin_bit_cast(f32x2, __builtin_amdgcn_raw_buffer_load_b64(rs, (int)WS_MLB + ((b * NSP + (j & 7)) * 32 + h3) * 8, 0, 16));
#pragma unroll
      for (int s = 0; s < NSP; ++s) pv[s] = __builtin_amdgcn_raw_buffer_load_b64(rs, (int)WS_CTXP + (((b * NSP + s) * 32 + h3) * 512 + 4 * c4) * 2, 0, 16);
      const float M = row16_max(ml[0]);
      float wt = __builtin_amdgcn_exp2f(ml[0] - M);
      wt *= 2.f / row16_sum(wt * ml[1]);
      float c0 = 0.f, c1 = 0.f, c2 = 0.f, c3 = 0.f;
#pragma unroll
      for (int s = 0; s < NSP; ++s) { const float ws_ = rdlane(wt, s); c0 = fmaf(ws_, bf_lo(pv[s][0]), c0); c1 = fmaf(ws_, bf_hi(pv[s][0]), c1); c2 = fmaf(ws_, bf_lo(pv[s][1]), c2); c3 = fmaf(ws_, bf_hi(pv[s][1]), c3); }
      const u32 h0 = pk_bf16(c0, c1), h1 = pk_bf16(c2, c3);
      if (BCUT == 5) { KEEPRW(); asm volatile("" :: "v"(h0), "v"(h1)); return; }
      *reinterpret_cast<u32x2*>(Am2 + (2 * b) * AM2_LD + 4 * c4) = u32x2{h0, h1};
      *reinterpret_cast<u32x2*>(Am2 + (2 * b + 1) * AM2_LD + 4 * c4) = u32x2{pk_bf16(c0 - bf_lo(h0), c1 - bf_hi(h0)), pk_bf16(c2 - bf_lo(h1), c3 - bf_hi(h1))};
      __syncthreads();
      __syncthreads();
      const int t2 = tid - 512;
      float s = 0.f;
#pragma unroll
      for (int ww = 0; ww < 8; ++ww) s += cpart[ww * 512 + t2];
      u16* Am = reinterpret_cast<u16*>(lds + L_AM);
      const int bb = t2 >> 7, d = t2 & 127;
      const u16 hi = to_bf16(s);
      Am[(4 * bb) * AM_LD + d] = hi; Am[(4 * bb + 1) * AM_LD + d] = to_bf16(s - bf2f(hi));
    } else {
      __syncthreads();
      f32x4 acc[8];
#pragma unroll
      for (int t = 0; t < 8; ++t) acc[t] = f32x4{0.f, 0.f, 0.f, 0.f};
#pragma unroll
      for (int k2 = 0; k2 < 2; ++k2) {
        const u32x4 af = *reinterpret_cast<const u32x4*>(Am2 + j * AM2_LD + 64 * w + 32 * k2 + 8 * g);
#pragma unroll
        for (int t = 0; t < 8; ++t) acc[t] = mfma16(af, tcol8(RW[2 * k2 + (t >> 2)], t & 3), acc[t]);
      }
      if (g < 2) {
#pragma unroll
        for (int bb = 0; bb < 2; ++bb) {
          float* p = cpart + (w * 4 + 2 * g + bb) * 128 + 8 * j;
          *reinterpret_cast<f32x4*>(p) = f32x4{acc[0][2 * bb] + acc[0][2 * bb + 1], acc[1][2 * bb] + acc[1][2 * bb + 1], acc[2][2 * bb] + acc[2][2 * bb + 1], acc[3][2 * bb] + acc[3][2 * bb + 1]};
          *reinterpret_cast<f32x4*>(p + 4) = f32x4{acc[4][2 * bb] + acc[4][2 * bb + 1], acc[5][2 * bb] + acc[5][2 * bb + 1], acc[6][2 * bb] + acc[6][2 * bb + 1], acc[7][2 * bb] + acc[7][2 * bb + 1]};
        }
      }
      __builtin_amdgcn_sched_barrier(0);
      __syncthreads();
      if (w == 0) {
#pragma unroll
        for (int s = 0; s < 4; ++s)
#pragma unroll
          for (int jj = 0; jj < 8; ++jj) RW[s][jj] = *reinterpret_cast<const u32x2*>(lds + L_W8 + (32 * s + 8 * g + jj) * 144 + 8 * j);
      }
    }
    __syncthreads();
    if (LD || w == 0) {
      f32x4 ya[4];
      wo_mfma(RW, lds, lane, ya);
      if (YDEF) { while ((int)(rt32() - (tb0 + YDEF)) < 0) __builtin_amdgcn_s_sleep(4); }
      st16_y(rs, (int)WS_Y + (((8 * (LD ? w - 8 : 8) + (j >> 1)) * 2 + (g >> 1)) * 256 + h3 * 8 + 7) * 64 + (g & 1) * 32 + (j & 1) * 16, yrow(ya));
    }
    if constexpr (!LD) { ST(7); }
  }
}

DI float red4(f32x4 x) {
  auto s0 = __builtin_amdgcn_permlane32_swap(__float_as_uint(x[0]), __float_as_uint(x[1]), false, false);
  auto s1 = __builtin_amdgcn_permlane32_swap(__float_as_uint(x[2]), __float_as_uint(x[3]), false, false);
  const float r0 = __uint_as_float(s0[0]) + __uint_as_float(s0[1]), r1 = __uint_as_float(s1[0]) + __uint_as_float(s1[1]);
  auto s2 = __builtin_amdgcn_permlane16_swap(__float_as_uint(r0), __float_as_uint(r1), false, false);
  float r = __uint_as_float(s2[0]) + __uint_as_float(s2[1]);
  r += dpp<0x124>(r); r += dpp<0x128>(r);
  return r;
}
extern "C" __global__ void __launch_bounds__(256) k_final(unsigned char* ws, u16* out) {
  __shared__ float red[4][32];
  u64 t_fentry; asm volatile("s_memrealtime %0\n s_waitcnt lgkmcnt(0)" : "=s"(t_fentry) :: "memory");
  const int tid = threadIdx.x, lane = tid & 63, wv = tid >> 6, id = blockIdx.x;
  const int c8 = id >> 1, bp = id & 1, half = (lane >> 3) & 1, hd = 8 * wv + 2 * (lane >> 4) + ((lane >> 2) & 1);
  u32 yo = (u32)WS_Y + id * 16384 + hd * 512 + half * 64 + (lane & 3) * 16, mo = (u32)WS_MLA + bp * 4096 + hd * 128 + half * 64 + (lane & 2) * 16;
  asm volatile("" : "+v"(yo), "+v"(mo));
  f32x4 v[4], ml[2];
#pragma unroll
  for (int i = 0; i < 2; ++i) ml[i] = *reinterpret_cast<const f32x4*>(ws + mo + i * 16);
#pragma unroll
  for (int i = 0; i < 4; ++i) v[i] = *reinterpret_cast<const f32x4*>(ws + yo + i * 128);
  __builtin_amdgcn_sched_barrier(0);
  if (id == 0 && tid < 128) reinterpret_cast<u64*>(ws)[tid] = 0ull;
  const float m3 = half ? -INFINITY : ml[1][2], l3 = half ? 0.f : ml[1][3];
  float M = fmaxf(fmaxf(ml[0][0], ml[0][2]), fmaxf(ml[1][0], m3));
  M = fmaxf(M, dpp<0x128>(M));
  const float e0 = __builtin_amdgcn_exp2f(ml[0][0] - M), e1 = __builtin_amdgcn_exp2f(ml[0][2] - M), e2 = __builtin_amdgcn_exp2f(ml[1][0] - M), e3 = __builtin_amdgcn_exp2f(m3 - M);
  float L = fmaf(e0, ml[0][1], e1 * ml[0][3]) + fmaf(e2, ml[1][1], e3 * l3);
  L += dpp<0x128>(L);
  const f32x4 z = {0.f, 0.f, 0.f, 0.f};
  const f32x4 ya = ((v[0] * e0 + v[1] * e1) + (v[2] * e2 + (half ? z : v[3]) * e3)) * __builtin_amdgcn_rcpf(L);
  const float ra = red4(ya), rb = red4(half ? v[3] : z);
  if ((lane & 8) == 0) red[wv][(lane & 4) * 4 + (lane & 3) * 4 + (((lane >> 4) & 1) * 2 + (lane >> 5))] = (lane & 4) ? rb : ra;
  __syncthreads();
  if (tid < 32) out[(2 * bp + ((tid >> 3) & 1)) * 1152 + (tid >> 4) * 576 + c8 * 8 + (tid & 7)] = to_bf16((red[0][tid] + red[1][tid]) + (red[2][tid] + red[3][tid]));
  if (tid == 0) reinterpret_cast<u64*>(ws + (19u << 20))[id * 2 + 1] = __builtin_amdgcn_s_memrealtime();
  if (id == 0) {
    const u64 t_end = __builtin_amdgcn_s_memrealtime();
    __shared__ u64 sh[256]; __shared__ u64 sx[256];
    const u64* st = reinterpret_cast<const u64*>(ws + (16u << 20)) + tid * 16;
    sh[tid] = st[15];
    __syncthreads();
    u64 t0 = ~0ull; for (int i = 0; i < 256; ++i) t0 = sh[i] < t0 ? sh[i] : t0;
    const bool isB = tid < NBW;
    u64 x = 0; int mode = 0;
    if (DIAG == 1) x = t_end - t0;
    if (DIAG == 2) x = st[isB ? 7 : 4] - t0;
    if (DIAG == 3) { x = isB ? 0 : st[1] - t0; mode = 1; }
    if (DIAG == 4) { x = isB ? 0 : st[4] - t0; mode = 1; }
    if (DIAG == 5) x = isB ? st[1] - t0 : 0;
    if (DIAG == 6) x = isB ? st[3] - t0 : 0;
    if (DIAG == 7) x = st[15] - t0;
    if (DIAG == 8) x = isB ? 0 : st[4] - t0;
    if (DIAG == 9) x = isB ? st[7] - t0 : 0;
    if (DIAG == 10) { x = isB ? 0 : st[0] - t0; mode = 1; }
    if (DIAG == 11) { x = isB ? st[2] - t0 : 0; }
    if (DIAG == 12) x = 10 * (t_end - t_fentry);
    if (DIAG == 13) { x = st[isB ? 7 : 4]; mode = 3; }
    sx[tid] = x;
    __syncthreads();
    u64 X = 0;
    if (mode == 0) { for (int i = 0; i < 256; ++i) X = sx[i] > X ? sx[i] : X; }
    else { for (int i = 0; i < 256; ++i) X += sx[i]; X /= (mode == 1 ? 224 : 32); }
    if (mode == 3) { X = 0; for (int i = 0; i < 256; ++i) X = sx[i] > X ? sx[i] : X; X = 10 * (t_fentry - X); }
    if (X > 20000) X = 20000;
    const u64 tgt = t0 + 10000 + X;
    for (int i = 0; i < 2000000; ++i) { if (__builtin_amdgcn_s_memrealtime() >= tgt) break; __builtin_amdgcn_s_sleep(1); }
  }
}

__global__ void __launch_bounds__(1024, 4) k_main(Args a) {
  __shared__ __attribute__((aligned(16))) unsigned char lds[L_TOTAL];
  u64 t_entry; asm volatile("s_memrealtime %0\n s_waitcnt lgkmcnt(0)" : "=s"(t_entry) :: "memory");
  u32 pf;
  { u64 pc; u32 len;
    asm volatile("s_getpc_b64 %0\n.Lpf%=:\n\ts_mov_b32 %1, k_tail_mark-.Lpf%=" : "=s"(pc), "=s"(len));
    pf = __builtin_amdgcn_raw_buffer_load_b32(__builtin_amdgcn_make_buffer_rsrc(reinterpret_cast<void*>(pc), 0, len < 131072u ? len : 0u, 0x00020000), CPFM ? (threadIdx.x < 16 ? ((blockIdx.x >> 3) * 16 + threadIdx.x) * 128 : 0x7ffff000) : threadIdx.x * 128, 0, 0);
    __builtin_amdgcn_sched_barrier(0); }
  u64 tag = (hw_dispatch_id() + 1ull) * 0x9E3779B97F4A7C15ull;
  tag ^= ((u64)__builtin_amdgcn_dispatch_ptr() >> 6) * 0xC2B2AE3D27D4EB4Full;
  tag |= 1ull << 63;
  if (GT0 && blockIdx.x == 0 && (threadIdx.x >> 6) == 7) {
    const u32 t = rt32();
    __hip_atomic_store(reinterpret_cast<u64*>(a.ws + WS_ACC) + (threadIdx.x & 63), 0ull, RLX_AGENT);
    asm volatile("s_waitcnt vmcnt(0)" ::: "memory");
    if ((threadIdx.x & 63) == 0) __hip_atomic_store(reinterpret_cast<u64*>(a.ws + WS_T0), (tag & 0xffffffff00000000ull) | t, RLX_AGENT);
  }
  if (__builtin_expect(blockIdx.x >= NBW, 1)) part_a(a, lds, blockIdx.x - NBW, tag);
  else if (__builtin_amdgcn_readfirstlane(threadIdx.x >> 6) < 8) part_b<false>(a, lds, blockIdx.x, tag);
  else part_b<true>(a, lds, blockIdx.x, tag);
  asm volatile("" :: "v"(pf));
  if (threadIdx.x == 0) reinterpret_cast<u64*>(a.ws + (16u << 20))[blockIdx.x * 16 + 15] = t_entry;
}

extern "C" __global__ void k_tail_mark() {}

static void* g_ws;
extern "C" void kernel_launch(void* const* d_in, const int* in_sizes, int n_in, void* d_out, int out_size, void* d_ws, size_t ws_size,
                              hipStream_t stream) {
  Args a;
  a.xA = (const u16*)d_in[0]; a.K = (const u16*)d_in[1]; a.V = (const u16*)d_in[2]; a.wq = (const u16*)d_in[3]; a.wo = (const u16*)d_in[4];
  a.xB = (const u16*)d_in[5]; a.C = (const u16*)d_in[6]; a.wqB = (const u16*)d_in[7]; a.wuk = (const u16*)d_in[8]; a.wuv = (const u16*)d_in[9]; a.woB = (const u16*)d_in[10];
#define WSTOP 32
  if (WSTOP && ws_size >= ((size_t)WSTOP << 20)) d_ws = (unsigned char*)d_ws + ((ws_size - ((size_t)WSTOP << 20)) & ~(size_t)4095);
  g_ws = d_ws;
  a.ws = (unsigned char*)d_ws;
  a.out = (u16*)d_out;
  k_main<<<NBW + NAW, 1024, 0, stream>>>(a);
  k_final<<<144, 256, 0, stream>>>((unsigned char*)d_ws, (u16*)d_out);
}
```

```cpp
#include <hip/hip_runtime.h>
#include <stdint.h>
#include <utility>

typedef unsigned short u16;
typedef unsigned u32;
typedef unsigned long long u64;
typedef u32 u32x4 __attribute__((ext_vector_type(4)));
typedef u32 u32x2 __attribute__((ext_vector_type(2)));
typedef float f32x4 __attribute__((ext_vector_type(4)));
typedef float f32x2 __attribute__((ext_vector_type(2)));
typedef short s16x4 __attribute__((ext_vector_type(4)));
typedef __bf16 bf16x8 __attribute__((ext_vector_type(8)));
typedef __bf16 bf16x2 __attribute__((ext_vector_type(2)));
#define DI __device__ __forceinline__
#define RLX_AGENT __ATOMIC_RELAXED, __HIP_MEMORY_SCOPE_AGENT
extern "C" __device__ u64 hw_dispatch_id() __asm("llvm.amdgcn.dispatch.id");

template <int... Is, class F> DI void static_for_impl(std::integer_sequence<int, Is...>, F&& f) { (f(std::integral_constant<int, Is>{}), ...); }
template <int N, class F> DI void static_for(F&& f) { static_for_impl(std::make_integer_sequence<int, N>{}, f); }

constexpr int H = 32, SK = 4096, DM = 576, DC = 512;
constexpr int ACU = 7;
constexpr int NBW = 32;
constexpr int NAW = H * ACU;
constexpr int NSP = 8;
constexpr float C2 = 0.08838834764831845f * 1.4426950408889634f;

constexpr size_t WS_F1 = 0, WS_F2 = 512;
constexpr size_t WS_QABS = 4096;
constexpr size_t WS_CTXP = WS_QABS + 4 * 32 * 512 * 2;
constexpr size_t WS_MLB = WS_CTXP + (size_t)4 * NSP * 32 * 512 * 2;
constexpr size_t WS_Y = WS_MLB + 4 * NSP * 32 * 2 * 4;
constexpr size_t WS_MLA = WS_Y + (size_t)72 * 256 * 128;
constexpr size_t WS_END = WS_MLA + 256 * 32;

constexpr int CRS = 1056;
constexpr int CBUF = 64 * CRS;
constexpr int L_BIG = 0;
constexpr int L_AM = 2 * CBUF;
constexpr int L_QS = L_AM + 16 * 272;
constexpr int L_ST = L_QS + 1024;
constexpr int L_PF = L_ST + 4096;
constexpr int L_TOTAL = L_PF + 4096;
constexpr int AM_LD = 136;
constexpr int AM2_LD = 520;
constexpr int L_AM2 = 69632, L_CP = 88064, L_W8 = 106496;

struct Args {
  const u16 *xA, *K, *V, *wq, *wo, *xB, *C, *wqB, *wuk, *wuv, *woB;
  unsigned char* ws;
  u16* out;
};

DI float bf_lo(u32 x) { return __uint_as_float(x << 16); }
DI float bf_hi(u32 x) { return __uint_as_float(x & 0xffff0000u); }
DI u32 pk_bf16(float lo, float hi) { bf16x2 v = {(__bf16)lo, (__bf16)hi}; return __builtin_bit_cast(u32, v); }
DI u16 to_bf16(float x) { return __builtin_bit_cast(u16, (__bf16)x); }
DI float bf2f(u16 x) { return __uint_as_float(((u32)x) << 16); }
DI float dot2(u32 a, u32 b, float c) { return __builtin_amdgcn_fdot2_f32_bf16(__builtin_bit_cast(bf16x2, a), __builtin_bit_cast(bf16x2, b), c, false); }
template <int CTRL> DI float dpp(float x) { return __builtin_bit_cast(float, __builtin_amdgcn_update_dpp(0, __builtin_bit_cast(int, x), CTRL, 0xf, 0xf, true)); }
DI float row16_sum(float x) { x += dpp<0x128>(x); x += dpp<0x124>(x); x += dpp<0x122>(x); x += dpp<0x121>(x); return x; }
DI float row16_max(float x) { x = fmaxf(x, dpp<0x128>(x)); x = fmaxf(x, dpp<0x124>(x)); x = fmaxf(x, dpp<0x122>(x)); x = fmaxf(x, dpp<0x121>(x)); return x; }
DI float xg_max(float x) {
  auto s = __builtin_amdgcn_permlane16_swap(__float_as_uint(x), __float_as_uint(x), false, false);
  x = fmaxf(__uint_as_float(s[0]), __uint_as_float(s[1]));
  auto t = __builtin_amdgcn_permlane32_swap(__float_as_uint(x), __float_as_uint(x), false, false);
  return fmaxf(__uint_as_float(t[0]), __uint_as_float(t[1]));
}
DI float xg_sum(float x) {
  auto s = __builtin_amdgcn_permlane16_swap(__float_as_uint(x), __float_as_uint(x), false, false);
  x = __uint_as_float(s[0]) + __uint_as_float(s[1]);
  auto t = __builtin_amdgcn_permlane32_swap(__float_as_uint(x), __float_as_uint(x), false, false);
  return __uint_as_float(t[0]) + __uint_as_float(t[1]);
}
DI f32x4 mfma16(u32x4 a, u32x4 b, f32x4 c) { return __builtin_amdgcn_mfma_f32_16x16x32_bf16(__builtin_bit_cast(bf16x8, a), __builtin_bit_cast(bf16x8, b), c, 0, 0, 0); }
DI u32x4 ld16(const u16* p) { return __builtin_nontemporal_load(reinterpret_cast<const u32x4*>(p)); }
DI u32x2 ld8(const u16* p) { return __builtin_nontemporal_load(reinterpret_cast<const u32x2*>(p)); }
DI u32x2 trread(const unsigned char* p) {
  return __builtin_bit_cast(u32x2, __builtin_amdgcn_ds_read_tr16_b64_v4i16((__attribute__((address_space(3))) s16x4*)(p)));
}
DI u32 prm(u32 hi, u32 lo, int odd) { return __builtin_amdgcn_perm(hi, lo, odd ? 0x07060302u : 0x05040100u); }

DI u32x4 tcol16(const u32x4 (&R)[8], int t) {
  u32x4 T;
#pragma unroll
  for (int q = 0; q < 4; ++q) T[q] = prm(R[2 * q + 1][t >> 1], R[2 * q][t >> 1], t & 1);
  return T;
}
DI u32x4 tcol8(const u32x2 (&R)[8], int t) {
  u32x4 T;
#pragma unroll
  for (int q = 0; q < 4; ++q) T[q] = prm(R[2 * q + 1][t >> 1], R[2 * q][t >> 1], t & 1);
  return T;
}

constexpr u32 SPIN_CAP = 1u << 22;
DI void wave_wait(const u64* f, bool active, u64 tag) {
  for (u32 spins = 0; spins < SPIN_CAP; ++spins) {
    const u64 v = active ? __hip_atomic_load(f, RLX_AGENT) : tag;
    if (__all(v == tag)) break;
    __builtin_amdgcn_s_sleep(2);
  }
  __builtin_amdgcn_fence(__ATOMIC_ACQUIRE, "agent");
}
DI void wg_wait64(const unsigned char* flags, u64 tag, int pw = 0) {
  if ((int)(threadIdx.x >> 6) == pw) {
    wave_wait(reinterpret_cast<const u64*>(flags) + (threadIdx.x & (NBW - 1)), true, tag);
    asm volatile("s_waitcnt vmcnt(0)" ::: "memory");
  }
  __syncthreads();
}
DI void st16_sc1(__amdgpu_buffer_rsrc_t rs, size_t off, u32x4 v) { __builtin_amdgcn_raw_buffer_store_b128(v, rs, (int)off, 0, 16); }
DI void st8_sc1(unsigned char* p, f32x2 v) { __hip_atomic_store(reinterpret_cast<u64*>(p), __builtin_bit_cast(u64, v), RLX_AGENT); }

struct QPre { u32x4 R0[8]; u32x4 R1; u32x2 xa, xb; u32 x1; };
DI void qproj_load(QPre& Q, const u16* x, const u16* Wh, int w, int lane) {
  const int j = lane & 15, g = lane >> 4;
  Q.xa = u32x2{0u, 0u}; Q.xb = u32x2{0u, 0u}; Q.x1 = 0u;
  if (j < 4) { Q.xa = ld8(x + j * DM + 36 * w + 8 * g); Q.xb = ld8(x + j * DM + 36 * w + 8 * g + 4); Q.x1 = x[j * DM + 36 * w + 32 + g]; }
#pragma unroll
  for (int jj = 0; jj < 8; ++jj) Q.R0[jj] = ld16(Wh + (size_t)(36 * w + 8 * g + jj) * 4096 + 8 * j);
  Q.R1 = ld16(Wh + (size_t)(36 * w + 32 + g) * 4096 + 8 * j);
}
DI void qproj_mfma(const QPre& Q, unsigned char* lds, int w, int lane) {
  const int j = lane & 15, g = lane >> 4;
  f32x4 acc[8];
  {
    const u32x4 a = {Q.xa[0], Q.xa[1], Q.xb[0], Q.xb[1]};
#pragma unroll
    for (int t = 0; t < 8; ++t) acc[t] = mfma16(a, tcol16(Q.R0, t), f32x4{0.f, 0.f, 0.f, 0.f});
    const u32x4 a1 = {Q.x1, 0u, 0u, 0u};
#pragma unroll
    for (int t = 0; t < 8; ++t) { const u32x4 bb = {(t & 1) ? (Q.R1[t >> 1] >> 16) : (Q.R1[t >> 1] & 0xffffu), 0u, 0u, 0u}; acc[t] = mfma16(a1, bb, acc[t]); }
  }
  float* qpart = reinterpret_cast<float*>(lds + L_BIG);
  if (g == 0) {
#pragma unroll
    for (int r = 0; r < 4; ++r) {
      float* p = qpart + (w * 4 + r) * 128 + 8 * j;
      *reinterpret_cast<f32x4*>(p) = f32x4{acc[0][r], acc[1][r], acc[2][r], acc[3][r]};
      *reinterpret_cast<f32x4*>(p + 4) = f32x4{acc[4][r], acc[5][r], acc[6][r], acc[7][r]};
    }
  }
  __builtin_amdgcn_sched_barrier(0);
}
DI void qproj_finish(unsigned char* lds) {
  const float* qpart = reinterpret_cast<const float*>(lds + L_BIG);
  __syncthreads();
  if (threadIdx.x < 512) {
    float s = 0.f;
#pragma unroll
    for (int ww = 0; ww < 16; ++ww) s += qpart[ww * 512 + threadIdx.x];
    reinterpret_cast<u16*>(lds + L_QS)[threadIdx.x] = to_bf16(s);
  }
  __syncthreads();
}

DI void wo_load(const u16* Wo, u32x2 (&R)[4][8], int nb, int lane, int s0, int s1) {
  const int j = lane & 15, g = lane >> 4;
#pragma unroll
  for (int s = 0; s < 4; ++s) if (s >= s0 && s < s1)
#pragma unroll
    for (int jj = 0; jj < 8; ++jj) R[s][jj] = ld8(Wo + (size_t)(32 * s + 8 * g + jj) * DM + 64 * nb + 4 * j);
}
DI void wo_mfma(const u32x2 (&R)[4][8], const unsigned char* lds, int lane, f32x4 (&acc)[4]) {
  const int j = lane & 15, g = lane >> 4;
#pragma unroll
  for (int t = 0; t < 4; ++t) acc[t] = f32x4{0.f, 0.f, 0.f, 0.f};
#pragma unroll
  for (int s = 0; s < 4; ++s) {
    const u32x4 a = *reinterpret_cast<const u32x4*>(lds + L_AM + j * 272 + (32 * s + 8 * g) * 2);
#pragma unroll
    for (int t = 0; t < 4; ++t) acc[t] = mfma16(a, tcol8(R[s], t), acc[t]);
  }
}
DI u32x4 yrow(const f32x4 (&acc)[4]) {
  const f32x4 o = {acc[0][0] + acc[0][1], acc[1][0] + acc[1][1], acc[2][0] + acc[2][1], acc[3][0] + acc[3][1]};
  return __builtin_bit_cast(u32x4, o);
}
DI void am_zero(unsigned char* lds, int tid) {
  if (tid < 136) { const int pr = tid / 34; *reinterpret_cast<u32x4*>(lds + L_AM + (4 * pr + 2) * 272 + (tid - 34 * pr) * 16) = u32x4{0u, 0u, 0u, 0u}; }
}
DI void st16_y(__amdgpu_buffer_rsrc_t rs, int off, u32x4 v) { __builtin_amdgcn_raw_buffer_store_b128(v, rs, off, 0, 16); }
DI float rdlane(float x, int l) { return __builtin_bit_cast(float, __builtin_amdgcn_readlane(__builtin_bit_cast(int, x), l)); }

constexpr int NS = 10;
constexpr int NSEQ = 8 * NS;
constexpr int DEPTH = 2, RING = 12, RAMP = 2;
constexpr int issued(int i) {
  int d = RING - (i + RAMP - 1) / RAMP; if (d < DEPTH) d = DEPTH;
  int e = i + d; if (e < RING) e = RING; return e > NSEQ ? NSEQ : e;
}
DI void part_a(const Args& a, unsigned char* lds, int idx, u64 tag) {
  const int tid = threadIdx.x, lane = tid & 63, w = __builtin_amdgcn_readfirstlane(tid >> 6);
  const int j = lane & 15, g = lane >> 4;
  const int h = (idx & 7) * 4 + ((idx >> 3) & 3), ci = idx >> 5;
  const int U0 = (ci * 1024) / ACU, NUN = ((ci + 1) * 1024) / ACU - U0;
  const int rot = (h + ci) % NS;
  const int bad_sl = (16 * (NS - 1) + w < NUN) ? -1 : NS - 1 - rot;
  const __amdgpu_buffer_rsrc_t rs = __builtin_amdgcn_make_buffer_rsrc(a.ws, 0, (int)WS_END, 0x00020000);
  const u16* Wq = a.wq + h * 128;
  QPre Q;
  qproj_load(Q, a.xA, Wq, w, lane);
  const size_t base = ((size_t)h * SK + 4 * U0) * 128;
  const int voff = lane * 16;
  u32x4 R[RING];
#define A_ISSUE(i) R[(i) % RING] = __builtin_amdgcn_raw_buffer_load_b128( \
    __builtin_amdgcn_make_buffer_rsrc(const_cast<u16*>(((((i) / NS) & 1) ? a.V : a.K) + base + (size_t)((i) / (2 * NS)) * H * SK * 128), 0, NUN * 1024, 0x00020000), \
    voff, (((i) % NS) + rot >= NS ? ((i) % NS) + rot - NS : ((i) % NS) + rot) * 16384 + w * 1024, 2)
  constexpr int PRE = RING / 2;
#pragma unroll
  for (int t = 0; t < PRE; ++t) A_ISSUE(t);
  __builtin_amdgcn_sched_barrier(0);
  am_zero(lds, tid);
  qproj_mfma(Q, lds, w, lane);
#pragma unroll
  for (int t = PRE; t < RING; ++t) A_ISSUE(t);
  __builtin_amdgcn_sched_barrier(0);
  qproj_finish(lds);
  float* pacc = reinterpret_cast<float*>(lds + L_BIG);
  float* pml = reinterpret_cast<float*>(lds + L_ST);
  u32x4 q;
  float m, l, acc[8], s[NS];
  u32x2 RW[4][8];
  const u16* Wo = a.wo + (size_t)h * 128 * DM;
  static_for<NSEQ>([&](auto ic) __attribute__((always_inline)) {
    constexpr int i = ic.value, bb = i / (2 * NS), pass = (i / NS) & 1, sl = i % NS;
    const u32x4 r = R[i % RING];
    if constexpr (pass == 0) {
      if constexpr (sl == 0) { q = *reinterpret_cast<const u32x4*>(lds + L_QS + (bb * 128 + 8 * j) * 2); m = -INFINITY; }
      float d = dot2(r[0], q[0], 0.f); d = dot2(r[1], q[1], d); d = dot2(r[2], q[2], d); d = dot2(r[3], q[3], d);
      asm volatile("" : "+v"(d));
      if constexpr (issued(i + 1) > issued(i)) A_ISSUE(issued(i));
      d = row16_sum(d) * C2;
      d = (sl == bad_sl) ? -INFINITY : d;
      s[sl] = d; m = fmaxf(m, d);
      if constexpr (sl == NS - 1) {
        m = xg_max(m); l = 0.f;
#pragma unroll
        for (int t = 0; t < NS; ++t) { s[t] = __builtin_amdgcn_exp2f(s[t] - m); l += s[t]; }
        l = xg_sum(l);
        if (lane == 0) *reinterpret_cast<f32x2*>(pml + (bb * 16 + w) * 2) = f32x2{m, l};
#pragma unroll
        for (int k = 0; k < 8; ++k) acc[k] = 0.f;
      }
    } else {
      const float p = s[sl];
#pragma unroll
      for (int k = 0; k < 4; ++k) { acc[2 * k] = fmaf(p, bf_lo(r[k]), acc[2 * k]); acc[2 * k + 1] = fmaf(p, bf_hi(r[k]), acc[2 * k + 1]); }
      asm volatile("" : "+v"(acc[0]), "+v"(acc[1]), "+v"(acc[2]), "+v"(acc[3]), "+v"(acc[4]), "+v"(acc[5]), "+v"(acc[6]), "+v"(acc[7]));
      if constexpr (issued(i + 1) > issued(i)) A_ISSUE(issued(i));
      if (sl == 4) { if (w < 9) wo_load(Wo, RW, w, lane, bb, bb + 1); }
      if constexpr (sl == NS - 1) {
#pragma unroll
        for (int k = 0; k < 8; ++k) acc[k] = xg_sum(acc[k]);
        if (g == 0) {
          float* pp = pacc + (bb * 16 + w) * 128 + 8 * j;
          *reinterpret_cast<f32x4*>(pp) = f32x4{acc[0], acc[1], acc[2], acc[3]};
          *reinterpret_cast<f32x4*>(pp + 4) = f32x4{acc[4], acc[5], acc[6], acc[7]};
        }
      }
    }
    __builtin_amdgcn_sched_barrier(0);
  });
#undef A_ISSUE
  __syncthreads();
  if (w < 8) {
    const int bb = w >> 1, d = tid & 127;
    u16* Am = reinterpret_cast<u16*>(lds + L_AM);
    const f32x2 ml = *reinterpret_cast<const f32x2*>(pml + (bb * 16 + j) * 2);
    float pv[16];
#pragma unroll
    for (int i = 0; i < 16; ++i) pv[i] = pacc[(bb * 16 + i) * 128 + d];
    const float wt = __builtin_amdgcn_exp2f(ml[0] - row16_max(ml[0]));
    float o0 = 0.f, o1 = 0.f;
#pragma unroll
    for (int i = 0; i < 16; i += 2) { o0 = fmaf(rdlane(wt, i), pv[i], o0); o1 = fmaf(rdlane(wt, i + 1), pv[i + 1], o1); }
    const float o = o0 + o1;
    const u16 hi = to_bf16(o); const u16 lo = to_bf16(o - bf2f(hi));
    Am[(4 * bb) * AM_LD + d] = hi; Am[(4 * bb + 1) * AM_LD + d] = lo;
  } else if (w == 9) {
    const f32x2 ml = *reinterpret_cast<const f32x2*>(pml + lane * 2);
    const float M = row16_max(ml[0]);
    const float L = row16_sum(__builtin_amdgcn_exp2f(ml[0] - M) * ml[1]);
    if (j == 0) __builtin_amdgcn_raw_buffer_store_b64(__builtin_bit_cast(u32x2, f32x2{M, L}), rs, (int)WS_MLA + (((g >> 1) * 32 + h) * 2 + (ci >> 2)) * 64 + (g & 1) * 32 + (ci & 3) * 8, 0, 16);
  }
  __syncthreads();
  if (w < 9) {
    f32x4 ya[4];
    wo_mfma(RW, lds, lane, ya);
    st16_y(rs, (int)WS_Y + (((8 * w + (j >> 1)) * 2 + (g >> 1)) * 256 + h * 8 + 2 * (ci & 3) + (ci >> 2)) * 64 + (g & 1) * 32 + (j & 1) * 16, yrow(ya));
  }
}

template <bool LD> DI void part_b(const Args& a, unsigned char* lds, int wg, u64 tag) {
  const int tid = threadIdx.x, lane = tid & 63, w = __builtin_amdgcn_readfirstlane(tid >> 6);
  const int j = lane & 15, g = lane >> 4;
  const __amdgpu_buffer_rsrc_t rs = __builtin_amdgcn_make_buffer_rsrc(a.ws, 0, (int)WS_END, 0x00020000);
  const int h = wg;
  const int h3 = wg ^ 2;
  const int b2 = wg >> 3, sp = wg & 7;
  const u16* cl = a.C + ((size_t)b2 * SK + sp * 512 + 8 * (w - 8)) * DC + lane * 8;
  u32x4 X[16];
  u32x2 RW[4][8];
  u32x4 W8[2];
  u32* cnt = reinterpret_cast<u32*>(lds + L_ST + 4064);
#define C_ISSUE(T) _Pragma("unroll") for (int r = 0; r < 8; ++r) X[8 * ((T) & 1) + r] = ld16(cl + (size_t)(64 * (T) + r) * DC)
  u32 sb0 = L_BIG + 8 * (w - 8) * CRS + lane * 16, sb1 = sb0 + CBUF;
  asm volatile("" : "+v"(sb0), "+v"(sb1));
#define C_STAGE(T) _Pragma("unroll") for (int r = 0; r < 8; ++r) *reinterpret_cast<u32x4*>(lds + (((T) & 1) ? sb1 : sb0) + r * CRS) = X[8 * ((T) & 1) + r]
  {
    const u16* Wq = a.wqB + h * 128;
    u32x4 U[8];
    QPre Q;
    qproj_load(Q, a.xB, Wq, w, lane);
#pragma unroll
    for (int i = 0; i < 8; ++i) U[i] = ld16(a.wuk + ((size_t)(32 * w + 4 * i + g) * H + h) * 128 + 8 * j);
    __builtin_amdgcn_sched_barrier(0);
    if constexpr (!LD) am_zero(lds, tid);
    qproj_mfma(Q, lds, w, lane);
    if constexpr (LD) { C_ISSUE(0); }
    __builtin_amdgcn_sched_barrier(0);
    qproj_finish(lds);
    u32x4 qv[4];
#pragma unroll
    for (int bb = 0; bb < 4; ++bb) qv[bb] = *reinterpret_cast<const u32x4*>(lds + L_QS + (bb * 128 + 8 * j) * 2);
    u16* qa = reinterpret_cast<u16*>(lds + L_PF);
#pragma unroll
    for (int i = 0; i < 8; ++i) {
      float v = 0.f;
#pragma unroll
      for (int bb = 0; bb < 4; ++bb) {
        float d = dot2(U[i][0], qv[bb][0], 0.f); d = dot2(U[i][1], qv[bb][1], d); d = dot2(U[i][2], qv[bb][2], d); d = dot2(U[i][3], qv[bb][3], d);
        d = row16_sum(d);
        v = (j == bb) ? d : v;
      }
      if (j < 4) qa[j * 512 + 32 * w + 4 * i + g] = to_bf16(v);
    }
    __builtin_amdgcn_sched_barrier(0);
    if constexpr (LD) { asm volatile("s_waitcnt vmcnt(0)" ::: "memory"); C_ISSUE(1); }
    __syncthreads();
    if constexpr (!LD) {
      if (w == 0) {
#pragma unroll
        for (int bb = 0; bb < 4; ++bb) st16_sc1(rs, WS_QABS + ((bb * H + h) * 512 + lane * 8) * 2, *reinterpret_cast<const u32x4*>(qa + bb * 512 + lane * 8));
        asm volatile("s_waitcnt vmcnt(0)" ::: "memory");
        if (lane == 0) { __hip_atomic_store(reinterpret_cast<u64*>(a.ws + WS_F1 + wg * 8), tag, RLX_AGENT); *cnt = 0u; }
      }
      __builtin_amdgcn_sched_barrier(0);
      __builtin_amdgcn_s_barrier();
    } else {
      C_STAGE(0);
      __builtin_amdgcn_sched_barrier(0);
      __builtin_amdgcn_s_barrier();
      C_ISSUE(2);
    }
  }
  {
    const int b = b2;
    if constexpr (!LD) wg_wait64(a.ws + WS_F1, tag); else __syncthreads();
    const int kt = w & 3, hh = (w >> 2) & 1;
    float* stmax = reinterpret_cast<float*>(lds + L_ST);
    float* stsum = stmax + 128;
    float* salpha = stmax + 256;
    unsigned char* Pf = lds + L_PF;
    float M = -INFINITY, lsum = 0.f;
    f32x4 acc[2][2];
#pragma unroll
    for (int ct = 0; ct < 2; ++ct) { acc[ct][0] = f32x4{0.f, 0.f, 0.f, 0.f}; acc[ct][1] = f32x4{0.f, 0.f, 0.f, 0.f}; }
    const int troff = (4 * g + (j >> 2)) * CRS + (32 * w + 4 * (j & 3)) * 2;
    auto pv_step = [&](const unsigned char* cb) __attribute__((always_inline)) {
      const f32x4 al0 = *reinterpret_cast<const f32x4*>(salpha + 4 * g), al1 = *reinterpret_cast<const f32x4*>(salpha + 16 + 4 * g);
#pragma unroll
      for (int ct = 0; ct < 2; ++ct) { acc[ct][0] *= al0; acc[ct][1] *= al1; }
#pragma unroll
      for (int blk = 0; blk < 2; ++blk) {
        const u32x4 p0 = *reinterpret_cast<const u32x4*>(Pf + ((blk * 2 + 0) * 64 + lane) * 16);
        const u32x4 p1 = *reinterpret_cast<const u32x4*>(Pf + ((blk * 2 + 1) * 64 + lane) * 16);
#pragma unroll
        for (int ct = 0; ct < 2; ++ct) {
          const u32x2 lo = trread(cb + troff + (32 * blk) * CRS + 32 * ct);
          const u32x2 hi = trread(cb + troff + (32 * blk + 16) * CRS + 32 * ct);
          const u32x4 bf = {lo[0], lo[1], hi[0], hi[1]};
          acc[ct][0] = mfma16(p0, bf, acc[ct][0]); acc[ct][1] = mfma16(p1, bf, acc[ct][1]);
        }
      }
    };
    unsigned char* Qf = lds + L_BIG + CBUF;
    if constexpr (!LD) {
      u32x4 qv[4];
#pragma unroll
      for (int k = 0; k < 4; ++k) qv[k] = *reinterpret_cast<const u32x4*>(a.ws + WS_QABS + ((size_t)b * H * 512 + (tid + 512 * k) * 8) * 2);
#pragma unroll
      for (int k = 0; k < 4; ++k) { const int e = tid + 512 * k, head = e >> 6, ch = e & 63;
        *reinterpret_cast<u32x4*>(Qf + ((head >> 4) * 16 + (ch >> 2)) * 1040 + ((ch & 3) * 16 + (head & 15)) * 16) = qv[k]; }
    }
    __syncthreads();
    if constexpr (!LD) {
      u32x4 QF[16];
#pragma unroll
      for (int s = 0; s < 16; ++s) QF[s] = *reinterpret_cast<const u32x4*>(Qf + (hh * 16 + s) * 1040 + lane * 16);
      auto wuv_load = [&]() __attribute__((always_inline)) {
#pragma unroll
        for (int k2 = 0; k2 < 2; ++k2)
#pragma unroll
          for (int jj = 0; jj < 8; ++jj) {
            const u32x4 v = ld16(a.wuv + ((size_t)(64 * w + 32 * k2 + 8 * g + jj) * H + h3) * 128 + 8 * j);
            RW[2 * k2][jj] = u32x2{v[0], v[1]}; RW[2 * k2 + 1][jj] = u32x2{v[2], v[3]};
          }
      };
      auto s_tile = [&](const unsigned char* cb, bool last) __attribute__((always_inline)) {
        __syncthreads();
        f32x4 s0 = {0.f, 0.f, 0.f, 0.f}, s1 = {0.f, 0.f, 0.f, 0.f};
#pragma unroll
        for (int s = 0; s < 16; s += 2) {
          s0 = mfma16(*reinterpret_cast<const u32x4*>(cb + (16 * kt + j) * CRS + (32 * s + 8 * g) * 2), QF[s], s0);
          s1 = mfma16(*reinterpret_cast<const u32x4*>(cb + (16 * kt + j) * CRS + (32 * s + 32 + 8 * g) * 2), QF[s + 1], s1);
        }
        const f32x4 sv = (s0 + s1) * C2;
        const float v = xg_max(fmaxf(fmaxf(sv[0], sv[1]), fmaxf(sv[2], sv[3])));
        if (g == 0) stmax[kt * 32 + 16 * hh + j] = v;
        __syncthreads();
        const float* sm = stmax + 16 * hh + j;
        const float Mn = fmaxf(fmaxf(M, fmaxf(sm[0], sm[32])), fmaxf(sm[64], sm[96]));
        const float alpha = __builtin_amdgcn_exp2f(M - Mn);
        M = Mn;
        u32x2 pk;
        pk[0] = pk_bf16(__builtin_amdgcn_exp2f(sv[0] - Mn), __builtin_amdgcn_exp2f(sv[1] - Mn));
        pk[1] = pk_bf16(__builtin_amdgcn_exp2f(sv[2] - Mn), __builtin_amdgcn_exp2f(sv[3] - Mn));
        lsum = lsum * alpha + ((bf_lo(pk[0]) + bf_hi(pk[0])) + (bf_lo(pk[1]) + bf_hi(pk[1])));
        *reinterpret_cast<u32x2*>(Pf + (((kt >> 1) * 2 + hh) * 64 + lane) * 16 + (kt & 1) * 8) = pk;
        if (kt == 0 && g == 0) salpha[16 * hh + j] = alpha;
        __syncthreads();
        if (last) { __builtin_amdgcn_sched_barrier(0); wuv_load(); __builtin_amdgcn_sched_barrier(0); }
        pv_step(cb);
      };
#pragma unroll 1
      for (int T = 0; T < 6; T += 2) { s_tile(lds + L_BIG, false); s_tile(lds + L_BIG + CBUF, false); }
      s_tile(lds + L_BIG, false); s_tile(lds + L_BIG + CBUF, true);
    } else {
#pragma unroll
      for (int T = 0; T < 8; ++T) {
        __syncthreads();
        if (T + 1 < 8) { C_STAGE(T + 1); if (T + 3 < 8) { C_ISSUE(T + 3); } }
        if (T == 5) wo_load(a.woB + (size_t)h3 * 128 * DM, RW, w - 8, lane, 0, 2);
        if (T == 6) wo_load(a.woB + (size_t)h3 * 128 * DM, RW, w - 8, lane, 2, 4);
        if (T == 6) {
#pragma unroll
          for (int k = 0; k < 2; ++k) W8[k] = ld16(a.woB + ((size_t)h3 * 128 + 16 * (w - 8) + 8 * k + (lane >> 3)) * DM + 512 + (lane & 7) * 8);
        }
        __syncthreads();
        __syncthreads();
        pv_step(lds + L_BIG + (T & 1) * CBUF);
      }
    }
#undef C_ISSUE
#undef C_STAGE
    __syncthreads();
    if constexpr (!LD) { lsum = xg_sum(lsum); if (g == 0) stsum[kt * 32 + 16 * hh + j] = lsum; if (kt == 0 && g == 0) salpha[32 + 16 * hh + j] = M; }
    float* tb = reinterpret_cast<float*>(lds + L_BIG);
#pragma unroll
    for (int ct = 0; ct < 2; ++ct)
#pragma unroll
      for (int h2 = 0; h2 < 2; ++h2)
#pragma unroll
        for (int r = 0; r < 4; ++r) tb[(16 * h2 + 4 * g + r) * 516 + 32 * w + 16 * ct + j] = acc[ct][h2][r];
    if constexpr (!LD) { for (int i = tid; i < 8 * AM2_LD * 2 / 16; i += 512) reinterpret_cast<u32x4*>(lds + L_AM2 + 8 * AM2_LD * 2)[i] = u32x4{0u, 0u, 0u, 0u}; }
    __syncthreads();
    if constexpr (LD) {
      const int t2 = tid - 512;
#pragma unroll
      for (int k = 0; k < 4; ++k) {
        const int e = t2 + 512 * k, head = e >> 6, c8 = e & 63;
        const f32x4 v0 = *reinterpret_cast<const f32x4*>(tb + head * 516 + 8 * c8), v1 = *reinterpret_cast<const f32x4*>(tb + head * 516 + 8 * c8 + 4);
        st16_sc1(rs, WS_CTXP + ((size_t)((b * NSP + sp) * 32 + head) * 512 + 8 * c8) * 2, u32x4{pk_bf16(v0[0], v0[1]), pk_bf16(v0[2], v0[3]), pk_bf16(v1[0], v1[1]), pk_bf16(v1[2], v1[3])});
      }
      if (t2 < 32) {
        const float L = (stsum[t2] + stsum[32 + t2]) + (stsum[64 + t2] + stsum[96 + t2]);
        st8_sc1(a.ws + WS_MLB + ((size_t)(b * NSP + sp) * 32 + t2) * 8, f32x2{salpha[32 + t2], L});
      }
      asm volatile("s_waitcnt vmcnt(0)" ::: "memory");
      if (lane == 0) {
        if (__hip_atomic_fetch_add(cnt, 1u, __ATOMIC_RELAXED, __HIP_MEMORY_SCOPE_WORKGROUP) == 7u) {
          __hip_atomic_store(reinterpret_cast<u64*>(a.ws + WS_F2 + wg * 8), tag, RLX_AGENT);
        }
      }
#pragma unroll
      for (int k = 0; k < 2; ++k) *reinterpret_cast<u32x4*>(lds + L_W8 + (16 * (w - 8) + 8 * k + (lane >> 3)) * 144 + (lane & 7) * 16) = W8[k];
    }
  }
  {
    int tid = threadIdx.x; asm volatile("" : "+v"(tid));
    const int lane = tid & 63, j = lane & 15, g = lane >> 4;
    u16* Am2 = reinterpret_cast<u16*>(lds + L_AM2);
    float* cpart = reinterpret_cast<float*>(lds + L_CP);
    __builtin_amdgcn_sched_barrier(0);
    if constexpr (LD) {
      wg_wait64(a.ws + WS_F2, tag, 8);
      const int b = (tid >> 7) & 3, c4 = tid & 127;
      u32x2 pv[NSP];
      const f32x2 ml = *reinterpret_cast<const f32x2*>(a.ws + WS_MLB + ((size_t)(b * NSP + (j & 7)) * 32 + h3) * 8);
#pragma unroll
      for (int s = 0; s < NSP; ++s) pv[s] = *reinterpret_cast<const u32x2*>(a.ws + WS_CTXP + (((size_t)(b * NSP + s) * 32 + h3) * 512 + 4 * c4) * 2);
      const float M = row16_max(ml[0]);
      float wt = __builtin_amdgcn_exp2f(ml[0] - M);
      wt *= 2.f / row16_sum(wt * ml[1]);
      float c0 = 0.f, c1 = 0.f, c2 = 0.f, c3 = 0.f;
#pragma unroll
      for (int s = 0; s < NSP; ++s) { const float ws_ = rdlane(wt, s); c0 = fmaf(ws_, bf_lo(pv[s][0]), c0); c1 = fmaf(ws_, bf_hi(pv[s][0]), c1); c2 = fmaf(ws_, bf_lo(pv[s][1]), c2); c3 = fmaf(ws_, bf_hi(pv[s][1]), c3); }
      const u32 h0 = pk_bf16(c0, c1), h1 = pk_bf16(c2, c3);
      *reinterpret_cast<u32x2*>(Am2 + (2 * b) * AM2_LD + 4 * c4) = u32x2{h0, h1};
      *reinterpret_cast<u32x2*>(Am2 + (2 * b + 1) * AM2_LD + 4 * c4) = u32x2{pk_bf16(c0 - bf_lo(h0), c1 - bf_hi(h0)), pk_bf16(c2 - bf_lo(h1), c3 - bf_hi(h1))};
      __syncthreads();
      __syncthreads();
      const int t2 = tid - 512;
      float s = 0.f;
#pragma unroll
      for (int ww = 0; ww < 8; ++ww) s += cpart[ww * 512 + t2];
      u16* Am = reinterpret_cast<u16*>(lds + L_AM);
      const int bb = t2 >> 7, d = t2 & 127;
      const u16 hi = to_bf16(s);
      Am[(4 * bb) * AM_LD + d] = hi; Am[(4 * bb + 1) * AM_LD + d] = to_bf16(s - bf2f(hi));
    } else {
      __builtin_amdgcn_s_barrier();
      __syncthreads();
      f32x4 acc[8];
#pragma unroll
      for (int t = 0; t < 8; ++t) acc[t] = f32x4{0.f, 0.f, 0.f, 0.f};
#pragma unroll
      for (int k2 = 0; k2 < 2; ++k2) {
        const u32x4 af = *reinterpret_cast<const u32x4*>(Am2 + j * AM2_LD + 64 * w + 32 * k2 + 8 * g);
#pragma unroll
        for (int t = 0; t < 8; ++t) acc[t] = mfma16(af, tcol8(RW[2 * k2 + (t >> 2)], t & 3), acc[t]);
      }
      if (g < 2) {
#pragma unroll
        for (int bb = 0; bb < 2; ++bb) {
          float* p = cpart + (w * 4 + 2 * g + bb) * 128 + 8 * j;
          *reinterpret_cast<f32x4*>(p) = f32x4{acc[0][2 * bb] + acc[0][2 * bb + 1], acc[1][2 * bb] + acc[1][2 * bb + 1], acc[2][2 * bb] + acc[2][2 * bb + 1], acc[3][2 * bb] + acc[3][2 * bb + 1]};
          *reinterpret_cast<f32x4*>(p + 4) = f32x4{acc[4][2 * bb] + acc[4][2 * bb + 1], acc[5][2 * bb] + acc[5][2 * bb + 1], acc[6][2 * bb] + acc[6][2 * bb + 1], acc[7][2 * bb] + acc[7][2 * bb + 1]};
        }
      }
      __builtin_amdgcn_sched_barrier(0);
      __syncthreads();
      if (w == 0) {
#pragma unroll
        for (int s = 0; s < 4; ++s)
#pragma unroll
          for (int jj = 0; jj < 8; ++jj) RW[s][jj] = *reinterpret_cast<const u32x2*>(lds + L_W8 + (32 * s + 8 * g + jj) * 144 + 8 * j);
      }
    }
    __syncthreads();
    if (LD || w == 0) {
      f32x4 ya[4];
      wo_mfma(RW, lds, lane, ya);
      st16_y(rs, (int)WS_Y + (((8 * (LD ? w - 8 : 8) + (j >> 1)) * 2 + (g >> 1)) * 256 + h3 * 8 + 7) * 64 + (g & 1) * 32 + (j & 1) * 16, yrow(ya));
    }
  }
}

DI float red4(f32x4 x) {
  auto s0 = __builtin_amdgcn_permlane32_swap(__float_as_uint(x[0]), __float_as_uint(x[1]), false, false);
  auto s1 = __builtin_amdgcn_permlane32_swap(__float_as_uint(x[2]), __float_as_uint(x[3]), false, false);
  const float r0 = __uint_as_float(s0[0]) + __uint_as_float(s0[1]), r1 = __uint_as_float(s1[0]) + __uint_as_float(s1[1]);
  auto s2 = __builtin_amdgcn_permlane16_swap(__float_as_uint(r0), __float_as_uint(r1), false, false);
  float r = __uint_as_float(s2[0]) + __uint_as_float(s2[1]);
  r += dpp<0x124>(r); r += dpp<0x128>(r);
  return r;
}
extern "C" __global__ void __launch_bounds__(256) k_final(unsigned char* ws, u16* out) {
  __shared__ float red[4][32];
  const int tid = threadIdx.x, lane = tid & 63, wv = tid >> 6, id = blockIdx.x;
  const int c8 = id >> 1, bp = id & 1, half = (lane >> 3) & 1, hd = 8 * wv + 2 * (lane >> 4) + ((lane >> 2) & 1);
  u32 yo = (u32)WS_Y + id * 16384 + hd * 512 + half * 64 + (lane & 3) * 16, mo = (u32)WS_MLA + bp * 4096 + hd * 128 + half * 64 + (lane & 2) * 16;
  asm volatile("" : "+v"(yo), "+v"(mo));
  f32x4 v[4], ml[2];
#pragma unroll
  for (int i = 0; i < 2; ++i) ml[i] = *reinterpret_cast<const f32x4*>(ws + mo + i * 16);
#pragma unroll
  for (int i = 0; i < 4; ++i) v[i] = *reinterpret_cast<const f32x4*>(ws + yo + i * 128);
  __builtin_amdgcn_sched_barrier(0);
  if (id == 0 && tid < 128) reinterpret_cast<u64*>(ws)[tid] = 0ull;
  const float m3 = half ? -INFINITY : ml[1][2], l3 = half ? 0.f : ml[1][3];
  float M = fmaxf(fmaxf(ml[0][0], ml[0][2]), fmaxf(ml[1][0], m3));
  M = fmaxf(M, dpp<0x128>(M));
  const float e0 = __builtin_amdgcn_exp2f(ml[0][0] - M), e1 = __builtin_amdgcn_exp2f(ml[0][2] - M), e2 = __builtin_amdgcn_exp2f(ml[1][0] - M), e3 = __builtin_amdgcn_exp2f(m3 - M);
  float L = fmaf(e0, ml[0][1], e1 * ml[0][3]) + fmaf(e2, ml[1][1], e3 * l3);
  L += dpp<0x128>(L);
  const f32x4 z = {0.f, 0.f, 0.f, 0.f};
  const f32x4 ya = ((v[0] * e0 + v[1] * e1) + (v[2] * e2 + (half ? z : v[3]) * e3)) * __builtin_amdgcn_rcpf(L);
  const float ra = red4(ya), rb = red4(half ? v[3] : z);
  if ((lane & 8) == 0) red[wv][(lane & 4) * 4 + (lane & 3) * 4 + (((lane >> 4) & 1) * 2 + (lane >> 5))] = (lane & 4) ? rb : ra;
  __syncthreads();
  if (id == 0) { u64 d = reinterpret_cast<u64*>(ws + (16u << 20))[1]; if (d > 20) d = 20; const u64 t0 = __builtin_amdgcn_s_memrealtime(); while (__builtin_amdgcn_s_memrealtime() - t0 < 1000 * d) __builtin_amdgcn_s_sleep(1); }
  if (tid < 32) out[(2 * bp + ((tid >> 3) & 1)) * 1152 + (tid >> 4) * 576 + c8 * 8 + (tid & 7)] = to_bf16((red[0][tid] + red[1][tid]) + (red[2][tid] + red[3][tid]));
}

__global__ void __launch_bounds__(1024, 4) k_main(Args a) {
  __shared__ __attribute__((aligned(16))) unsigned char lds[L_TOTAL];
  u32 pf;
  { u64 pc; u32 len;
    asm volatile("s_getpc_b64 %0\n.Lpf%=:\n\ts_mov_b32 %1, k_tail_mark-.Lpf%=" : "=s"(pc), "=s"(len));
    pf = __builtin_amdgcn_raw_buffer_load_b32(__builtin_amdgcn_make_buffer_rsrc(reinterpret_cast<void*>(pc), 0, len < 131072u ? len : 0u, 0x00020000), threadIdx.x * 128, 0, 0);
    __builtin_amdgcn_sched_barrier(0); }
  u64 tag = (hw_dispatch_id() + 1ull) * 0x9E3779B97F4A7C15ull;
  tag ^= ((u64)__builtin_amdgcn_dispatch_ptr() >> 6) * 0xC2B2AE3D27D4EB4Full;
  tag |= 1ull << 63;
  if (blockIdx.x == 0 && threadIdx.x == 0) { u64* p = reinterpret_cast<u64*>(a.ws + (16u << 20)); const u64 id = hw_dispatch_id(); const u64 d = id - p[0]; p[1] = d; p[0] = id; }
  if (__builtin_expect(blockIdx.x >= NBW, 1)) part_a(a, lds, blockIdx.x - NBW, tag);
  else if (__builtin_amdgcn_readfirstlane(threadIdx.x >> 6) < 8) part_b<false>(a, lds, blockIdx.x, tag);
  else part_b<true>(a, lds, blockIdx.x, tag);
  asm volatile("" :: "v"(pf));
}

extern "C" __global__ void k_tail_mark() {}

extern "C" void kernel_launch(void* const* d_in, const int* in_sizes, int n_in, void* d_out, int out_size, void* d_ws, size_t ws_size,
                              hipStream_t stream) {
  Args a;
  a.xA = (const u16*)d_in[0]; a.K = (const u16*)d_in[1]; a.V = (const u16*)d_in[2]; a.wq = (const u16*)d_in[3]; a.wo = (const u16*)d_in[4];
  a.xB = (const u16*)d_in[5]; a.C = (const u16*)d_in[6]; a.wqB = (const u16*)d_in[7]; a.wuk = (const u16*)d_in[8]; a.wuv = (const u16*)d_in[9]; a.woB = (const u16*)d_in[10];
  a.ws = (unsigned char*)d_ws;
  a.out = (u16*)d_out;
  k_main<<<NBW + NAW, 1024, 0, stream>>>(a);
  k_final<<<144, 256, 0, stream>>>((unsigned char*)d_ws, (u16*)d_out);
}
```

```cpp
#include <hip/hip_runtime.h>
#include <stdint.h>
#include <utility>

typedef unsigned short u16;
typedef unsigned u32;
typedef unsigned long long u64;
typedef u32 u32x4 __attribute__((ext_vector_type(4)));
typedef u32 u32x2 __attribute__((ext_vector_type(2)));
typedef float f32x4 __attribute__((ext_vector_type(4)));
typedef float f32x2 __attribute__((ext_vector_type(2)));
typedef short s16x4 __attribute__((ext_vector_type(4)));
typedef __bf16 bf16x8 __attribute__((ext_vector_type(8)));
typedef __bf16 bf16x2 __attribute__((ext_vector_type(2)));
#define DI __device__ __forceinline__
#define RLX_AGENT __ATOMIC_RELAXED, __HIP_MEMORY_SCOPE_AGENT
extern "C" __device__ u64 hw_dispatch_id() __asm("llvm.amdgcn.dispatch.id");

template <int... Is, class F> DI void static_for_impl(std::integer_sequence<int, Is...>, F&& f) { (f(std::integral_constant<int, Is>{}), ...); }
template <int N, class F> DI void static_for(F&& f) { static_for_impl(std::make_integer_sequence<int, N>{}, f); }

constexpr int H = 32, SK = 4096, DM = 576, DC = 512;
constexpr int ACU = 7;
constexpr int NBW = 32;
constexpr int NAW = H * ACU;
constexpr int NSP = 8;
constexpr float C2 = 0.08838834764831845f * 1.4426950408889634f;

constexpr size_t WS_F1 = 0, WS_F2 = 512;
constexpr size_t WS_QABS = 4096;
constexpr size_t WS_CTXP = WS_QABS + 4 * 32 * 512 * 2;
constexpr size_t WS_MLB = WS_CTXP + (size_t)4 * NSP * 32 * 512 * 2;
constexpr size_t WS_Y = WS_MLB + 4 * NSP * 32 * 2 * 4;
constexpr size_t WS_MLA = WS_Y + (size_t)72 * 256 * 128;
constexpr size_t WS_RPT = WS_MLA + 256 * 128;
constexpr size_t WS_END = WS_RPT + 64 * 4480;

constexpr int CRS = 1056;
constexpr int CBUF = 64 * CRS;
constexpr int L_BIG = 0;
constexpr int L_AM = 2 * CBUF;
constexpr int L_QS = L_AM + 16 * 272;
constexpr int L_ST = L_QS + 1024;
constexpr int L_PF = L_ST + 4096;
constexpr int L_TOTAL = L_PF + 4096;
constexpr int AM_LD = 136;
constexpr int AM2_LD = 520;
constexpr int L_AM2 = 69632, L_CP = 88064, L_W8 = 106496;

#define ST(i) do {} while (0)
struct Args {
  const u16 *xA, *K, *V, *wq, *wo, *xB, *C, *wqB, *wuk, *wuv, *woB;
  unsigned char* ws;
  u16* out;
};

DI float bf_lo(u32 x) { return __uint_as_float(x << 16); }
DI float bf_hi(u32 x) { return __uint_as_float(x & 0xffff0000u); }
DI u32 pk_bf16(float lo, float hi) { bf16x2 v = {(__bf16)lo, (__bf16)hi}; return __builtin_bit_cast(u32, v); }
DI u16 to_bf16(float x) { return __builtin_bit_cast(u16, (__bf16)x); }
DI float bf2f(u16 x) { return __uint_as_float(((u32)x) << 16); }
DI float dot2(u32 a, u32 b, float c) { return __builtin_amdgcn_fdot2_f32_bf16(__builtin_bit_cast(bf16x2, a), __builtin_bit_cast(bf16x2, b), c, false); }
template <int CTRL> DI float dpp(float x) { return __builtin_bit_cast(float, __builtin_amdgcn_update_dpp(0, __builtin_bit_cast(int, x), CTRL, 0xf, 0xf, true)); }
DI float row16_sum(float x) { x += dpp<0x128>(x); x += dpp<0x124>(x); x += dpp<0x122>(x); x += dpp<0x121>(x); return x; }
DI float row16_max(float x) { x = fmaxf(x, dpp<0x128>(x)); x = fmaxf(x, dpp<0x124>(x)); x = fmaxf(x, dpp<0x122>(x)); x = fmaxf(x, dpp<0x121>(x)); return x; }
DI float xg_max(float x) {
  auto s = __builtin_amdgcn_permlane16_swap(__float_as_uint(x), __float_as_uint(x), false, false);
  x = fmaxf(__uint_as_float(s[0]), __uint_as_float(s[1]));
  auto t = __builtin_amdgcn_permlane32_swap(__float_as_uint(x), __float_as_uint(x), false, false);
  return fmaxf(__uint_as_float(t[0]), __uint_as_float(t[1]));
}
DI float xg_sum(float x) {
  auto s = __builtin_amdgcn_permlane16_swap(__float_as_uint(x), __float_as_uint(x), false, false);
  x = __uint_as_float(s[0]) + __uint_as_float(s[1]);
  auto t = __builtin_amdgcn_permlane32_swap(__float_as_uint(x), __float_as_uint(x), false, false);
  return __uint_as_float(t[0]) + __uint_as_float(t[1]);
}
DI f32x4 mfma16(u32x4 a, u32x4 b, f32x4 c) { return __builtin_amdgcn_mfma_f32_16x16x32_bf16(__builtin_bit_cast(bf16x8, a), __builtin_bit_cast(bf16x8, b), c, 0, 0, 0); }
DI u32x4 ld16(const u16* p) { return __builtin_nontemporal_load(reinterpret_cast<const u32x4*>(p)); }
DI u32x2 ld8(const u16* p) { return __builtin_nontemporal_load(reinterpret_cast<const u32x2*>(p)); }
DI u32x2 trread(const unsigned char* p) {
  return __builtin_bit_cast(u32x2, __builtin_amdgcn_ds_read_tr16_b64_v4i16((__attribute__((address_space(3))) s16x4*)(p)));
}
DI u32 prm(u32 hi, u32 lo, int odd) { return __builtin_amdgcn_perm(hi, lo, odd ? 0x07060302u : 0x05040100u); }

DI u32x4 tcol16(const u32x4 (&R)[8], int t) {
  u32x4 T;
#pragma unroll
  for (int q = 0; q < 4; ++q) T[q] = prm(R[2 * q + 1][t >> 1], R[2 * q][t >> 1], t & 1);
  return T;
}
DI u32x4 tcol8(const u32x2 (&R)[8], int t) {
  u32x4 T;
#pragma unroll
  for (int q = 0; q < 4; ++q) T[q] = prm(R[2 * q + 1][t >> 1], R[2 * q][t >> 1], t & 1);
  return T;
}

#define SPINLOG 22
#define PSL 2
constexpr u32 SPIN_CAP = 1u << SPINLOG;
DI void wave_wait(const u64* f, bool active, u64 tag) {
  for (u32 spins = 0; spins < SPIN_CAP; ++spins) {
    const u64 v = active ? __hip_atomic_load(f, RLX_AGENT) : tag;
    if (__all(v == tag)) break;
    __builtin_amdgcn_s_sleep(PSL);
  }
}
DI void wg_wait64(const unsigned char* flags, u64 tag, int pw = 0) {
  if ((int)(threadIdx.x >> 6) == pw) {
    wave_wait(reinterpret_cast<const u64*>(flags) + (threadIdx.x & (NBW - 1)), true, tag);
    asm volatile("s_waitcnt vmcnt(0)" ::: "memory");
  }
  __syncthreads();
}
DI void st16_sc1(__amdgpu_buffer_rsrc_t rs, size_t off, u32x4 v) { __builtin_amdgcn_raw_buffer_store_b128(v, rs, (int)off, 0, 16); }
DI void st8_sc1(unsigned char* p, f32x2 v) { __hip_atomic_store(reinterpret_cast<u64*>(p), __builtin_bit_cast(u64, v), RLX_AGENT); }

struct QPre { u32x4 R0[8]; u32x4 R1; u32x2 xa, xb; u32 x1; };
DI void qproj_load(QPre& Q, const u16* x, const u16* Wh, int w, int lane) {
  const int j = lane & 15, g = lane >> 4;
  Q.xa = u32x2{0u, 0u}; Q.xb = u32x2{0u, 0u}; Q.x1 = 0u;
  if (j < 4) { Q.xa = ld8(x + j * DM + 36 * w + 8 * g); Q.xb = ld8(x + j * DM + 36 * w + 8 * g + 4); Q.x1 = x[j * DM + 36 * w + 32 + g]; }
#pragma unroll
  for (int jj = 0; jj < 8; ++jj) Q.R0[jj] = ld16(Wh + (size_t)(36 * w + 8 * g + jj) * 4096 + 8 * j);
  Q.R1 = ld16(Wh + (size_t)(36 * w + 32 + g) * 4096 + 8 * j);
}
DI void qproj_mfma(const QPre& Q, unsigned char* lds, int w, int lane) {
  const int j = lane & 15, g = lane >> 4;
  float* qpart = reinterpret_cast<float*>(lds + L_BIG);
  const u32x4 a = {Q.xa[0], Q.xa[1], Q.xb[0], Q.xb[1]}, a1 = {Q.x1, 0u, 0u, 0u};
#pragma unroll
  for (int hf = 0; hf < 2; ++hf) {
    f32x4 acc[4];
#pragma unroll
    for (int t = 0; t < 4; ++t) acc[t] = mfma16(a, tcol16(Q.R0, 4 * hf + t), f32x4{0.f, 0.f, 0.f, 0.f});
#pragma unroll
    for (int t = 0; t < 4; ++t) { const u32x4 bb = {(t & 1) ? (Q.R1[2 * hf + (t >> 1)] >> 16) : (Q.R1[2 * hf + (t >> 1)] & 0xffffu), 0u, 0u, 0u}; acc[t] = mfma16(a1, bb, acc[t]); }
    if (g == 0) {
#pragma unroll
      for (int r = 0; r < 4; ++r) *reinterpret_cast<f32x4*>(qpart + (w * 4 + r) * 128 + 8 * j + 4 * hf) = f32x4{acc[0][r], acc[1][r], acc[2][r], acc[3][r]};
    }
    __builtin_amdgcn_sched_barrier(0);
  }
}
DI void wo_load(const u16* Wo, u32x2 (&R)[4][8], int nb, int lane, int s0, int s1) {
  const int j = lane & 15, g = lane >> 4;
#pragma unroll
  for (int s = 0; s < 4; ++s) if (s >= s0 && s < s1)
#pragma unroll
    for (int jj = 0; jj < 8; ++jj) R[s][jj] = ld8(Wo + (size_t)(32 * s + 8 * g + jj) * DM + 64 * nb + 4 * j);
}
DI void wo_mfma(const u32x2 (&R)[4][8], const unsigned char* lds, int lane, f32x4 (&acc)[4]) {
  const int j = lane & 15, g = lane >> 4;
#pragma unroll
  for (int t = 0; t < 4; ++t) acc[t] = f32x4{0.f, 0.f, 0.f, 0.f};
#pragma unroll
  for (int s = 0; s < 4; ++s) {
    const u32x4 a = *reinterpret_cast<const u32x4*>(lds + L_AM + j * 272 + (32 * s + 8 * g) * 2);
#pragma unroll
    for (int t = 0; t < 4; ++t) acc[t] = mfma16(a, tcol8(R[s], t), acc[t]);
  }
}
DI u32x4 yrow(const f32x4 (&acc)[4], u32 tg) {
  return u32x4{pk_bf16(acc[0][0] + acc[0][1], acc[1][0] + acc[1][1]), tg, pk_bf16(acc[2][0] + acc[2][1], acc[3][0] + acc[3][1]), tg};
}
DI void am_zero(unsigned char* lds, int tid) {
  if (tid < 136) { const int pr = tid / 34; *reinterpret_cast<u32x4*>(lds + L_AM + (4 * pr + 2) * 272 + (tid - 34 * pr) * 16) = u32x4{0u, 0u, 0u, 0u}; }
}
DI float rdlane(float x, int l) { return __builtin_bit_cast(float, __builtin_amdgcn_readlane(__builtin_bit_cast(int, x), l)); }

constexpr int NS = 10;
constexpr int NSEQ = 8 * NS;
#define ORDN 0
#define ORD "KWWKWWKWWKWWKWK"
constexpr char ord[] = ORD;
constexpr int ord_len() { int n = 0; while (ord[n]) ++n; return n; }
constexpr int ord_cnt(int n, char c) { int k = 0; for (int i = 0; i < n; ++i) k += ord[i] == c; return k; }
#define POST 3
#define DEP 2
constexpr int DEPTH = DEP, PRE = ord_cnt(ord_len(), 'K'), RING = PRE + POST, RAMP = 2;
static_assert(ord_cnt(ord_len(), 'W') == 9, "");
constexpr int issued(int i) {
  int d = RING - (i + RAMP - 1) / RAMP; if (d < DEPTH) d = DEPTH;
  int e = i + d; if (e < RING) e = RING; return e > NSEQ ? NSEQ : e;
}
#define PACE 1
#define YDEF 4400
#define WO0 20
#define WON 32
#define PAT 1
#define TA1 535
#define TA2 450
#define NK 68
#define ORG0 240
#define LEAD 5
#define BETA 10
#define SLMAX 80
#define MAXGAP 70
constexpr int cmin(int a, int b) { return a < b ? a : b; }
#define RSH 1
#define RD 4
constexpr int due_at(int n) { return (n <= NK ? n * TA1 : NK * TA1 + (n - NK) * TA2) / 10; }
#define RTAB 20, 30, 40, 50, 60, 70
#define GTAB 0, 12, 6, 12, 12, 12
#define BTAB 8, 12, 6, 12, 12, 0
#define SSYNC 1
#define MISS 40
#define DB 10
constexpr int rtab[] = {RTAB}, gtab[] = {GTAB}, btab[] = {BTAB};
constexpr int NRND = sizeof(rtab) / sizeof(int);
constexpr int rgap(int k) { return k ? rtab[k] - rtab[k - 1] : rtab[0] - RING; }
constexpr int rnd_at(int n) { for (int k = 0; k < NRND; ++k) if (rtab[k] == n) return k; return -1; }
static_assert(rtab[0] >= RING + 2 && rtab[NRND - 1] + RD + 2 < NSEQ, "");
constexpr int wo_step(int k) { return WO0 + k * WON / 32; }
#define PRIO 2
#define XW 13
#define XR 50
#define FDLY 8
#define FSL 2
#define STEPST 0
#define CPFM 1
#define STAG 16
DI u32 rt32() { return (u32)__builtin_amdgcn_s_memrealtime(); }
DI void part_a(const Args& a, unsigned char* lds, int idx, u64 tag) {
  const int tid = threadIdx.x, lane = tid & 63, w = __builtin_amdgcn_readfirstlane(tid >> 6);
  u32 T0 = rt32();
  const u32 toff = (u32)(w * TA2) / (STAG * 10);
  u32* lorg = reinterpret_cast<u32*>(lds + L_ST + 2048);
  if (tid < 32) lorg[tid] = tid == 0 ? ORG0 : tid == 2 ? T0 : 0u;
  __syncthreads();
  __builtin_amdgcn_s_setprio(PRIO);
  const u32 tg = (u32)(tag >> 32);
  u32 last = T0;
  u32x2 rv = {0u, 0u}; u32 rt = 0;
  const int j = lane & 15, g = lane >> 4;
  const int h = (idx & 7) * 4 + ((idx >> 3) & 3), ci = idx >> 5;
  const int U0 = 0, NUN = 1024;
  const int rot = h % NS;
  const int l0 = (ci * 16) / ACU, ln = ((ci + 1) * 16) / ACU - l0;
  const int bad_sl = (w < ln) ? -1 : NS - 1 - rot;
  const int sstr = 7 * 16384, s0 = ci * 16384 + w * 1024, s9 = (w < ln) ? (1008 + l0 + w) * 1024 : 1 << 20;
  const __amdgpu_buffer_rsrc_t rs = __builtin_amdgcn_make_buffer_rsrc(a.ws, 0, (int)WS_END, 0x00020000);
  const u16* Wq = a.wq + h * 128;
  QPre Q;
  ST(0);
  if (SSYNC) __builtin_amdgcn_raw_buffer_store_b64(u32x2{T0, (u32)tag + 15u}, rs, (ci == 3 && (h & 1) == 0 && tid == 0) ? (int)WS_RPT + (h >> 1) * 4352 : 0x7ffff000, 128 * 15 * 35, 16);
  Q.xa = u32x2{0u, 0u}; Q.xb = u32x2{0u, 0u}; Q.x1 = 0u;
  if (j < 4) { Q.xa = ld8(a.xA + j * DM + 36 * w + 8 * g); Q.xb = ld8(a.xA + j * DM + 36 * w + 8 * g + 4); Q.x1 = a.xA[j * DM + 36 * w + 32 + g]; }
  const size_t base = ((size_t)h * SK + 4 * U0) * 128;
  const int voff = lane * 16;
  u32x4 R[RING];
#define A_ISSUE(i) R[(i) % RING] = __builtin_amdgcn_raw_buffer_load_b128( \
    __builtin_amdgcn_make_buffer_rsrc(const_cast<u16*>((((i) >= 4 * NS) ? a.V : a.K) + base + (size_t)(((i) / NS) & 3) * H * SK * 128), 0, NUN * 1024, 0x00020000), \
    voff, ((i) % NS) + rot == NS - 1 ? s9 : (((i) % NS) + rot >= NS ? ((i) % NS) + rot - NS : ((i) % NS) + rot) * sstr + s0, 2)
#define GATE(n) do { if (PACE) { u32 due = T0 + toff + org + ((osl * cmin(n, NK)) >> 4) + (u32)due_at(n); if (MAXGAP && (int)(due - (last + MAXGAP)) > 0) due = last + MAXGAP; \
    while ((int)(now - due) < 0) { __builtin_amdgcn_s_sleep(1); now = rt32(); } last = now; } } while (0)
#define CTRL(n) do { if constexpr (PACE) { \
    if constexpr (rnd_at((n) - RD - 2) >= 0) { constexpr int k = rnd_at((n) - RD - 2); \
      const u32 dt = (rv[1] == (u32)tag + k) ? rv[0] - T0 : rt; \
      const float sm = row16_sum((float)(int)dt); \
      const int o = (int)((RSH ? rdlane(sm, 0) * (1.f / 16.f) : (rdlane(sm, 0) + rdlane(sm, 16)) * (1.f / 32.f))) - due_at(rtab[k]); \
      if (w == ((5 * k + 11) & 15) && lane == 0 && o > 0 && o < 2000) { \
        int e = o - (int)org - (int)((osl * cmin(rtab[k], NK)) >> 4) - ((k || SSYNC) ? DB : LEAD); if ((k || SSYNC) && e < 0) e = 0; \
        int sl_ = ((int)osl * gtab[k]) / 16 + (e * btab[k]) / rgap(k); sl_ = sl_ > SLMAX ? SLMAX : sl_; \
        if (STEPST) lorg[16 + k] = (u32)(e & 0xffff) | ((u32)sl_ << 16); \
        __hip_atomic_store(reinterpret_cast<u64*>(lorg), __builtin_bit_cast(u64, u32x2{(u32)((int)org + e + ((((int)osl - sl_) * cmin(n, NK)) >> 4)), (u32)sl_}), __ATOMIC_RELAXED, __HIP_MEMORY_SCOPE_WORKGROUP); } } \
    if constexpr (rnd_at(n) >= 0) { constexpr int k = rnd_at(n); \
      u32 tot = 0; \
      if (ci == k % ACU && (h & ((1 << RSH) - 1)) == (k & ((1 << RSH) - 1))) { const u32 mine = (1u << 24) + ((rt32() - toff - T0) & 0xfffffu); \
        if (lane == 0) tot = __hip_atomic_fetch_add(lorg + 8 + k, mine, __ATOMIC_RELAXED, __HIP_MEMORY_SCOPE_WORKGROUP) + mine; } \
      __builtin_amdgcn_raw_buffer_store_b64(u32x2{T0 + ((tot & 0xffffffu) >> 4), (u32)tag + k}, rs, (tot >> 24) == 16u ? (int)WS_RPT + (h >> RSH) * 4352 : 0x7ffff000, 128 * k * 35, 16); } \
    if constexpr (rnd_at((n) - RD) >= 0) { constexpr int k = rnd_at((n) - RD); \
      rv = __builtin_amdgcn_raw_buffer_load_b64(rs, (w == ((5 * k + 11) & 15) && lane < (32 >> RSH)) ? (int)WS_RPT + lane * 4352 : 0x7ffff000, 128 * k * 35, 16); rt = rt32() - T0 - MISS; } \
  } } while (0)
  static_for<ord_len()>([&](auto oc) __attribute__((always_inline)) {
    constexpr int o = oc.value, kk = ord_cnt(o, 'K'), wk = ord_cnt(o, 'W');
    if constexpr (ord[o] == 'K') A_ISSUE(kk);
    else if constexpr (wk < 8) Q.R0[wk] = ld16(Wq + (size_t)(36 * w + 8 * g + wk) * 4096 + 8 * j);
    else Q.R1 = ld16(Wq + (size_t)(36 * w + 32 + g) * 4096 + 8 * j);
    __builtin_amdgcn_sched_barrier(0);
    if (STEPST == 2) { const u32 t_ = rt32(); if (lane == 0) reinterpret_cast<u32*>(lds + 98304)[w * 32 + o] = t_; __builtin_amdgcn_sched_barrier(0); }
  });
  am_zero(lds, tid);
  qproj_mfma(Q, lds, w, lane);
  if (STEPST == 2) { const u32 t_ = rt32(); if (lane == 0) reinterpret_cast<u32*>(lds + 98304)[w * 32 + 30] = t_; }
  asm volatile("" ::: "memory");
  {
    u32 arr = 0;
    if (lane == 0) arr = __hip_atomic_fetch_add(lorg + 4, 1u, __ATOMIC_RELAXED, __HIP_MEMORY_SCOPE_WORKGROUP);
    if (__builtin_amdgcn_readfirstlane(arr) == 15u) {
      asm volatile("" ::: "memory");
      const float* qpart = reinterpret_cast<const float*>(lds + L_BIG) + 4 * lane;
      f32x4 s0 = {0.f, 0.f, 0.f, 0.f}, s1 = {0.f, 0.f, 0.f, 0.f};
#pragma unroll
      for (int c = 0; c < 4; ++c) {
#pragma unroll
        for (int ww = 4 * c; ww < 4 * c + 4; ++ww) { s0 += *reinterpret_cast<const f32x4*>(qpart + ww * 512); s1 += *reinterpret_cast<const f32x4*>(qpart + ww * 512 + 256); }
        __builtin_amdgcn_sched_barrier(0);
      }
      *reinterpret_cast<u32x2*>(lds + L_QS + 8 * lane) = u32x2{pk_bf16(s0[0], s0[1]), pk_bf16(s0[2], s0[3])};
      *reinterpret_cast<u32x2*>(lds + L_QS + 512 + 8 * lane) = u32x2{pk_bf16(s1[0], s1[1]), pk_bf16(s1[2], s1[3])};
      asm volatile("" ::: "memory");
      if (lane == 0) __hip_atomic_store(lorg + 5, 1u, __ATOMIC_RELAXED, __HIP_MEMORY_SCOPE_WORKGROUP);
    }
  }
  __builtin_amdgcn_s_setprio(0);
  if (SSYNC) rv = __builtin_amdgcn_raw_buffer_load_b64(rs, tid < 16 ? (int)WS_RPT + lane * 4352 : 0x7ffff000, 128 * 15 * 35, 16);
  __builtin_amdgcn_sched_barrier(0);
#pragma unroll
  for (int t = PRE; t < RING; ++t) A_ISSUE(t);
  __builtin_amdgcn_sched_barrier(0);
  if (SSYNC && w == 0) {
    const bool ok = rv[1] == (u32)tag + 15u;
    const float sm = row16_sum(ok ? (float)(int)(rv[0] - T0) : 0.f), cn = row16_sum(ok ? 1.f : 0.f);
    const int o = ORG0 + (cn > 0.f ? (int)(sm / cn) : 0);
    if (lane == 0 && o > 0 && o < 1000) lorg[0] = (u32)o;
  }
  while (__hip_atomic_load(lorg + 5, __ATOMIC_RELAXED, __HIP_MEMORY_SCOPE_WORKGROUP) == 0u) __builtin_amdgcn_s_sleep(1);
  asm volatile("" ::: "memory");
  T0 = __builtin_amdgcn_readfirstlane(lorg[2]);
  ST(1);
  float* pacc = reinterpret_cast<float*>(lds + L_BIG);
  float* wtab = reinterpret_cast<float*>(lds + L_ST + 1024);
  u32x2 xv = {0u, 0u};
  const int xoff = (w == XW && lane < 8 * ACU) ? (int)WS_MLA + (h * 8 + (lane >> 3)) * 128 + (lane & 7) * 8 : 0x7ffff000;
  float* pml = reinterpret_cast<float*>(lds + L_ST);
  u32x4 q;
  float acc[8], S[4];
  u32x2 RW[4][8];
  const __amdgpu_buffer_rsrc_t rwo = __builtin_amdgcn_make_buffer_rsrc(const_cast<u16*>(a.wo + (size_t)h * 128 * DM), 0, w < 9 ? 128 * DM * 2 : 0, 0x00020000);
  const int wvo = (8 * g * DM + 64 * w + 4 * j) * 2;
#define WO_ISSUE() static_for<32>([&](auto kc) __attribute__((always_inline)) { constexpr int k = kc.value; \
    if constexpr (wo_step(k) == i) RW[k >> 3][k & 7] = __builtin_amdgcn_raw_buffer_load_b64(rwo, wvo, (32 * (k >> 3) + (k & 7)) * DM * 2, 2); })
  static_for<NSEQ>([&](auto ic) __attribute__((always_inline)) {
    constexpr int i = ic.value, bb = (i / NS) & 3, pass = i / (4 * NS), sl = i % NS;
    u32 now = 0; if (PACE + STEPST > 0) now = rt32();
    u32 org = 0, osl = 0; if (PACE) { const u32x2 ov = __builtin_bit_cast(u32x2, __hip_atomic_load(reinterpret_cast<u64*>(lorg), __ATOMIC_RELAXED, __HIP_MEMORY_SCOPE_WORKGROUP)); org = __builtin_amdgcn_readfirstlane(ov[0]); osl = __builtin_amdgcn_readfirstlane(ov[1]); }
    if (STEPST && lane == 0) reinterpret_cast<u32*>(lds + 65536)[w * 96 + i] = now;
    __builtin_amdgcn_sched_barrier(0);
    const u32x4 r = R[i % RING];
    if constexpr (pass == 0) {
      if constexpr (sl == 0) { q = *reinterpret_cast<const u32x4*>(lds + L_QS + (bb * 128 + 8 * j) * 2); S[bb] = -INFINITY; }
      float d = dot2(r[0], q[0], 0.f); d = dot2(r[1], q[1], d); d = dot2(r[2], q[2], d); d = dot2(r[3], q[3], d);
      asm volatile("" : "+v"(d));
      if constexpr (issued(i + 1) > issued(i)) { GATE(issued(i)); WO_ISSUE(); A_ISSUE(issued(i)); CTRL(issued(i)); } else { WO_ISSUE(); }
      d = row16_sum(d);
      S[bb] = (j == sl) ? d : S[bb];
      if constexpr (sl == NS - 1) {
        const float sc = (j == bad_sl) ? -INFINITY : S[bb] * C2;
        const float m = xg_max(row16_max(sc));
        S[bb] = __builtin_amdgcn_exp2f(sc - m);
        const float l = xg_sum(row16_sum(S[bb]));
        if (lane == 0) *reinterpret_cast<f32x2*>(pml + (bb * 16 + w) * 2) = f32x2{m, l};
      }
      if constexpr (i == 4 * NS - 1) {
        asm volatile("" ::: "memory");
        u32 arr = 0;
        if (lane == 0) arr = __hip_atomic_fetch_add(lorg + 6, 1u, __ATOMIC_RELAXED, __HIP_MEMORY_SCOPE_WORKGROUP);
        arr = __builtin_amdgcn_readfirstlane(arr);
        asm volatile("" ::: "memory");
        const f32x2 ml = *reinterpret_cast<const f32x2*>(pml + lane * 2);
        const float M = row16_max(ml[0]);
        const float L = row16_sum(__builtin_amdgcn_exp2f(ml[0] - M) * ml[1]);
        __builtin_amdgcn_raw_buffer_store_b64(u32x2{__float_as_uint(j ? L : M), tg}, rs, (arr == 15u && j < 2) ? (int)WS_MLA + (h * 8 + ci) * 128 + g * 16 + j * 8 : 0x7ffff000, 0, 16);
      }
    } else {
      const float p = dpp<0x150 + sl>(S[bb]);
      if constexpr (sl == 0) {
#pragma unroll
        for (int k = 0; k < 8; ++k) acc[k] = 0.f;
      }
#pragma unroll
      for (int k = 0; k < 4; ++k) { acc[2 * k] = fmaf(p, bf_lo(r[k]), acc[2 * k]); acc[2 * k + 1] = fmaf(p, bf_hi(r[k]), acc[2 * k + 1]); }
      asm volatile("" : "+v"(acc[0]), "+v"(acc[1]), "+v"(acc[2]), "+v"(acc[3]), "+v"(acc[4]), "+v"(acc[5]), "+v"(acc[6]), "+v"(acc[7]));
      if constexpr (issued(i + 1) > issued(i)) { GATE(issued(i)); WO_ISSUE(); A_ISSUE(issued(i)); CTRL(issued(i)); } else { WO_ISSUE(); }
      if constexpr (i == XR) xv = __builtin_amdgcn_raw_buffer_load_b64(rs, xoff, 0, 16);
      if constexpr (i == XR + 2) {
        if (w == XW) {
          if (__builtin_expect(!__all(lane >= 8 * ACU || xv[1] == tg), 0)) {
#pragma unroll 1
            for (u32 spins = 0; spins < SPIN_CAP; ++spins) {
              __builtin_amdgcn_s_sleep(8); xv = __builtin_amdgcn_raw_buffer_load_b64(rs, xoff, 0, 16);
              if (__all(lane >= 8 * ACU || xv[1] == tg)) break;
            }
          }
          const float x = __uint_as_float(xv[0]);
          const float m = lane < 8 * ACU ? dpp<0xA0>(x) : -INFINITY, l = lane < 8 * ACU ? dpp<0xF5>(x) : 0.f;
          const float M = xg_max(fmaxf(m, dpp<0x128>(m)));
          float t = __builtin_amdgcn_exp2f(m - M) * l; t = xg_sum(t + dpp<0x128>(t));
          if (lane < 8 && !(lane & 1)) *reinterpret_cast<f32x2*>(wtab + 64 + lane) = f32x2{M, 1.f / t};
          asm volatile("" ::: "memory");
          const f32x2 gm = *reinterpret_cast<const f32x2*>(wtab + 64 + 2 * g);
          wtab[lane] = __builtin_amdgcn_exp2f(pml[lane * 2] - gm[0]) * gm[1];
        }
      }
      if constexpr (sl == NS - 1) {
#pragma unroll
        for (int k = 0; k < 8; ++k) acc[k] = xg_sum(acc[k]);
        if (g == 0) {
          float* pp = pacc + (bb * 16 + w) * 128 + 8 * j;
          *reinterpret_cast<f32x4*>(pp) = f32x4{acc[0], acc[1], acc[2], acc[3]};
          *reinterpret_cast<f32x4*>(pp + 4) = f32x4{acc[4], acc[5], acc[6], acc[7]};
        }
      }
    }
    __builtin_amdgcn_sched_barrier(0);
  });
#undef A_ISSUE
  if (STEPST) { if (lane == 0) { reinterpret_cast<u32*>(lds + 65536)[w * 96 + NSEQ] = rt32(); reinterpret_cast<u32*>(lds + 65536)[w * 96 + NSEQ + 1] = lorg[0]; for (int k = 0; k < 8; ++k) reinterpret_cast<u32*>(lds + 65536)[w * 96 + NSEQ + 2 + k] = lorg[16 + k]; }
    if (STEPST == 2 && lane < 32) reinterpret_cast<u32*>(a.ws + (22u << 20))[(blockIdx.x * 16 + w) * 32 + lane] = reinterpret_cast<u32*>(lds + 98304)[w * 32 + lane];
    for (int k = lane; k < 96; k += 64) reinterpret_cast<u32*>(a.ws + (20u << 20))[(blockIdx.x * 16 + w) * 96 + k] = reinterpret_cast<u32*>(lds + 65536)[w * 96 + k]; }
  ST(2);
  __syncthreads();
  ST(3);
  if (w < 8) {
    const int bb = w >> 1, d = tid & 127;
    u16* Am = reinterpret_cast<u16*>(lds + L_AM);
    const float wt = wtab[bb * 16 + j];
    float pv[16];
#pragma unroll
    for (int i = 0; i < 16; ++i) pv[i] = pacc[(bb * 16 + i) * 128 + d];
    float o0 = 0.f, o1 = 0.f;
#pragma unroll
    for (int i = 0; i < 16; i += 2) { o0 = fmaf(rdlane(wt, i), pv[i], o0); o1 = fmaf(rdlane(wt, i + 1), pv[i + 1], o1); }
    const float o = o0 + o1;
    const u16 hi = to_bf16(o); const u16 lo = to_bf16(o - bf2f(hi));
    Am[(4 * bb) * AM_LD + d] = hi; Am[(4 * bb + 1) * AM_LD + d] = lo;
  }
  __syncthreads();
  if (w < 9) {
    f32x4 ya[4];
    wo_mfma(RW, lds, lane, ya);
    st16_sc1(rs, WS_Y + ((8 * w + (j >> 1)) * 256 + idx) * 128 + g * 32 + (j & 1) * 16, yrow(ya, tg));
  }
  ST(4);
  if (idx < 72) {
    __builtin_amdgcn_sched_barrier(0);
    __builtin_amdgcn_s_barrier();
    __builtin_amdgcn_s_sleep(FDLY);
    const int yo = (int)WS_Y + (idx * 256 + 16 * w) * 128 + lane * 16;
    u32x4 v0, v1;
#pragma unroll 1
    for (u32 spins = 0; spins < SPIN_CAP; ++spins) {
      v0 = __builtin_amdgcn_raw_buffer_load_b128(rs, yo, 0, 16); v1 = __builtin_amdgcn_raw_buffer_load_b128(rs, yo + 1024, 0, 16);
      if (__all(v0[1] == tg && v0[3] == tg && v1[1] == tg && v1[3] == tg)) break;
      __builtin_amdgcn_s_sleep(FSL);
    }
    f32x4 y = {bf_lo(v0[0]) + bf_lo(v1[0]), bf_hi(v0[0]) + bf_hi(v1[0]), bf_lo(v0[2]) + bf_lo(v1[2]), bf_hi(v0[2]) + bf_hi(v1[2])};
#pragma unroll
    for (int k = 0; k < 4; ++k) y[k] = xg_sum(y[k] + dpp<0x128>(y[k]));
    float* red = reinterpret_cast<float*>(lds + L_PF);
    if (lane < 8) *reinterpret_cast<f32x4*>(red + w * 32 + lane * 4) = y;
    __syncthreads();
    if (tid < 64) {
      const int e = tid & 31;
      float r[14];
#pragma unroll
      for (int k = 0; k < 14; ++k) r[k] = red[(tid < 32 ? k : 14 + (k & 1)) * 32 + e];
      float sa = r[0] + r[1];
      float sb = ((r[2] + r[3]) + (r[4] + r[5])) + ((r[6] + r[7]) + (r[8] + r[9])) + ((r[10] + r[11]) + (r[12] + r[13]));
      a.out[(e >> 3) * 1152 + (tid >> 5) * 576 + idx * 8 + (e & 7)] = to_bf16(tid < 32 ? sa + sb : sa);
    }
  }
  ST(5);
}

#define WPD 4
template <bool LD> DI void part_b(const Args& a, unsigned char* lds, int wg, u64 tag) {
  const int tid = threadIdx.x, lane = tid & 63, w = __builtin_amdgcn_readfirstlane(tid >> 6);
  const int j = lane & 15, g = lane >> 4;
  const __amdgpu_buffer_rsrc_t rs = __builtin_amdgcn_make_buffer_rsrc(a.ws, 0, (int)WS_END, 0x00020000);
  const u32 tb0 = rt32();
  const int h = wg;
  const int h3 = wg ^ 2;
  const int b2 = wg >> 3, sp = wg & 7;
  const u16* cl = a.C + ((size_t)b2 * SK + sp * 512 + 8 * (w - 8)) * DC + lane * 8;
  u32x4 X[16];
  u32x2 RW[4][8];
  u32x4 W8[2];
  u32 tv = 0;
#define BST(n) do { if (STEPST && LD) { __builtin_amdgcn_sched_barrier(0); const u32 t_ = rt32(); tv = (lane == (n)) ? t_ : tv; __builtin_amdgcn_sched_barrier(0); } } while (0)
  u32* cnt = reinterpret_cast<u32*>(lds + L_ST + 4064);
#define C_ISSUE(T) _Pragma("unroll") for (int r = 0; r < 8; ++r) X[8 * ((T) & 1) + r] = ld16(cl + (size_t)(64 * (T) + r) * DC)
#define C_HALF(T, hf) _Pragma("unroll") for (int r = 4 * (hf); r < 4 * (hf) + 4; ++r) X[8 * ((T) & 1) + r] = ld16(cl + (size_t)(64 * (T) + r) * DC)
#define C_WIN(T, r0, r1) _Pragma("unroll") for (int r = r0; r < r1; ++r) { asm volatile("s_waitcnt vmcnt(%0)" :: "n"(WPD - 1) : "memory"); X[8 * ((T) & 1) + r] = ld16(cl + (size_t)(64 * (T) + r) * DC); __builtin_amdgcn_sched_barrier(0); }
  u32 sb0 = L_BIG + 8 * (w - 8) * CRS + lane * 16, sb1 = sb0 + CBUF;
  asm volatile("" : "+v"(sb0), "+v"(sb1));
#define C_STAGE(T) _Pragma("unroll") for (int r = 0; r < 8; ++r) *reinterpret_cast<u32x4*>(lds + (((T) & 1) ? sb1 : sb0) + r * CRS) = X[8 * ((T) & 1) + r]
  {
    const u16* Wq = a.wqB + h * 128;
    u32x4 U[8];
    QPre Q;
    if constexpr (!LD) { ST(0); }
    qproj_load(Q, a.xB, Wq, w, lane);
#pragma unroll
    for (int i = 0; i < 8; ++i) U[i] = ld16(a.wuk + ((size_t)(32 * w + 4 * i + (j & 3)) * H + h) * 128 + 8 * ((j & 12) + g));
    __builtin_amdgcn_sched_barrier(0);
    BST(0);
    if constexpr (!LD) am_zero(lds, tid);
    qproj_mfma(Q, lds, w, lane);
    BST(1);
    __syncthreads();
    BST(2);
    if constexpr (LD) { C_HALF(0, 0); }
    else {
      const float* qpart = reinterpret_cast<const float*>(lds + L_BIG);
      float s = 0.f;
#pragma unroll
      for (int ww = 0; ww < 16; ++ww) s += qpart[ww * 512 + tid];
      reinterpret_cast<u16*>(lds + L_QS)[tid] = to_bf16(s);
    }
    __builtin_amdgcn_sched_barrier(0);
    __syncthreads();
    BST(3);
    const u32x4 af = *reinterpret_cast<const u32x4*>(lds + L_QS + ((j >> 2) * 128 + ((j & 3) * 4 + g) * 8) * 2);
    u16* qa = reinterpret_cast<u16*>(lds + L_PF);
    float v2[2] = {0.f, 0.f};
#pragma unroll
    for (int i = 0; i < 8; ++i) {
      const f32x4 d4 = mfma16(af, U[i], f32x4{0.f, 0.f, 0.f, 0.f});
      float d = (j & 8) ? ((j & 4) ? d4[3] : d4[2]) : ((j & 4) ? d4[1] : d4[0]);
      d += dpp<0x124>(d); d += dpp<0x128>(d);
      v2[i >> 2] = ((j >> 2) == (i & 3)) ? d : v2[i >> 2];
    }
    qa[g * 512 + 32 * w + j] = to_bf16(v2[0]); qa[g * 512 + 32 * w + 16 + j] = to_bf16(v2[1]);
    __builtin_amdgcn_sched_barrier(0);
    BST(4);
    __syncthreads();
    if constexpr (!LD) {
      if (w == 0) {
#pragma unroll
        for (int bb = 0; bb < 4; ++bb) st16_sc1(rs, WS_QABS + ((bb * H + h) * 512 + lane * 8) * 2, *reinterpret_cast<const u32x4*>(qa + bb * 512 + lane * 8));
      }
      __builtin_amdgcn_sched_barrier(0);
      __builtin_amdgcn_s_barrier();
      if (w == 0) {
        asm volatile("s_waitcnt vmcnt(0)" ::: "memory");
        if (lane == 0) { __hip_atomic_store(reinterpret_cast<u64*>(a.ws + WS_F1 + wg * 8), tag, RLX_AGENT); cnt[0] = 0u; cnt[1] = 0u; }
      }
    } else {
      __builtin_amdgcn_s_barrier();
      BST(5);
      C_WIN(0, 4, 8); C_WIN(1, 0, 8);
      BST(6);
      C_STAGE(0);
      __builtin_amdgcn_sched_barrier(0);
      BST(7);
      C_WIN(2, 0, 8);
      BST(8);
    }
    if constexpr (!LD) { ST(1); }
  }
  {
    const int b = b2;
    if constexpr (!LD) wg_wait64(a.ws + WS_F1, tag); else __syncthreads();
    if constexpr (!LD) { ST(2); }
    const int kt = w & 3, hh = (w >> 2) & 1;
    float* stmax = reinterpret_cast<float*>(lds + L_ST);
    float* stsum = stmax + 128;
    float* salpha = stmax + 256;
    unsigned char* Pf = lds + L_PF;
    float M = -INFINITY, lsum = 0.f;
    f32x4 acc[2][2];
#pragma unroll
    for (int ct = 0; ct < 2; ++ct) { acc[ct][0] = f32x4{0.f, 0.f, 0.f, 0.f}; acc[ct][1] = f32x4{0.f, 0.f, 0.f, 0.f}; }
    const int troff = (4 * g + (j >> 2)) * CRS + (32 * w + 4 * (j & 3)) * 2;
    auto pv_step = [&](const unsigned char* cb) __attribute__((always_inline)) {
      const f32x4 al0 = *reinterpret_cast<const f32x4*>(salpha + 4 * g), al1 = *reinterpret_cast<const f32x4*>(salpha + 16 + 4 * g);
#pragma unroll
      for (int ct = 0; ct < 2; ++ct) { acc[ct][0] *= al0; acc[ct][1] *= al1; }
#pragma unroll
      for (int blk = 0; blk < 2; ++blk) {
        const u32x4 p0 = *reinterpret_cast<const u32x4*>(Pf + ((blk * 2 + 0) * 64 + lane) * 16);
        const u32x4 p1 = *reinterpret_cast<const u32x4*>(Pf + ((blk * 2 + 1) * 64 + lane) * 16);
#pragma unroll
        for (int ct = 0; ct < 2; ++ct) {
          const u32x2 lo = trread(cb + troff + (32 * blk) * CRS + 32 * ct);
          const u32x2 hi = trread(cb + troff + (32 * blk + 16) * CRS + 32 * ct);
          const u32x4 bf = {lo[0], lo[1], hi[0], hi[1]};
          acc[ct][0] = mfma16(p0, bf, acc[ct][0]); acc[ct][1] = mfma16(p1, bf, acc[ct][1]);
        }
      }
    };
    unsigned char* Qf = lds + L_BIG + CBUF;
    if constexpr (!LD) {
      u32x4 qv[4];
#pragma unroll
      for (int k = 0; k < 4; ++k) qv[k] = __builtin_amdgcn_raw_buffer_load_b128(rs, (int)WS_QABS + (b * H * 512 + (tid + 512 * k) * 8) * 2, 0, 16);
#pragma unroll
      for (int k = 0; k < 4; ++k) { const int e = tid + 512 * k, head = e >> 6, ch = e & 63;
        *reinterpret_cast<u32x4*>(Qf + ((head >> 4) * 16 + (ch >> 2)) * 1040 + ((ch & 3) * 16 + (head & 15)) * 16) = qv[k]; }
    }
    __syncthreads();
    if constexpr (!LD) {
      u32x4 QF[16];
#pragma unroll
      for (int s = 0; s < 16; ++s) QF[s] = *reinterpret_cast<const u32x4*>(Qf + (hh * 16 + s) * 1040 + lane * 16);
      auto s_tile = [&](const unsigned char* cb) __attribute__((always_inline)) {
        __syncthreads();
        f32x4 s0 = {0.f, 0.f, 0.f, 0.f}, s1 = {0.f, 0.f, 0.f, 0.f};
#pragma unroll
        for (int s = 0; s < 16; s += 2) {
          s0 = mfma16(*reinterpret_cast<const u32x4*>(cb + (16 * kt + j) * CRS + (32 * s + 8 * g) * 2), QF[s], s0);
          s1 = mfma16(*reinterpret_cast<const u32x4*>(cb + (16 * kt + j) * CRS + (32 * s + 32 + 8 * g) * 2), QF[s + 1], s1);
        }
        const f32x4 sv = (s0 + s1) * C2;
        const float v = xg_max(fmaxf(fmaxf(sv[0], sv[1]), fmaxf(sv[2], sv[3])));
        if (g == 0) stmax[kt * 32 + 16 * hh + j] = v;
        __syncthreads();
        const float* sm = stmax + 16 * hh + j;
        const float Mn = fmaxf(fmaxf(M, fmaxf(sm[0], sm[32])), fmaxf(sm[64], sm[96]));
        const float alpha = __builtin_amdgcn_exp2f(M - Mn);
        M = Mn;
        u32x2 pk;
        pk[0] = pk_bf16(__builtin_amdgcn_exp2f(sv[0] - Mn), __builtin_amdgcn_exp2f(sv[1] - Mn));
        pk[1] = pk_bf16(__builtin_amdgcn_exp2f(sv[2] - Mn), __builtin_amdgcn_exp2f(sv[3] - Mn));
        lsum = lsum * alpha + ((bf_lo(pk[0]) + bf_hi(pk[0])) + (bf_lo(pk[1]) + bf_hi(pk[1])));
        *reinterpret_cast<u32x2*>(Pf + (((kt >> 1) * 2 + hh) * 64 + lane) * 16 + (kt & 1) * 8) = pk;
        if (kt == 0 && g == 0) salpha[16 * hh + j] = alpha;
        __syncthreads();
        pv_step(cb);
      };
#pragma unroll 1
      for (int T = 0; T < 8; T += 2) { s_tile(lds + L_BIG); s_tile(lds + L_BIG + CBUF); }
    } else {
#pragma unroll
      for (int T = 0; T < 8; ++T) {
        __syncthreads();
        BST(10 + 4 * T);
        if (T + 1 < 8) { C_STAGE(T + 1); }
        BST(11 + 4 * T);
        if (T + 3 < 8) { C_HALF(T + 3, 0); }
        if (T == 5 || T == 6) wo_load(a.woB + (size_t)h3 * 128 * DM, RW, w - 8, lane, 2 * (T - 5), 2 * (T - 5) + 1);
        if (T == 7) {
#pragma unroll
          for (int k = 0; k < 2; ++k) W8[k] = ld16(a.woB + ((size_t)h3 * 128 + 16 * (w - 8) + 8 * k + (lane >> 3)) * DM + 512 + (lane & 7) * 8);
        }
        BST(12 + 4 * T);
        __syncthreads();
        __syncthreads();
        BST(13 + 4 * T);
        if (T + 3 < 8) { C_HALF(T + 3, 1); }
        if (T == 5 || T == 6) wo_load(a.woB + (size_t)h3 * 128 * DM, RW, w - 8, lane, 2 * (T - 5) + 1, 2 * (T - 5) + 2);
        __builtin_amdgcn_sched_barrier(0);
        pv_step(lds + L_BIG + (T & 1) * CBUF);
      }
      BST(42);
      if (STEPST) reinterpret_cast<u32*>(a.ws + (20u << 20))[(blockIdx.x * 16 + w) * 96 + lane] = tv;
    }
#undef C_ISSUE
#undef C_STAGE
    if constexpr (!LD) { ST(3); }
    __syncthreads();
    if constexpr (!LD) { lsum = xg_sum(lsum); if (g == 0) stsum[kt * 32 + 16 * hh + j] = lsum; if (kt == 0 && g == 0) salpha[32 + 16 * hh + j] = M; }
    float* tb = reinterpret_cast<float*>(lds + L_BIG);
#pragma unroll
    for (int ct = 0; ct < 2; ++ct)
#pragma unroll
      for (int h2 = 0; h2 < 2; ++h2)
#pragma unroll
        for (int r = 0; r < 4; ++r) tb[(16 * h2 + 4 * g + r) * 516 + 32 * w + 16 * ct + j] = acc[ct][h2][r];
    if constexpr (!LD) { for (int i = tid; i < 8 * AM2_LD * 2 / 16; i += 512) reinterpret_cast<u32x4*>(lds + L_AM2 + 8 * AM2_LD * 2)[i] = u32x4{0u, 0u, 0u, 0u}; }
    __syncthreads();
    if constexpr (!LD) { ST(4); }
    if constexpr (LD) {
      const int t2 = tid - 512;
#pragma unroll
      for (int k = 0; k < 4; ++k) {
        const int e = t2 + 512 * k, head = e >> 6, c8 = e & 63;
        const f32x4 v0 = *reinterpret_cast<const f32x4*>(tb + head * 516 + 8 * c8), v1 = *reinterpret_cast<const f32x4*>(tb + head * 516 + 8 * c8 + 4);
        __builtin_amdgcn_raw_buffer_store_b128(u32x4{pk_bf16(v0[0], v0[1]), pk_bf16(v0[2], v0[3]), pk_bf16(v1[0], v1[1]), pk_bf16(v1[2], v1[3])}, rs, (int)WS_CTXP + (((b * NSP + sp) * 32 + head) * 512 + 8 * c8) * 2, 0, 16);
      }
      if (t2 < 32) {
        const float L = (stsum[t2] + stsum[32 + t2]) + (stsum[64 + t2] + stsum[96 + t2]);
        st8_sc1(a.ws + WS_MLB + ((size_t)(b * NSP + sp) * 32 + t2) * 8, f32x2{salpha[32 + t2], L});
      }
      __builtin_amdgcn_sched_barrier(0);
      __builtin_amdgcn_s_barrier();
      asm volatile("s_waitcnt vmcnt(0)" ::: "memory");
      if (lane == 0) {
        if (__hip_atomic_fetch_add(cnt, 1u, __ATOMIC_RELAXED, __HIP_MEMORY_SCOPE_WORKGROUP) == 7u) {
          __hip_atomic_store(reinterpret_cast<u64*>(a.ws + WS_F2 + wg * 8), tag, RLX_AGENT);
        }
      }
#pragma unroll
      for (int k = 0; k < 2; ++k) *reinterpret_cast<u32x4*>(lds + L_W8 + (16 * (w - 8) + 8 * k + (lane >> 3)) * 144 + (lane & 7) * 16) = W8[k];
    } else {
      __builtin_amdgcn_s_barrier();
      static_for<16>([&](auto ic) __attribute__((always_inline)) {
        constexpr int k2 = ic.value >> 3, jj = ic.value & 7;
        if constexpr (ic.value >= WPD) asm volatile("s_waitcnt vmcnt(%0)" :: "n"(WPD - 1) : "memory");
        const u32x4 v = ld16(a.wuv + ((size_t)(64 * w + 32 * k2 + 8 * g + jj) * H + h3) * 128 + 8 * j);
        RW[2 * k2][jj] = u32x2{v[0], v[1]}; RW[2 * k2 + 1][jj] = u32x2{v[2], v[3]};
        __builtin_amdgcn_sched_barrier(0);
      });
    }
  }
  {
    int tid = threadIdx.x; asm volatile("" : "+v"(tid));
    const int lane = tid & 63, j = lane & 15, g = lane >> 4;
    u16* Am2 = reinterpret_cast<u16*>(lds + L_AM2);
    float* cpart = reinterpret_cast<float*>(lds + L_CP);
    __builtin_amdgcn_sched_barrier(0);
    if constexpr (LD) {
      if (w == 8) {
        wave_wait(reinterpret_cast<const u64*>(a.ws + WS_F2) + (lane & (NBW - 1)), true, tag);
        asm volatile("s_waitcnt vmcnt(0)" ::: "memory");
        if (lane == 0) __hip_atomic_store(cnt + 1, 1u, __ATOMIC_RELEASE, __HIP_MEMORY_SCOPE_WORKGROUP);
      } else {
        for (u32 spins = 0; spins < 64u * SPIN_CAP; ++spins) { if (__hip_atomic_load(cnt + 1, __ATOMIC_ACQUIRE, __HIP_MEMORY_SCOPE_WORKGROUP) != 0u) break; __builtin_amdgcn_s_sleep(1); }
      }
      __builtin_amdgcn_sched_barrier(0);
      const int b = (tid >> 7) & 3, c4 = tid & 127;
      u32x2 pv[NSP];
      const f32x2 ml = __builtin_bit_cast(f32x2, __builtin_amdgcn_raw_buffer_load_b64(rs, (int)WS_MLB + ((b * NSP + (j & 7)) * 32 + h3) * 8, 0, 16));
#pragma unroll
      for (int s = 0; s < NSP; ++s) pv[s] = __builtin_amdgcn_raw_buffer_load_b64(rs, (int)WS_CTXP + (((b * NSP + s) * 32 + h3) * 512 + 4 * c4) * 2, 0, 16);
      const float M = row16_max(ml[0]);
      float wt = __builtin_amdgcn_exp2f(ml[0] - M);
      wt *= 2.f / row16_sum(wt * ml[1]);
      float c0 = 0.f, c1 = 0.f, c2 = 0.f, c3 = 0.f;
#pragma unroll
      for (int s = 0; s < NSP; ++s) { const float ws_ = rdlane(wt, s); c0 = fmaf(ws_, bf_lo(pv[s][0]), c0); c1 = fmaf(ws_, bf_hi(pv[s][0]), c1); c2 = fmaf(ws_, bf_lo(pv[s][1]), c2); c3 = fmaf(ws_, bf_hi(pv[s][1]), c3); }
      const u32 h0 = pk_bf16(c0, c1), h1 = pk_bf16(c2, c3);
      *reinterpret_cast<u32x2*>(Am2 + (2 * b) * AM2_LD + 4 * c4) = u32x2{h0, h1};
      *reinterpret_cast<u32x2*>(Am2 + (2 * b + 1) * AM2_LD + 4 * c4) = u32x2{pk_bf16(c0 - bf_lo(h0), c1 - bf_hi(h0)), pk_bf16(c2 - bf_lo(h1), c3 - bf_hi(h1))};
      __syncthreads();
      __syncthreads();
      const int t2 = tid - 512;
      float s = 0.f;
#pragma unroll
      for (int ww = 0; ww < 8; ++ww) s += cpart[ww * 512 + t2];
      u16* Am = reinterpret_cast<u16*>(lds + L_AM);
      const int bb = t2 >> 7, d = t2 & 127;
      const u16 hi = to_bf16(s);
      Am[(4 * bb) * AM_LD + d] = hi; Am[(4 * bb + 1) * AM_LD + d] = to_bf16(s - bf2f(hi));
    } else {
      __syncthreads();
      f32x4 acc[8];
#pragma unroll
      for (int t = 0; t < 8; ++t) acc[t] = f32x4{0.f, 0.f, 0.f, 0.f};
#pragma unroll
      for (int k2 = 0; k2 < 2; ++k2) {
        const u32x4 af = *reinterpret_cast<const u32x4*>(Am2 + j * AM2_LD + 64 * w + 32 * k2 + 8 * g);
#pragma unroll
        for (int t = 0; t < 8; ++t) acc[t] = mfma16(af, tcol8(RW[2 * k2 + (t >> 2)], t & 3), acc[t]);
      }
      if (g < 2) {
#pragma unroll
        for (int bb = 0; bb < 2; ++bb) {
          float* p = cpart + (w * 4 + 2 * g + bb) * 128 + 8 * j;
          *reinterpret_cast<f32x4*>(p) = f32x4{acc[0][2 * bb] + acc[0][2 * bb + 1], acc[1][2 * bb] + acc[1][2 * bb + 1], acc[2][2 * bb] + acc[2][2 * bb + 1], acc[3][2 * bb] + acc[3][2 * bb + 1]};
          *reinterpret_cast<f32x4*>(p + 4) = f32x4{acc[4][2 * bb] + acc[4][2 * bb + 1], acc[5][2 * bb] + acc[5][2 * bb + 1], acc[6][2 * bb] + acc[6][2 * bb + 1], acc[7][2 * bb] + acc[7][2 * bb + 1]};
        }
      }
      __builtin_amdgcn_sched_barrier(0);
      __syncthreads();
      if (w == 0) {
#pragma unroll
        for (int s = 0; s < 4; ++s)
#pragma unroll
          for (int jj = 0; jj < 8; ++jj) RW[s][jj] = *reinterpret_cast<const u32x2*>(lds + L_W8 + (32 * s + 8 * g + jj) * 144 + 8 * j);
      }
    }
    __syncthreads();
    if (LD || w == 0) {
      f32x4 ya[4];
      wo_mfma(RW, lds, lane, ya);
      if (YDEF) { while ((int)(rt32() - (tb0 + YDEF)) < 0) __builtin_amdgcn_s_sleep(4); }
      st16_sc1(rs, WS_Y + ((8 * (LD ? w - 8 : 8) + (j >> 1)) * 256 + NAW + h3) * 128 + g * 32 + (j & 1) * 16, yrow(ya, (u32)(tag >> 32)));
    }
    if constexpr (!LD) { ST(7); }
  }
}

__global__ void __launch_bounds__(1024, 4) k_main(Args a) {
  __shared__ __attribute__((aligned(16))) unsigned char lds[L_TOTAL];
  u32 pf;
  { u64 pc; u32 len;
    asm volatile("s_getpc_b64 %0\n.Lpf%=:\n\ts_mov_b32 %1, k_tail_mark-.Lpf%=" : "=s"(pc), "=s"(len));
    pf = __builtin_amdgcn_raw_buffer_load_b32(__builtin_amdgcn_make_buffer_rsrc(reinterpret_cast<void*>(pc), 0, len < 131072u ? len : 0u, 0x00020000), CPFM ? (threadIdx.x < 16 ? ((blockIdx.x >> 3) * 16 + threadIdx.x) * 128 : 0x7ffff000) : threadIdx.x * 128, 0, 0);
    __builtin_amdgcn_sched_barrier(0); }
  u64 tag = (hw_dispatch_id() + 1ull) * 0x9E3779B97F4A7C15ull;
  tag ^= ((u64)__builtin_amdgcn_dispatch_ptr() >> 6) * 0xC2B2AE3D27D4EB4Full;
  tag |= 1ull << 63;
  if (__builtin_expect(blockIdx.x >= NBW, 1)) part_a(a, lds, blockIdx.x - NBW, tag);
  else if (__builtin_amdgcn_readfirstlane(threadIdx.x >> 6) < 8) part_b<false>(a, lds, blockIdx.x, tag);
  else part_b<true>(a, lds, blockIdx.x, tag);
  asm volatile("" :: "v"(pf));
}

extern "C" __global__ void k_tail_mark() {}

static void* g_ws;
extern "C" void kernel_launch(void* const* d_in, const int* in_sizes, int n_in, void* d_out, int out_size, void* d_ws, size_t ws_size,
                              hipStream_t stream) {
  Args a;
  a.xA = (const u16*)d_in[0]; a.K = (const u16*)d_in[1]; a.V = (const u16*)d_in[2]; a.wq = (const u16*)d_in[3]; a.wo = (const u16*)d_in[4];
  a.xB = (const u16*)d_in[5]; a.C = (const u16*)d_in[6]; a.wqB = (const u16*)d_in[7]; a.wuk = (const u16*)d_in[8]; a.wuv = (const u16*)d_in[9]; a.woB = (const u16*)d_in[10];
#define WSTOP 32
  if (WSTOP && ws_size >= ((size_t)WSTOP << 20)) d_ws = (unsigned char*)d_ws + ((ws_size - ((size_t)WSTOP << 20)) & ~(size_t)4095);
  g_ws = d_ws;
  a.ws = (unsigned char*)d_ws;
  a.out = (u16*)d_out;
  k_main<<<NBW + NAW, 1024, 0, stream>>>(a);
}
```

```cpp
#include <hip/hip_runtime.h>
#include <stdint.h>
#include <utility>

typedef unsigned short u16;
typedef unsigned u32;
typedef unsigned long long u64;
typedef u32 u32x4 __attribute__((ext_vector_type(4)));
typedef u32 u32x2 __attribute__((ext_vector_type(2)));
typedef float f32x4 __attribute__((ext_vector_type(4)));
typedef float f32x2 __attribute__((ext_vector_type(2)));
typedef short s16x4 __attribute__((ext_vector_type(4)));
typedef __bf16 bf16x8 __attribute__((ext_vector_type(8)));
typedef __bf16 bf16x2 __attribute__((ext_vector_type(2)));
#define DI __device__ __forceinline__
#define RLX_AGENT __ATOMIC_RELAXED, __HIP_MEMORY_SCOPE_AGENT
extern "C" __device__ u64 hw_dispatch_id() __asm("llvm.amdgcn.dispatch.id");

template <int... Is, class F> DI void static_for_impl(std::integer_sequence<int, Is...>, F&& f) { (f(std::integral_constant<int, Is>{}), ...); }
template <int N, class F> DI void static_for(F&& f) { static_for_impl(std::make_integer_sequence<int, N>{}, f); }

constexpr int H = 32, SK = 4096, DM = 576, DC = 512;
constexpr int ACU = 7;
constexpr int NBW = 32;
constexpr int NAW = H * ACU;
constexpr int NSP = 8;
constexpr float C2 = 0.08838834764831845f * 1.4426950408889634f;

constexpr size_t WS_F1 = 0, WS_F2 = 512;
constexpr size_t WS_QABS = 4096;
constexpr size_t WS_CTXP = WS_QABS + 4 * 32 * 512 * 2;
constexpr size_t WS_MLB = WS_CTXP + (size_t)4 * NSP * 32 * 512 * 2;
constexpr size_t WS_Y = WS_MLB + 4 * NSP * 32 * 2 * 4;
constexpr size_t WS_MLA = WS_Y + (size_t)72 * 256 * 128;
constexpr size_t WS_RPT = WS_MLA + 256 * 128;
constexpr size_t WS_END = WS_RPT + 64 * 4480;

constexpr int CRS = 1056;
constexpr int CBUF = 64 * CRS;
constexpr int L_BIG = 0;
constexpr int L_AM = 2 * CBUF;
constexpr int L_QS = L_AM + 16 * 272;
constexpr int L_ST = L_QS + 1024;
constexpr int L_PF = L_ST + 4096;
constexpr int L_TOTAL = L_PF + 4096;
constexpr int AM_LD = 136;
constexpr int AM2_LD = 520;
constexpr int L_AM2 = 69632, L_CP = 88064, L_W8 = 106496;

struct Args {
  const u16 *xA, *K, *V, *wq, *wo, *xB, *C, *wqB, *wuk, *wuv, *woB;
  unsigned char* ws;
  u16* out;
};

DI float bf_lo(u32 x) { return __uint_as_float(x << 16); }
DI float bf_hi(u32 x) { return __uint_as_float(x & 0xffff0000u); }
DI u32 pk_bf16(float lo, float hi) { bf16x2 v = {(__bf16)lo, (__bf16)hi}; return __builtin_bit_cast(u32, v); }
DI u16 to_bf16(float x) { return __builtin_bit_cast(u16, (__bf16)x); }
DI float bf2f(u16 x) { return __uint_as_float(((u32)x) << 16); }
DI float dot2(u32 a, u32 b, float c) { return __builtin_amdgcn_fdot2_f32_bf16(__builtin_bit_cast(bf16x2, a), __builtin_bit_cast(bf16x2, b), c, false); }
template <int CTRL> DI float dpp(float x) { return __builtin_bit_cast(float, __builtin_amdgcn_update_dpp(0, __builtin_bit_cast(int, x), CTRL, 0xf, 0xf, true)); }
DI float row16_sum(float x) { x += dpp<0x128>(x); x += dpp<0x124>(x); x += dpp<0x122>(x); x += dpp<0x121>(x); return x; }
DI float row16_max(float x) { x = fmaxf(x, dpp<0x128>(x)); x = fmaxf(x, dpp<0x124>(x)); x = fmaxf(x, dpp<0x122>(x)); x = fmaxf(x, dpp<0x121>(x)); return x; }
DI float xg_max(float x) {
  auto s = __builtin_amdgcn_permlane16_swap(__float_as_uint(x), __float_as_uint(x), false, false);
  x = fmaxf(__uint_as_float(s[0]), __uint_as_float(s[1]));
  auto t = __builtin_amdgcn_permlane32_swap(__float_as_uint(x), __float_as_uint(x), false, false);
  return fmaxf(__uint_as_float(t[0]), __uint_as_float(t[1]));
}
DI float xg_sum(float x) {
  auto s = __builtin_amdgcn_permlane16_swap(__float_as_uint(x), __float_as_uint(x), false, false);
  x = __uint_as_float(s[0]) + __uint_as_float(s[1]);
  auto t = __builtin_amdgcn_permlane32_swap(__float_as_uint(x), __float_as_uint(x), false, false);
  return __uint_as_float(t[0]) + __uint_as_float(t[1]);
}
DI float sw32(float a, float b) {
  auto s = __builtin_amdgcn_permlane32_swap(__float_as_uint(a), __float_as_uint(b), false, false);
  return __uint_as_float(s[0]) + __uint_as_float(s[1]);
}
DI float sw16(float a, float b) {
  auto s = __builtin_amdgcn_permlane16_swap(__float_as_uint(a), __float_as_uint(b), false, false);
  return __uint_as_float(s[0]) + __uint_as_float(s[1]);
}
DI f32x4 mfma16(u32x4 a, u32x4 b, f32x4 c) { return __builtin_amdgcn_mfma_f32_16x16x32_bf16(__builtin_bit_cast(bf16x8, a), __builtin_bit_cast(bf16x8, b), c, 0, 0, 0); }
DI u32x4 ld16(const u16* p) { return __builtin_nontemporal_load(reinterpret_cast<const u32x4*>(p)); }
DI u32x2 ld8(const u16* p) { return __builtin_nontemporal_load(reinterpret_cast<const u32x2*>(p)); }
DI u32x2 trread(const unsigned char* p) {
  return __builtin_bit_cast(u32x2, __builtin_amdgcn_ds_read_tr16_b64_v4i16((__attribute__((address_space(3))) s16x4*)(p)));
}
DI u32 prm(u32 hi, u32 lo, int odd) { return __builtin_amdgcn_perm(hi, lo, odd ? 0x07060302u : 0x05040100u); }

DI u32x4 tcol16(const u32x4 (&R)[8], int t) {
  u32x4 T;
#pragma unroll
  for (int q = 0; q < 4; ++q) T[q] = prm(R[2 * q + 1][t >> 1], R[2 * q][t >> 1], t & 1);
  return T;
}
DI u32x4 tcol8(const u32x2 (&R)[8], int t) {
  u32x4 T;
#pragma unroll
  for (int q = 0; q < 4; ++q) T[q] = prm(R[2 * q + 1][t >> 1], R[2 * q][t >> 1], t & 1);
  return T;
}

#define SPINLOG 22
#define PSL 2
constexpr u32 SPIN_CAP = 1u << SPINLOG;
DI void wave_wait(const u64* f, bool active, u64 tag) {
  for (u32 spins = 0; spins < SPIN_CAP; ++spins) {
    const u64 v = active ? __hip_atomic_load(f, RLX_AGENT) : tag;
    if (__all(v == tag)) break;
    __builtin_amdgcn_s_sleep(PSL);
  }
}
DI void wg_wait64(const unsigned char* flags, u64 tag, int pw = 0) {
  if ((int)(threadIdx.x >> 6) == pw) {
    wave_wait(reinterpret_cast<const u64*>(flags) + (threadIdx.x & (NBW - 1)), true, tag);
    asm volatile("s_waitcnt vmcnt(0)" ::: "memory");
  }
  __syncthreads();
}
DI void st16_sc1(__amdgpu_buffer_rsrc_t rs, size_t off, u32x4 v) { __builtin_amdgcn_raw_buffer_store_b128(v, rs, (int)off, 0, 16); }
DI void st8_sc1(unsigned char* p, f32x2 v) { __hip_atomic_store(reinterpret_cast<u64*>(p), __builtin_bit_cast(u64, v), RLX_AGENT); }

struct QPre { u32x4 R0[8]; u32x4 R1; u32x2 xa, xb; u32 x1; };
DI void qproj_load(QPre& Q, const u16* x, const u16* Wh, int w, int lane) {
  const int j = lane & 15, g = lane >> 4;
  Q.xa = u32x2{0u, 0u}; Q.xb = u32x2{0u, 0u}; Q.x1 = 0u;
  if (j < 4) { Q.xa = ld8(x + j * DM + 36 * w + 8 * g); Q.xb = ld8(x + j * DM + 36 * w + 8 * g + 4); Q.x1 = x[j * DM + 36 * w + 32 + g]; }
#pragma unroll
  for (int jj = 0; jj < 8; ++jj) Q.R0[jj] = ld16(Wh + (size_t)(36 * w + 8 * g + jj) * 4096 + 8 * j);
  Q.R1 = ld16(Wh + (size_t)(36 * w + 32 + g) * 4096 + 8 * j);
}
DI void qproj_mfma(const QPre& Q, unsigned char* lds, int w, int lane) {
  const int j = lane & 15, g = lane >> 4;
  float* qpart = reinterpret_cast<float*>(lds + L_BIG);
  const u32x4 a = {Q.xa[0], Q.xa[1], Q.xb[0], Q.xb[1]}, a1 = {Q.x1, 0u, 0u, 0u};
#pragma unroll
  for (int hf = 0; hf < 2; ++hf) {
    f32x4 acc[4];
#pragma unroll
    for (int t = 0; t < 4; ++t) acc[t] = mfma16(a, tcol16(Q.R0, 4 * hf + t), f32x4{0.f, 0.f, 0.f, 0.f});
#pragma unroll
    for (int t = 0; t < 4; ++t) { const u32x4 bb = {(t & 1) ? (Q.R1[2 * hf + (t >> 1)] >> 16) : (Q.R1[2 * hf + (t >> 1)] & 0xffffu), 0u, 0u, 0u}; acc[t] = mfma16(a1, bb, acc[t]); }
    if (g == 0) {
#pragma unroll
      for (int r = 0; r < 4; ++r) *reinterpret_cast<f32x4*>(qpart + (w * 4 + r) * 128 + 8 * j + 4 * hf) = f32x4{acc[0][r], acc[1][r], acc[2][r], acc[3][r]};
    }
    __builtin_amdgcn_sched_barrier(0);
  }
}
DI void wo_load(const u16* Wo, u32x2 (&R)[4][8], int nb, int lane, int s0, int s1) {
  const int j = lane & 15, g = lane >> 4;
#pragma unroll
  for (int s = 0; s < 4; ++s) if (s >= s0 && s < s1)
#pragma unroll
    for (int jj = 0; jj < 8; ++jj) R[s][jj] = ld8(Wo + (size_t)(32 * s + 8 * g + jj) * DM + 64 * nb + 4 * j);
}
DI void wo_mfma(const u32x2 (&R)[4][8], const unsigned char* lds, int lane, f32x4 (&acc)[4]) {
  const int j = lane & 15, g = lane >> 4;
#pragma unroll
  for (int t = 0; t < 4; ++t) acc[t] = f32x4{0.f, 0.f, 0.f, 0.f};
#pragma unroll
  for (int s = 0; s < 4; ++s) {
    const u32x4 a = *reinterpret_cast<const u32x4*>(lds + L_AM + j * 272 + (32 * s + 8 * g) * 2);
#pragma unroll
    for (int t = 0; t < 4; ++t) acc[t] = mfma16(a, tcol8(R[s], t), acc[t]);
  }
}
DI u32x4 yrow(const f32x4 (&acc)[4], u32 tg) {
  return u32x4{pk_bf16(acc[0][0] + acc[0][1], acc[1][0] + acc[1][1]), tg, pk_bf16(acc[2][0] + acc[2][1], acc[3][0] + acc[3][1]), tg};
}
DI void am_zero(unsigned char* lds, int tid) {
  if (tid < 136) { const int pr = tid / 34; *reinterpret_cast<u32x4*>(lds + L_AM + (4 * pr + 2) * 272 + (tid - 34 * pr) * 16) = u32x4{0u, 0u, 0u, 0u}; }
}
DI float rdlane(float x, int l) { return __builtin_bit_cast(float, __builtin_amdgcn_readlane(__builtin_bit_cast(int, x), l)); }

constexpr int NS = 10;
constexpr int NSEQ = 8 * NS;
#define ORD "KWWKWWKWWKWWKWK"
constexpr char ord[] = ORD;
constexpr int ord_len() { int n = 0; while (ord[n]) ++n; return n; }
constexpr int ord_cnt(int n, char c) { int k = 0; for (int i = 0; i < n; ++i) k += ord[i] == c; return k; }
#define POST 3
#define D3FROM 66
#define D4FROM 99
#define ROTM 1
#define OAUX 0
#define DEP 2
constexpr int DEPTH = DEP, PRE = ord_cnt(ord_len(), 'K'), RING = PRE + POST, RAMP = 2;
static_assert(ord_cnt(ord_len(), 'W') == 9, "");
constexpr int issued(int i) {
  int d = RING - (i + RAMP - 1) / RAMP; if (d < DEPTH) d = DEPTH;
  if (i >= D3FROM && d < 3) d = 3;
  if (i >= D4FROM && d < 4) d = 4;
  int e = i + d; if (e < RING) e = RING; return e > NSEQ ? NSEQ : e;
}
#define PACE 1
#define YDEF 4400
#define WO0 20
#define WON 32
#define TA1 535
#define TA2 440
#define NK 68
#define ORG0 200
#define LEAD 5
#define SLMAX 80
#define MAXGAP 70
constexpr int cmin(int a, int b) { return a < b ? a : b; }
#define RSH 1
#define RD 4
constexpr int due_at(int n) { return (n <= NK ? n * TA1 : NK * TA1 + (n - NK) * TA2) / 10; }
#define RTAB 20, 30, 40, 50, 60, 70
#define GTAB 0, 12, 6, 12, 12, 12
#define BTAB 8, 12, 6, 12, 12, 0
#define SSYNC 1
#define MISS 40
#define DB 10
constexpr int rtab[] = {RTAB}, gtab[] = {GTAB}, btab[] = {BTAB};
constexpr int NRND = sizeof(rtab) / sizeof(int);
constexpr int rgap(int k) { return k ? rtab[k] - rtab[k - 1] : rtab[0] - RING; }
constexpr int rnd_at(int n) { for (int k = 0; k < NRND; ++k) if (rtab[k] == n) return k; return -1; }
static_assert(rtab[0] >= RING + 2 && rtab[NRND - 1] + RD + 2 < NSEQ, "");
constexpr int wo_step(int k) { return WO0 + k * WON / 32; }
#define WTS 54
#define PJ0 58
#define PJ1 63
#define PJ2 72
#define PRIO 2
#define XW 13
#define XR 50
#define FDLY 8
#define FSL 2
#define CPFM 1
#define STAG 16
DI u32 rt32() { return (u32)__builtin_amdgcn_s_memrealtime(); }
constexpr int OOB = 0x7ffff000;
constexpr int L_AB = 32768;
DI void part_a(const Args& a, unsigned char* lds, int idx, u64 tag, u32 pf) {
  const u64 T64 = __builtin_amdgcn_s_memrealtime();
  __builtin_amdgcn_sched_barrier(0);
  const int tid = threadIdx.x, lane = tid & 63, w = __builtin_amdgcn_readfirstlane(tid >> 6);
  const int j = lane & 15, g = lane >> 4;
  const int h = (idx & 7) * 4 + ((idx >> 3) & 3), ci = idx >> 5;
  const int rr = (((h & 3) * 5) >> 1) + (h >> 2), rot = ROTM ? (rr >= NS ? rr - NS : rr) : (h * 5) >> 4, l0 = (ci * 37) >> 4, ln = ((ci * 37 + 37) >> 4) - l0;
  const int bad_sl = (w < ln) ? -1 : NS - 1 - rot;
  int vtab;
  { const int p = lane + rot >= NS ? lane + rot - NS : lane + rot;
    vtab = p == NS - 1 ? (w < ln ? (h << 20) + (1008 + l0 + w) * 1024 : 1 << 30) : (h << 20) + (p * 7 + ci) * 16384 + w * 1024; }
  int voff = lane * 16, wqo = ((36 * w + 8 * g) * 4096 + h * 128 + 8 * j) * 2, wq1 = ((36 * w + 32 + g) * 4096 + h * 128 + 8 * j) * 2;
  int xo = j < 4 ? (j * DM + 36 * w + 8 * g) * 2 : OOB, x1o = j < 4 ? (j * DM + 36 * w + 32 + g) * 2 : OOB;
  int so0 = __builtin_amdgcn_readlane(vtab, 0);
  asm volatile("" : "+v"(vtab), "+v"(voff), "+v"(wqo), "+v"(wq1), "+v"(xo), "+v"(x1o), "+s"(so0));
  __builtin_amdgcn_sched_barrier(0);
  asm volatile("" :: "s"(a.xB), "s"(a.C), "s"(a.wqB), "s"(a.wuk), "s"(a.wuv), "s"(a.woB), "s"(T64));
  u32 T0 = (u32)T64;
  const u32 toff = (u32)(w * TA2) / (STAG * 10);
  u32* lorg = reinterpret_cast<u32*>(lds + L_ST + 2048);
  __builtin_amdgcn_s_setprio(PRIO);
  const u32 tg = (u32)(tag >> 32);
  u32 last = T0;
  u32x2 rv = {0u, 0u}; u32 rt = 0;
  const __amdgpu_buffer_rsrc_t rs = __builtin_amdgcn_make_buffer_rsrc(a.ws, 0, (int)WS_END, 0x00020000);
  const __amdgpu_buffer_rsrc_t rk = __builtin_amdgcn_make_buffer_rsrc(const_cast<u16*>(a.K), 0, 1 << 27, 0x00020000);
  const __amdgpu_buffer_rsrc_t rvv = __builtin_amdgcn_make_buffer_rsrc(const_cast<u16*>(a.V), 0, 1 << 27, 0x00020000);
  const __amdgpu_buffer_rsrc_t rw = __builtin_amdgcn_make_buffer_rsrc(const_cast<u16*>(a.wq), 0, DM * 4096 * 2, 0x00020000);
  const __amdgpu_buffer_rsrc_t rx = __builtin_amdgcn_make_buffer_rsrc(const_cast<u16*>(a.xA), 0, 4 * DM * 2, 0x00020000);
  QPre Q;
  u32x4 R[RING];
#define A_ISSUE(i) R[(i) % RING] = __builtin_amdgcn_raw_buffer_load_b128((i) >= 4 * NS ? rvv : rk, voff, ((i) == 0 ? so0 : __builtin_amdgcn_readlane(vtab, (i) % NS)) + ((((i) / NS) & 3) << 25), 2)
#define GATE(n) do { if (PACE) { u32 due = T0 + toff + org + ((osl * cmin(n, NK)) >> 4) + (u32)due_at(n); if (MAXGAP && (int)(due - (last + MAXGAP)) > 0) due = last + MAXGAP; \
    while ((int)(now - due) < 0) { __builtin_amdgcn_s_sleep(1); now = rt32(); } last = now; } } while (0)
#define CTRL(n) do { if constexpr (PACE) { \
    if constexpr (rnd_at((n) - RD - 2) >= 0) { constexpr int k = rnd_at((n) - RD - 2); \
      const u32 dt = (rv[1] == (u32)tag + k) ? rv[0] - T0 : rt; \
      const float sm = row16_sum((float)(int)dt); \
      const int o = (int)((RSH ? rdlane(sm, 0) * (1.f / 16.f) : (rdlane(sm, 0) + rdlane(sm, 16)) * (1.f / 32.f))) - due_at(rtab[k]); \
      if (w == ((5 * k + 11) & 15) && lane == 0 && o > 0 && o < 2000) { \
        int e = o - (int)org - (int)((osl * cmin(rtab[k], NK)) >> 4) - ((k || SSYNC) ? DB : LEAD); if ((k || SSYNC) && e < 0) e = 0; \
        int sl_ = ((int)osl * gtab[k]) / 16 + (e * btab[k]) / rgap(k); sl_ = sl_ > SLMAX ? SLMAX : sl_; \
        __hip_atomic_store(reinterpret_cast<u64*>(lorg), __builtin_bit_cast(u64, u32x2{(u32)((int)org + e + ((((int)osl - sl_) * cmin(n, NK)) >> 4)), (u32)sl_}), __ATOMIC_RELAXED, __HIP_MEMORY_SCOPE_WORKGROUP); } } \
    if constexpr (rnd_at(n) >= 0) { constexpr int k = rnd_at(n); \
      u32 tot = 0; \
      if (ci == k % ACU && (h & ((1 << RSH) - 1)) == (k & ((1 << RSH) - 1))) { const u32 mine = (1u << 24) + ((rt32() - toff - T0) & 0xfffffu); \
        if (lane == 0) tot = __hip_atomic_fetch_add(lorg + 8 + k, mine, __ATOMIC_RELAXED, __HIP_MEMORY_SCOPE_WORKGROUP) + mine; } \
      __builtin_amdgcn_raw_buffer_store_b64(u32x2{T0 + ((tot & 0xffffffu) >> 4), (u32)tag + k}, rs, (tot >> 24) == 16u ? (int)WS_RPT + (h >> RSH) * 4352 : OOB, 128 * k * 35, 16); } \
    if constexpr (rnd_at((n) - RD) >= 0) { constexpr int k = rnd_at((n) - RD); \
      rv = __builtin_amdgcn_raw_buffer_load_b64(rs, (w == ((5 * k + 11) & 15) && lane < (32 >> RSH)) ? (int)WS_RPT + lane * 4352 : OOB, 128 * k * 35, 16); rt = rt32() - T0 - MISS; } \
  } } while (0)
  static_for<ord_len()>([&](auto oc) __attribute__((always_inline)) {
    constexpr int o = oc.value, kk = ord_cnt(o, 'K'), wk = ord_cnt(o, 'W');
    if constexpr (ord[o] == 'K') A_ISSUE(kk);
    else if constexpr (wk < 8) Q.R0[wk] = __builtin_amdgcn_raw_buffer_load_b128(rw, wqo, wk * 8192, 2);
    else Q.R1 = __builtin_amdgcn_raw_buffer_load_b128(rw, wq1, 0, 2);
    if constexpr (o == 1) {
      __builtin_amdgcn_sched_barrier(0);
      if (tid < 32) lorg[tid] = tid == 0 ? ORG0 : tid == 2 ? T0 : 0u;
      __syncthreads();
      const u32x4 xx = __builtin_amdgcn_raw_buffer_load_b128(rx, xo, 0, 2);
      Q.xa = u32x2{xx[0], xx[1]}; Q.xb = u32x2{xx[2], xx[3]}; Q.x1 = __builtin_amdgcn_raw_buffer_load_b16(rx, x1o, 0, 2);
      if (SSYNC) __builtin_amdgcn_raw_buffer_store_b64(u32x2{T0, (u32)tag + 15u}, rs, (ci == 3 && (h & 1) == 0 && tid == 0) ? (int)WS_RPT + (h >> 1) * 4352 : OOB, 128 * 15 * 35, 16);
    }
    __builtin_amdgcn_sched_barrier(0);
  });
  qproj_mfma(Q, lds, w, lane);
  asm volatile("" :: "v"(pf));
  asm volatile("" ::: "memory");
  {
    u32 arr = 0;
    if (lane == 0) arr = __hip_atomic_fetch_add(lorg + 4, 1u, __ATOMIC_RELAXED, __HIP_MEMORY_SCOPE_WORKGROUP);
    if (__builtin_amdgcn_readfirstlane(arr) == 15u) {
      asm volatile("" ::: "memory");
      const float* qpart = reinterpret_cast<const float*>(lds + L_BIG) + 4 * lane;
      f32x4 s0 = {0.f, 0.f, 0.f, 0.f}, s1 = {0.f, 0.f, 0.f, 0.f};
#pragma unroll
      for (int c = 0; c < 4; ++c) {
#pragma unroll
        for (int ww = 4 * c; ww < 4 * c + 4; ++ww) { s0 += *reinterpret_cast<const f32x4*>(qpart + ww * 512); s1 += *reinterpret_cast<const f32x4*>(qpart + ww * 512 + 256); }
        __builtin_amdgcn_sched_barrier(0);
      }
      *reinterpret_cast<u32x2*>(lds + L_QS + 8 * lane) = u32x2{pk_bf16(s0[0], s0[1]), pk_bf16(s0[2], s0[3])};
      *reinterpret_cast<u32x2*>(lds + L_QS + 512 + 8 * lane) = u32x2{pk_bf16(s1[0], s1[1]), pk_bf16(s1[2], s1[3])};
      asm volatile("" ::: "memory");
      if (lane == 0) __hip_atomic_store(lorg + 5, 1u, __ATOMIC_RELAXED, __HIP_MEMORY_SCOPE_WORKGROUP);
    }
  }
  __builtin_amdgcn_s_setprio(0);
  if (SSYNC) rv = __builtin_amdgcn_raw_buffer_load_b64(rs, tid < 16 ? (int)WS_RPT + lane * 4352 : OOB, 128 * 15 * 35, 16);
  __builtin_amdgcn_sched_barrier(0);
#pragma unroll
  for (int t = PRE; t < RING; ++t) A_ISSUE(t);
  __builtin_amdgcn_sched_barrier(0);
  if (SSYNC && w == 0) {
    const bool ok = rv[1] == (u32)tag + 15u;
    const float sm = row16_sum(ok ? (float)(int)(rv[0] - T0) : 0.f), cn = row16_sum(ok ? 1.f : 0.f);
    const int o = ORG0 + (cn > 0.f ? (int)(sm / cn) : 0);
    if (lane == 0 && o > 0 && o < 1000) lorg[0] = (u32)o;
  }
  while (__hip_atomic_load(lorg + 5, __ATOMIC_RELAXED, __HIP_MEMORY_SCOPE_WORKGROUP) == 0u) __builtin_amdgcn_s_sleep(1);
  asm volatile("" ::: "memory");
  T0 = __builtin_amdgcn_readfirstlane(lorg[2]);
  float* wtab = reinterpret_cast<float*>(lds + L_ST + 1024);
  u32x2 xv = {0u, 0u};
  const int xoff = (w == XW && lane < 8 * ACU) ? (int)WS_MLA + (h * 8 + (lane >> 3)) * 128 + (lane & 7) * 8 : OOB;
  float* pml = reinterpret_cast<float*>(lds + L_ST);
  u32x4 q;
  float acc[8], S[4];
  u32x2 RW[4][8];
  u32x4 WT[4][4];
  float Y[4];
  auto project = [&](int b) __attribute__((always_inline)) {
    f32x4 d4[4];
#pragma unroll
    for (int t = 0; t < 4; ++t) d4[t] = f32x4{0.f, 0.f, 0.f, 0.f};
    const f32x4 wt = *reinterpret_cast<const f32x4*>(wtab + b * 16 + 4 * g);
#pragma unroll
    for (int s = 0; s < 4; ++s) {
      const u32x4 af = *reinterpret_cast<const u32x4*>(lds + L_AB + (b * 16 + j) * 272 + (32 * s + 8 * g) * 2);
#pragma unroll
      for (int t = 0; t < 4; ++t) d4[t] = mfma16(af, WT[s][t], d4[t]);
    }
    float y[4];
#pragma unroll
    for (int t = 0; t < 4; ++t) y[t] = fmaf(d4[t][0], wt[0], d4[t][1] * wt[1]) + fmaf(d4[t][2], wt[2], d4[t][3] * wt[3]);
    Y[b] = sw16(sw32(y[0], y[2]), sw32(y[1], y[3]));
  };
  const __amdgpu_buffer_rsrc_t rwo = __builtin_amdgcn_make_buffer_rsrc(const_cast<u16*>(a.wo + (size_t)h * 128 * DM), 0, w < 9 ? 128 * DM * 2 : 0, 0x00020000);
  const int wvo = (8 * g * DM + 64 * w + 4 * j) * 2;
#define WO_ISSUE() static_for<32>([&](auto kc) __attribute__((always_inline)) { constexpr int k = kc.value; \
    if constexpr (wo_step(k) == i) RW[k >> 3][k & 7] = __builtin_amdgcn_raw_buffer_load_b64(rwo, wvo, (32 * (k >> 3) + (k & 7)) * DM * 2, 2); })
  static_for<NSEQ>([&](auto ic) __attribute__((always_inline)) {
    constexpr int i = ic.value, bb = (i / NS) & 3, pass = i / (4 * NS), sl = i % NS;
    u32 now = 0; if (PACE) now = rt32();
    u32 org = 0, osl = 0; if (PACE) { const u32x2 ov = __builtin_bit_cast(u32x2, __hip_atomic_load(reinterpret_cast<u64*>(lorg), __ATOMIC_RELAXED, __HIP_MEMORY_SCOPE_WORKGROUP)); org = __builtin_amdgcn_readfirstlane(ov[0]); osl = __builtin_amdgcn_readfirstlane(ov[1]); }
    __builtin_amdgcn_sched_barrier(0);
    const u32x4 r = R[i % RING];
    if constexpr (pass == 0) {
      if constexpr (sl == 0) { q = *reinterpret_cast<const u32x4*>(lds + L_QS + (bb * 128 + 8 * j) * 2); S[bb] = -INFINITY; }
      float d = dot2(r[0], q[0], 0.f); d = dot2(r[1], q[1], d); d = dot2(r[2], q[2], d); d = dot2(r[3], q[3], d);
      asm volatile("" : "+v"(d));
      if constexpr (issued(i + 1) > issued(i)) { GATE(issued(i)); WO_ISSUE(); A_ISSUE(issued(i)); CTRL(issued(i)); if constexpr (issued(i + 1) > issued(i) + 1) { GATE(issued(i) + 1); A_ISSUE(issued(i) + 1); CTRL(issued(i) + 1); } } else { WO_ISSUE(); }
      d = row16_sum(d);
      S[bb] = (j == sl) ? d : S[bb];
      if constexpr (sl == NS - 1) {
        const float sc = (j == bad_sl) ? -INFINITY : S[bb] * C2;
        const float m = xg_max(row16_max(sc));
        S[bb] = __builtin_amdgcn_exp2f(sc - m);
        const float l = xg_sum(row16_sum(S[bb]));
        if (lane == 0) *reinterpret_cast<f32x2*>(pml + (bb * 16 + w) * 2) = f32x2{m, l};
      }
      if constexpr (i == 4 * NS - 1) {
        asm volatile("" ::: "memory");
        u32 arr = 0;
        if (lane == 0) arr = __hip_atomic_fetch_add(lorg + 6, 1u, __ATOMIC_RELAXED, __HIP_MEMORY_SCOPE_WORKGROUP);
        arr = __builtin_amdgcn_readfirstlane(arr);
        asm volatile("" ::: "memory");
        const f32x2 ml = *reinterpret_cast<const f32x2*>(pml + lane * 2);
        const float M = row16_max(ml[0]);
        const float L = row16_sum(__builtin_amdgcn_exp2f(ml[0] - M) * ml[1]);
        __builtin_amdgcn_raw_buffer_store_b64(u32x2{__float_as_uint(j ? L : M), tg}, rs, (arr == 15u && j < 2) ? (int)WS_MLA + (h * 8 + ci) * 128 + g * 16 + j * 8 : OOB, 0, 16);
      }
    } else {
      const float p = dpp<0x150 + sl>(S[bb]);
      if constexpr (sl == 0) {
#pragma unroll
        for (int k = 0; k < 8; ++k) acc[k] = 0.f;
      }
#pragma unroll
      for (int k = 0; k < 4; ++k) { acc[2 * k] = fmaf(p, bf_lo(r[k]), acc[2 * k]); acc[2 * k + 1] = fmaf(p, bf_hi(r[k]), acc[2 * k + 1]); }
      asm volatile("" : "+v"(acc[0]), "+v"(acc[1]), "+v"(acc[2]), "+v"(acc[3]), "+v"(acc[4]), "+v"(acc[5]), "+v"(acc[6]), "+v"(acc[7]));
      if constexpr (issued(i + 1) > issued(i)) { GATE(issued(i)); WO_ISSUE(); A_ISSUE(issued(i)); CTRL(issued(i)); if constexpr (issued(i + 1) > issued(i) + 1) { GATE(issued(i) + 1); A_ISSUE(issued(i) + 1); CTRL(issued(i) + 1); } } else { WO_ISSUE(); }
      if constexpr (i == XR) xv = __builtin_amdgcn_raw_buffer_load_b64(rs, xoff, 0, 16);
      if constexpr (i == XR + 2) {
        if (w == XW) {
          if (__builtin_expect(!__all(lane >= 8 * ACU || xv[1] == tg), 0)) {
#pragma unroll 1
            for (u32 spins = 0; spins < SPIN_CAP; ++spins) {
              __builtin_amdgcn_s_sleep(8); xv = __builtin_amdgcn_raw_buffer_load_b64(rs, xoff, 0, 16);
              if (__all(lane >= 8 * ACU || xv[1] == tg)) break;
            }
          }
          const float x = __uint_as_float(xv[0]);
          const float m = lane < 8 * ACU ? dpp<0xA0>(x) : -INFINITY, l = lane < 8 * ACU ? dpp<0xF5>(x) : 0.f;
          const float M = xg_max(fmaxf(m, dpp<0x128>(m)));
          float t = __builtin_amdgcn_exp2f(m - M) * l; t = xg_sum(t + dpp<0x128>(t));
          if (lane < 8 && !(lane & 1)) *reinterpret_cast<f32x2*>(wtab + 64 + lane) = f32x2{M, 1.f / t};
          asm volatile("" ::: "memory");
          const f32x2 gm = *reinterpret_cast<const f32x2*>(wtab + 64 + 2 * g);
          wtab[lane] = __builtin_amdgcn_exp2f(pml[lane * 2] - gm[0]) * gm[1];
          asm volatile("" ::: "memory");
          if (lane == 0) __hip_atomic_store(lorg + 7, 1u, __ATOMIC_RELAXED, __HIP_MEMORY_SCOPE_WORKGROUP);
        }
      }
      if constexpr (i >= WTS && i < WTS + 4) {
        constexpr int s = i - WTS;
#pragma unroll
        for (int t = 0; t < 4; ++t) WT[s][t] = tcol8(RW[s], t);
        asm volatile("" : "+v"(WT[s][0]), "+v"(WT[s][1]), "+v"(WT[s][2]), "+v"(WT[s][3]));
      }
      if constexpr (i == PJ0 || i == PJ1 || i == PJ2) {
        constexpr int b = i == PJ0 ? 0 : i == PJ1 ? 1 : 2;
        Y[b] = 0.f;
        if (w < 9) {
          while (__hip_atomic_load(lorg + 24 + b, __ATOMIC_RELAXED, __HIP_MEMORY_SCOPE_WORKGROUP) != 16u || __hip_atomic_load(lorg + 7, __ATOMIC_RELAXED, __HIP_MEMORY_SCOPE_WORKGROUP) == 0u) __builtin_amdgcn_s_sleep(1);
          asm volatile("" ::: "memory");
          project(b);
        }
        asm volatile("" : "+v"(Y[b]));
      }
      if constexpr (sl == NS - 1) {
        const float t0 = sw16(sw32(acc[0], acc[4]), sw32(acc[2], acc[6])), t1 = sw16(sw32(acc[1], acc[5]), sw32(acc[3], acc[7]));
        *reinterpret_cast<u32*>(lds + L_AB + (bb * 16 + w) * 272 + 16 * j + 4 * g) = pk_bf16(t0, t1);
        if constexpr (bb < 3) { asm volatile("" ::: "memory"); if (lane == 0) __hip_atomic_fetch_add(lorg + 24 + bb, 1u, __ATOMIC_RELAXED, __HIP_MEMORY_SCOPE_WORKGROUP); }
      }
    }
    __builtin_amdgcn_sched_barrier(0);
  });
#undef A_ISSUE
  __syncthreads();
  if (w < 9) {
    project(3);
    st16_sc1(rs, WS_Y + ((8 * w + (j >> 1)) * 256 + idx) * 128 + ((j & 1) * 4 + g) * 16, u32x4{pk_bf16(Y[0], Y[1]), tg, pk_bf16(Y[2], Y[3]), tg});
  }
  if (idx < 72) {
    __builtin_amdgcn_sched_barrier(0);
    __builtin_amdgcn_s_barrier();
    __builtin_amdgcn_s_sleep(FDLY);
    const int yo = (int)WS_Y + (idx * 256 + 16 * w) * 128 + lane * 16;
    u32x4 v0, v1;
#pragma unroll 1
    for (u32 spins = 0; spins < SPIN_CAP; ++spins) {
      v0 = __builtin_amdgcn_raw_buffer_load_b128(rs, yo, 0, 16); v1 = __builtin_amdgcn_raw_buffer_load_b128(rs, yo + 1024, 0, 16);
      if (__all(v0[1] == tg && v0[3] == tg && v1[1] == tg && v1[3] == tg)) break;
      __builtin_amdgcn_s_sleep(FSL);
    }
    float y = sw16(sw32(bf_lo(v0[0]) + bf_lo(v1[0]), bf_lo(v0[2]) + bf_lo(v1[2])), sw32(bf_hi(v0[0]) + bf_hi(v1[0]), bf_hi(v0[2]) + bf_hi(v1[2])));
    y += dpp<0x128>(y);
    float* red = reinterpret_cast<float*>(lds + L_PF);
    if (!(lane & 8)) red[((lane & 7) * 4 + g) * 20 + w] = y;
    __syncthreads();
    if (tid < 32) {
      const f32x4 r0 = *reinterpret_cast<const f32x4*>(red + tid * 20), r1 = *reinterpret_cast<const f32x4*>(red + tid * 20 + 4);
      const f32x4 r2 = *reinterpret_cast<const f32x4*>(red + tid * 20 + 8), r3 = *reinterpret_cast<const f32x4*>(red + tid * 20 + 12);
      const f32x4 r = r0 + r1 + r2;
      const __amdgpu_buffer_rsrc_t ro = __builtin_amdgcn_make_buffer_rsrc(a.out, 0, 4 * 1152 * 2, 0x00020000);
      __builtin_amdgcn_raw_buffer_store_b16(to_bf16(((r[0] + r[1]) + (r[2] + r[3])) + (r3[0] + r3[1])), ro, ((tid & 3) * 1152 + idx * 8 + (tid >> 2)) * 2, 0, OAUX);
      __builtin_amdgcn_raw_buffer_store_b16(to_bf16(r3[2] + r3[3]), ro, ((tid >> 3) * 1152 + 576 + idx * 8 + (tid & 7)) * 2, 0, OAUX);
    }
  }
}

#define IA 2
#define IB 4
#define IC 6
#define EUV 4
#define YOFF 90
#define WPD 4
template <bool LD> DI void part_b(const Args& a, unsigned char* lds, int wg, u64 tag, u32 pf) {
  const int tid = threadIdx.x, lane = tid & 63, w = __builtin_amdgcn_readfirstlane(tid >> 6);
  const int j = lane & 15, g = lane >> 4;
  const __amdgpu_buffer_rsrc_t rs = __builtin_amdgcn_make_buffer_rsrc(a.ws, 0, (int)WS_END, 0x00020000);
  const u32 tb0 = rt32();
  const int h = wg;
  const int h3 = wg ^ 2;
  const int b2 = wg >> 3, sp = wg & 7;
  const u16* cl = a.C + ((size_t)b2 * SK + sp * 512 + 8 * (w - 8)) * DC + lane * 8;
  u32x4 X[16];
  u32x2 RW[4][8];
  u32x4 W8[2];
  u32* cnt = reinterpret_cast<u32*>(lds + L_ST + 4064);
#define C_ISSUE(T) _Pragma("unroll") for (int r = 0; r < 8; ++r) X[8 * ((T) & 1) + r] = ld16(cl + (size_t)(64 * (T) + r) * DC)
#define C_HALF(T, hf) _Pragma("unroll") for (int r = 4 * (hf); r < 4 * (hf) + 4; ++r) X[8 * ((T) & 1) + r] = ld16(cl + (size_t)(64 * (T) + r) * DC)
#define L_PART(T, r0, r1) do { __builtin_amdgcn_sched_barrier(0); \
    if ((T) + 3 < 8) { _Pragma("unroll") for (int r = r0; r < r1; ++r) X[8 * (((T) + 3) & 1) + r] = ld16(cl + (size_t)(64 * ((T) + 3) + r) * DC); } \
    if ((T) == 5 || (T) == 6) { _Pragma("unroll") for (int k = 2 * (r0); k < 2 * (r1); ++k) \
      RW[2 * (((T) - 5) & 1) + (k >> 3)][k & 7] = ld8(a.woB + ((size_t)h3 * 128 + 32 * (2 * (((T) - 5) & 1) + (k >> 3)) + 8 * g + (k & 7)) * DM + 64 * (w - 8) + 4 * j); } \
    __builtin_amdgcn_sched_barrier(0); } while (0)
#define UV_LOAD(n) do { const u32x4 v_ = ld16(a.wuv + ((size_t)(64 * w + 32 * ((n) >> 3) + 8 * g + ((n) & 7)) * H + h3) * 128 + 8 * j); \
    RW[2 * ((n) >> 3)][(n) & 7] = u32x2{v_[0], v_[1]}; RW[2 * ((n) >> 3) + 1][(n) & 7] = u32x2{v_[2], v_[3]}; } while (0)
#define C_WIN(T, r0, r1) _Pragma("unroll") for (int r = r0; r < r1; ++r) { asm volatile("s_waitcnt vmcnt(%0)" :: "n"(WPD - 1) : "memory"); X[8 * ((T) & 1) + r] = ld16(cl + (size_t)(64 * (T) + r) * DC); __builtin_amdgcn_sched_barrier(0); }
  u32 sb0 = L_BIG + 8 * (w - 8) * CRS + lane * 16, sb1 = sb0 + CBUF;
  asm volatile("" : "+v"(sb0), "+v"(sb1));
#define C_STAGE(T) _Pragma("unroll") for (int r = 0; r < 8; ++r) *reinterpret_cast<u32x4*>(lds + (((T) & 1) ? sb1 : sb0) + r * CRS) = X[8 * ((T) & 1) + r]
  {
    const u16* Wq = a.wqB + h * 128;
    u32x4 U[8];
    QPre Q;
    qproj_load(Q, a.xB, Wq, w, lane);
#pragma unroll
    for (int i = 0; i < 8; ++i) U[i] = ld16(a.wuk + ((size_t)(32 * w + 4 * i + (j & 3)) * H + h) * 128 + 8 * ((j & 12) + g));
    __builtin_amdgcn_sched_barrier(0);
    if constexpr (!LD) am_zero(lds, tid);
    qproj_mfma(Q, lds, w, lane);
    asm volatile("" :: "v"(pf));
    __syncthreads();
    if constexpr (LD) { C_HALF(0, 0); }
    else {
      const float* qpart = reinterpret_cast<const float*>(lds + L_BIG);
      float s = 0.f;
#pragma unroll
      for (int ww = 0; ww < 16; ++ww) s += qpart[ww * 512 + tid];
      reinterpret_cast<u16*>(lds + L_QS)[tid] = to_bf16(s);
    }
    __builtin_amdgcn_sched_barrier(0);
    __syncthreads();
    const u32x4 af = *reinterpret_cast<const u32x4*>(lds + L_QS + ((j >> 2) * 128 + ((j & 3) * 4 + g) * 8) * 2);
    u16* qa = reinterpret_cast<u16*>(lds + L_PF);
    float v2[2] = {0.f, 0.f};
#pragma unroll
    for (int i = 0; i < 8; ++i) {
      const f32x4 d4 = mfma16(af, U[i], f32x4{0.f, 0.f, 0.f, 0.f});
      float d = (j & 8) ? ((j & 4) ? d4[3] : d4[2]) : ((j & 4) ? d4[1] : d4[0]);
      d += dpp<0x124>(d); d += dpp<0x128>(d);
      v2[i >> 2] = ((j >> 2) == (i & 3)) ? d : v2[i >> 2];
    }
    qa[g * 512 + 32 * w + j] = to_bf16(v2[0]); qa[g * 512 + 32 * w + 16 + j] = to_bf16(v2[1]);
    __builtin_amdgcn_sched_barrier(0);
    __syncthreads();
    if constexpr (!LD) {
      if (w == 0) {
#pragma unroll
        for (int bb = 0; bb < 4; ++bb) st16_sc1(rs, WS_QABS + ((bb * H + h) * 512 + lane * 8) * 2, *reinterpret_cast<const u32x4*>(qa + bb * 512 + lane * 8));
      }
      __builtin_amdgcn_sched_barrier(0);
      __builtin_amdgcn_s_barrier();
      if (w == 0) {
        asm volatile("s_waitcnt vmcnt(0)" ::: "memory");
        if (lane == 0) { __hip_atomic_store(reinterpret_cast<u64*>(a.ws + WS_F1 + wg * 8), tag, RLX_AGENT); cnt[0] = 0u; cnt[1] = 0u; }
      }
    } else {
      __builtin_amdgcn_s_barrier();
      C_WIN(0, 4, 8); C_WIN(1, 0, 8);
      C_STAGE(0);
      __builtin_amdgcn_sched_barrier(0);
      C_WIN(2, 0, 8);
    }
  }
  {
    const int b = b2;
    if constexpr (!LD) wg_wait64(a.ws + WS_F1, tag); else __syncthreads();
    const int kt = w & 3, hh = (w >> 2) & 1;
    float* stmax = reinterpret_cast<float*>(lds + L_ST);
    float* stsum = stmax + 128;
    float* salpha = stmax + 256;
    unsigned char* Pf = lds + L_PF;
    float M = -INFINITY, lsum = 0.f;
    f32x4 acc[2][2];
#pragma unroll
    for (int ct = 0; ct < 2; ++ct) { acc[ct][0] = f32x4{0.f, 0.f, 0.f, 0.f}; acc[ct][1] = f32x4{0.f, 0.f, 0.f, 0.f}; }
    const int troff = (4 * g + (j >> 2)) * CRS + (32 * w + 4 * (j & 3)) * 2;
    auto pv_step = [&](const unsigned char* cb) __attribute__((always_inline)) {
      const f32x4 al0 = *reinterpret_cast<const f32x4*>(salpha + 4 * g), al1 = *reinterpret_cast<const f32x4*>(salpha + 16 + 4 * g);
#pragma unroll
      for (int ct = 0; ct < 2; ++ct) { acc[ct][0] *= al0; acc[ct][1] *= al1; }
#pragma unroll
      for (int blk = 0; blk < 2; ++blk) {
        const u32x4 p0 = *reinterpret_cast<const u32x4*>(Pf + ((blk * 2 + 0) * 64 + lane) * 16);
        const u32x4 p1 = *reinterpret_cast<const u32x4*>(Pf + ((blk * 2 + 1) * 64 + lane) * 16);
#pragma unroll
        for (int ct = 0; ct < 2; ++ct) {
          const u32x2 lo = trread(cb + troff + (32 * blk) * CRS + 32 * ct);
          const u32x2 hi = trread(cb + troff + (32 * blk + 16) * CRS + 32 * ct);
          const u32x4 bf = {lo[0], lo[1], hi[0], hi[1]};
          acc[ct][0] = mfma16(p0, bf, acc[ct][0]); acc[ct][1] = mfma16(p1, bf, acc[ct][1]);
        }
      }
    };
    unsigned char* Qf = lds + L_BIG + CBUF;
    if constexpr (!LD) {
      u32x4 qv[4];
#pragma unroll
      for (int k = 0; k < 4; ++k) qv[k] = __builtin_amdgcn_raw_buffer_load_b128(rs, (int)WS_QABS + (b * H * 512 + (tid + 512 * k) * 8) * 2, 0, 16);
#pragma unroll
      for (int k = 0; k < 4; ++k) { const int e = tid + 512 * k, head = e >> 6, ch = e & 63;
        *reinterpret_cast<u32x4*>(Qf + ((head >> 4) * 16 + (ch >> 2)) * 1040 + ((ch & 3) * 16 + (head & 15)) * 16) = qv[k]; }
    }
    __syncthreads();
    if constexpr (!LD) {
      u32x4 QF[16];
#pragma unroll
      for (int s = 0; s < 16; ++s) QF[s] = *reinterpret_cast<const u32x4*>(Qf + (hh * 16 + s) * 1040 + lane * 16);
      auto s_tile = [&](const unsigned char* cb, bool last) __attribute__((always_inline)) {
        __syncthreads();
        f32x4 s0 = {0.f, 0.f, 0.f, 0.f}, s1 = {0.f, 0.f, 0.f, 0.f};
#pragma unroll
        for (int s = 0; s < 16; s += 2) {
          s0 = mfma16(*reinterpret_cast<const u32x4*>(cb + (16 * kt + j) * CRS + (32 * s + 8 * g) * 2), QF[s], s0);
          s1 = mfma16(*reinterpret_cast<const u32x4*>(cb + (16 * kt + j) * CRS + (32 * s + 32 + 8 * g) * 2), QF[s + 1], s1);
        }
        const f32x4 sv = (s0 + s1) * C2;
        if (last) { __builtin_amdgcn_sched_barrier(0); _Pragma("unroll") for (int n = 0; n < EUV; ++n) UV_LOAD(n); __builtin_amdgcn_sched_barrier(0); }
        const float v = xg_max(fmaxf(fmaxf(sv[0], sv[1]), fmaxf(sv[2], sv[3])));
        if (g == 0) stmax[kt * 32 + 16 * hh + j] = v;
        __syncthreads();
        const float* sm = stmax + 16 * hh + j;
        const float Mn = fmaxf(fmaxf(M, fmaxf(sm[0], sm[32])), fmaxf(sm[64], sm[96]));
        const float alpha = __builtin_amdgcn_exp2f(M - Mn);
        M = Mn;
        u32x2 pk;
        pk[0] = pk_bf16(__builtin_amdgcn_exp2f(sv[0] - Mn), __builtin_amdgcn_exp2f(sv[1] - Mn));
        pk[1] = pk_bf16(__builtin_amdgcn_exp2f(sv[2] - Mn), __builtin_amdgcn_exp2f(sv[3] - Mn));
        lsum = lsum * alpha + ((bf_lo(pk[0]) + bf_hi(pk[0])) + (bf_lo(pk[1]) + bf_hi(pk[1])));
        *reinterpret_cast<u32x2*>(Pf + (((kt >> 1) * 2 + hh) * 64 + lane) * 16 + (kt & 1) * 8) = pk;
        if (kt == 0 && g == 0) salpha[16 * hh + j] = alpha;
        __syncthreads();
        pv_step(cb);
      };
#pragma unroll 1
      for (int T = 0; T < 6; T += 2) { s_tile(lds + L_BIG, false); s_tile(lds + L_BIG + CBUF, false); }
      s_tile(lds + L_BIG, false); s_tile(lds + L_BIG + CBUF, true);
    } else {
#pragma unroll
      for (int T = 0; T < 8; ++T) {
        __syncthreads();
        if (T + 1 < 8) { C_STAGE(T + 1); }
        L_PART(T, 0, IA);
        if (T == 7) {
#pragma unroll
          for (int k = 0; k < 2; ++k) W8[k] = ld16(a.woB + ((size_t)h3 * 128 + 16 * (w - 8) + 8 * k + (lane >> 3)) * DM + 512 + (lane & 7) * 8);
        }
        __syncthreads();
        L_PART(T, IA, IB);
        __syncthreads();
        L_PART(T, IB, IC);
        pv_step(lds + L_BIG + (T & 1) * CBUF);
        L_PART(T, IC, 8);
      }
    }
#undef C_ISSUE
#undef C_STAGE
    __syncthreads();
    if constexpr (!LD) { lsum = xg_sum(lsum); if (g == 0) stsum[kt * 32 + 16 * hh + j] = lsum; if (kt == 0 && g == 0) salpha[32 + 16 * hh + j] = M; }
    float* tb = reinterpret_cast<float*>(lds + L_BIG);
#pragma unroll
    for (int ct = 0; ct < 2; ++ct)
#pragma unroll
      for (int h2 = 0; h2 < 2; ++h2)
#pragma unroll
        for (int r = 0; r < 4; ++r) tb[(16 * h2 + 4 * g + r) * 516 + 32 * w + 16 * ct + j] = acc[ct][h2][r];
    if constexpr (!LD) { for (int i = tid; i < 8 * AM2_LD * 2 / 16; i += 512) reinterpret_cast<u32x4*>(lds + L_AM2 + 8 * AM2_LD * 2)[i] = u32x4{0u, 0u, 0u, 0u}; }
    __syncthreads();
    if constexpr (LD) {
      const int t2 = tid - 512;
#pragma unroll
      for (int k = 0; k < 4; ++k) {
        const int e = t2 + 512 * k, head = e >> 6, c8 = e & 63;
        const f32x4 v0 = *reinterpret_cast<const f32x4*>(tb + head * 516 + 8 * c8), v1 = *reinterpret_cast<const f32x4*>(tb + head * 516 + 8 * c8 + 4);
        __builtin_amdgcn_raw_buffer_store_b128(u32x4{pk_bf16(v0[0], v0[1]), pk_bf16(v0[2], v0[3]), pk_bf16(v1[0], v1[1]), pk_bf16(v1[2], v1[3])}, rs, (int)WS_CTXP + (((b * NSP + sp) * 32 + head) * 512 + 8 * c8) * 2, 0, 16);
      }
      if (t2 < 32) {
        const float L = (stsum[t2] + stsum[32 + t2]) + (stsum[64 + t2] + stsum[96 + t2]);
        st8_sc1(a.ws + WS_MLB + ((size_t)(b * NSP + sp) * 32 + t2) * 8, f32x2{salpha[32 + t2], L});
      }
      __builtin_amdgcn_sched_barrier(0);
      __builtin_amdgcn_s_barrier();
      asm volatile("s_waitcnt vmcnt(0)" ::: "memory");
      if (lane == 0) {
        if (__hip_atomic_fetch_add(cnt, 1u, __ATOMIC_RELAXED, __HIP_MEMORY_SCOPE_WORKGROUP) == 7u) {
          __hip_atomic_store(reinterpret_cast<u64*>(a.ws + WS_F2 + wg * 8), tag, RLX_AGENT);
        }
      }
#pragma unroll
      for (int k = 0; k < 2; ++k) *reinterpret_cast<u32x4*>(lds + L_W8 + (16 * (w - 8) + 8 * k + (lane >> 3)) * 144 + (lane & 7) * 16) = W8[k];
    } else {
      __builtin_amdgcn_s_barrier();
      static_for<16 - EUV>([&](auto ic) __attribute__((always_inline)) {
        asm volatile("s_waitcnt vmcnt(%0)" :: "n"(WPD - 1) : "memory");
        UV_LOAD(EUV + ic.value);
        __builtin_amdgcn_sched_barrier(0);
      });
    }
  }
  {
    int tid = threadIdx.x; asm volatile("" : "+v"(tid));
    const int lane = tid & 63, j = lane & 15, g = lane >> 4;
    u16* Am2 = reinterpret_cast<u16*>(lds + L_AM2);
    float* cpart = reinterpret_cast<float*>(lds + L_CP);
    __builtin_amdgcn_sched_barrier(0);
    if constexpr (LD) {
      if (w == 8) {
        wave_wait(reinterpret_cast<const u64*>(a.ws + WS_F2) + (lane & (NBW - 1)), true, tag);
        asm volatile("s_waitcnt vmcnt(0)" ::: "memory");
        if (lane == 0) __hip_atomic_store(cnt + 1, 1u, __ATOMIC_RELEASE, __HIP_MEMORY_SCOPE_WORKGROUP);
      } else {
        for (u32 spins = 0; spins < 64u * SPIN_CAP; ++spins) { if (__hip_atomic_load(cnt + 1, __ATOMIC_ACQUIRE, __HIP_MEMORY_SCOPE_WORKGROUP) != 0u) break; __builtin_amdgcn_s_sleep(1); }
      }
      __builtin_amdgcn_sched_barrier(0);
      const int b = (tid >> 7) & 3, c4 = tid & 127;
      u32x2 pv[NSP];
      const f32x2 ml = __builtin_bit_cast(f32x2, __builtin_amdgcn_raw_buffer_load_b64(rs, (int)WS_MLB + ((b * NSP + (j & 7)) * 32 + h3) * 8, 0, 16));
#pragma unroll
      for (int s = 0; s < NSP; ++s) pv[s] = __builtin_amdgcn_raw_buffer_load_b64(rs, (int)WS_CTXP + (((b * NSP + s) * 32 + h3) * 512 + 4 * c4) * 2, 0, 16);
      const float M = row16_max(ml[0]);
      float wt = __builtin_amdgcn_exp2f(ml[0] - M);
      wt *= 2.f / row16_sum(wt * ml[1]);
      float c0 = 0.f, c1 = 0.f, c2 = 0.f, c3 = 0.f;
#pragma unroll
      for (int s = 0; s < NSP; ++s) { const float ws_ = rdlane(wt, s); c0 = fmaf(ws_, bf_lo(pv[s][0]), c0); c1 = fmaf(ws_, bf_hi(pv[s][0]), c1); c2 = fmaf(ws_, bf_lo(pv[s][1]), c2); c3 = fmaf(ws_, bf_hi(pv[s][1]), c3); }
      const u32 h0 = pk_bf16(c0, c1), h1 = pk_bf16(c2, c3);
      *reinterpret_cast<u32x2*>(Am2 + (2 * b) * AM2_LD + 4 * c4) = u32x2{h0, h1};
      *reinterpret_cast<u32x2*>(Am2 + (2 * b + 1) * AM2_LD + 4 * c4) = u32x2{pk_bf16(c0 - bf_lo(h0), c1 - bf_hi(h0)), pk_bf16(c2 - bf_lo(h1), c3 - bf_hi(h1))};
      __syncthreads();
      __syncthreads();
      const int t2 = tid - 512;
      float s = 0.f;
#pragma unroll
      for (int ww = 0; ww < 8; ++ww) s += cpart[ww * 512 + t2];
      u16* Am = reinterpret_cast<u16*>(lds + L_AM);
      const int bb = t2 >> 7, d = t2 & 127;
      const u16 hi = to_bf16(s);
      Am[(4 * bb) * AM_LD + d] = hi; Am[(4 * bb + 1) * AM_LD + d] = to_bf16(s - bf2f(hi));
    } else {
      __syncthreads();
      f32x4 acc[8];
#pragma unroll
      for (int t = 0; t < 8; ++t) acc[t] = f32x4{0.f, 0.f, 0.f, 0.f};
#pragma unroll
      for (int k2 = 0; k2 < 2; ++k2) {
        const u32x4 af = *reinterpret_cast<const u32x4*>(Am2 + j * AM2_LD + 64 * w + 32 * k2 + 8 * g);
#pragma unroll
        for (int t = 0; t < 8; ++t) acc[t] = mfma16(af, tcol8(RW[2 * k2 + (t >> 2)], t & 3), acc[t]);
      }
      if (g < 2) {
#pragma unroll
        for (int bb = 0; bb < 2; ++bb) {
          float* p = cpart + (w * 4 + 2 * g + bb) * 128 + 8 * j;
          *reinterpret_cast<f32x4*>(p) = f32x4{acc[0][2 * bb] + acc[0][2 * bb + 1], acc[1][2 * bb] + acc[1][2 * bb + 1], acc[2][2 * bb] + acc[2][2 * bb + 1], acc[3][2 * bb] + acc[3][2 * bb + 1]};
          *reinterpret_cast<f32x4*>(p + 4) = f32x4{acc[4][2 * bb] + acc[4][2 * bb + 1], acc[5][2 * bb] + acc[5][2 * bb + 1], acc[6][2 * bb] + acc[6][2 * bb + 1], acc[7][2 * bb] + acc[7][2 * bb + 1]};
        }
      }
      __builtin_amdgcn_sched_barrier(0);
      __syncthreads();
      if (YDEF == 1 && w == 1) {
        u32 dl = tb0 + 5200u;
#pragma unroll 1
        while ((int)(rt32() - dl) < 0) {
          const u32x2 rv = __builtin_amdgcn_raw_buffer_load_b64(rs, lane < 16 ? (int)WS_RPT + lane * 4352 : OOB, 128 * (NRND - 1) * 35, 16);
          const bool ok = rv[1] == (u32)tag + (NRND - 1);
          const float cn = rdlane(row16_sum(ok ? 1.f : 0.f), 0);
          if (cn >= 8.f) { dl = tb0 + (u32)(int)(rdlane(row16_sum(ok ? (float)(int)(rv[0] - tb0) : 0.f), 0) / cn) + (u32)(due_at(NSEQ - 1) - due_at(rtab[NRND - 1]) + YOFF); break; }
          __builtin_amdgcn_s_sleep(32);
        }
        if (lane == 0) cnt[2] = (int)(dl - tb0) > 5200 ? tb0 : dl;
      }
      if (w == 0) {
#pragma unroll
        for (int s = 0; s < 4; ++s)
#pragma unroll
          for (int jj = 0; jj < 8; ++jj) RW[s][jj] = *reinterpret_cast<const u32x2*>(lds + L_W8 + (32 * s + 8 * g + jj) * 144 + 8 * j);
      }
    }
    __syncthreads();
    if (LD || w == 0) {
      f32x4 ya[4];
      wo_mfma(RW, lds, lane, ya);
      if (YDEF) { const u32 dl = YDEF == 1 ? __builtin_amdgcn_readfirstlane(cnt[2]) : tb0 + YDEF; while ((int)(rt32() - dl) < 0) __builtin_amdgcn_s_sleep(4); }
      st16_sc1(rs, WS_Y + ((8 * (LD ? w - 8 : 8) + (j >> 1)) * 256 + NAW + h3) * 128 + g * 32 + (j & 1) * 16, yrow(ya, (u32)(tag >> 32)));
    }
  }
}

__global__ void __launch_bounds__(1024, 4) k_main(Args a) {
  __shared__ __attribute__((aligned(16))) unsigned char lds[L_TOTAL];
  u32 pf;
  { u64 pc; u32 len;
    asm volatile("s_getpc_b64 %0\n.Lpf%=:\n\ts_mov_b32 %1, k_tail_mark-.Lpf%=" : "=s"(pc), "=s"(len));
    pf = __builtin_amdgcn_raw_buffer_load_b32(__builtin_amdgcn_make_buffer_rsrc(reinterpret_cast<void*>(pc), 0, len < 131072u ? len : 0u, 0x00020000), CPFM ? (threadIdx.x < 16 ? ((blockIdx.x >> 3) * 16 + threadIdx.x) * 128 : 0x7ffff000) : threadIdx.x * 128, 0, 0);
    __builtin_amdgcn_sched_barrier(0); }
  u64 tag = (hw_dispatch_id() + 1ull) * 0x9E3779B97F4A7C15ull;
  tag ^= ((u64)__builtin_amdgcn_dispatch_ptr() >> 6) * 0xC2B2AE3D27D4EB4Full;
  tag |= 1ull << 63;
  if (__builtin_expect(blockIdx.x >= NBW, 1)) part_a(a, lds, blockIdx.x - NBW, tag, pf);
  else if (__builtin_amdgcn_readfirstlane(threadIdx.x >> 6) < 8) part_b<false>(a, lds, blockIdx.x, tag, pf);
  else part_b<true>(a, lds, blockIdx.x, tag, pf);
}

extern "C" __global__ void k_tail_mark() {}

extern "C" void kernel_launch(void* const* d_in, const int* in_sizes, int n_in, void* d_out, int out_size, void* d_ws, size_t ws_size,
                              hipStream_t stream) {
  Args a;
  a.xA = (const u16*)d_in[0]; a.K = (const u16*)d_in[1]; a.V = (const u16*)d_in[2]; a.wq = (const u16*)d_in[3]; a.wo = (const u16*)d_in[4];
  a.xB = (const u16*)d_in[5]; a.C = (const u16*)d_in[6]; a.wqB = (const u16*)d_in[7]; a.wuk = (const u16*)d_in[8]; a.wuv = (const u16*)d_in[9]; a.woB = (const u16*)d_in[10];
#define WSTOP 32
  if (WSTOP && ws_size >= ((size_t)WSTOP << 20)) d_ws = (unsigned char*)d_ws + ((ws_size - ((size_t)WSTOP << 20)) & ~(size_t)4095);
  a.ws = (unsigned char*)d_ws;
  a.out = (u16*)d_out;
  k_main<<<NBW + NAW, 1024, 0, stream>>>(a);
}
```

```cpp
#include <hip/hip_runtime.h>
#include <stdint.h>
#include <utility>

typedef unsigned short u16;
typedef unsigned u32;
typedef unsigned long long u64;
typedef u32 u32x4 __attribute__((ext_vector_type(4)));
typedef u32 u32x2 __attribute__((ext_vector_type(2)));
typedef float f32x4 __attribute__((ext_vector_type(4)));
typedef float f32x2 __attribute__((ext_vector_type(2)));
typedef short s16x4 __attribute__((ext_vector_type(4)));
typedef __bf16 bf16x8 __attribute__((ext_vector_type(8)));
typedef __bf16 bf16x2 __attribute__((ext_vector_type(2)));
#define DI __device__ __forceinline__
#define RLX_AGENT __ATOMIC_RELAXED, __HIP_MEMORY_SCOPE_AGENT
extern "C" __device__ u64 hw_dispatch_id() __asm("llvm.amdgcn.dispatch.id");

template <int... Is, class F> DI void static_for_impl(std::integer_sequence<int, Is...>, F&& f) { (f(std::integral_constant<int, Is>{}), ...); }
template <int N, class F> DI void static_for(F&& f) { static_for_impl(std::make_integer_sequence<int, N>{}, f); }

constexpr int H = 32, SK = 4096, DM = 576, DC = 512;
constexpr int ACU = 7;
constexpr int NBW = 32;
constexpr int NAW = H * ACU;
constexpr int NSP = 8;
constexpr float C2 = 0.08838834764831845f * 1.4426950408889634f;

constexpr size_t WS_F1 = 0, WS_F2 = 512;
constexpr size_t WS_QABS = 4096;
constexpr size_t WS_CTXP = WS_QABS + 4 * 32 * 512 * 2;
constexpr size_t WS_MLB = WS_CTXP + (size_t)4 * NSP * 32 * 512 * 2;
constexpr size_t WS_Y = WS_MLB + 4 * NSP * 32 * 2 * 4;
constexpr size_t WS_MLA = WS_Y + (size_t)72 * 256 * 128;
constexpr size_t WS_RPT = WS_MLA + 256 * 128;
constexpr size_t WS_QA = WS_RPT + 64 * 4480;
constexpr size_t WS_END = WS_QA + 256 * 2048;

constexpr int CRS = 1056;
constexpr int CBUF = 64 * CRS;
constexpr int L_BIG = 0;
constexpr int L_AM = 2 * CBUF;
constexpr int L_QS = L_AM + 16 * 272;
constexpr int L_ST = L_QS + 1024;
constexpr int L_PF = L_ST + 4096;
constexpr int L_TOTAL = L_PF + 4096;
constexpr int AM_LD = 136;
constexpr int AM2_LD = 520;
constexpr int L_AM2 = 69632, L_CP = 88064, L_W8 = 106496;

struct Args {
  const u16 *xA, *K, *V, *wq, *wo, *xB, *C, *wqB, *wuk, *wuv, *woB;
  unsigned char* ws;
  u16* out;
};

DI float bf_lo(u32 x) { return __uint_as_float(x << 16); }
DI float bf_hi(u32 x) { return __uint_as_float(x & 0xffff0000u); }
DI u32 pk_bf16(float lo, float hi) { bf16x2 v = {(__bf16)lo, (__bf16)hi}; return __builtin_bit_cast(u32, v); }
DI u16 to_bf16(float x) { return __builtin_bit_cast(u16, (__bf16)x); }
DI float bf2f(u16 x) { return __uint_as_float(((u32)x) << 16); }
DI float dot2(u32 a, u32 b, float c) { return __builtin_amdgcn_fdot2_f32_bf16(__builtin_bit_cast(bf16x2, a), __builtin_bit_cast(bf16x2, b), c, false); }
template <int CTRL> DI float dpp(float x) { return __builtin_bit_cast(float, __builtin_amdgcn_update_dpp(0, __builtin_bit_cast(int, x), CTRL, 0xf, 0xf, true)); }
DI float row16_sum(float x) { x += dpp<0x128>(x); x += dpp<0x124>(x); x += dpp<0x122>(x); x += dpp<0x121>(x); return x; }
DI float row16_max(float x) { x = fmaxf(x, dpp<0x128>(x)); x = fmaxf(x, dpp<0x124>(x)); x = fmaxf(x, dpp<0x122>(x)); x = fmaxf(x, dpp<0x121>(x)); return x; }
DI float xg_max(float x) {
  auto s = __builtin_amdgcn_permlane16_swap(__float_as_uint(x), __float_as_uint(x), false, false);
  x = fmaxf(__uint_as_float(s[0]), __uint_as_float(s[1]));
  auto t = __builtin_amdgcn_permlane32_swap(__float_as_uint(x), __float_as_uint(x), false, false);
  return fmaxf(__uint_as_float(t[0]), __uint_as_float(t[1]));
}
DI float xg_sum(float x) {
  auto s = __builtin_amdgcn_permlane16_swap(__float_as_uint(x), __float_as_uint(x), false, false);
  x = __uint_as_float(s[0]) + __uint_as_float(s[1]);
  auto t = __builtin_amdgcn_permlane32_swap(__float_as_uint(x), __float_as_uint(x), false, false);
  return __uint_as_float(t[0]) + __uint_as_float(t[1]);
}
DI float sw32(float a, float b) {
  auto s = __builtin_amdgcn_permlane32_swap(__float_as_uint(a), __float_as_uint(b), false, false);
  return __uint_as_float(s[0]) + __uint_as_float(s[1]);
}
DI float sw16(float a, float b) {
  auto s = __builtin_amdgcn_permlane16_swap(__float_as_uint(a), __float_as_uint(b), false, false);
  return __uint_as_float(s[0]) + __uint_as_float(s[1]);
}
DI f32x4 mfma16(u32x4 a, u32x4 b, f32x4 c) { return __builtin_amdgcn_mfma_f32_16x16x32_bf16(__builtin_bit_cast(bf16x8, a), __builtin_bit_cast(bf16x8, b), c, 0, 0, 0); }
DI u32x4 ld16(const u16* p) { return __builtin_nontemporal_load(reinterpret_cast<const u32x4*>(p)); }
DI u32x2 ld8(const u16* p) { return __builtin_nontemporal_load(reinterpret_cast<const u32x2*>(p)); }
DI u32x2 trread(const unsigned char* p) {
  return __builtin_bit_cast(u32x2, __builtin_amdgcn_ds_read_tr16_b64_v4i16((__attribute__((address_space(3))) s16x4*)(p)));
}
DI u32 prm(u32 hi, u32 lo, int odd) { return __builtin_amdgcn_perm(hi, lo, odd ? 0x07060302u : 0x05040100u); }

DI u32x4 tcol16(const u32x4 (&R)[8], int t) {
  u32x4 T;
#pragma unroll
  for (int q = 0; q < 4; ++q) T[q] = prm(R[2 * q + 1][t >> 1], R[2 * q][t >> 1], t & 1);
  return T;
}
DI u32x4 tcol8(const u32x2 (&R)[8], int t) {
  u32x4 T;
#pragma unroll
  for (int q = 0; q < 4; ++q) T[q] = prm(R[2 * q + 1][t >> 1], R[2 * q][t >> 1], t & 1);
  return T;
}

#define SPINLOG 22
#define PSL 2
constexpr u32 SPIN_CAP = 1u << SPINLOG;
DI void wave_wait(const u64* f, bool active, u64 tag) {
  for (u32 spins = 0; spins < SPIN_CAP; ++spins) {
    const u64 v = active ? __hip_atomic_load(f, RLX_AGENT) : tag;
    if (__all(v == tag)) break;
    __builtin_amdgcn_s_sleep(PSL);
  }
}
DI void wg_wait64(const unsigned char* flags, u64 tag, int pw = 0) {
  if ((int)(threadIdx.x >> 6) == pw) {
    wave_wait(reinterpret_cast<const u64*>(flags) + (threadIdx.x & (NBW - 1)), true, tag);
    asm volatile("s_waitcnt vmcnt(0)" ::: "memory");
  }
  __syncthreads();
}
DI void st16_sc1(__amdgpu_buffer_rsrc_t rs, size_t off, u32x4 v) { __builtin_amdgcn_raw_buffer_store_b128(v, rs, (int)off, 0, 16); }
DI void st8_sc1(unsigned char* p, f32x2 v) { __hip_atomic_store(reinterpret_cast<u64*>(p), __builtin_bit_cast(u64, v), RLX_AGENT); }

struct QPre { u32x4 R0[8]; u32x4 R1; u32x2 xa, xb; u32 x1; };
DI void qproj_load(QPre& Q, const u16* x, const u16* Wh, int w, int lane) {
  const int j = lane & 15, g = lane >> 4;
  Q.xa = u32x2{0u, 0u}; Q.xb = u32x2{0u, 0u}; Q.x1 = 0u;
  if (j < 4) { Q.xa = ld8(x + j * DM + 36 * w + 8 * g); Q.xb = ld8(x + j * DM + 36 * w + 8 * g + 4); Q.x1 = x[j * DM + 36 * w + 32 + g]; }
#pragma unroll
  for (int jj = 0; jj < 8; ++jj) Q.R0[jj] = ld16(Wh + (size_t)(36 * w + 8 * g + jj) * 4096 + 8 * j);
  Q.R1 = ld16(Wh + (size_t)(36 * w + 32 + g) * 4096 + 8 * j);
}
DI void qproj_mfma(const QPre& Q, unsigned char* lds, int w, int lane) {
  const int j = lane & 15, g = lane >> 4;
  float* qpart = reinterpret_cast<float*>(lds + L_BIG);
  const u32x4 a = {Q.xa[0], Q.xa[1], Q.xb[0], Q.xb[1]}, a1 = {Q.x1, 0u, 0u, 0u};
#pragma unroll
  for (int hf = 0; hf < 2; ++hf) {
    f32x4 acc[4];
#pragma unroll
    for (int t = 0; t < 4; ++t) acc[t] = mfma16(a, tcol16(Q.R0, 4 * hf + t), f32x4{0.f, 0.f, 0.f, 0.f});
#pragma unroll
    for (int t = 0; t < 4; ++t) { const u32x4 bb = {(t & 1) ? (Q.R1[2 * hf + (t >> 1)] >> 16) : (Q.R1[2 * hf + (t >> 1)] & 0xffffu), 0u, 0u, 0u}; acc[t] = mfma16(a1, bb, acc[t]); }
    if (g == 0) {
#pragma unroll
      for (int r = 0; r < 4; ++r) *reinterpret_cast<f32x4*>(qpart + (w * 4 + r) * 128 + 8 * j + 4 * hf) = f32x4{acc[0][r], acc[1][r], acc[2][r], acc[3][r]};
    }
    __builtin_amdgcn_sched_barrier(0);
  }
}
DI void wo_load(const u16* Wo, u32x2 (&R)[4][8], int nb, int lane, int s0, int s1) {
  const int j = lane & 15, g = lane >> 4;
#pragma unroll
  for (int s = 0; s < 4; ++s) if (s >= s0 && s < s1)
#pragma unroll
    for (int jj = 0; jj < 8; ++jj) R[s][jj] = ld8(Wo + (size_t)(32 * s + 8 * g + jj) * DM + 64 * nb + 4 * j);
}
DI void wo_mfma(const u32x2 (&R)[4][8], const unsigned char* lds, int lane, f32x4 (&acc)[4]) {
  const int j = lane & 15, g = lane >> 4;
#pragma unroll
  for (int t = 0; t < 4; ++t) acc[t] = f32x4{0.f, 0.f, 0.f, 0.f};
#pragma unroll
  for (int s = 0; s < 4; ++s) {
    const u32x4 a = *reinterpret_cast<const u32x4*>(lds + L_AM + j * 272 + (32 * s + 8 * g) * 2);
#pragma unroll
    for (int t = 0; t < 4; ++t) acc[t] = mfma16(a, tcol8(R[s], t), acc[t]);
  }
}
DI u32x4 yrow(const f32x4 (&acc)[4], u32 tg) {
  return u32x4{pk_bf16(acc[0][0] + acc[0][1], acc[1][0] + acc[1][1]), tg, pk_bf16(acc[2][0] + acc[2][1], acc[3][0] + acc[3][1]), tg};
}
DI void am_zero(unsigned char* lds, int tid) {
  if (tid < 136) { const int pr = tid / 34; *reinterpret_cast<u32x4*>(lds + L_AM + (4 * pr + 2) * 272 + (tid - 34 * pr) * 16) = u32x4{0u, 0u, 0u, 0u}; }
}
DI float rdlane(float x, int l) { return __builtin_bit_cast(float, __builtin_amdgcn_readlane(__builtin_bit_cast(int, x), l)); }

constexpr int NS = 10;
constexpr int NSEQ = 8 * NS;
#define RNG 9
#define NP 3
#define QP 5
#define SP 4
#define BPF 2
#define K1G 1
#define PSKIP 0
#define D3FROM 66
#define D4FROM 99
#define ROTM 1
#define OAUX 0
#define DEP 2
constexpr int DEPTH = DEP, RING = RNG, RAMP = 2;
constexpr int issued(int i) {
  int d = RING - (i + RAMP - 1) / RAMP; if (d < DEPTH) d = DEPTH;
  if (i >= D3FROM && d < 3) d = 3;
  if (i >= D4FROM && d < 4) d = 4;
  int e = i + d; if (e < RING) e = RING; return e > NSEQ ? NSEQ : e;
}
#define PACE 1
#define YDEF 1
#define WO0 16
#define WON 36
#define TA1 535
#define TA2 440
#define NK 68
#define ORG0 130
#define SLMAX 80
#define MAXGAP 70
constexpr int cmin(int a, int b) { return a < b ? a : b; }
#define RSH 1
#define RD 4
#define BMAP 1
#define EBIG 9999
#define NJ 54
#define TA3 515
constexpr int due_at(int n) { return (n <= NJ ? n * TA1 : n <= NK ? NJ * TA1 + (n - NJ) * TA3 : NJ * TA1 + (NK - NJ) * TA3 + (n - NK) * TA2) / 10; }
#define RTAB 20, 30, 40, 50, 60, 70
#define GTAB 0, 12, 6, 12, 12, 12
#define BTAB 8, 12, 6, 12, 12, 0
#define MISS 40
#define DB 10
#define DTAB 20, 20, 20, 15, 5, 3, 3, 3
constexpr int rtab[] = {RTAB}, gtab[] = {GTAB}, btab[] = {BTAB}, dtab[] = {DTAB};
constexpr int NRND = sizeof(rtab) / sizeof(int);
constexpr int rgap(int k) { return k ? rtab[k] - rtab[k - 1] : rtab[0] - RING; }
constexpr int rnd_at(int n) { for (int k = 0; k < NRND; ++k) if (rtab[k] == n) return k; return -1; }
static_assert(rtab[0] >= RING + 2 && rtab[NRND - 1] + RD + 2 < NSEQ, "");
constexpr int wo_step(int k) { return WO0 + k * WON / 32; }
#define WTS 54
#define PJ0 58
#define PJ1 63
#define PJ2 72
#define XW 13
#define XR 50
#define FDLY 8
#define FSL 2
#define CPFM 1
#define STAG 16
DI u32 rt32() { return (u32)__builtin_amdgcn_s_memrealtime(); }
constexpr int OOB = 0x7ffff000;
constexpr int L_AB = 32768;
DI void part_a(const Args& a, unsigned char* lds, int idx, u64 tag, u32 pf) {
  const u64 T64 = __builtin_amdgcn_s_memrealtime();
  __builtin_amdgcn_sched_barrier(0);
  const int tid = threadIdx.x, lane = tid & 63, w = __builtin_amdgcn_readfirstlane(tid >> 6);
  const int j = lane & 15, g = lane >> 4;
  const int h = (idx & 7) * 4 + ((idx >> 3) & 3), ci = idx >> 5;
  const int rr = (((h & 3) * 5) >> 1) + (h >> 2), rot = ROTM ? (rr >= NS ? rr - NS : rr) : (h * 5) >> 4, l0 = (ci * 37) >> 4, ln = ((ci * 37 + 37) >> 4) - l0;
  const int bad_sl = (w < ln) ? -1 : NS - 1 - rot;
  int vtab;
  { const int p = lane + rot >= NS ? lane + rot - NS : lane + rot;
    vtab = p == NS - 1 ? (w < ln ? (h << 20) + (1008 + l0 + w) * 1024 : 1 << 30) : (h << 20) + (p * 7 + ci) * 16384 + w * 1024; }
  const bool prod = ci < 2 * NP && w < 9 / NP;
  const int r0 = ((ci >> 1) * (9 / NP) + w) * 64;
  int voff = lane * 16, wqo = ((r0 + 32 * (j >> 3) + 8 * g) * 4096 + h * 128 + (ci & 1) * 64 + 8 * (j & 7)) * 2;
  int xo = j < 8 ? ((j & 3) * DM + r0 + 32 * (j >> 2) + 8 * g) * 2 : OOB;
  const int prow = (idx >> 3) * 40 + (w - 3) * 4;
  int pfo = (w >= 3 && w < 13 && prow < DM + DC && prow >= PSKIP) ? ((prow < DM ? prow : prow - DM) + g) * 8192 + (idx & 7) * 1024 + j * 64 : OOB;
  int so0 = __builtin_amdgcn_readlane(vtab, 0);
  asm volatile("" : "+v"(vtab), "+v"(voff), "+v"(wqo), "+v"(xo), "+v"(pfo), "+s"(so0));
  __builtin_amdgcn_sched_barrier(0);
  asm volatile("" :: "s"(a.xB), "s"(a.C), "s"(a.wqB), "s"(a.wuk), "s"(a.wuv), "s"(a.woB), "s"(T64));
  u32 T0 = (u32)T64;
  const u32 toff = (u32)(w * TA2) / (STAG * 10);
  u32* lorg = reinterpret_cast<u32*>(lds + L_ST + 2048);
  const u32 tg = (u32)(tag >> 32);
  u32 last = T0;
  u32x2 rv = {0u, 0u}; u32 rt = 0;
  const __amdgpu_buffer_rsrc_t rs = __builtin_amdgcn_make_buffer_rsrc(a.ws, 0, (int)WS_END, 0x00020000);
  const __amdgpu_buffer_rsrc_t rk = __builtin_amdgcn_make_buffer_rsrc(const_cast<u16*>(a.K), 0, 1 << 27, 0x00020000);
  const __amdgpu_buffer_rsrc_t rvv = __builtin_amdgcn_make_buffer_rsrc(const_cast<u16*>(a.V), 0, 1 << 27, 0x00020000);
  const __amdgpu_buffer_rsrc_t rw = __builtin_amdgcn_make_buffer_rsrc(const_cast<u16*>(a.wq), 0, DM * 4096 * 2, 0x00020000);
  const __amdgpu_buffer_rsrc_t rx = __builtin_amdgcn_make_buffer_rsrc(const_cast<u16*>(a.xA), 0, 4 * DM * 2, 0x00020000);
  u32x4 R[RING];
#define A_ISSUE(i) R[(i) % RING] = __builtin_amdgcn_raw_buffer_load_b128((i) >= 4 * NS ? rvv : rk, voff, ((i) == 0 ? so0 : __builtin_amdgcn_readlane(vtab, (i) % NS)) + ((((i) / NS) & 3) << 25), 2)
#define GATE(n) do { if (PACE) { u32 due = T0 + toff + org + ((osl * cmin(n, NK)) >> 4) + (u32)due_at(n); if (MAXGAP && (int)(due - (last + MAXGAP)) > 0) due = last + MAXGAP; \
    while ((int)(now - due) < 0) { __builtin_amdgcn_s_sleep(1); now = rt32(); } last = now; } } while (0)
#define CTRL(n) do { if constexpr (PACE) { \
    if constexpr (rnd_at((n) - RD - 2) >= 0) { constexpr int k = rnd_at((n) - RD - 2); \
      const u32 dt = (rv[1] == (u32)tag + k) ? rv[0] - T0 : rt; \
      const float sm = row16_sum((float)(int)dt); \
      const int o = (int)((RSH ? rdlane(sm, 0) * (1.f / 16.f) : (rdlane(sm, 0) + rdlane(sm, 16)) * (1.f / 32.f))) - due_at(rtab[k]); \
      if (w == ((5 * k + 11) & 15) && lane == 0 && o > -300 && o < 2000) { \
        int e = o - (int)org - (int)((osl * cmin(rtab[k], NK)) >> 4) - dtab[k]; if (e < 0) e = 0; \
        int sl_ = ((int)osl * gtab[k]) / 16 + ((e > EBIG ? 0 : e) * btab[k]) / rgap(k); sl_ = sl_ > SLMAX ? SLMAX : sl_; \
        __hip_atomic_store(reinterpret_cast<u64*>(lorg), __builtin_bit_cast(u64, u32x2{(u32)((int)org + e + ((((int)osl - sl_) * cmin(n, NK)) >> 4)), (u32)sl_}), __ATOMIC_RELAXED, __HIP_MEMORY_SCOPE_WORKGROUP); } } \
    if constexpr (rnd_at(n) >= 0) { constexpr int k = rnd_at(n); \
      u32 tot = 0; \
      if (ci == k % ACU && (h & ((1 << RSH) - 1)) == (k & ((1 << RSH) - 1))) { const u32 mine = (1u << 24) + ((rt32() - toff - T0) & 0xfffffu); \
        if (lane == 0) tot = __hip_atomic_fetch_add(lorg + 8 + k, mine, __ATOMIC_RELAXED, __HIP_MEMORY_SCOPE_WORKGROUP) + mine; } \
      __builtin_amdgcn_raw_buffer_store_b64(u32x2{T0 + ((tot & 0xffffffu) >> 4), (u32)tag + k}, rs, (tot >> 24) == 16u ? (int)WS_RPT + (h >> RSH) * 4352 : OOB, 128 * k * 35, 16); } \
    if constexpr (rnd_at((n) - RD) >= 0) { constexpr int k = rnd_at((n) - RD); \
      rv = __builtin_amdgcn_raw_buffer_load_b64(rs, (w == ((5 * k + 11) & 15) && lane < (32 >> RSH)) ? (int)WS_RPT + lane * 4352 : OOB, 128 * k * 35, 16); rt = rt32() - T0 - MISS; } \
  } } while (0)
  u32x4 Wr[8], xf;
  u32 pf2 = 0;
  if (BPF == 1) pf2 = __builtin_amdgcn_raw_buffer_load_b32(__builtin_amdgcn_make_buffer_rsrc(const_cast<u16*>(prow < DM ? a.wqB : a.wuk), 0, DM * 4096 * 2, 0x00020000), pfo, 0, 2);
  A_ISSUE(0);
  __builtin_amdgcn_sched_barrier(0);
  if (prod) {
#pragma unroll
    for (int k = 0; k < 8; ++k) Wr[k] = __builtin_amdgcn_raw_buffer_load_b128(rw, wqo, k * 8192, 2);
    xf = __builtin_amdgcn_raw_buffer_load_b128(rx, xo, 0, 2);
  }
  __builtin_amdgcn_sched_barrier(0);
  if (BPF == 2) pf2 = __builtin_amdgcn_raw_buffer_load_b32(__builtin_amdgcn_make_buffer_rsrc(const_cast<u16*>(prow < DM ? a.wqB : a.wuk), 0, DM * 4096 * 2, 0x00020000), pfo, 0, 2);
  if (!K1G) A_ISSUE(1);
  __builtin_amdgcn_raw_buffer_store_b64(u32x2{T0, (u32)tag + 15u}, rs, (ci == 6 && (h & 1) == 0 && tid == 0) ? (int)WS_RPT + (h >> 1) * 4352 : OOB, 128 * 15 * 35, 16);
  __builtin_amdgcn_sched_barrier(0);
  if (tid < 32) lorg[tid] = tid == 0 ? ORG0 : tid == 2 ? T0 : 0u;
  __syncthreads();
  T0 = __builtin_amdgcn_readfirstlane(lorg[2]);
  if (prod) {
    float* qpart = reinterpret_cast<float*>(lds + L_BIG);
#pragma unroll
    for (int hf = 0; hf < 2; ++hf) {
      f32x4 d4[4];
#pragma unroll
      for (int t = 0; t < 4; ++t) d4[t] = mfma16(xf, tcol16(Wr, 4 * hf + t), f32x4{0.f, 0.f, 0.f, 0.f});
      if (g == (j >> 3)) {
#pragma unroll
        for (int r = 0; r < 4; ++r) *reinterpret_cast<f32x4*>(qpart + ((w * 2 + g) * 4 + r) * 64 + 8 * (j & 7) + 4 * hf) = f32x4{d4[0][r], d4[1][r], d4[2][r], d4[3][r]};
      }
    }
    asm volatile("" ::: "memory");
    u32 arr = 0;
    if (lane == 0) arr = __hip_atomic_fetch_add(lorg + 4, 1u, __ATOMIC_RELAXED, __HIP_MEMORY_SCOPE_WORKGROUP);
    if (__builtin_amdgcn_readfirstlane(arr) == 9 / NP - 1) {
      asm volatile("" ::: "memory");
      f32x4 s0 = {0.f, 0.f, 0.f, 0.f};
#pragma unroll
      for (int k = 0; k < 18 / NP; ++k) s0 += *reinterpret_cast<const f32x4*>(qpart + k * 256 + 4 * lane);
      const int qo = (int)WS_QA + (h * 8 + ci) * 2048 + lane * 16;
      st16_sc1(rs, qo, u32x4{__float_as_uint(s0[0]), tg, __float_as_uint(s0[1]), tg});
      st16_sc1(rs, qo + 1024, u32x4{__float_as_uint(s0[2]), tg, __float_as_uint(s0[3]), tg});
    }
  }
  static_for<RING - 2 + K1G>([&](auto tc) __attribute__((always_inline)) {
    constexpr int t = tc.value + 2 - K1G;
    u32 now = rt32();
    const u32 org = __builtin_amdgcn_readfirstlane(__hip_atomic_load(lorg, __ATOMIC_RELAXED, __HIP_MEMORY_SCOPE_WORKGROUP)), osl = 0;
    GATE(t);
    A_ISSUE(t);
    __builtin_amdgcn_sched_barrier(0);
    if constexpr (t == SP) {
      if (w == 13) {
        const u32x2 sv = __builtin_amdgcn_raw_buffer_load_b64(rs, lane < 16 ? (int)WS_RPT + lane * 4352 : OOB, 128 * 15 * 35, 16);
        const bool ok = sv[1] == (u32)tag + 15u;
        const float sm = row16_sum(ok ? (float)(int)(sv[0] - T0) : 0.f), cn = row16_sum(ok ? 1.f : 0.f);
        const int o = ORG0 + (cn > 0.f ? (int)(sm / cn) : 0);
        if (lane == 0 && o > -300 && o < 1000) lorg[0] = (u32)o;
      }
    }
    if constexpr (t == QP) {
      if (w >= 14) {
        const int qo = (int)WS_QA + (h * 8 + (w & 1)) * 2048 + lane * 16;
        u32x4 v[2 * NP];
#pragma unroll 1
        for (u32 spins = 0; spins < SPIN_CAP; ++spins) {
          bool ok = true;
#pragma unroll
          for (int k = 0; k < 2 * NP; ++k) v[k] = __builtin_amdgcn_raw_buffer_load_b128(rs, qo, (k >> 1) * 4096 + (k & 1) * 1024, 16);
#pragma unroll
          for (int k = 0; k < 2 * NP; ++k) ok = ok && v[k][1] == tg && v[k][3] == tg;
          if (__all(ok)) break;
          __builtin_amdgcn_s_sleep(4);
        }
        f32x4 s0 = {0.f, 0.f, 0.f, 0.f};
#pragma unroll
        for (int k = 0; k < NP; ++k) s0 += f32x4{__uint_as_float(v[2 * k][0]), __uint_as_float(v[2 * k][2]), __uint_as_float(v[2 * k + 1][0]), __uint_as_float(v[2 * k + 1][2])};
        *reinterpret_cast<u32x2*>(lds + L_QS + (g * 128 + (w & 1) * 64 + 4 * j) * 2) = u32x2{pk_bf16(s0[0], s0[1]), pk_bf16(s0[2], s0[3])};
        asm volatile("" ::: "memory");
        if (lane == 0) __hip_atomic_fetch_add(lorg + 5, 1u, __ATOMIC_RELAXED, __HIP_MEMORY_SCOPE_WORKGROUP);
      }
    }
    __builtin_amdgcn_sched_barrier(0);
  });
  while (__hip_atomic_load(lorg + 5, __ATOMIC_RELAXED, __HIP_MEMORY_SCOPE_WORKGROUP) != 2u) __builtin_amdgcn_s_sleep(1);
  asm volatile("" :: "v"(pf), "v"(pf2) : "memory");
  float* wtab = reinterpret_cast<float*>(lds + L_ST + 1024);
  u32x2 xv = {0u, 0u};
  const int xoff = (w == XW && lane < 8 * ACU) ? (int)WS_MLA + (h * 8 + (lane >> 3)) * 128 + (lane & 7) * 8 : OOB;
  float* pml = reinterpret_cast<float*>(lds + L_ST);
  u32x4 q;
  float acc[8], S[4];
  u32x2 RW[4][8];
  u32x4 WT[4][4];
  float Y[4];
  auto project = [&](int b) __attribute__((always_inline)) {
    f32x4 d4[4];
#pragma unroll
    for (int t = 0; t < 4; ++t) d4[t] = f32x4{0.f, 0.f, 0.f, 0.f};
    const f32x4 wt = *reinterpret_cast<const f32x4*>(wtab + b * 16 + 4 * g);
#pragma unroll
    for (int s = 0; s < 4; ++s) {
      const u32x4 af = *reinterpret_cast<const u32x4*>(lds + L_AB + (b * 16 + j) * 272 + (32 * s + 8 * g) * 2);
#pragma unroll
      for (int t = 0; t < 4; ++t) d4[t] = mfma16(af, WT[s][t], d4[t]);
    }
    float y[4];
#pragma unroll
    for (int t = 0; t < 4; ++t) y[t] = fmaf(d4[t][0], wt[0], d4[t][1] * wt[1]) + fmaf(d4[t][2], wt[2], d4[t][3] * wt[3]);
    Y[b] = sw16(sw32(y[0], y[2]), sw32(y[1], y[3]));
  };
  const __amdgpu_buffer_rsrc_t rwo = __builtin_amdgcn_make_buffer_rsrc(const_cast<u16*>(a.wo + (size_t)h * 128 * DM), 0, w < 9 ? 128 * DM * 2 : 0, 0x00020000);
  const int wvo = (8 * g * DM + 64 * w + 4 * j) * 2;
#define WO_ISSUE() static_for<32>([&](auto kc) __attribute__((always_inline)) { constexpr int k = kc.value; \
    if constexpr (wo_step(k) == i) RW[k >> 3][k & 7] = __builtin_amdgcn_raw_buffer_load_b64(rwo, wvo, (32 * (k >> 3) + (k & 7)) * DM * 2, 2); })
  static_for<NSEQ>([&](auto ic) __attribute__((always_inline)) {
    constexpr int i = ic.value, bb = (i / NS) & 3, pass = i / (4 * NS), sl = i % NS;
    u32 now = 0; if (PACE) now = rt32();
    u32 org = 0, osl = 0; if (PACE) { const u32x2 ov = __builtin_bit_cast(u32x2, __hip_atomic_load(reinterpret_cast<u64*>(lorg), __ATOMIC_RELAXED, __HIP_MEMORY_SCOPE_WORKGROUP)); org = __builtin_amdgcn_readfirstlane(ov[0]); osl = __builtin_amdgcn_readfirstlane(ov[1]); }
    __builtin_amdgcn_sched_barrier(0);
    const u32x4 r = R[i % RING];
    if constexpr (pass == 0) {
      if constexpr (sl == 0) { q = *reinterpret_cast<const u32x4*>(lds + L_QS + (bb * 128 + 8 * j) * 2); S[bb] = -INFINITY; }
      float d = dot2(r[0], q[0], 0.f); d = dot2(r[1], q[1], d); d = dot2(r[2], q[2], d); d = dot2(r[3], q[3], d);
      asm volatile("" : "+v"(d));
      if constexpr (issued(i + 1) > issued(i)) { GATE(issued(i)); WO_ISSUE(); A_ISSUE(issued(i)); CTRL(issued(i)); if constexpr (issued(i + 1) > issued(i) + 1) { GATE(issued(i) + 1); A_ISSUE(issued(i) + 1); CTRL(issued(i) + 1); } } else { WO_ISSUE(); }
      d = row16_sum(d);
      S[bb] = (j == sl) ? d : S[bb];
      if constexpr (sl == NS - 1) {
        const float sc = (j == bad_sl) ? -INFINITY : S[bb] * C2;
        const float m = xg_max(row16_max(sc));
        S[bb] = __builtin_amdgcn_exp2f(sc - m);
        const float l = xg_sum(row16_sum(S[bb]));
        if (lane == 0) *reinterpret_cast<f32x2*>(pml + (bb * 16 + w) * 2) = f32x2{m, l};
      }
      if constexpr (i == 4 * NS - 1) {
        asm volatile("" ::: "memory");
        u32 arr = 0;
        if (lane == 0) arr = __hip_atomic_fetch_add(lorg + 6, 1u, __ATOMIC_RELAXED, __HIP_MEMORY_SCOPE_WORKGROUP);
        arr = __builtin_amdgcn_readfirstlane(arr);
        asm volatile("" ::: "memory");
        const f32x2 ml = *reinterpret_cast<const f32x2*>(pml + lane * 2);
        const float M = row16_max(ml[0]);
        const float L = row16_sum(__builtin_amdgcn_exp2f(ml[0] - M) * ml[1]);
        __builtin_amdgcn_raw_buffer_store_b64(u32x2{__float_as_uint(j ? L : M), tg}, rs, (arr == 15u && j < 2) ? (int)WS_MLA + (h * 8 + ci) * 128 + g * 16 + j * 8 : OOB, 0, 16);
      }
    } else {
      const float p = dpp<0x150 + sl>(S[bb]);
      if constexpr (sl == 0) {
#pragma unroll
        for (int k = 0; k < 8; ++k) acc[k] = 0.f;
      }
#pragma unroll
      for (int k = 0; k < 4; ++k) { acc[2 * k] = fmaf(p, bf_lo(r[k]), acc[2 * k]); acc[2 * k + 1] = fmaf(p, bf_hi(r[k]), acc[2 * k + 1]); }
      asm volatile("" : "+v"(acc[0]), "+v"(acc[1]), "+v"(acc[2]), "+v"(acc[3]), "+v"(acc[4]), "+v"(acc[5]), "+v"(acc[6]), "+v"(acc[7]));
      if constexpr (issued(i + 1) > issued(i)) { GATE(issued(i)); WO_ISSUE(); A_ISSUE(issued(i)); CTRL(issued(i)); if constexpr (issued(i + 1) > issued(i) + 1) { GATE(issued(i) + 1); A_ISSUE(issued(i) + 1); CTRL(issued(i) + 1); } } else { WO_ISSUE(); }
      if constexpr (i == XR) xv = __builtin_amdgcn_raw_buffer_load_b64(rs, xoff, 0, 16);
      if constexpr (i == XR + 2) {
        if (w == XW) {
          if (__builtin_expect(!__all(lane >= 8 * ACU || xv[1] == tg), 0)) {
#pragma unroll 1
            for (u32 spins = 0; spins < SPIN_CAP; ++spins) {
              __builtin_amdgcn_s_sleep(8); xv = __builtin_amdgcn_raw_buffer_load_b64(rs, xoff, 0, 16);
              if (__all(lane >= 8 * ACU || xv[1] == tg)) break;
            }
          }
          const float x = __uint_as_float(xv[0]);
          const float m = lane < 8 * ACU ? dpp<0xA0>(x) : -INFINITY, l = lane < 8 * ACU ? dpp<0xF5>(x) : 0.f;
          const float M = xg_max(fmaxf(m, dpp<0x128>(m)));
          float t = __builtin_amdgcn_exp2f(m - M) * l; t = xg_sum(t + dpp<0x128>(t));
          if (lane < 8 && !(lane & 1)) *reinterpret_cast<f32x2*>(wtab + 64 + lane) = f32x2{M, 1.f / t};
          asm volatile("" ::: "memory");
          const f32x2 gm = *reinterpret_cast<const f32x2*>(wtab + 64 + 2 * g);
          wtab[lane] = __builtin_amdgcn_exp2f(pml[lane * 2] - gm[0]) * gm[1];
          asm volatile("" ::: "memory");
          if (lane == 0) __hip_atomic_store(lorg + 7, 1u, __ATOMIC_RELAXED, __HIP_MEMORY_SCOPE_WORKGROUP);
        }
      }
      if constexpr (i >= WTS && i < WTS + 4) {
        constexpr int s = i - WTS;
#pragma unroll
        for (int t = 0; t < 4; ++t) WT[s][t] = tcol8(RW[s], t);
        asm volatile("" : "+v"(WT[s][0]), "+v"(WT[s][1]), "+v"(WT[s][2]), "+v"(WT[s][3]));
      }
      if constexpr (i == PJ0 || i == PJ1 || i == PJ2) {
        constexpr int b = i == PJ0 ? 0 : i == PJ1 ? 1 : 2;
        Y[b] = 0.f;
        if (w < 9) {
          while (__hip_atomic_load(lorg + 24 + b, __ATOMIC_RELAXED, __HIP_MEMORY_SCOPE_WORKGROUP) != 16u || __hip_atomic_load(lorg + 7, __ATOMIC_RELAXED, __HIP_MEMORY_SCOPE_WORKGROUP) == 0u) __builtin_amdgcn_s_sleep(1);
          asm volatile("" ::: "memory");
          project(b);
        }
        asm volatile("" : "+v"(Y[b]));
      }
      if constexpr (sl == NS - 1) {
        const float t0 = sw16(sw32(acc[0], acc[4]), sw32(acc[2], acc[6])), t1 = sw16(sw32(acc[1], acc[5]), sw32(acc[3], acc[7]));
        *reinterpret_cast<u32*>(lds + L_AB + (bb * 16 + w) * 272 + 16 * j + 4 * g) = pk_bf16(t0, t1);
        if constexpr (bb < 3) { asm volatile("" ::: "memory"); if (lane == 0) __hip_atomic_fetch_add(lorg + 24 + bb, 1u, __ATOMIC_RELAXED, __HIP_MEMORY_SCOPE_WORKGROUP); }
      }
    }
    __builtin_amdgcn_sched_barrier(0);
  });
#undef A_ISSUE
  __syncthreads();
  if (w < 9) {
    project(3);
    st16_sc1(rs, WS_Y + ((8 * w + (j >> 1)) * 256 + idx) * 128 + ((j & 1) * 4 + g) * 16, u32x4{pk_bf16(Y[0], Y[1]), tg, pk_bf16(Y[2], Y[3]), tg});
  }
  if (idx < 72) {
    __builtin_amdgcn_sched_barrier(0);
    __builtin_amdgcn_s_barrier();
    __builtin_amdgcn_s_sleep(FDLY);
    const int yo = (int)WS_Y + (idx * 256 + 16 * w) * 128 + lane * 16;
    u32x4 v0, v1;
#pragma unroll 1
    for (u32 spins = 0; spins < SPIN_CAP; ++spins) {
      v0 = __builtin_amdgcn_raw_buffer_load_b128(rs, yo, 0, 16); v1 = __builtin_amdgcn_raw_buffer_load_b128(rs, yo + 1024, 0, 16);
      if (__all(v0[1] == tg && v0[3] == tg && v1[1] == tg && v1[3] == tg)) break;
      __builtin_amdgcn_s_sleep(FSL);
    }
    float y = sw16(sw32(bf_lo(v0[0]) + bf_lo(v1[0]), bf_lo(v0[2]) + bf_lo(v1[2])), sw32(bf_hi(v0[0]) + bf_hi(v1[0]), bf_hi(v0[2]) + bf_hi(v1[2])));
    y += dpp<0x128>(y);
    float* red = reinterpret_cast<float*>(lds + L_PF);
    if (!(lane & 8)) red[((lane & 7) * 4 + g) * 20 + w] = y;
    __syncthreads();
    if (tid < 32) {
      const f32x4 r0 = *reinterpret_cast<const f32x4*>(red + tid * 20), r1 = *reinterpret_cast<const f32x4*>(red + tid * 20 + 4);
      const f32x4 r2 = *reinterpret_cast<const f32x4*>(red + tid * 20 + 8), r3 = *reinterpret_cast<const f32x4*>(red + tid * 20 + 12);
      const f32x4 r = r0 + r1 + r2;
      const __amdgpu_buffer_rsrc_t ro = __builtin_amdgcn_make_buffer_rsrc(a.out, 0, 4 * 1152 * 2, 0x00020000);
      __builtin_amdgcn_raw_buffer_store_b16(to_bf16(((r[0] + r[1]) + (r[2] + r[3])) + (r3[0] + r3[1])), ro, ((tid & 3) * 1152 + idx * 8 + (tid >> 2)) * 2, 0, OAUX);
      __builtin_amdgcn_raw_buffer_store_b16(to_bf16(r3[2] + r3[3]), ro, ((tid >> 3) * 1152 + 576 + idx * 8 + (tid & 7)) * 2, 0, OAUX);
    }
  }
}

#define IA 2
#define IB 4
#define IC 6
#define EUV 4
#define YOFF 90
#define WPD 4
template <bool LD> DI void part_b(const Args& a, unsigned char* lds, int wg, u64 tag, u32 pf) {
  const int tid = threadIdx.x, lane = tid & 63, w = __builtin_amdgcn_readfirstlane(tid >> 6);
  const int j = lane & 15, g = lane >> 4;
  const __amdgpu_buffer_rsrc_t rs = __builtin_amdgcn_make_buffer_rsrc(a.ws, 0, (int)WS_END, 0x00020000);
  const u32 tb0 = rt32();
  const int h = BPF ? (wg & 7) * 4 + (wg >> 3) : wg;
  const int h3 = h ^ 2;
  const int b2 = BMAP ? (wg & 7) >> 1 : wg >> 3, sp = BMAP ? (wg >> 3) * 2 + (wg & 1) : wg & 7;
  const u16* cl = a.C + ((size_t)b2 * SK + sp * 512 + 8 * (w - 8)) * DC + lane * 8;
  u32x4 X[16];
  u32x2 RW[4][8];
  u32x4 W8[2];
  u32* cnt = reinterpret_cast<u32*>(lds + L_ST + 4064);
#define C_ISSUE(T) _Pragma("unroll") for (int r = 0; r < 8; ++r) X[8 * ((T) & 1) + r] = ld16(cl + (size_t)(64 * (T) + r) * DC)
#define C_HALF(T, hf) _Pragma("unroll") for (int r = 4 * (hf); r < 4 * (hf) + 4; ++r) X[8 * ((T) & 1) + r] = ld16(cl + (size_t)(64 * (T) + r) * DC)
#define L_PART(T, r0, r1) do { __builtin_amdgcn_sched_barrier(0); \
    if ((T) + 3 < 8) { _Pragma("unroll") for (int r = r0; r < r1; ++r) X[8 * (((T) + 3) & 1) + r] = ld16(cl + (size_t)(64 * ((T) + 3) + r) * DC); } \
    if ((T) == 5 || (T) == 6) { _Pragma("unroll") for (int k = 2 * (r0); k < 2 * (r1); ++k) \
      RW[2 * (((T) - 5) & 1) + (k >> 3)][k & 7] = ld8(a.woB + ((size_t)h3 * 128 + 32 * (2 * (((T) - 5) & 1) + (k >> 3)) + 8 * g + (k & 7)) * DM + 64 * (w - 8) + 4 * j); } \
    __builtin_amdgcn_sched_barrier(0); } while (0)
#define UV_LOAD(n) do { const u32x4 v_ = ld16(a.wuv + ((size_t)(64 * w + 32 * ((n) >> 3) + 8 * g + ((n) & 7)) * H + h3) * 128 + 8 * j); \
    RW[2 * ((n) >> 3)][(n) & 7] = u32x2{v_[0], v_[1]}; RW[2 * ((n) >> 3) + 1][(n) & 7] = u32x2{v_[2], v_[3]}; } while (0)
#define C_WIN(T, r0, r1) _Pragma("unroll") for (int r = r0; r < r1; ++r) { asm volatile("s_waitcnt vmcnt(%0)" :: "n"(WPD - 1) : "memory"); X[8 * ((T) & 1) + r] = ld16(cl + (size_t)(64 * (T) + r) * DC); __builtin_amdgcn_sched_barrier(0); }
  u32 sb0 = L_BIG + 8 * (w - 8) * CRS + lane * 16, sb1 = sb0 + CBUF;
  asm volatile("" : "+v"(sb0), "+v"(sb1));
#define C_STAGE(T) _Pragma("unroll") for (int r = 0; r < 8; ++r) *reinterpret_cast<u32x4*>(lds + (((T) & 1) ? sb1 : sb0) + r * CRS) = X[8 * ((T) & 1) + r]
  {
    const u16* Wq = a.wqB + h * 128;
    u32x4 U[8];
    QPre Q;
    qproj_load(Q, a.xB, Wq, w, lane);
#pragma unroll
    for (int i = 0; i < 8; ++i) U[i] = ld16(a.wuk + ((size_t)(32 * w + 4 * i + (j & 3)) * H + h) * 128 + 8 * ((j & 12) + g));
    __builtin_amdgcn_sched_barrier(0);
    if constexpr (!LD) am_zero(lds, tid);
    qproj_mfma(Q, lds, w, lane);
    asm volatile("" :: "v"(pf));
    __syncthreads();
    if constexpr (LD) { C_HALF(0, 0); }
    else {
      const float* qpart = reinterpret_cast<const float*>(lds + L_BIG);
      float s = 0.f;
#pragma unroll
      for (int ww = 0; ww < 16; ++ww) s += qpart[ww * 512 + tid];
      reinterpret_cast<u16*>(lds + L_QS)[tid] = to_bf16(s);
    }
    __builtin_amdgcn_sched_barrier(0);
    __syncthreads();
    const u32x4 af = *reinterpret_cast<const u32x4*>(lds + L_QS + ((j >> 2) * 128 + ((j & 3) * 4 + g) * 8) * 2);
    u16* qa = reinterpret_cast<u16*>(lds + L_PF);
    float v2[2] = {0.f, 0.f};
#pragma unroll
    for (int i = 0; i < 8; ++i) {
      const f32x4 d4 = mfma16(af, U[i], f32x4{0.f, 0.f, 0.f, 0.f});
      float d = (j & 8) ? ((j & 4) ? d4[3] : d4[2]) : ((j & 4) ? d4[1] : d4[0]);
      d += dpp<0x124>(d); d += dpp<0x128>(d);
      v2[i >> 2] = ((j >> 2) == (i & 3)) ? d : v2[i >> 2];
    }
    qa[g * 512 + 32 * w + j] = to_bf16(v2[0]); qa[g * 512 + 32 * w + 16 + j] = to_bf16(v2[1]);
    __builtin_amdgcn_sched_barrier(0);
    __syncthreads();
    if constexpr (!LD) {
      if (w == 0) {
#pragma unroll
        for (int bb = 0; bb < 4; ++bb) st16_sc1(rs, WS_QABS + ((bb * H + h) * 512 + lane * 8) * 2, *reinterpret_cast<const u32x4*>(qa + bb * 512 + lane * 8));
      }
      __builtin_amdgcn_sched_barrier(0);
      __builtin_amdgcn_s_barrier();
      if (w == 0) {
        asm volatile("s_waitcnt vmcnt(0)" ::: "memory");
        if (lane == 0) { __hip_atomic_store(reinterpret_cast<u64*>(a.ws + WS_F1 + wg * 8), tag, RLX_AGENT); cnt[0] = 0u; cnt[1] = 0u; }
      }
    } else {
      __builtin_amdgcn_s_barrier();
      C_WIN(0, 4, 8); C_WIN(1, 0, 8);
      C_STAGE(0);
      __builtin_amdgcn_sched_barrier(0);
      C_WIN(2, 0, 8);
    }
  }
  {
    const int b = b2;
    if constexpr (!LD) wg_wait64(a.ws + WS_F1, tag); else __syncthreads();
    const int kt = w & 3, hh = (w >> 2) & 1;
    float* stmax = reinterpret_cast<float*>(lds + L_ST);
    float* stsum = stmax + 128;
    float* salpha = stmax + 256;
    unsigned char* Pf = lds + L_PF;
    float M = -INFINITY, lsum = 0.f;
    f32x4 acc[2][2];
#pragma unroll
    for (int ct = 0; ct < 2; ++ct) { acc[ct][0] = f32x4{0.f, 0.f, 0.f, 0.f}; acc[ct][1] = f32x4{0.f, 0.f, 0.f, 0.f}; }
    const int troff = (4 * g + (j >> 2)) * CRS + (32 * w + 4 * (j & 3)) * 2;
    auto pv_step = [&](const unsigned char* cb) __attribute__((always_inline)) {
      const f32x4 al0 = *reinterpret_cast<const f32x4*>(salpha + 4 * g), al1 = *reinterpret_cast<const f32x4*>(salpha + 16 + 4 * g);
#pragma unroll
      for (int ct = 0; ct < 2; ++ct) { acc[ct][0] *= al0; acc[ct][1] *= al1; }
#pragma unroll
      for (int blk = 0; blk < 2; ++blk) {
        const u32x4 p0 = *reinterpret_cast<const u32x4*>(Pf + ((blk * 2 + 0) * 64 + lane) * 16);
        const u32x4 p1 = *reinterpret_cast<const u32x4*>(Pf + ((blk * 2 + 1) * 64 + lane) * 16);
#pragma unroll
        for (int ct = 0; ct < 2; ++ct) {
          const u32x2 lo = trread(cb + troff + (32 * blk) * CRS + 32 * ct);
          const u32x2 hi = trread(cb + troff + (32 * blk + 16) * CRS + 32 * ct);
          const u32x4 bf = {lo[0], lo[1], hi[0], hi[1]};
          acc[ct][0] = mfma16(p0, bf, acc[ct][0]); acc[ct][1] = mfma16(p1, bf, acc[ct][1]);
        }
      }
    };
    unsigned char* Qf = lds + L_BIG + CBUF;
    if constexpr (!LD) {
      u32x4 qv[4];
#pragma unroll
      for (int k = 0; k < 4; ++k) qv[k] = __builtin_amdgcn_raw_buffer_load_b128(rs, (int)WS_QABS + (b * H * 512 + (tid + 512 * k) * 8) * 2, 0, 16);
#pragma unroll
      for (int k = 0; k < 4; ++k) { const int e = tid + 512 * k, head = e >> 6, ch = e & 63;
        *reinterpret_cast<u32x4*>(Qf + ((head >> 4) * 16 + (ch >> 2)) * 1040 + ((ch & 3) * 16 + (head & 15)) * 16) = qv[k]; }
    }
    __syncthreads();
    if constexpr (!LD) {
      u32x4 QF[16];
#pragma unroll
      for (int s = 0; s < 16; ++s) QF[s] = *reinterpret_cast<const u32x4*>(Qf + (hh * 16 + s) * 1040 + lane * 16);
      auto s_tile = [&](const unsigned char* cb, bool last) __attribute__((always_inline)) {
        __syncthreads();
        f32x4 s0 = {0.f, 0.f, 0.f, 0.f}, s1 = {0.f, 0.f, 0.f, 0.f};
#pragma unroll
        for (int s = 0; s < 16; s += 2) {
          s0 = mfma16(*reinterpret_cast<const u32x4*>(cb + (16 * kt + j) * CRS + (32 * s + 8 * g) * 2), QF[s], s0);
          s1 = mfma16(*reinterpret_cast<const u32x4*>(cb + (16 * kt + j) * CRS + (32 * s + 32 + 8 * g) * 2), QF[s + 1], s1);
        }
        const f32x4 sv = (s0 + s1) * C2;
        if (last) { __builtin_amdgcn_sched_barrier(0); _Pragma("unroll") for (int n = 0; n < EUV; ++n) UV_LOAD(n); __builtin_amdgcn_sched_barrier(0); }
        const float v = xg_max(fmaxf(fmaxf(sv[0], sv[1]), fmaxf(sv[2], sv[3])));
        if (g == 0) stmax[kt * 32 + 16 * hh + j] = v;
        __syncthreads();
        const float* sm = stmax + 16 * hh + j;
        const float Mn = fmaxf(fmaxf(M, fmaxf(sm[0], sm[32])), fmaxf(sm[64], sm[96]));
        const float alpha = __builtin_amdgcn_exp2f(M - Mn);
        M = Mn;
        u32x2 pk;
        pk[0] = pk_bf16(__builtin_amdgcn_exp2f(sv[0] - Mn), __builtin_amdgcn_exp2f(sv[1] - Mn));
        pk[1] = pk_bf16(__builtin_amdgcn_exp2f(sv[2] - Mn), __builtin_amdgcn_exp2f(sv[3] - Mn));
        lsum = lsum * alpha + ((bf_lo(pk[0]) + bf_hi(pk[0])) + (bf_lo(pk[1]) + bf_hi(pk[1])));
        *reinterpret_cast<u32x2*>(Pf + (((kt >> 1) * 2 + hh) * 64 + lane) * 16 + (kt & 1) * 8) = pk;
        if (kt == 0 && g == 0) salpha[16 * hh + j] = alpha;
        __syncthreads();
        pv_step(cb);
      };
#pragma unroll 1
      for (int T = 0; T < 6; T += 2) { s_tile(lds + L_BIG, false); s_tile(lds + L_BIG + CBUF, false); }
      s_tile(lds + L_BIG, false); s_tile(lds + L_BIG + CBUF, true);
    } else {
#pragma unroll
      for (int T = 0; T < 8; ++T) {
        __syncthreads();
        if (T + 1 < 8) { C_STAGE(T + 1); }
        L_PART(T, 0, IA);
        if (T == 7) {
#pragma unroll
          for (int k = 0; k < 2; ++k) W8[k] = ld16(a.woB + ((size_t)h3 * 128 + 16 * (w - 8) + 8 * k + (lane >> 3)) * DM + 512 + (lane & 7) * 8);
        }
        __syncthreads();
        L_PART(T, IA, IB);
        __syncthreads();
        L_PART(T, IB, IC);
        pv_step(lds + L_BIG + (T & 1) * CBUF);
        L_PART(T, IC, 8);
      }
    }
#undef C_ISSUE
#undef C_STAGE
    __syncthreads();
    if constexpr (!LD) { lsum = xg_sum(lsum); if (g == 0) stsum[kt * 32 + 16 * hh + j] = lsum; if (kt == 0 && g == 0) salpha[32 + 16 * hh + j] = M; }
    float* tb = reinterpret_cast<float*>(lds + L_BIG);
#pragma unroll
    for (int ct = 0; ct < 2; ++ct)
#pragma unroll
      for (int h2 = 0; h2 < 2; ++h2)
#pragma unroll
        for (int r = 0; r < 4; ++r) tb[(16 * h2 + 4 * g + r) * 516 + 32 * w + 16 * ct + j] = acc[ct][h2][r];
    if constexpr (!LD) { for (int i = tid; i < 8 * AM2_LD * 2 / 16; i += 512) reinterpret_cast<u32x4*>(lds + L_AM2 + 8 * AM2_LD * 2)[i] = u32x4{0u, 0u, 0u, 0u}; }
    __syncthreads();
    if constexpr (LD) {
      const int t2 = tid - 512;
#pragma unroll
      for (int k = 0; k < 4; ++k) {
        const int e = t2 + 512 * k, head = e >> 6, c8 = e & 63;
        const f32x4 v0 = *reinterpret_cast<const f32x4*>(tb + head * 516 + 8 * c8), v1 = *reinterpret_cast<const f32x4*>(tb + head * 516 + 8 * c8 + 4);
        __builtin_amdgcn_raw_buffer_store_b128(u32x4{pk_bf16(v0[0], v0[1]), pk_bf16(v0[2], v0[3]), pk_bf16(v1[0], v1[1]), pk_bf16(v1[2], v1[3])}, rs, (int)WS_CTXP + (((b * NSP + sp) * 32 + head) * 512 + 8 * c8) * 2, 0, 16);
      }
      if (t2 < 32) {
        const float L = (stsum[t2] + stsum[32 + t2]) + (stsum[64 + t2] + stsum[96 + t2]);
        st8_sc1(a.ws + WS_MLB + ((size_t)(b * NSP + sp) * 32 + t2) * 8, f32x2{salpha[32 + t2], L});
      }
      __builtin_amdgcn_sched_barrier(0);
      __builtin_amdgcn_s_barrier();
      asm volatile("s_waitcnt vmcnt(0)" ::: "memory");
      if (lane == 0) {
        if (__hip_atomic_fetch_add(cnt, 1u, __ATOMIC_RELAXED, __HIP_MEMORY_SCOPE_WORKGROUP) == 7u) {
          __hip_atomic_store(reinterpret_cast<u64*>(a.ws + WS_F2 + wg * 8), tag, RLX_AGENT);
        }
      }
#pragma unroll
      for (int k = 0; k < 2; ++k) *reinterpret_cast<u32x4*>(lds + L_W8 + (16 * (w - 8) + 8 * k + (lane >> 3)) * 144 + (lane & 7) * 16) = W8[k];
    } else {
      __builtin_amdgcn_s_barrier();
      static_for<16 - EUV>([&](auto ic) __attribute__((always_inline)) {
        asm volatile("s_waitcnt vmcnt(%0)" :: "n"(WPD - 1) : "memory");
        UV_LOAD(EUV + ic.value);
        __builtin_amdgcn_sched_barrier(0);
      });
    }
  }
  {
    int tid = threadIdx.x; asm volatile("" : "+v"(tid));
    const int lane = tid & 63, j = lane & 15, g = lane >> 4;
    u16* Am2 = reinterpret_cast<u16*>(lds + L_AM2);
    float* cpart = reinterpret_cast<float*>(lds + L_CP);
    __builtin_amdgcn_sched_barrier(0);
    if constexpr (LD) {
      if (w == 8) {
        wave_wait(reinterpret_cast<const u64*>(a.ws + WS_F2) + (lane & (NBW - 1)), true, tag);
        asm volatile("s_waitcnt vmcnt(0)" ::: "memory");
        if (lane == 0) __hip_atomic_store(cnt + 1, 1u, __ATOMIC_RELEASE, __HIP_MEMORY_SCOPE_WORKGROUP);
      } else {
        for (u32 spins = 0; spins < 64u * SPIN_CAP; ++spins) { if (__hip_atomic_load(cnt + 1, __ATOMIC_ACQUIRE, __HIP_MEMORY_SCOPE_WORKGROUP) != 0u) break; __builtin_amdgcn_s_sleep(1); }
      }
      __builtin_amdgcn_sched_barrier(0);
      const int b = (tid >> 7) & 3, c4 = tid & 127;
      u32x2 pv[NSP];
      const f32x2 ml = __builtin_bit_cast(f32x2, __builtin_amdgcn_raw_buffer_load_b64(rs, (int)WS_MLB + ((b * NSP + (j & 7)) * 32 + h3) * 8, 0, 16));
#pragma unroll
      for (int s = 0; s < NSP; ++s) pv[s] = __builtin_amdgcn_raw_buffer_load_b64(rs, (int)WS_CTXP + (((b * NSP + s) * 32 + h3) * 512 + 4 * c4) * 2, 0, 16);
      const float M = row16_max(ml[0]);
      float wt = __builtin_amdgcn_exp2f(ml[0] - M);
      wt *= 2.f / row16_sum(wt * ml[1]);
      float c0 = 0.f, c1 = 0.f, c2 = 0.f, c3 = 0.f;
#pragma unroll
      for (int s = 0; s < NSP; ++s) { const float ws_ = rdlane(wt, s); c0 = fmaf(ws_, bf_lo(pv[s][0]), c0); c1 = fmaf(ws_, bf_hi(pv[s][0]), c1); c2 = fmaf(ws_, bf_lo(pv[s][1]), c2); c3 = fmaf(ws_, bf_hi(pv[s][1]), c3); }
      const u32 h0 = pk_bf16(c0, c1), h1 = pk_bf16(c2, c3);
      *reinterpret_cast<u32x2*>(Am2 + (2 * b) * AM2_LD + 4 * c4) = u32x2{h0, h1};
      *reinterpret_cast<u32x2*>(Am2 + (2 * b + 1) * AM2_LD + 4 * c4) = u32x2{pk_bf16(c0 - bf_lo(h0), c1 - bf_hi(h0)), pk_bf16(c2 - bf_lo(h1), c3 - bf_hi(h1))};
      __syncthreads();
      __syncthreads();
      const int t2 = tid - 512;
      float s = 0.f;
#pragma unroll
      for (int ww = 0; ww < 8; ++ww) s += cpart[ww * 512 + t2];
      u16* Am = reinterpret_cast<u16*>(lds + L_AM);
      const int bb = t2 >> 7, d = t2 & 127;
      const u16 hi = to_bf16(s);
      Am[(4 * bb) * AM_LD + d] = hi; Am[(4 * bb + 1) * AM_LD + d] = to_bf16(s - bf2f(hi));
    } else {
      __syncthreads();
      f32x4 acc[8];
#pragma unroll
      for (int t = 0; t < 8; ++t) acc[t] = f32x4{0.f, 0.f, 0.f, 0.f};
#pragma unroll
      for (int k2 = 0; k2 < 2; ++k2) {
        const u32x4 af = *reinterpret_cast<const u32x4*>(Am2 + j * AM2_LD + 64 * w + 32 * k2 + 8 * g);
#pragma unroll
        for (int t = 0; t < 8; ++t) acc[t] = mfma16(af, tcol8(RW[2 * k2 + (t >> 2)], t & 3), acc[t]);
      }
      if (g < 2) {
#pragma unroll
        for (int bb = 0; bb < 2; ++bb) {
          float* p = cpart + (w * 4 + 2 * g + bb) * 128 + 8 * j;
          *reinterpret_cast<f32x4*>(p) = f32x4{acc[0][2 * bb] + acc[0][2 * bb + 1], acc[1][2 * bb] + acc[1][2 * bb + 1], acc[2][2 * bb] + acc[2][2 * bb + 1], acc[3][2 * bb] + acc[3][2 * bb + 1]};
          *reinterpret_cast<f32x4*>(p + 4) = f32x4{acc[4][2 * bb] + acc[4][2 * bb + 1], acc[5][2 * bb] + acc[5][2 * bb + 1], acc[6][2 * bb] + acc[6][2 * bb + 1], acc[7][2 * bb] + acc[7][2 * bb + 1]};
        }
      }
      __builtin_amdgcn_sched_barrier(0);
      __syncthreads();
      if (YDEF == 1 && w == 1) {
        u32 dl = tb0 + 5200u;
#pragma unroll 1
        while ((int)(rt32() - dl) < 0) {
          const u32x2 rv = __builtin_amdgcn_raw_buffer_load_b64(rs, lane < 16 ? (int)WS_RPT + lane * 4352 : OOB, 128 * (NRND - 1) * 35, 16);
          const bool ok = rv[1] == (u32)tag + (NRND - 1);
          const float cn = rdlane(row16_sum(ok ? 1.f : 0.f), 0);
          if (cn >= 8.f) { dl = tb0 + (u32)(int)(rdlane(row16_sum(ok ? (float)(int)(rv[0] - tb0) : 0.f), 0) / cn) + (u32)(due_at(NSEQ - 1) - due_at(rtab[NRND - 1]) + YOFF); break; }
          __builtin_amdgcn_s_sleep(32);
        }
        if (lane == 0) cnt[2] = (int)(dl - tb0) > 5200 ? tb0 : dl;
      }
      if (w == 0) {
#pragma unroll
        for (int s = 0; s < 4; ++s)
#pragma unroll
          for (int jj = 0; jj < 8; ++jj) RW[s][jj] = *reinterpret_cast<const u32x2*>(lds + L_W8 + (32 * s + 8 * g + jj) * 144 + 8 * j);
      }
    }
    __syncthreads();
    if (LD || w == 0) {
      f32x4 ya[4];
      wo_mfma(RW, lds, lane, ya);
      if (YDEF) { const u32 dl = YDEF == 1 ? __builtin_amdgcn_readfirstlane(cnt[2]) : tb0 + YDEF; while ((int)(rt32() - dl) < 0) __builtin_amdgcn_s_sleep(4); }
      st16_sc1(rs, WS_Y + ((8 * (LD ? w - 8 : 8) + (j >> 1)) * 256 + NAW + h3) * 128 + g * 32 + (j & 1) * 16, yrow(ya, (u32)(tag >> 32)));
    }
  }
}

__global__ void __launch_bounds__(1024, 4) k_main(Args a) {
  __shared__ __attribute__((aligned(16))) unsigned char lds[L_TOTAL];
  u32 pf;
  { u64 pc; u32 len;
    asm volatile("s_getpc_b64 %0\n.Lpf%=:\n\ts_mov_b32 %1, k_tail_mark-.Lpf%=" : "=s"(pc), "=s"(len));
    pf = __builtin_amdgcn_raw_buffer_load_b32(__builtin_amdgcn_make_buffer_rsrc(reinterpret_cast<void*>(pc), 0, len < 131072u ? len : 0u, 0x00020000), CPFM ? (threadIdx.x < 16 ? ((blockIdx.x >> 3) * 16 + threadIdx.x) * 128 : 0x7ffff000) : threadIdx.x * 128, 0, 0);
    __builtin_amdgcn_sched_barrier(0); }
  u64 tag = (hw_dispatch_id() + 1ull) * 0x9E3779B97F4A7C15ull;
  tag ^= ((u64)__builtin_amdgcn_dispatch_ptr() >> 6) * 0xC2B2AE3D27D4EB4Full;
  tag |= 1ull << 63;
  if (__builtin_expect(blockIdx.x >= NBW, 1)) part_a(a, lds, blockIdx.x - NBW, tag, pf);
  else if (__builtin_amdgcn_readfirstlane(threadIdx.x >> 6) < 8) part_b<false>(a, lds, blockIdx.x, tag, pf);
  else part_b<true>(a, lds, blockIdx.x, tag, pf);
}

extern "C" __global__ void k_tail_mark() {}

extern "C" void kernel_launch(void* const* d_in, const int* in_sizes, int n_in, void* d_out, int out_size, void* d_ws, size_t ws_size,
                              hipStream_t stream) {
  Args a;
  a.xA = (const u16*)d_in[0]; a.K = (const u16*)d_in[1]; a.V = (const u16*)d_in[2]; a.wq = (const u16*)d_in[3]; a.wo = (const u16*)d_in[4];
  a.xB = (const u16*)d_in[5]; a.C = (const u16*)d_in[6]; a.wqB = (const u16*)d_in[7]; a.wuk = (const u16*)d_in[8]; a.wuv = (const u16*)d_in[9]; a.woB = (const u16*)d_in[10];
#define WSTOP 32
  if (WSTOP && ws_size >= ((size_t)WSTOP << 20)) d_ws = (unsigned char*)d_ws + ((ws_size - ((size_t)WSTOP << 20)) & ~(size_t)4095);
  a.ws = (unsigned char*)d_ws;
  a.out = (u16*)d_out;
  k_main<<<NBW + NAW, 1024, 0, stream>>>(a);
}
```

```cpp
#define DIAG 0
#include <hip/hip_runtime.h>
#include <stdint.h>
#include <utility>

typedef unsigned short u16;
typedef unsigned u32;
typedef unsigned long long u64;
typedef u32 u32x4 __attribute__((ext_vector_type(4)));
typedef u32 u32x2 __attribute__((ext_vector_type(2)));
typedef float f32x4 __attribute__((ext_vector_type(4)));
typedef float f32x2 __attribute__((ext_vector_type(2)));
typedef short s16x4 __attribute__((ext_vector_type(4)));
typedef __bf16 bf16x8 __attribute__((ext_vector_type(8)));
typedef __bf16 bf16x2 __attribute__((ext_vector_type(2)));
#define DI __device__ __forceinline__
#define RLX_AGENT __ATOMIC_RELAXED, __HIP_MEMORY_SCOPE_AGENT
extern "C" __device__ u64 hw_dispatch_id() __asm("llvm.amdgcn.dispatch.id");

template <int... Is, class F> DI void static_for_impl(std::integer_sequence<int, Is...>, F&& f) { (f(std::integral_constant<int, Is>{}), ...); }
template <int N, class F> DI void static_for(F&& f) { static_for_impl(std::make_integer_sequence<int, N>{}, f); }

constexpr int H = 32, SK = 4096, DM = 576, DC = 512;
constexpr int ACU = 7;
constexpr int NBW = 32;
constexpr int NAW = H * ACU;
constexpr int NSP = 8;
constexpr float C2 = 0.08838834764831845f * 1.4426950408889634f;

constexpr size_t WS_F1 = 0, WS_F2 = 512;
constexpr size_t WS_QABS = 4096;
constexpr size_t WS_CTXP = WS_QABS + 4 * 32 * 512 * 2;
constexpr size_t WS_MLB = WS_CTXP + (size_t)4 * NSP * 32 * 512 * 2;
constexpr size_t WS_Y = WS_MLB + 4 * NSP * 32 * 2 * 4;
constexpr size_t WS_MLA = WS_Y + (size_t)72 * 256 * 128;
constexpr size_t WS_RPT = WS_MLA + 256 * 128;
constexpr size_t WS_QA = WS_RPT + 64 * 4480;
constexpr size_t WS_END = WS_QA + 256 * 2048;

constexpr int CRS = 1056;
constexpr int CBUF = 64 * CRS;
constexpr int L_BIG = 0;
constexpr int L_AM = 2 * CBUF;
constexpr int L_QS = L_AM + 16 * 272;
constexpr int L_ST = L_QS + 1024;
constexpr int L_PF = L_ST + 4096;
constexpr int L_TOTAL = L_PF + 4096;
constexpr int AM_LD = 136;
constexpr int AM2_LD = 520;
constexpr int L_AM2 = 69632, L_CP = 88064, L_W8 = 106496;

#define ST(i) do { if (threadIdx.x == 0) reinterpret_cast<u32*>(lds + L_ST + 3072)[i] = (u32)__builtin_amdgcn_s_memrealtime(); } while (0)
struct Args {
  const u16 *xA, *K, *V, *wq, *wo, *xB, *C, *wqB, *wuk, *wuv, *woB;
  unsigned char* ws;
  u16* out;
};

DI float bf_lo(u32 x) { return __uint_as_float(x << 16); }
DI float bf_hi(u32 x) { return __uint_as_float(x & 0xffff0000u); }
DI u32 pk_bf16(float lo, float hi) { bf16x2 v = {(__bf16)lo, (__bf16)hi}; return __builtin_bit_cast(u32, v); }
DI u16 to_bf16(float x) { return __builtin_bit_cast(u16, (__bf16)x); }
DI float bf2f(u16 x) { return __uint_as_float(((u32)x) << 16); }
DI float dot2(u32 a, u32 b, float c) { return __builtin_amdgcn_fdot2_f32_bf16(__builtin_bit_cast(bf16x2, a), __builtin_bit_cast(bf16x2, b), c, false); }
template <int CTRL> DI float dpp(float x) { return __builtin_bit_cast(float, __builtin_amdgcn_update_dpp(0, __builtin_bit_cast(int, x), CTRL, 0xf, 0xf, true)); }
DI float row16_sum(float x) { x += dpp<0x128>(x); x += dpp<0x124>(x); x += dpp<0x122>(x); x += dpp<0x121>(x); return x; }
DI float row16_max(float x) { x = fmaxf(x, dpp<0x128>(x)); x = fmaxf(x, dpp<0x124>(x)); x = fmaxf(x, dpp<0x122>(x)); x = fmaxf(x, dpp<0x121>(x)); return x; }
DI float xg_max(float x) {
  auto s = __builtin_amdgcn_permlane16_swap(__float_as_uint(x), __float_as_uint(x), false, false);
  x = fmaxf(__uint_as_float(s[0]), __uint_as_float(s[1]));
  auto t = __builtin_amdgcn_permlane32_swap(__float_as_uint(x), __float_as_uint(x), false, false);
  return fmaxf(__uint_as_float(t[0]), __uint_as_float(t[1]));
}
DI float xg_sum(float x) {
  auto s = __builtin_amdgcn_permlane16_swap(__float_as_uint(x), __float_as_uint(x), false, false);
  x = __uint_as_float(s[0]) + __uint_as_float(s[1]);
  auto t = __builtin_amdgcn_permlane32_swap(__float_as_uint(x), __float_as_uint(x), false, false);
  return __uint_as_float(t[0]) + __uint_as_float(t[1]);
}
DI float sw32(float a, float b) {
  auto s = __builtin_amdgcn_permlane32_swap(__float_as_uint(a), __float_as_uint(b), false, false);
  return __uint_as_float(s[0]) + __uint_as_float(s[1]);
}
DI float sw16(float a, float b) {
  auto s = __builtin_amdgcn_permlane16_swap(__float_as_uint(a), __float_as_uint(b), false, false);
  return __uint_as_float(s[0]) + __uint_as_float(s[1]);
}
DI f32x4 mfma16(u32x4 a, u32x4 b, f32x4 c) { return __builtin_amdgcn_mfma_f32_16x16x32_bf16(__builtin_bit_cast(bf16x8, a), __builtin_bit_cast(bf16x8, b), c, 0, 0, 0); }
DI u32x4 ld16(const u16* p) { return __builtin_nontemporal_load(reinterpret_cast<const u32x4*>(p)); }
DI u32x2 ld8(const u16* p) { return __builtin_nontemporal_load(reinterpret_cast<const u32x2*>(p)); }
DI u32x2 trread(const unsigned char* p) {
  return __builtin_bit_cast(u32x2, __builtin_amdgcn_ds_read_tr16_b64_v4i16((__attribute__((address_space(3))) s16x4*)(p)));
}
DI u32 prm(u32 hi, u32 lo, int odd) { return __builtin_amdgcn_perm(hi, lo, odd ? 0x07060302u : 0x05040100u); }

DI u32x4 tcol16(const u32x4 (&R)[8], int t) {
  u32x4 T;
#pragma unroll
  for (int q = 0; q < 4; ++q) T[q] = prm(R[2 * q + 1][t >> 1], R[2 * q][t >> 1], t & 1);
  return T;
}
DI u32x4 tcol8(const u32x2 (&R)[8], int t) {
  u32x4 T;
#pragma unroll
  for (int q = 0; q < 4; ++q) T[q] = prm(R[2 * q + 1][t >> 1], R[2 * q][t >> 1], t & 1);
  return T;
}

#define SPINLOG 22
#define PSL 2
constexpr u32 SPIN_CAP = 1u << SPINLOG;
DI void wave_wait(const u64* f, bool active, u64 tag) {
  for (u32 spins = 0; spins < SPIN_CAP; ++spins) {
    const u64 v = active ? __hip_atomic_load(f, RLX_AGENT) : tag;
    if (__all(v == tag)) break;
    __builtin_amdgcn_s_sleep(PSL);
  }
}
DI void wg_wait64(const unsigned char* flags, u64 tag, int pw = 0) {
  if ((int)(threadIdx.x >> 6) == pw) {
    wave_wait(reinterpret_cast<const u64*>(flags) + (threadIdx.x & (NBW - 1)), true, tag);
    asm volatile("s_waitcnt vmcnt(0)" ::: "memory");
  }
  __syncthreads();
}
DI void st16_sc1(__amdgpu_buffer_rsrc_t rs, size_t off, u32x4 v) { __builtin_amdgcn_raw_buffer_store_b128(v, rs, (int)off, 0, 16); }
DI void st8_sc1(unsigned char* p, f32x2 v) { __hip_atomic_store(reinterpret_cast<u64*>(p), __builtin_bit_cast(u64, v), RLX_AGENT); }

struct QPre { u32x4 R0[8]; u32x4 R1; u32x2 xa, xb; u32 x1; };
DI void qproj_load(QPre& Q, const u16* x, const u16* Wh, int w, int lane) {
  const int j = lane & 15, g = lane >> 4;
  Q.xa = u32x2{0u, 0u}; Q.xb = u32x2{0u, 0u}; Q.x1 = 0u;
  if (j < 4) { Q.xa = ld8(x + j * DM + 36 * w + 8 * g); Q.xb = ld8(x + j * DM + 36 * w + 8 * g + 4); Q.x1 = x[j * DM + 36 * w + 32 + g]; }
#pragma unroll
  for (int jj = 0; jj < 8; ++jj) Q.R0[jj] = ld16(Wh + (size_t)(36 * w + 8 * g + jj) * 4096 + 8 * j);
  Q.R1 = ld16(Wh + (size_t)(36 * w + 32 + g) * 4096 + 8 * j);
}
DI void qproj_mfma(const QPre& Q, unsigned char* lds, int w, int lane) {
  const int j = lane & 15, g = lane >> 4;
  float* qpart = reinterpret_cast<float*>(lds + L_BIG);
  const u32x4 a = {Q.xa[0], Q.xa[1], Q.xb[0], Q.xb[1]}, a1 = {Q.x1, 0u, 0u, 0u};
#pragma unroll
  for (int hf = 0; hf < 2; ++hf) {
    f32x4 acc[4];
#pragma unroll
    for (int t = 0; t < 4; ++t) acc[t] = mfma16(a, tcol16(Q.R0, 4 * hf + t), f32x4{0.f, 0.f, 0.f, 0.f});
#pragma unroll
    for (int t = 0; t < 4; ++t) { const u32x4 bb = {(t & 1) ? (Q.R1[2 * hf + (t >> 1)] >> 16) : (Q.R1[2 * hf + (t >> 1)] & 0xffffu), 0u, 0u, 0u}; acc[t] = mfma16(a1, bb, acc[t]); }
    if (g == 0) {
#pragma unroll
      for (int r = 0; r < 4; ++r) *reinterpret_cast<f32x4*>(qpart + (w * 4 + r) * 128 + 8 * j + 4 * hf) = f32x4{acc[0][r], acc[1][r], acc[2][r], acc[3][r]};
    }
    __builtin_amdgcn_sched_barrier(0);
  }
}
DI void wo_load(const u16* Wo, u32x2 (&R)[4][8], int nb, int lane, int s0, int s1) {
  const int j = lane & 15, g = lane >> 4;
#pragma unroll
  for (int s = 0; s < 4; ++s) if (s >= s0 && s < s1)
#pragma unroll
    for (int jj = 0; jj < 8; ++jj) R[s][jj] = ld8(Wo + (size_t)(32 * s + 8 * g + jj) * DM + 64 * nb + 4 * j);
}
DI void wo_mfma(const u32x2 (&R)[4][8], const unsigned char* lds, int lane, f32x4 (&acc)[4]) {
  const int j = lane & 15, g = lane >> 4;
#pragma unroll
  for (int t = 0; t < 4; ++t) acc[t] = f32x4{0.f, 0.f, 0.f, 0.f};
#pragma unroll
  for (int s = 0; s < 4; ++s) {
    const u32x4 a = *reinterpret_cast<const u32x4*>(lds + L_AM + j * 272 + (32 * s + 8 * g) * 2);
#pragma unroll
    for (int t = 0; t < 4; ++t) acc[t] = mfma16(a, tcol8(R[s], t), acc[t]);
  }
}
DI u32x4 yrow(const f32x4 (&acc)[4], u32 tg) {
  return u32x4{pk_bf16(acc[0][0] + acc[0][1], acc[1][0] + acc[1][1]), tg, pk_bf16(acc[2][0] + acc[2][1], acc[3][0] + acc[3][1]), tg};
}
DI void am_zero(unsigned char* lds, int tid) {
  if (tid < 136) { const int pr = tid / 34; *reinterpret_cast<u32x4*>(lds + L_AM + (4 * pr + 2) * 272 + (tid - 34 * pr) * 16) = u32x4{0u, 0u, 0u, 0u}; }
}
DI float rdlane(float x, int l) { return __builtin_bit_cast(float, __builtin_amdgcn_readlane(__builtin_bit_cast(int, x), l)); }

constexpr int NS = 10;
constexpr int NSEQ = 8 * NS;
#define RNG 9
#define NP 3
#define QP 5
#define SP 4
#define BPF 2
#define K1G 1
#define PSKIP 0
#define D3FROM 66
#define D4FROM 99
#define ROTM 1
#define OAUX 0
#define DEP 2
constexpr int DEPTH = DEP, RING = RNG, RAMP = 2;
constexpr int issued(int i) {
  int d = RING - (i + RAMP - 1) / RAMP; if (d < DEPTH) d = DEPTH;
  if (i >= D3FROM && d < 3) d = 3;
  if (i >= D4FROM && d < 4) d = 4;
  int e = i + d; if (e < RING) e = RING; return e > NSEQ ? NSEQ : e;
}
#define PACE 1
#define YDEF 1
#define WO0 16
#define WON 36
#define TA1 535
#define TA2 440
#define NK 68
#define ORG0 130
#define SLMAX 80
#define MAXGAP 70
constexpr int cmin(int a, int b) { return a < b ? a : b; }
#define RSH 1
#define RD 4
#define BMAP 1
#define EBIG 9999
#define NJ 54
#define TA3 515
constexpr int due_at(int n) { return (n <= NJ ? n * TA1 : n <= NK ? NJ * TA1 + (n - NJ) * TA3 : NJ * TA1 + (NK - NJ) * TA3 + (n - NK) * TA2) / 10; }
#define RTAB 20, 30, 40, 50, 60, 70
#define GTAB 0, 12, 6, 12, 12, 12
#define BTAB 8, 12, 6, 12, 12, 0
#define MISS 40
#define DB 10
#define DTAB 20, 20, 20, 15, 5, 3, 3, 3
constexpr int rtab[] = {RTAB}, gtab[] = {GTAB}, btab[] = {BTAB}, dtab[] = {DTAB};
constexpr int NRND = sizeof(rtab) / sizeof(int);
constexpr int rgap(int k) { return k ? rtab[k] - rtab[k - 1] : rtab[0] - RING; }
constexpr int rnd_at(int n) { for (int k = 0; k < NRND; ++k) if (rtab[k] == n) return k; return -1; }
static_assert(rtab[0] >= RING + 2 && rtab[NRND - 1] + RD + 2 < NSEQ, "");
constexpr int wo_step(int k) { return WO0 + k * WON / 32; }
#define WTS 54
#define PJ0 58
#define PJ1 63
#define PJ2 72
#define XW 13
#define XR 50
#define FDLY 8
#define FSL 2
#define STEPST 0
#define CPFM 1
#define STAG 16
DI u32 rt32() { return (u32)__builtin_amdgcn_s_memrealtime(); }
constexpr int OOB = 0x7ffff000;
constexpr int L_AB = 32768;
DI void part_a(const Args& a, unsigned char* lds, int idx, u64 tag, u32 pf) {
  const u64 T64 = __builtin_amdgcn_s_memrealtime();
  __builtin_amdgcn_sched_barrier(0);
  const int tid = threadIdx.x, lane = tid & 63, w = __builtin_amdgcn_readfirstlane(tid >> 6);
  const int j = lane & 15, g = lane >> 4;
  const int h = (idx & 7) * 4 + ((idx >> 3) & 3), ci = idx >> 5;
  const int rr = (((h & 3) * 5) >> 1) + (h >> 2), rot = ROTM ? (rr >= NS ? rr - NS : rr) : (h * 5) >> 4, l0 = (ci * 37) >> 4, ln = ((ci * 37 + 37) >> 4) - l0;
  const int bad_sl = (w < ln) ? -1 : NS - 1 - rot;
  int vtab;
  { const int p = lane + rot >= NS ? lane + rot - NS : lane + rot;
    vtab = p == NS - 1 ? (w < ln ? (h << 20) + (1008 + l0 + w) * 1024 : 1 << 30) : (h << 20) + (p * 7 + ci) * 16384 + w * 1024; }
  const bool prod = ci < 2 * NP && w < 9 / NP;
  const int r0 = ((ci >> 1) * (9 / NP) + w) * 64;
  int voff = lane * 16, wqo = ((r0 + 32 * (j >> 3) + 8 * g) * 4096 + h * 128 + (ci & 1) * 64 + 8 * (j & 7)) * 2;
  int xo = j < 8 ? ((j & 3) * DM + r0 + 32 * (j >> 2) + 8 * g) * 2 : OOB;
  const int prow = (idx >> 3) * 40 + (w - 3) * 4;
  int pfo = (w >= 3 && w < 13 && prow < DM + DC && prow >= PSKIP) ? ((prow < DM ? prow : prow - DM) + g) * 8192 + (idx & 7) * 1024 + j * 64 : OOB;
  int so0 = __builtin_amdgcn_readlane(vtab, 0);
  asm volatile("" : "+v"(vtab), "+v"(voff), "+v"(wqo), "+v"(xo), "+v"(pfo), "+s"(so0));
  __builtin_amdgcn_sched_barrier(0);
  asm volatile("" :: "s"(a.xB), "s"(a.C), "s"(a.wqB), "s"(a.wuk), "s"(a.wuv), "s"(a.woB), "s"(T64));
  u32 T0 = (u32)T64;
  const u32 toff = (u32)(w * TA2) / (STAG * 10);
  u32* lorg = reinterpret_cast<u32*>(lds + L_ST + 2048);
  const u32 tg = (u32)(tag >> 32);
  u32 last = T0;
  u32x2 rv = {0u, 0u}; u32 rt = 0;
  const __amdgpu_buffer_rsrc_t rs = __builtin_amdgcn_make_buffer_rsrc(a.ws, 0, (int)WS_END, 0x00020000);
  const __amdgpu_buffer_rsrc_t rk = __builtin_amdgcn_make_buffer_rsrc(const_cast<u16*>(a.K), 0, 1 << 27, 0x00020000);
  const __amdgpu_buffer_rsrc_t rvv = __builtin_amdgcn_make_buffer_rsrc(const_cast<u16*>(a.V), 0, 1 << 27, 0x00020000);
  const __amdgpu_buffer_rsrc_t rw = __builtin_amdgcn_make_buffer_rsrc(const_cast<u16*>(a.wq), 0, DM * 4096 * 2, 0x00020000);
  const __amdgpu_buffer_rsrc_t rx = __builtin_amdgcn_make_buffer_rsrc(const_cast<u16*>(a.xA), 0, 4 * DM * 2, 0x00020000);
  ST(0);
  u32x4 R[RING];
#define A_ISSUE(i) R[(i) % RING] = __builtin_amdgcn_raw_buffer_load_b128((i) >= 4 * NS ? rvv : rk, voff, ((i) == 0 ? so0 : __builtin_amdgcn_readlane(vtab, (i) % NS)) + ((((i) / NS) & 3) << 25), 2)
#define GATE(n) do { if (PACE) { u32 due = T0 + toff + org + ((osl * cmin(n, NK)) >> 4) + (u32)due_at(n); if (MAXGAP && (int)(due - (last + MAXGAP)) > 0) due = last + MAXGAP; \
    while ((int)(now - due) < 0) { __builtin_amdgcn_s_sleep(1); now = rt32(); } last = now; } } while (0)
#define CTRL(n) do { if constexpr (PACE) { \
    if constexpr (rnd_at((n) - RD - 2) >= 0) { constexpr int k = rnd_at((n) - RD - 2); \
      const u32 dt = (rv[1] == (u32)tag + k) ? rv[0] - T0 : rt; \
      const float sm = row16_sum((float)(int)dt); \
      const int o = (int)((RSH ? rdlane(sm, 0) * (1.f / 16.f) : (rdlane(sm, 0) + rdlane(sm, 16)) * (1.f / 32.f))) - due_at(rtab[k]); \
      if (w == ((5 * k + 11) & 15) && lane == 0 && o > -300 && o < 2000) { \
        int e = o - (int)org - (int)((osl * cmin(rtab[k], NK)) >> 4) - dtab[k]; if (e < 0) e = 0; \
        int sl_ = ((int)osl * gtab[k]) / 16 + ((e > EBIG ? 0 : e) * btab[k]) / rgap(k); sl_ = sl_ > SLMAX ? SLMAX : sl_; \
        if (STEPST) lorg[16 + k] = (u32)(e & 0xffff) | ((u32)sl_ << 16); \
        __hip_atomic_store(reinterpret_cast<u64*>(lorg), __builtin_bit_cast(u64, u32x2{(u32)((int)org + e + ((((int)osl - sl_) * cmin(n, NK)) >> 4)), (u32)sl_}), __ATOMIC_RELAXED, __HIP_MEMORY_SCOPE_WORKGROUP); } } \
    if constexpr (rnd_at(n) >= 0) { constexpr int k = rnd_at(n); \
      u32 tot = 0; \
      if (ci == k % ACU && (h & ((1 << RSH) - 1)) == (k & ((1 << RSH) - 1))) { const u32 mine = (1u << 24) + ((rt32() - toff - T0) & 0xfffffu); \
        if (lane == 0) tot = __hip_atomic_fetch_add(lorg + 8 + k, mine, __ATOMIC_RELAXED, __HIP_MEMORY_SCOPE_WORKGROUP) + mine; } \
      __builtin_amdgcn_raw_buffer_store_b64(u32x2{T0 + ((tot & 0xffffffu) >> 4), (u32)tag + k}, rs, (tot >> 24) == 16u ? (int)WS_RPT + (h >> RSH) * 4352 : OOB, 128 * k * 35, 16); } \
    if constexpr (rnd_at((n) - RD) >= 0) { constexpr int k = rnd_at((n) - RD); \
      rv = __builtin_amdgcn_raw_buffer_load_b64(rs, (w == ((5 * k + 11) & 15) && lane < (32 >> RSH)) ? (int)WS_RPT + lane * 4352 : OOB, 128 * k * 35, 16); rt = rt32() - T0 - MISS; } \
  } } while (0)
  u32x4 Wr[8], xf;
  u32 pf2 = 0;
  if (BPF == 1) pf2 = __builtin_amdgcn_raw_buffer_load_b32(__builtin_amdgcn_make_buffer_rsrc(const_cast<u16*>(prow < DM ? a.wqB : a.wuk), 0, DM * 4096 * 2, 0x00020000), pfo, 0, 2);
  A_ISSUE(0);
  __builtin_amdgcn_sched_barrier(0);
  if (prod) {
#pragma unroll
    for (int k = 0; k < 8; ++k) Wr[k] = __builtin_amdgcn_raw_buffer_load_b128(rw, wqo, k * 8192, 2);
    xf = __builtin_amdgcn_raw_buffer_load_b128(rx, xo, 0, 2);
  }
  __builtin_amdgcn_sched_barrier(0);
  if (BPF == 2) pf2 = __builtin_amdgcn_raw_buffer_load_b32(__builtin_amdgcn_make_buffer_rsrc(const_cast<u16*>(prow < DM ? a.wqB : a.wuk), 0, DM * 4096 * 2, 0x00020000), pfo, 0, 2);
  if (!K1G) A_ISSUE(1);
  __builtin_amdgcn_raw_buffer_store_b64(u32x2{T0, (u32)tag + 15u}, rs, (ci == 6 && (h & 1) == 0 && tid == 0) ? (int)WS_RPT + (h >> 1) * 4352 : OOB, 128 * 15 * 35, 16);
  __builtin_amdgcn_sched_barrier(0);
  if (tid < 32) lorg[tid] = tid == 0 ? ORG0 : tid == 2 ? T0 : 0u;
  __syncthreads();
  T0 = __builtin_amdgcn_readfirstlane(lorg[2]);
  if (prod) {
    float* qpart = reinterpret_cast<float*>(lds + L_BIG);
#pragma unroll
    for (int hf = 0; hf < 2; ++hf) {
      f32x4 d4[4];
#pragma unroll
      for (int t = 0; t < 4; ++t) d4[t] = mfma16(xf, tcol16(Wr, 4 * hf + t), f32x4{0.f, 0.f, 0.f, 0.f});
      if (g == (j >> 3)) {
#pragma unroll
        for (int r = 0; r < 4; ++r) *reinterpret_cast<f32x4*>(qpart + ((w * 2 + g) * 4 + r) * 64 + 8 * (j & 7) + 4 * hf) = f32x4{d4[0][r], d4[1][r], d4[2][r], d4[3][r]};
      }
    }
    asm volatile("" ::: "memory");
    u32 arr = 0;
    if (lane == 0) arr = __hip_atomic_fetch_add(lorg + 4, 1u, __ATOMIC_RELAXED, __HIP_MEMORY_SCOPE_WORKGROUP);
    if (__builtin_amdgcn_readfirstlane(arr) == 9 / NP - 1) {
      asm volatile("" ::: "memory");
      f32x4 s0 = {0.f, 0.f, 0.f, 0.f};
#pragma unroll
      for (int k = 0; k < 18 / NP; ++k) s0 += *reinterpret_cast<const f32x4*>(qpart + k * 256 + 4 * lane);
      const int qo = (int)WS_QA + (h * 8 + ci) * 2048 + lane * 16;
      st16_sc1(rs, qo, u32x4{__float_as_uint(s0[0]), tg, __float_as_uint(s0[1]), tg});
      st16_sc1(rs, qo + 1024, u32x4{__float_as_uint(s0[2]), tg, __float_as_uint(s0[3]), tg});
    }
  }
  static_for<RING - 2 + K1G>([&](auto tc) __attribute__((always_inline)) {
    constexpr int t = tc.value + 2 - K1G;
    u32 now = rt32();
    const u32 org = __builtin_amdgcn_readfirstlane(__hip_atomic_load(lorg, __ATOMIC_RELAXED, __HIP_MEMORY_SCOPE_WORKGROUP)), osl = 0;
    GATE(t);
    A_ISSUE(t);
    __builtin_amdgcn_sched_barrier(0);
    if constexpr (t == SP) {
      if (w == 13) {
        const u32x2 sv = __builtin_amdgcn_raw_buffer_load_b64(rs, lane < 16 ? (int)WS_RPT + lane * 4352 : OOB, 128 * 15 * 35, 16);
        const bool ok = sv[1] == (u32)tag + 15u;
        const float sm = row16_sum(ok ? (float)(int)(sv[0] - T0) : 0.f), cn = row16_sum(ok ? 1.f : 0.f);
        const int o = ORG0 + (cn > 0.f ? (int)(sm / cn) : 0);
        if (lane == 0 && o > -300 && o < 1000) lorg[0] = (u32)o;
      }
    }
    if constexpr (t == QP) {
      if (w >= 14) {
        const int qo = (int)WS_QA + (h * 8 + (w & 1)) * 2048 + lane * 16;
        u32x4 v[2 * NP];
#pragma unroll 1
        for (u32 spins = 0; spins < SPIN_CAP; ++spins) {
          bool ok = true;
#pragma unroll
          for (int k = 0; k < 2 * NP; ++k) v[k] = __builtin_amdgcn_raw_buffer_load_b128(rs, qo, (k >> 1) * 4096 + (k & 1) * 1024, 16);
#pragma unroll
          for (int k = 0; k < 2 * NP; ++k) ok = ok && v[k][1] == tg && v[k][3] == tg;
          if (__all(ok)) break;
          __builtin_amdgcn_s_sleep(4);
        }
        f32x4 s0 = {0.f, 0.f, 0.f, 0.f};
#pragma unroll
        for (int k = 0; k < NP; ++k) s0 += f32x4{__uint_as_float(v[2 * k][0]), __uint_as_float(v[2 * k][2]), __uint_as_float(v[2 * k + 1][0]), __uint_as_float(v[2 * k + 1][2])};
        *reinterpret_cast<u32x2*>(lds + L_QS + (g * 128 + (w & 1) * 64 + 4 * j) * 2) = u32x2{pk_bf16(s0[0], s0[1]), pk_bf16(s0[2], s0[3])};
        asm volatile("" ::: "memory");
        if (lane == 0) __hip_atomic_fetch_add(lorg + 5, 1u, __ATOMIC_RELAXED, __HIP_MEMORY_SCOPE_WORKGROUP);
        if (STEPST && lane == 0) lorg[22 + (w & 1)] = rt32() - T0;
      }
    }
    __builtin_amdgcn_sched_barrier(0);
  });
  while (__hip_atomic_load(lorg + 5, __ATOMIC_RELAXED, __HIP_MEMORY_SCOPE_WORKGROUP) != 2u) __builtin_amdgcn_s_sleep(1);
  asm volatile("" :: "v"(pf), "v"(pf2) : "memory");
  ST(1);
  float* wtab = reinterpret_cast<float*>(lds + L_ST + 1024);
  u32x2 xv = {0u, 0u};
  const int xoff = (w == XW && lane < 8 * ACU) ? (int)WS_MLA + (h * 8 + (lane >> 3)) * 128 + (lane & 7) * 8 : OOB;
  float* pml = reinterpret_cast<float*>(lds + L_ST);
  u32x4 q;
  float acc[8], S[4];
  u32x2 RW[4][8];
  u32x4 WT[4][4];
  float Y[4];
  auto project = [&](int b) __attribute__((always_inline)) {
    f32x4 d4[4];
#pragma unroll
    for (int t = 0; t < 4; ++t) d4[t] = f32x4{0.f, 0.f, 0.f, 0.f};
    const f32x4 wt = *reinterpret_cast<const f32x4*>(wtab + b * 16 + 4 * g);
#pragma unroll
    for (int s = 0; s < 4; ++s) {
      const u32x4 af = *reinterpret_cast<const u32x4*>(lds + L_AB + (b * 16 + j) * 272 + (32 * s + 8 * g) * 2);
#pragma unroll
      for (int t = 0; t < 4; ++t) d4[t] = mfma16(af, WT[s][t], d4[t]);
    }
    float y[4];
#pragma unroll
    for (int t = 0; t < 4; ++t) y[t] = fmaf(d4[t][0], wt[0], d4[t][1] * wt[1]) + fmaf(d4[t][2], wt[2], d4[t][3] * wt[3]);
    Y[b] = sw16(sw32(y[0], y[2]), sw32(y[1], y[3]));
  };
  const __amdgpu_buffer_rsrc_t rwo = __builtin_amdgcn_make_buffer_rsrc(const_cast<u16*>(a.wo + (size_t)h * 128 * DM), 0, w < 9 ? 128 * DM * 2 : 0, 0x00020000);
  const int wvo = (8 * g * DM + 64 * w + 4 * j) * 2;
#define WO_ISSUE() static_for<32>([&](auto kc) __attribute__((always_inline)) { constexpr int k = kc.value; \
    if constexpr (wo_step(k) == i) RW[k >> 3][k & 7] = __builtin_amdgcn_raw_buffer_load_b64(rwo, wvo, (32 * (k >> 3) + (k & 7)) * DM * 2, 2); })
  static_for<NSEQ>([&](auto ic) __attribute__((always_inline)) {
    constexpr int i = ic.value, bb = (i / NS) & 3, pass = i / (4 * NS), sl = i % NS;
    u32 now = 0; if (PACE + STEPST > 0) now = rt32();
    u32 org = 0, osl = 0; if (PACE) { const u32x2 ov = __builtin_bit_cast(u32x2, __hip_atomic_load(reinterpret_cast<u64*>(lorg), __ATOMIC_RELAXED, __HIP_MEMORY_SCOPE_WORKGROUP)); org = __builtin_amdgcn_readfirstlane(ov[0]); osl = __builtin_amdgcn_readfirstlane(ov[1]); }
    if constexpr (i == 10 || i == 20 || i == 40 || i == 60) { if (tid == 0) reinterpret_cast<u32*>(lds + L_ST + 3072)[i == 10 ? 8 : i == 20 ? 9 : i == 40 ? 10 : 11] = now; }
    __builtin_amdgcn_sched_barrier(0);
    const u32x4 r = R[i % RING];
    if constexpr (pass == 0) {
      if constexpr (sl == 0) { q = *reinterpret_cast<const u32x4*>(lds + L_QS + (bb * 128 + 8 * j) * 2); S[bb] = -INFINITY; }
      float d = dot2(r[0], q[0], 0.f); d = dot2(r[1], q[1], d); d = dot2(r[2], q[2], d); d = dot2(r[3], q[3], d);
      asm volatile("" : "+v"(d));
      if constexpr (issued(i + 1) > issued(i)) { GATE(issued(i)); WO_ISSUE(); A_ISSUE(issued(i)); CTRL(issued(i)); if constexpr (issued(i + 1) > issued(i) + 1) { GATE(issued(i) + 1); A_ISSUE(issued(i) + 1); CTRL(issued(i) + 1); } } else { WO_ISSUE(); }
      d = row16_sum(d);
      S[bb] = (j == sl) ? d : S[bb];
      if constexpr (sl == NS - 1) {
        const float sc = (j == bad_sl) ? -INFINITY : S[bb] * C2;
        const float m = xg_max(row16_max(sc));
        S[bb] = __builtin_amdgcn_exp2f(sc - m);
        const float l = xg_sum(row16_sum(S[bb]));
        if (lane == 0) *reinterpret_cast<f32x2*>(pml + (bb * 16 + w) * 2) = f32x2{m, l};
      }
      if constexpr (i == 4 * NS - 1) {
        asm volatile("" ::: "memory");
        u32 arr = 0;
        if (lane == 0) arr = __hip_atomic_fetch_add(lorg + 6, 1u, __ATOMIC_RELAXED, __HIP_MEMORY_SCOPE_WORKGROUP);
        arr = __builtin_amdgcn_readfirstlane(arr);
        asm volatile("" ::: "memory");
        const f32x2 ml = *reinterpret_cast<const f32x2*>(pml + lane * 2);
        const float M = row16_max(ml[0]);
        const float L = row16_sum(__builtin_amdgcn_exp2f(ml[0] - M) * ml[1]);
        __builtin_amdgcn_raw_buffer_store_b64(u32x2{__float_as_uint(j ? L : M), tg}, rs, (arr == 15u && j < 2) ? (int)WS_MLA + (h * 8 + ci) * 128 + g * 16 + j * 8 : OOB, 0, 16);
      }
    } else {
      const float p = dpp<0x150 + sl>(S[bb]);
      if constexpr (sl == 0) {
#pragma unroll
        for (int k = 0; k < 8; ++k) acc[k] = 0.f;
      }
#pragma unroll
      for (int k = 0; k < 4; ++k) { acc[2 * k] = fmaf(p, bf_lo(r[k]), acc[2 * k]); acc[2 * k + 1] = fmaf(p, bf_hi(r[k]), acc[2 * k + 1]); }
      asm volatile("" : "+v"(acc[0]), "+v"(acc[1]), "+v"(acc[2]), "+v"(acc[3]), "+v"(acc[4]), "+v"(acc[5]), "+v"(acc[6]), "+v"(acc[7]));
      if constexpr (issued(i + 1) > issued(i)) { GATE(issued(i)); WO_ISSUE(); A_ISSUE(issued(i)); CTRL(issued(i)); if constexpr (issued(i + 1) > issued(i) + 1) { GATE(issued(i) + 1); A_ISSUE(issued(i) + 1); CTRL(issued(i) + 1); } } else { WO_ISSUE(); }
      if constexpr (i == XR) xv = __builtin_amdgcn_raw_buffer_load_b64(rs, xoff, 0, 16);
      if constexpr (i == XR + 2) {
        if (w == XW) {
          if (__builtin_expect(!__all(lane >= 8 * ACU || xv[1] == tg), 0)) {
#pragma unroll 1
            for (u32 spins = 0; spins < SPIN_CAP; ++spins) {
              __builtin_amdgcn_s_sleep(8); xv = __builtin_amdgcn_raw_buffer_load_b64(rs, xoff, 0, 16);
              if (__all(lane >= 8 * ACU || xv[1] == tg)) break;
            }
          }
          const float x = __uint_as_float(xv[0]);
          const float m = lane < 8 * ACU ? dpp<0xA0>(x) : -INFINITY, l = lane < 8 * ACU ? dpp<0xF5>(x) : 0.f;
          const float M = xg_max(fmaxf(m, dpp<0x128>(m)));
          float t = __builtin_amdgcn_exp2f(m - M) * l; t = xg_sum(t + dpp<0x128>(t));
          if (lane < 8 && !(lane & 1)) *reinterpret_cast<f32x2*>(wtab + 64 + lane) = f32x2{M, 1.f / t};
          asm volatile("" ::: "memory");
          const f32x2 gm = *reinterpret_cast<const f32x2*>(wtab + 64 + 2 * g);
          wtab[lane] = __builtin_amdgcn_exp2f(pml[lane * 2] - gm[0]) * gm[1];
          asm volatile("" ::: "memory");
          if (lane == 0) __hip_atomic_store(lorg + 7, 1u, __ATOMIC_RELAXED, __HIP_MEMORY_SCOPE_WORKGROUP);
        }
      }
      if constexpr (i >= WTS && i < WTS + 4) {
        constexpr int s = i - WTS;
#pragma unroll
        for (int t = 0; t < 4; ++t) WT[s][t] = tcol8(RW[s], t);
        asm volatile("" : "+v"(WT[s][0]), "+v"(WT[s][1]), "+v"(WT[s][2]), "+v"(WT[s][3]));
      }
      if constexpr (i == PJ0 || i == PJ1 || i == PJ2) {
        constexpr int b = i == PJ0 ? 0 : i == PJ1 ? 1 : 2;
        Y[b] = 0.f;
        if (w < 9) {
          while (__hip_atomic_load(lorg + 24 + b, __ATOMIC_RELAXED, __HIP_MEMORY_SCOPE_WORKGROUP) != 16u || __hip_atomic_load(lorg + 7, __ATOMIC_RELAXED, __HIP_MEMORY_SCOPE_WORKGROUP) == 0u) __builtin_amdgcn_s_sleep(1);
          asm volatile("" ::: "memory");
          project(b);
        }
        asm volatile("" : "+v"(Y[b]));
      }
      if constexpr (sl == NS - 1) {
        const float t0 = sw16(sw32(acc[0], acc[4]), sw32(acc[2], acc[6])), t1 = sw16(sw32(acc[1], acc[5]), sw32(acc[3], acc[7]));
        *reinterpret_cast<u32*>(lds + L_AB + (bb * 16 + w) * 272 + 16 * j + 4 * g) = pk_bf16(t0, t1);
        if constexpr (bb < 3) { asm volatile("" ::: "memory"); if (lane == 0) __hip_atomic_fetch_add(lorg + 24 + bb, 1u, __ATOMIC_RELAXED, __HIP_MEMORY_SCOPE_WORKGROUP); }
      }
    }
    __builtin_amdgcn_sched_barrier(0);
  });
#undef A_ISSUE
  if (STEPST) { if (lane == 0) { reinterpret_cast<u32*>(lds + 65536)[w * 96 + NSEQ] = rt32(); reinterpret_cast<u32*>(lds + 65536)[w * 96 + NSEQ + 1] = lorg[0]; for (int k = 0; k < 8; ++k) reinterpret_cast<u32*>(lds + 65536)[w * 96 + NSEQ + 2 + k] = lorg[16 + k]; }
    for (int k = lane; k < 96; k += 64) reinterpret_cast<u32*>(a.ws + (20u << 20))[(blockIdx.x * 16 + w) * 96 + k] = reinterpret_cast<u32*>(lds + 65536)[w * 96 + k]; }
  ST(2);
  __syncthreads();
  ST(3);
  if (w < 9) {
    project(3);
    st16_sc1(rs, WS_Y + ((8 * w + (j >> 1)) * 256 + idx) * 128 + ((j & 1) * 4 + g) * 16, u32x4{pk_bf16(Y[0], Y[1]), tg, pk_bf16(Y[2], Y[3]), tg});
  }
  ST(4);
  if (idx < 72) {
    __builtin_amdgcn_sched_barrier(0);
    __builtin_amdgcn_s_barrier();
    __builtin_amdgcn_s_sleep(FDLY);
    const int yo = (int)WS_Y + (idx * 256 + 16 * w) * 128 + lane * 16;
    u32x4 v0, v1;
#pragma unroll 1
    for (u32 spins = 0; spins < SPIN_CAP; ++spins) {
      v0 = __builtin_amdgcn_raw_buffer_load_b128(rs, yo, 0, 16); v1 = __builtin_amdgcn_raw_buffer_load_b128(rs, yo + 1024, 0, 16);
      if (__all(v0[1] == tg && v0[3] == tg && v1[1] == tg && v1[3] == tg)) break;
      __builtin_amdgcn_s_sleep(FSL);
    }
    ST(7);
    float y = sw16(sw32(bf_lo(v0[0]) + bf_lo(v1[0]), bf_lo(v0[2]) + bf_lo(v1[2])), sw32(bf_hi(v0[0]) + bf_hi(v1[0]), bf_hi(v0[2]) + bf_hi(v1[2])));
    y += dpp<0x128>(y);
    float* red = reinterpret_cast<float*>(lds + L_PF);
    if (!(lane & 8)) red[((lane & 7) * 4 + g) * 20 + w] = y;
    __syncthreads();
    if (tid < 32) {
      const f32x4 r0 = *reinterpret_cast<const f32x4*>(red + tid * 20), r1 = *reinterpret_cast<const f32x4*>(red + tid * 20 + 4);
      const f32x4 r2 = *reinterpret_cast<const f32x4*>(red + tid * 20 + 8), r3 = *reinterpret_cast<const f32x4*>(red + tid * 20 + 12);
      const f32x4 r = r0 + r1 + r2;
      const __amdgpu_buffer_rsrc_t ro = __builtin_amdgcn_make_buffer_rsrc(a.out, 0, 4 * 1152 * 2, 0x00020000);
      __builtin_amdgcn_raw_buffer_store_b16(to_bf16(((r[0] + r[1]) + (r[2] + r[3])) + (r3[0] + r3[1])), ro, ((tid & 3) * 1152 + idx * 8 + (tid >> 2)) * 2, 0, OAUX);
      __builtin_amdgcn_raw_buffer_store_b16(to_bf16(r3[2] + r3[3]), ro, ((tid >> 3) * 1152 + 576 + idx * 8 + (tid & 7)) * 2, 0, OAUX);
    }
  }
  ST(5);
}

#define IA 2
#define IB 4
#define IC 6
#define EUV 4
#define YOFF 90
#define WPD 4
template <bool LD> DI void part_b(const Args& a, unsigned char* lds, int wg, u64 tag, u32 pf) {
  const int tid = threadIdx.x, lane = tid & 63, w = __builtin_amdgcn_readfirstlane(tid >> 6);
  const int j = lane & 15, g = lane >> 4;
  const __amdgpu_buffer_rsrc_t rs = __builtin_amdgcn_make_buffer_rsrc(a.ws, 0, (int)WS_END, 0x00020000);
  const u32 tb0 = rt32();
  const int h = BPF ? (wg & 7) * 4 + (wg >> 3) : wg;
  const int h3 = h ^ 2;
  const int b2 = BMAP ? (wg & 7) >> 1 : wg >> 3, sp = BMAP ? (wg >> 3) * 2 + (wg & 1) : wg & 7;
  const u16* cl = a.C + ((size_t)b2 * SK + sp * 512 + 8 * (w - 8)) * DC + lane * 8;
  u32x4 X[16];
  u32x2 RW[4][8];
  u32x4 W8[2];
  u32 tv = 0;
#define BST(n) do { if (STEPST && LD) { __builtin_amdgcn_sched_barrier(0); const u32 t_ = rt32(); tv = (lane == (n)) ? t_ : tv; __builtin_amdgcn_sched_barrier(0); } } while (0)
  u32* cnt = reinterpret_cast<u32*>(lds + L_ST + 4064);
#define C_ISSUE(T) _Pragma("unroll") for (int r = 0; r < 8; ++r) X[8 * ((T) & 1) + r] = ld16(cl + (size_t)(64 * (T) + r) * DC)
#define C_HALF(T, hf) _Pragma("unroll") for (int r = 4 * (hf); r < 4 * (hf) + 4; ++r) X[8 * ((T) & 1) + r] = ld16(cl + (size_t)(64 * (T) + r) * DC)
#define L_PART(T, r0, r1) do { __builtin_amdgcn_sched_barrier(0); \
    if ((T) + 3 < 8) { _Pragma("unroll") for (int r = r0; r < r1; ++r) X[8 * (((T) + 3) & 1) + r] = ld16(cl + (size_t)(64 * ((T) + 3) + r) * DC); } \
    if ((T) == 5 || (T) == 6) { _Pragma("unroll") for (int k = 2 * (r0); k < 2 * (r1); ++k) \
      RW[2 * (((T) - 5) & 1) + (k >> 3)][k & 7] = ld8(a.woB + ((size_t)h3 * 128 + 32 * (2 * (((T) - 5) & 1) + (k >> 3)) + 8 * g + (k & 7)) * DM + 64 * (w - 8) + 4 * j); } \
    __builtin_amdgcn_sched_barrier(0); } while (0)
#define UV_LOAD(n) do { const u32x4 v_ = ld16(a.wuv + ((size_t)(64 * w + 32 * ((n) >> 3) + 8 * g + ((n) & 7)) * H + h3) * 128 + 8 * j); \
    RW[2 * ((n) >> 3)][(n) & 7] = u32x2{v_[0], v_[1]}; RW[2 * ((n) >> 3) + 1][(n) & 7] = u32x2{v_[2], v_[3]}; } while (0)
#define C_WIN(T, r0, r1) _Pragma("unroll") for (int r = r0; r < r1; ++r) { asm volatile("s_waitcnt vmcnt(%0)" :: "n"(WPD - 1) : "memory"); X[8 * ((T) & 1) + r] = ld16(cl + (size_t)(64 * (T) + r) * DC); __builtin_amdgcn_sched_barrier(0); }
  u32 sb0 = L_BIG + 8 * (w - 8) * CRS + lane * 16, sb1 = sb0 + CBUF;
  asm volatile("" : "+v"(sb0), "+v"(sb1));
#define C_STAGE(T) _Pragma("unroll") for (int r = 0; r < 8; ++r) *reinterpret_cast<u32x4*>(lds + (((T) & 1) ? sb1 : sb0) + r * CRS) = X[8 * ((T) & 1) + r]
  {
    const u16* Wq = a.wqB + h * 128;
    u32x4 U[8];
    QPre Q;
    if constexpr (!LD) { ST(0); }
    qproj_load(Q, a.xB, Wq, w, lane);
#pragma unroll
    for (int i = 0; i < 8; ++i) U[i] = ld16(a.wuk + ((size_t)(32 * w + 4 * i + (j & 3)) * H + h) * 128 + 8 * ((j & 12) + g));
    __builtin_amdgcn_sched_barrier(0);
    BST(0);
    if constexpr (!LD) am_zero(lds, tid);
    qproj_mfma(Q, lds, w, lane);
    asm volatile("" :: "v"(pf));
    BST(1);
    __syncthreads();
    BST(2);
    if constexpr (LD) { C_HALF(0, 0); }
    else {
      const float* qpart = reinterpret_cast<const float*>(lds + L_BIG);
      float s = 0.f;
#pragma unroll
      for (int ww = 0; ww < 16; ++ww) s += qpart[ww * 512 + tid];
      reinterpret_cast<u16*>(lds + L_QS)[tid] = to_bf16(s);
    }
    __builtin_amdgcn_sched_barrier(0);
    __syncthreads();
    BST(3);
    const u32x4 af = *reinterpret_cast<const u32x4*>(lds + L_QS + ((j >> 2) * 128 + ((j & 3) * 4 + g) * 8) * 2);
    u16* qa = reinterpret_cast<u16*>(lds + L_PF);
    float v2[2] = {0.f, 0.f};
#pragma unroll
    for (int i = 0; i < 8; ++i) {
      const f32x4 d4 = mfma16(af, U[i], f32x4{0.f, 0.f, 0.f, 0.f});
      float d = (j & 8) ? ((j & 4) ? d4[3] : d4[2]) : ((j & 4) ? d4[1] : d4[0]);
      d += dpp<0x124>(d); d += dpp<0x128>(d);
      v2[i >> 2] = ((j >> 2) == (i & 3)) ? d : v2[i >> 2];
    }
    qa[g * 512 + 32 * w + j] = to_bf16(v2[0]); qa[g * 512 + 32 * w + 16 + j] = to_bf16(v2[1]);
    __builtin_amdgcn_sched_barrier(0);
    BST(4);
    __syncthreads();
    if constexpr (!LD) {
      if (w == 0) {
#pragma unroll
        for (int bb = 0; bb < 4; ++bb) st16_sc1(rs, WS_QABS + ((bb * H + h) * 512 + lane * 8) * 2, *reinterpret_cast<const u32x4*>(qa + bb * 512 + lane * 8));
      }
      __builtin_amdgcn_sched_barrier(0);
      __builtin_amdgcn_s_barrier();
      if (w == 0) {
        asm volatile("s_waitcnt vmcnt(0)" ::: "memory");
        if (lane == 0) { __hip_atomic_store(reinterpret_cast<u64*>(a.ws + WS_F1 + wg * 8), tag, RLX_AGENT); cnt[0] = 0u; cnt[1] = 0u; }
      }
    } else {
      __builtin_amdgcn_s_barrier();
      BST(5);
      C_WIN(0, 4, 8); C_WIN(1, 0, 8);
      BST(6);
      C_STAGE(0);
      __builtin_amdgcn_sched_barrier(0);
      BST(7);
      C_WIN(2, 0, 8);
      BST(8);
    }
    if constexpr (!LD) { ST(1); }
  }
  {
    const int b = b2;
    if constexpr (!LD) wg_wait64(a.ws + WS_F1, tag); else __syncthreads();
    if constexpr (!LD) { ST(2); }
    const int kt = w & 3, hh = (w >> 2) & 1;
    float* stmax = reinterpret_cast<float*>(lds + L_ST);
    float* stsum = stmax + 128;
    float* salpha = stmax + 256;
    unsigned char* Pf = lds + L_PF;
    float M = -INFINITY, lsum = 0.f;
    f32x4 acc[2][2];
#pragma unroll
    for (int ct = 0; ct < 2; ++ct) { acc[ct][0] = f32x4{0.f, 0.f, 0.f, 0.f}; acc[ct][1] = f32x4{0.f, 0.f, 0.f, 0.f}; }
    const int troff = (4 * g + (j >> 2)) * CRS + (32 * w + 4 * (j & 3)) * 2;
    auto pv_step = [&](const unsigned char* cb) __attribute__((always_inline)) {
      const f32x4 al0 = *reinterpret_cast<const f32x4*>(salpha + 4 * g), al1 = *reinterpret_cast<const f32x4*>(salpha + 16 + 4 * g);
#pragma unroll
      for (int ct = 0; ct < 2; ++ct) { acc[ct][0] *= al0; acc[ct][1] *= al1; }
#pragma unroll
      for (int blk = 0; blk < 2; ++blk) {
        const u32x4 p0 = *reinterpret_cast<const u32x4*>(Pf + ((blk * 2 + 0) * 64 + lane) * 16);
        const u32x4 p1 = *reinterpret_cast<const u32x4*>(Pf + ((blk * 2 + 1) * 64 + lane) * 16);
#pragma unroll
        for (int ct = 0; ct < 2; ++ct) {
          const u32x2 lo = trread(cb + troff + (32 * blk) * CRS + 32 * ct);
          const u32x2 hi = trread(cb + troff + (32 * blk + 16) * CRS + 32 * ct);
          const u32x4 bf = {lo[0], lo[1], hi[0], hi[1]};
          acc[ct][0] = mfma16(p0, bf, acc[ct][0]); acc[ct][1] = mfma16(p1, bf, acc[ct][1]);
        }
      }
    };
    unsigned char* Qf = lds + L_BIG + CBUF;
    if constexpr (!LD) {
      u32x4 qv[4];
#pragma unroll
      for (int k = 0; k < 4; ++k) qv[k] = __builtin_amdgcn_raw_buffer_load_b128(rs, (int)WS_QABS + (b * H * 512 + (tid + 512 * k) * 8) * 2, 0, 16);
#pragma unroll
      for (int k = 0; k < 4; ++k) { const int e = tid + 512 * k, head = e >> 6, ch = e & 63;
        *reinterpret_cast<u32x4*>(Qf + ((head >> 4) * 16 + (ch >> 2)) * 1040 + ((ch & 3) * 16 + (head & 15)) * 16) = qv[k]; }
    }
    __syncthreads();
    if constexpr (!LD) {
      u32x4 QF[16];
#pragma unroll
      for (int s = 0; s < 16; ++s) QF[s] = *reinterpret_cast<const u32x4*>(Qf + (hh * 16 + s) * 1040 + lane * 16);
      auto s_tile = [&](const unsigned char* cb, bool last) __attribute__((always_inline)) {
        __syncthreads();
        f32x4 s0 = {0.f, 0.f, 0.f, 0.f}, s1 = {0.f, 0.f, 0.f, 0.f};
#pragma unroll
        for (int s = 0; s < 16; s += 2) {
          s0 = mfma16(*reinterpret_cast<const u32x4*>(cb + (16 * kt + j) * CRS + (32 * s + 8 * g) * 2), QF[s], s0);
          s1 = mfma16(*reinterpret_cast<const u32x4*>(cb + (16 * kt + j) * CRS + (32 * s + 32 + 8 * g) * 2), QF[s + 1], s1);
        }
        const f32x4 sv = (s0 + s1) * C2;
        if (last) { __builtin_amdgcn_sched_barrier(0); _Pragma("unroll") for (int n = 0; n < EUV; ++n) UV_LOAD(n); __builtin_amdgcn_sched_barrier(0); }
        const float v = xg_max(fmaxf(fmaxf(sv[0], sv[1]), fmaxf(sv[2], sv[3])));
        if (g == 0) stmax[kt * 32 + 16 * hh + j] = v;
        __syncthreads();
        const float* sm = stmax + 16 * hh + j;
        const float Mn = fmaxf(fmaxf(M, fmaxf(sm[0], sm[32])), fmaxf(sm[64], sm[96]));
        const float alpha = __builtin_amdgcn_exp2f(M - Mn);
        M = Mn;
        u32x2 pk;
        pk[0] = pk_bf16(__builtin_amdgcn_exp2f(sv[0] - Mn), __builtin_amdgcn_exp2f(sv[1] - Mn));
        pk[1] = pk_bf16(__builtin_amdgcn_exp2f(sv[2] - Mn), __builtin_amdgcn_exp2f(sv[3] - Mn));
        lsum = lsum * alpha + ((bf_lo(pk[0]) + bf_hi(pk[0])) + (bf_lo(pk[1]) + bf_hi(pk[1])));
        *reinterpret_cast<u32x2*>(Pf + (((kt >> 1) * 2 + hh) * 64 + lane) * 16 + (kt & 1) * 8) = pk;
        if (kt == 0 && g == 0) salpha[16 * hh + j] = alpha;
        __syncthreads();
        pv_step(cb);
      };
#pragma unroll 1
      for (int T = 0; T < 6; T += 2) { s_tile(lds + L_BIG, false); s_tile(lds + L_BIG + CBUF, false); }
      s_tile(lds + L_BIG, false); s_tile(lds + L_BIG + CBUF, true);
    } else {
#pragma unroll
      for (int T = 0; T < 8; ++T) {
        __syncthreads();
        BST(10 + 4 * T);
        if (T + 1 < 8) { C_STAGE(T + 1); }
        BST(11 + 4 * T);
        L_PART(T, 0, IA);
        if (T == 7) {
#pragma unroll
          for (int k = 0; k < 2; ++k) W8[k] = ld16(a.woB + ((size_t)h3 * 128 + 16 * (w - 8) + 8 * k + (lane >> 3)) * DM + 512 + (lane & 7) * 8);
        }
        BST(12 + 4 * T);
        __syncthreads();
        L_PART(T, IA, IB);
        __syncthreads();
        BST(13 + 4 * T);
        L_PART(T, IB, IC);
        pv_step(lds + L_BIG + (T & 1) * CBUF);
        L_PART(T, IC, 8);
      }
      BST(42);
      if (STEPST) reinterpret_cast<u32*>(a.ws + (20u << 20))[(blockIdx.x * 16 + w) * 96 + lane] = tv;
    }
#undef C_ISSUE
#undef C_STAGE
    if constexpr (!LD) { ST(3); }
    __syncthreads();
    if constexpr (!LD) { lsum = xg_sum(lsum); if (g == 0) stsum[kt * 32 + 16 * hh + j] = lsum; if (kt == 0 && g == 0) salpha[32 + 16 * hh + j] = M; }
    float* tb = reinterpret_cast<float*>(lds + L_BIG);
#pragma unroll
    for (int ct = 0; ct < 2; ++ct)
#pragma unroll
      for (int h2 = 0; h2 < 2; ++h2)
#pragma unroll
        for (int r = 0; r < 4; ++r) tb[(16 * h2 + 4 * g + r) * 516 + 32 * w + 16 * ct + j] = acc[ct][h2][r];
    if constexpr (!LD) { for (int i = tid; i < 8 * AM2_LD * 2 / 16; i += 512) reinterpret_cast<u32x4*>(lds + L_AM2 + 8 * AM2_LD * 2)[i] = u32x4{0u, 0u, 0u, 0u}; }
    __syncthreads();
    if constexpr (!LD) { ST(4); }
    if constexpr (LD) {
      const int t2 = tid - 512;
#pragma unroll
      for (int k = 0; k < 4; ++k) {
        const int e = t2 + 512 * k, head = e >> 6, c8 = e & 63;
        const f32x4 v0 = *reinterpret_cast<const f32x4*>(tb + head * 516 + 8 * c8), v1 = *reinterpret_cast<const f32x4*>(tb + head * 516 + 8 * c8 + 4);
        __builtin_amdgcn_raw_buffer_store_b128(u32x4{pk_bf16(v0[0], v0[1]), pk_bf16(v0[2], v0[3]), pk_bf16(v1[0], v1[1]), pk_bf16(v1[2], v1[3])}, rs, (int)WS_CTXP + (((b * NSP + sp) * 32 + head) * 512 + 8 * c8) * 2, 0, 16);
      }
      if (t2 < 32) {
        const float L = (stsum[t2] + stsum[32 + t2]) + (stsum[64 + t2] + stsum[96 + t2]);
        st8_sc1(a.ws + WS_MLB + ((size_t)(b * NSP + sp) * 32 + t2) * 8, f32x2{salpha[32 + t2], L});
      }
      __builtin_amdgcn_sched_barrier(0);
      __builtin_amdgcn_s_barrier();
      asm volatile("s_waitcnt vmcnt(0)" ::: "memory");
      if (lane == 0) {
        if (__hip_atomic_fetch_add(cnt, 1u, __ATOMIC_RELAXED, __HIP_MEMORY_SCOPE_WORKGROUP) == 7u) {
          __hip_atomic_store(reinterpret_cast<u64*>(a.ws + WS_F2 + wg * 8), tag, RLX_AGENT);
        }
      }
#pragma unroll
      for (int k = 0; k < 2; ++k) *reinterpret_cast<u32x4*>(lds + L_W8 + (16 * (w - 8) + 8 * k + (lane >> 3)) * 144 + (lane & 7) * 16) = W8[k];
    } else {
      __builtin_amdgcn_s_barrier();
      static_for<16 - EUV>([&](auto ic) __attribute__((always_inline)) {
        asm volatile("s_waitcnt vmcnt(%0)" :: "n"(WPD - 1) : "memory");
        UV_LOAD(EUV + ic.value);
        __builtin_amdgcn_sched_barrier(0);
      });
    }
  }
  {
    int tid = threadIdx.x; asm volatile("" : "+v"(tid));
    const int lane = tid & 63, j = lane & 15, g = lane >> 4;
    u16* Am2 = reinterpret_cast<u16*>(lds + L_AM2);
    float* cpart = reinterpret_cast<float*>(lds + L_CP);
    __builtin_amdgcn_sched_barrier(0);
    if constexpr (LD) {
      if (w == 8) {
        wave_wait(reinterpret_cast<const u64*>(a.ws + WS_F2) + (lane & (NBW - 1)), true, tag);
        asm volatile("s_waitcnt vmcnt(0)" ::: "memory");
        if (lane == 0) __hip_atomic_store(cnt + 1, 1u, __ATOMIC_RELEASE, __HIP_MEMORY_SCOPE_WORKGROUP);
      } else {
        for (u32 spins = 0; spins < 64u * SPIN_CAP; ++spins) { if (__hip_atomic_load(cnt + 1, __ATOMIC_ACQUIRE, __HIP_MEMORY_SCOPE_WORKGROUP) != 0u) break; __builtin_amdgcn_s_sleep(1); }
      }
      __builtin_amdgcn_sched_barrier(0);
      const int b = (tid >> 7) & 3, c4 = tid & 127;
      u32x2 pv[NSP];
      const f32x2 ml = __builtin_bit_cast(f32x2, __builtin_amdgcn_raw_buffer_load_b64(rs, (int)WS_MLB + ((b * NSP + (j & 7)) * 32 + h3) * 8, 0, 16));
#pragma unroll
      for (int s = 0; s < NSP; ++s) pv[s] = __builtin_amdgcn_raw_buffer_load_b64(rs, (int)WS_CTXP + (((b * NSP + s) * 32 + h3) * 512 + 4 * c4) * 2, 0, 16);
      const float M = row16_max(ml[0]);
      float wt = __builtin_amdgcn_exp2f(ml[0] - M);
      wt *= 2.f / row16_sum(wt * ml[1]);
      float c0 = 0.f, c1 = 0.f, c2 = 0.f, c3 = 0.f;
#pragma unroll
      for (int s = 0; s < NSP; ++s) { const float ws_ = rdlane(wt, s); c0 = fmaf(ws_, bf_lo(pv[s][0]), c0); c1 = fmaf(ws_, bf_hi(pv[s][0]), c1); c2 = fmaf(ws_, bf_lo(pv[s][1]), c2); c3 = fmaf(ws_, bf_hi(pv[s][1]), c3); }
      const u32 h0 = pk_bf16(c0, c1), h1 = pk_bf16(c2, c3);
      *reinterpret_cast<u32x2*>(Am2 + (2 * b) * AM2_LD + 4 * c4) = u32x2{h0, h1};
      *reinterpret_cast<u32x2*>(Am2 + (2 * b + 1) * AM2_LD + 4 * c4) = u32x2{pk_bf16(c0 - bf_lo(h0), c1 - bf_hi(h0)), pk_bf16(c2 - bf_lo(h1), c3 - bf_hi(h1))};
      __syncthreads();
      __syncthreads();
      const int t2 = tid - 512;
      float s = 0.f;
#pragma unroll
      for (int ww = 0; ww < 8; ++ww) s += cpart[ww * 512 + t2];
      u16* Am = reinterpret_cast<u16*>(lds + L_AM);
      const int bb = t2 >> 7, d = t2 & 127;
      const u16 hi = to_bf16(s);
      Am[(4 * bb) * AM_LD + d] = hi; Am[(4 * bb + 1) * AM_LD + d] = to_bf16(s - bf2f(hi));
    } else {
      __syncthreads();
      f32x4 acc[8];
#pragma unroll
      for (int t = 0; t < 8; ++t) acc[t] = f32x4{0.f, 0.f, 0.f, 0.f};
#pragma unroll
      for (int k2 = 0; k2 < 2; ++k2) {
        const u32x4 af = *reinterpret_cast<const u32x4*>(Am2 + j * AM2_LD + 64 * w + 32 * k2 + 8 * g);
#pragma unroll
        for (int t = 0; t < 8; ++t) acc[t] = mfma16(af, tcol8(RW[2 * k2 + (t >> 2)], t & 3), acc[t]);
      }
      if (g < 2) {
#pragma unroll
        for (int bb = 0; bb < 2; ++bb) {
          float* p = cpart + (w * 4 + 2 * g + bb) * 128 + 8 * j;
          *reinterpret_cast<f32x4*>(p) = f32x4{acc[0][2 * bb] + acc[0][2 * bb + 1], acc[1][2 * bb] + acc[1][2 * bb + 1], acc[2][2 * bb] + acc[2][2 * bb + 1], acc[3][2 * bb] + acc[3][2 * bb + 1]};
          *reinterpret_cast<f32x4*>(p + 4) = f32x4{acc[4][2 * bb] + acc[4][2 * bb + 1], acc[5][2 * bb] + acc[5][2 * bb + 1], acc[6][2 * bb] + acc[6][2 * bb + 1], acc[7][2 * bb] + acc[7][2 * bb + 1]};
        }
      }
      __builtin_amdgcn_sched_barrier(0);
      __syncthreads();
      if (YDEF == 1 && w == 1) {
        u32 dl = tb0 + 5200u;
#pragma unroll 1
        while ((int)(rt32() - dl) < 0) {
          const u32x2 rv = __builtin_amdgcn_raw_buffer_load_b64(rs, lane < 16 ? (int)WS_RPT + lane * 4352 : OOB, 128 * (NRND - 1) * 35, 16);
          const bool ok = rv[1] == (u32)tag + (NRND - 1);
          const float cn = rdlane(row16_sum(ok ? 1.f : 0.f), 0);
          if (cn >= 8.f) { dl = tb0 + (u32)(int)(rdlane(row16_sum(ok ? (float)(int)(rv[0] - tb0) : 0.f), 0) / cn) + (u32)(due_at(NSEQ - 1) - due_at(rtab[NRND - 1]) + YOFF); break; }
          __builtin_amdgcn_s_sleep(32);
        }
        if (lane == 0) cnt[2] = (int)(dl - tb0) > 5200 ? tb0 : dl;
      }
      if (w == 0) {
#pragma unroll
        for (int s = 0; s < 4; ++s)
#pragma unroll
          for (int jj = 0; jj < 8; ++jj) RW[s][jj] = *reinterpret_cast<const u32x2*>(lds + L_W8 + (32 * s + 8 * g + jj) * 144 + 8 * j);
      }
    }
    __syncthreads();
    if (LD || w == 0) {
      f32x4 ya[4];
      wo_mfma(RW, lds, lane, ya);
      if (YDEF) { const u32 dl = YDEF == 1 ? __builtin_amdgcn_readfirstlane(cnt[2]) : tb0 + YDEF; while ((int)(rt32() - dl) < 0) __builtin_amdgcn_s_sleep(4); }
      st16_sc1(rs, WS_Y + ((8 * (LD ? w - 8 : 8) + (j >> 1)) * 256 + NAW + h3) * 128 + g * 32 + (j & 1) * 16, yrow(ya, (u32)(tag >> 32)));
    }
    if constexpr (!LD) { ST(7); }
  }
}

__global__ void __launch_bounds__(1024, 4) k_main(Args a) {
  __shared__ __attribute__((aligned(16))) unsigned char lds[L_TOTAL];
  const u32 t_entry = (u32)__builtin_amdgcn_s_memrealtime();
  u32 pf;
  { u64 pc; u32 len;
    asm volatile("s_getpc_b64 %0\n.Lpf%=:\n\ts_mov_b32 %1, k_tail_mark-.Lpf%=" : "=s"(pc), "=s"(len));
    pf = __builtin_amdgcn_raw_buffer_load_b32(__builtin_amdgcn_make_buffer_rsrc(reinterpret_cast<void*>(pc), 0, len < 131072u ? len : 0u, 0x00020000), CPFM ? (threadIdx.x < 16 ? ((blockIdx.x >> 3) * 16 + threadIdx.x) * 128 : 0x7ffff000) : threadIdx.x * 128, 0, 0);
    __builtin_amdgcn_sched_barrier(0); }
  u64 tag = (hw_dispatch_id() + 1ull) * 0x9E3779B97F4A7C15ull;
  tag ^= ((u64)__builtin_amdgcn_dispatch_ptr() >> 6) * 0xC2B2AE3D27D4EB4Full;
  tag |= 1ull << 63;
  if (__builtin_expect(blockIdx.x >= NBW, 1)) part_a(a, lds, blockIdx.x - NBW, tag, pf);
  else if (__builtin_amdgcn_readfirstlane(threadIdx.x >> 6) < 8) part_b<false>(a, lds, blockIdx.x, tag, pf);
  else part_b<true>(a, lds, blockIdx.x, tag, pf);

  {
    const u32 tg = (u32)(tag >> 32); const int tid = threadIdx.x;
    u32* sl = reinterpret_cast<u32*>(lds + L_ST + 3072);
    const u32 t_end = (u32)__builtin_amdgcn_s_memrealtime();
    __syncthreads();
    if (tid == 0) { sl[15] = t_entry; sl[14] = t_end; sl[13] = reinterpret_cast<u32*>(lds + L_ST + 2048)[0]; sl[12] = reinterpret_cast<u32*>(lds + L_ST + 2048)[1]; }
    __syncthreads();
    u64* gl = reinterpret_cast<u64*>(a.ws + (16u << 20));
    while ((int)((u32)__builtin_amdgcn_s_memrealtime() - (t_entry + 7000u)) < 0) __builtin_amdgcn_s_sleep(8);
    if (tid < 16) __hip_atomic_store(gl + blockIdx.x * 16 + tid, ((u64)tg << 32) | sl[tid], RLX_AGENT);
    if (blockIdx.x == 0) {
      u32* sh = reinterpret_cast<u32*>(lds);
      u32 v[16];
      if (tid < 256) {
        for (int k = 0; k < 16; ++k) { u64 x; for (int s = 0; s < 1000000; ++s) { x = __hip_atomic_load(gl + tid * 16 + k, RLX_AGENT); if ((u32)(x >> 32) == tg) break; __builtin_amdgcn_s_sleep(4); } v[k] = (u32)x; }
        sh[tid] = v[15] - t_entry + 1000u;
      }
      __syncthreads();
      u32 t0 = ~0u; for (int i = 0; i < 256; ++i) t0 = sh[i] < t0 ? sh[i] : t0;
      t0 = t0 - 1000u + t_entry;
      __syncthreads();
      const bool isB = tid < NBW; u32 x = 0; int mode = 0;
      if (DIAG == 1) x = v[14] - t0;
      if (DIAG == 2) x = v[15] - t0;
      if (DIAG >= 100 && DIAG < 116) { x = isB ? 0 : v[DIAG - 100] - t0; mode = 1; }
      if (DIAG >= 200 && DIAG < 216) { x = isB ? 0 : v[DIAG - 200] - t0; }
      if (DIAG >= 300 && DIAG < 316) { x = isB ? v[DIAG - 300] - t0 : 0; }
      if (DIAG >= 400 && DIAG < 416) { x = isB ? v[DIAG - 400] - t0 : 0; mode = 2; }
      if (DIAG == 3) { x = isB ? 0 : v[13]; mode = 1; }
      if (tid < 256) sh[tid] = x;
      __syncthreads();
      u32 X = 0;
      if (mode == 0) { for (int i = 0; i < 256; ++i) X = sh[i] > X ? sh[i] : X; X *= 4; }
      else { for (int i = 0; i < 256; ++i) X += sh[i]; X = X * 4 / (mode == 1 ? 224 : 32); }
      if (X > 40000u) X = 40000u;
      const u32 tgt = t0 + 10000u + X;
      for (int i = 0; i < 4000000; ++i) { if ((int)((u32)__builtin_amdgcn_s_memrealtime() - tgt) >= 0) break; __builtin_amdgcn_s_sleep(1); }
    }
  }
}

extern "C" __global__ void k_tail_mark() {}

static void* g_ws;
extern "C" void kernel_launch(void* const* d_in, const int* in_sizes, int n_in, void* d_out, int out_size, void* d_ws, size_t ws_size,
                              hipStream_t stream) {
  Args a;
  a.xA = (const u16*)d_in[0]; a.K = (const u16*)d_in[1]; a.V = (const u16*)d_in[2]; a.wq = (const u16*)d_in[3]; a.wo = (const u16*)d_in[4];
  a.xB = (const u16*)d_in[5]; a.C = (const u16*)d_in[6]; a.wqB = (const u16*)d_in[7]; a.wuk = (const u16*)d_in[8]; a.wuv = (const u16*)d_in[9]; a.woB = (const u16*)d_in[10];
#define WSTOP 32
  if (WSTOP && ws_size >= ((size_t)WSTOP << 20)) d_ws = (unsigned char*)d_ws + ((ws_size - ((size_t)WSTOP << 20)) & ~(size_t)4095);
  g_ws = d_ws;
  a.ws = (unsigned char*)d_ws;
  a.out = (u16*)d_out;
  k_main<<<NBW + NAW, 1024, 0, stream>>>(a);
}
```

```cpp
#define DIAG 100
#include <hip/hip_runtime.h>
#include <stdint.h>
#include <utility>

typedef unsigned short u16;
typedef unsigned u32;
typedef unsigned long long u64;
typedef u32 u32x4 __attribute__((ext_vector_type(4)));
typedef u32 u32x2 __attribute__((ext_vector_type(2)));
typedef float f32x4 __attribute__((ext_vector_type(4)));
typedef float f32x2 __attribute__((ext_vector_type(2)));
typedef short s16x4 __attribute__((ext_vector_type(4)));
typedef __bf16 bf16x8 __attribute__((ext_vector_type(8)));
typedef __bf16 bf16x2 __attribute__((ext_vector_type(2)));
#define DI __device__ __forceinline__
#define RLX_AGENT __ATOMIC_RELAXED, __HIP_MEMORY_SCOPE_AGENT
extern "C" __device__ u64 hw_dispatch_id() __asm("llvm.amdgcn.dispatch.id");

template <int... Is, class F> DI void static_for_impl(std::integer_sequence<int, Is...>, F&& f) { (f(std::integral_constant<int, Is>{}), ...); }
template <int N, class F> DI void static_for(F&& f) { static_for_impl(std::make_integer_sequence<int, N>{}, f); }

constexpr int H = 32, SK = 4096, DM = 576, DC = 512;
constexpr int ACU = 7;
constexpr int NBW = 32;
constexpr int NAW = H * ACU;
constexpr int NSP = 8;
constexpr float C2 = 0.08838834764831845f * 1.4426950408889634f;

constexpr size_t WS_F1 = 0, WS_F2 = 512;
constexpr size_t WS_QABS = 4096;
constexpr size_t WS_CTXP = WS_QABS + 4 * 32 * 512 * 2;
constexpr size_t WS_MLB = WS_CTXP + (size_t)4 * NSP * 32 * 512 * 2;
constexpr size_t WS_Y = WS_MLB + 4 * NSP * 32 * 2 * 4;
constexpr size_t WS_MLA = WS_Y + (size_t)72 * 256 * 128;
constexpr size_t WS_RPT = WS_MLA + 256 * 128;
constexpr size_t WS_QA = WS_RPT + 64 * 4480;
constexpr size_t WS_END = WS_QA + 256 * 2048;

constexpr int CRS = 1056;
constexpr int CBUF = 64 * CRS;
constexpr int L_BIG = 0;
constexpr int L_AM = 2 * CBUF;
constexpr int L_QS = L_AM + 16 * 272;
constexpr int L_ST = L_QS + 1024;
constexpr int L_PF = L_ST + 4096;
constexpr int L_TOTAL = L_PF + 4096;
constexpr int AM_LD = 136;
constexpr int AM2_LD = 520;
constexpr int L_AM2 = 69632, L_CP = 88064, L_W8 = 106496;

#define ST(i) do { if (threadIdx.x == 0) reinterpret_cast<u32*>(lds + L_ST + 3072)[i] = (u32)__builtin_amdgcn_s_memrealtime(); } while (0)
struct Args {
  const u16 *xA, *K, *V, *wq, *wo, *xB, *C, *wqB, *wuk, *wuv, *woB;
  unsigned char* ws;
  u16* out;
};

DI float bf_lo(u32 x) { return __uint_as_float(x << 16); }
DI float bf_hi(u32 x) { return __uint_as_float(x & 0xffff0000u); }
DI u32 pk_bf16(float lo, float hi) { bf16x2 v = {(__bf16)lo, (__bf16)hi}; return __builtin_bit_cast(u32, v); }
DI u16 to_bf16(float x) { return __builtin_bit_cast(u16, (__bf16)x); }
DI float bf2f(u16 x) { return __uint_as_float(((u32)x) << 16); }
DI float dot2(u32 a, u32 b, float c) { return __builtin_amdgcn_fdot2_f32_bf16(__builtin_bit_cast(bf16x2, a), __builtin_bit_cast(bf16x2, b), c, false); }
template <int CTRL> DI float dpp(float x) { return __builtin_bit_cast(float, __builtin_amdgcn_update_dpp(0, __builtin_bit_cast(int, x), CTRL, 0xf, 0xf, true)); }
DI float row16_sum(float x) { x += dpp<0x128>(x); x += dpp<0x124>(x); x += dpp<0x122>(x); x += dpp<0x121>(x); return x; }
DI float row16_max(float x) { x = fmaxf(x, dpp<0x128>(x)); x = fmaxf(x, dpp<0x124>(x)); x = fmaxf(x, dpp<0x122>(x)); x = fmaxf(x, dpp<0x121>(x)); return x; }
DI float xg_max(float x) {
  auto s = __builtin_amdgcn_permlane16_swap(__float_as_uint(x), __float_as_uint(x), false, false);
  x = fmaxf(__uint_as_float(s[0]), __uint_as_float(s[1]));
  auto t = __builtin_amdgcn_permlane32_swap(__float_as_uint(x), __float_as_uint(x), false, false);
  return fmaxf(__uint_as_float(t[0]), __uint_as_float(t[1]));
}
DI float xg_sum(float x) {
  auto s = __builtin_amdgcn_permlane16_swap(__float_as_uint(x), __float_as_uint(x), false, false);
  x = __uint_as_float(s[0]) + __uint_as_float(s[1]);
  auto t = __builtin_amdgcn_permlane32_swap(__float_as_uint(x), __float_as_uint(x), false, false);
  return __uint_as_float(t[0]) + __uint_as_float(t[1]);
}
DI float sw32(float a, float b) {
  auto s = __builtin_amdgcn_permlane32_swap(__float_as_uint(a), __float_as_uint(b), false, false);
  return __uint_as_float(s[0]) + __uint_as_float(s[1]);
}
DI float sw16(float a, float b) {
  auto s = __builtin_amdgcn_permlane16_swap(__float_as_uint(a), __float_as_uint(b), false, false);
  return __uint_as_float(s[0]) + __uint_as_float(s[1]);
}
DI f32x4 mfma16(u32x4 a, u32x4 b, f32x4 c) { return __builtin_amdgcn_mfma_f32_16x16x32_bf16(__builtin_bit_cast(bf16x8, a), __builtin_bit_cast(bf16x8, b), c, 0, 0, 0); }
DI u32x4 ld16(const u16* p) { return __builtin_nontemporal_load(reinterpret_cast<const u32x4*>(p)); }
DI u32x2 ld8(const u16* p) { return __builtin_nontemporal_load(reinterpret_cast<const u32x2*>(p)); }
DI u32x2 trread(const unsigned char* p) {
  return __builtin_bit_cast(u32x2, __builtin_amdgcn_ds_read_tr16_b64_v4i16((__attribute__((address_space(3))) s16x4*)(p)));
}
DI u32 prm(u32 hi, u32 lo, int odd) { return __builtin_amdgcn_perm(hi, lo, odd ? 0x07060302u : 0x05040100u); }

DI u32x4 tcol16(const u32x4 (&R)[8], int t) {
  u32x4 T;
#pragma unroll
  for (int q = 0; q < 4; ++q) T[q] = prm(R[2 * q + 1][t >> 1], R[2 * q][t >> 1], t & 1);
  return T;
}
DI u32x4 tcol8(const u32x2 (&R)[8], int t) {
  u32x4 T;
#pragma unroll
  for (int q = 0; q < 4; ++q) T[q] = prm(R[2 * q + 1][t >> 1], R[2 * q][t >> 1], t & 1);
  return T;
}

#define SPINLOG 22
#define PSL 2
constexpr u32 SPIN_CAP = 1u << SPINLOG;
DI void wave_wait(const u64* f, bool active, u64 tag) {
  for (u32 spins = 0; spins < SPIN_CAP; ++spins) {
    const u64 v = active ? __hip_atomic_load(f, RLX_AGENT) : tag;
    if (__all(v == tag)) break;
    __builtin_amdgcn_s_sleep(PSL);
  }
}
DI void wg_wait64(const unsigned char* flags, u64 tag, int pw = 0) {
  if ((int)(threadIdx.x >> 6) == pw) {
    wave_wait(reinterpret_cast<const u64*>(flags) + (threadIdx.x & (NBW - 1)), true, tag);
    asm volatile("s_waitcnt vmcnt(0)" ::: "memory");
  }
  __syncthreads();
}
DI void st16_sc1(__amdgpu_buffer_rsrc_t rs, size_t off, u32x4 v) { __builtin_amdgcn_raw_buffer_store_b128(v, rs, (int)off, 0, 16); }
DI void st8_sc1(unsigned char* p, f32x2 v) { __hip_atomic_store(reinterpret_cast<u64*>(p), __builtin_bit_cast(u64, v), RLX_AGENT); }

struct QPre { u32x4 R0[8]; u32x4 R1; u32x2 xa, xb; u32 x1; };
DI void qproj_load(QPre& Q, const u16* x, const u16* Wh, int w, int lane) {
  const int j = lane & 15, g = lane >> 4;
  Q.xa = u32x2{0u, 0u}; Q.xb = u32x2{0u, 0u}; Q.x1 = 0u;
  if (j < 4) { Q.xa = ld8(x + j * DM + 36 * w + 8 * g); Q.xb = ld8(x + j * DM + 36 * w + 8 * g + 4); Q.x1 = x[j * DM + 36 * w + 32 + g]; }
#pragma unroll
  for (int jj = 0; jj < 8; ++jj) Q.R0[jj] = ld16(Wh + (size_t)(36 * w + 8 * g + jj) * 4096 + 8 * j);
  Q.R1 = ld16(Wh + (size_t)(36 * w + 32 + g) * 4096 + 8 * j);
}
DI void qproj_mfma(const QPre& Q, unsigned char* lds, int w, int lane) {
  const int j = lane & 15, g = lane >> 4;
  float* qpart = reinterpret_cast<float*>(lds + L_BIG);
  const u32x4 a = {Q.xa[0], Q.xa[1], Q.xb[0], Q.xb[1]}, a1 = {Q.x1, 0u, 0u, 0u};
#pragma unroll
  for (int hf = 0; hf < 2; ++hf) {
    f32x4 acc[4];
#pragma unroll
    for (int t = 0; t < 4; ++t) acc[t] = mfma16(a, tcol16(Q.R0, 4 * hf + t), f32x4{0.f, 0.f, 0.f, 0.f});
#pragma unroll
    for (int t = 0; t < 4; ++t) { const u32x4 bb = {(t & 1) ? (Q.R1[2 * hf + (t >> 1)] >> 16) : (Q.R1[2 * hf + (t >> 1)] & 0xffffu), 0u, 0u, 0u}; acc[t] = mfma16(a1, bb, acc[t]); }
    if (g == 0) {
#pragma unroll
      for (int r = 0; r < 4; ++r) *reinterpret_cast<f32x4*>(qpart + (w * 4 + r) * 128 + 8 * j + 4 * hf) = f32x4{acc[0][r], acc[1][r], acc[2][r], acc[3][r]};
    }
    __builtin_amdgcn_sched_barrier(0);
  }
}
DI void wo_load(const u16* Wo, u32x2 (&R)[4][8], int nb, int lane, int s0, int s1) {
  const int j = lane & 15, g = lane >> 4;
#pragma unroll
  for (int s = 0; s < 4; ++s) if (s >= s0 && s < s1)
#pragma unroll
    for (int jj = 0; jj < 8; ++jj) R[s][jj] = ld8(Wo + (size_t)(32 * s + 8 * g + jj) * DM + 64 * nb + 4 * j);
}
DI void wo_mfma(const u32x2 (&R)[4][8], const unsigned char* lds, int lane, f32x4 (&acc)[4]) {
  const int j = lane & 15, g = lane >> 4;
#pragma unroll
  for (int t = 0; t < 4; ++t) acc[t] = f32x4{0.f, 0.f, 0.f, 0.f};
#pragma unroll
  for (int s = 0; s < 4; ++s) {
    const u32x4 a = *reinterpret_cast<const u32x4*>(lds + L_AM + j * 272 + (32 * s + 8 * g) * 2);
#pragma unroll
    for (int t = 0; t < 4; ++t) acc[t] = mfma16(a, tcol8(R[s], t), acc[t]);
  }
}
DI u32x4 yrow(const f32x4 (&acc)[4], u32 tg) {
  return u32x4{pk_bf16(acc[0][0] + acc[0][1], acc[1][0] + acc[1][1]), tg, pk_bf16(acc[2][0] + acc[2][1], acc[3][0] + acc[3][1]), tg};
}
DI void am_zero(unsigned char* lds, int tid) {
  if (tid < 136) { const int pr = tid / 34; *reinterpret_cast<u32x4*>(lds + L_AM + (4 * pr + 2) * 272 + (tid - 34 * pr) * 16) = u32x4{0u, 0u, 0u, 0u}; }
}
DI float rdlane(float x, int l) { return __builtin_bit_cast(float, __builtin_amdgcn_readlane(__builtin_bit_cast(int, x), l)); }

constexpr int NS = 10;
constexpr int NSEQ = 8 * NS;
#define RNG 9
#define NP 3
#define QP 5
#define SP 4
#define BPF 2
#define K1G 1
#define PSKIP 0
#define D3FROM 66
#define D4FROM 99
#define ROTM 1
#define OAUX 0
#define DEP 2
constexpr int DEPTH = DEP, RING = RNG, RAMP = 2;
constexpr int issued(int i) {
  int d = RING - (i + RAMP - 1) / RAMP; if (d < DEPTH) d = DEPTH;
  if (i >= D3FROM && d < 3) d = 3;
  if (i >= D4FROM && d < 4) d = 4;
  int e = i + d; if (e < RING) e = RING; return e > NSEQ ? NSEQ : e;
}
#define PACE 1
#define YDEF 1
#define WO0 16
#define WON 36
#define TA1 535
#define TA2 440
#define NK 68
#define ORG0 130
#define SLMAX 80
#define MAXGAP 70
constexpr int cmin(int a, int b) { return a < b ? a : b; }
#define RSH 1
#define RD 4
#define BMAP 1
#define EBIG 9999
#define NJ 54
#define TA3 515
constexpr int due_at(int n) { return (n <= NJ ? n * TA1 : n <= NK ? NJ * TA1 + (n - NJ) * TA3 : NJ * TA1 + (NK - NJ) * TA3 + (n - NK) * TA2) / 10; }
#define RTAB 20, 30, 40, 50, 60, 70
#define GTAB 0, 12, 6, 12, 12, 12
#define BTAB 8, 12, 6, 12, 12, 0
#define MISS 40
#define DB 10
#define DTAB 20, 20, 20, 15, 5, 3, 3, 3
constexpr int rtab[] = {RTAB}, gtab[] = {GTAB}, btab[] = {BTAB}, dtab[] = {DTAB};
constexpr int NRND = sizeof(rtab) / sizeof(int);
constexpr int rgap(int k) { return k ? rtab[k] - rtab[k - 1] : rtab[0] - RING; }
constexpr int rnd_at(int n) { for (int k = 0; k < NRND; ++k) if (rtab[k] == n) return k; return -1; }
static_assert(rtab[0] >= RING + 2 && rtab[NRND - 1] + RD + 2 < NSEQ, "");
constexpr int wo_step(int k) { return WO0 + k * WON / 32; }
#define WTS 54
#define PJ0 58
#define PJ1 63
#define PJ2 72
#define XW 13
#define XR 50
#define FDLY 8
#define FSL 2
#define STEPST 0
#define CPFM 1
#define STAG 16
DI u32 rt32() { return (u32)__builtin_amdgcn_s_memrealtime(); }
constexpr int OOB = 0x7ffff000;
constexpr int L_AB = 32768;
DI void part_a(const Args& a, unsigned char* lds, int idx, u64 tag, u32 pf) {
  const u64 T64 = __builtin_amdgcn_s_memrealtime();
  __builtin_amdgcn_sched_barrier(0);
  const int tid = threadIdx.x, lane = tid & 63, w = __builtin_amdgcn_readfirstlane(tid >> 6);
  const int j = lane & 15, g = lane >> 4;
  const int h = (idx & 7) * 4 + ((idx >> 3) & 3), ci = idx >> 5;
  const int rr = (((h & 3) * 5) >> 1) + (h >> 2), rot = ROTM ? (rr >= NS ? rr - NS : rr) : (h * 5) >> 4, l0 = (ci * 37) >> 4, ln = ((ci * 37 + 37) >> 4) - l0;
  const int bad_sl = (w < ln) ? -1 : NS - 1 - rot;
  int vtab;
  { const int p = lane + rot >= NS ? lane + rot - NS : lane + rot;
    vtab = p == NS - 1 ? (w < ln ? (h << 20) + (1008 + l0 + w) * 1024 : 1 << 30) : (h << 20) + (p * 7 + ci) * 16384 + w * 1024; }
  const bool prod = ci < 2 * NP && w < 9 / NP;
  const int r0 = ((ci >> 1) * (9 / NP) + w) * 64;
  int voff = lane * 16, wqo = ((r0 + 32 * (j >> 3) + 8 * g) * 4096 + h * 128 + (ci & 1) * 64 + 8 * (j & 7)) * 2;
  int xo = j < 8 ? ((j & 3) * DM + r0 + 32 * (j >> 2) + 8 * g) * 2 : OOB;
  const int prow = (idx >> 3) * 40 + (w - 3) * 4;
  int pfo = (w >= 3 && w < 13 && prow < DM + DC && prow >= PSKIP) ? ((prow < DM ? prow : prow - DM) + g) * 8192 + (idx & 7) * 1024 + j * 64 : OOB;
  int so0 = __builtin_amdgcn_readlane(vtab, 0);
  asm volatile("" : "+v"(vtab), "+v"(voff), "+v"(wqo), "+v"(xo), "+v"(pfo), "+s"(so0));
  __builtin_amdgcn_sched_barrier(0);
  asm volatile("" :: "s"(a.xB), "s"(a.C), "s"(a.wqB), "s"(a.wuk), "s"(a.wuv), "s"(a.woB), "s"(T64));
  u32 T0 = (u32)T64;
  const u32 toff = (u32)(w * TA2) / (STAG * 10);
  u32* lorg = reinterpret_cast<u32*>(lds + L_ST + 2048);
  const u32 tg = (u32)(tag >> 32);
  u32 last = T0;
  u32x2 rv = {0u, 0u}; u32 rt = 0;
  const __amdgpu_buffer_rsrc_t rs = __builtin_amdgcn_make_buffer_rsrc(a.ws, 0, (int)WS_END, 0x00020000);
  const __amdgpu_buffer_rsrc_t rk = __builtin_amdgcn_make_buffer_rsrc(const_cast<u16*>(a.K), 0, 1 << 27, 0x00020000);
  const __amdgpu_buffer_rsrc_t rvv = __builtin_amdgcn_make_buffer_rsrc(const_cast<u16*>(a.V), 0, 1 << 27, 0x00020000);
  const __amdgpu_buffer_rsrc_t rw = __builtin_amdgcn_make_buffer_rsrc(const_cast<u16*>(a.wq), 0, DM * 4096 * 2, 0x00020000);
  const __amdgpu_buffer_rsrc_t rx = __builtin_amdgcn_make_buffer_rsrc(const_cast<u16*>(a.xA), 0, 4 * DM * 2, 0x00020000);
  ST(0);
  u32x4 R[RING];
#define A_ISSUE(i) R[(i) % RING] = __builtin_amdgcn_raw_buffer_load_b128((i) >= 4 * NS ? rvv : rk, voff, ((i) == 0 ? so0 : __builtin_amdgcn_readlane(vtab, (i) % NS)) + ((((i) / NS) & 3) << 25), 2)
#define GATE(n) do { if (PACE) { u32 due = T0 + toff + org + ((osl * cmin(n, NK)) >> 4) + (u32)due_at(n); if (MAXGAP && (int)(due - (last + MAXGAP)) > 0) due = last + MAXGAP; \
    while ((int)(now - due) < 0) { __builtin_amdgcn_s_sleep(1); now = rt32(); } last = now; } } while (0)
#define CTRL(n) do { if constexpr (PACE) { \
    if constexpr (rnd_at((n) - RD - 2) >= 0) { constexpr int k = rnd_at((n) - RD - 2); \
      const u32 dt = (rv[1] == (u32)tag + k) ? rv[0] - T0 : rt; \
      const float sm = row16_sum((float)(int)dt); \
      const int o = (int)((RSH ? rdlane(sm, 0) * (1.f / 16.f) : (rdlane(sm, 0) + rdlane(sm, 16)) * (1.f / 32.f))) - due_at(rtab[k]); \
      if (w == ((5 * k + 11) & 15) && lane == 0 && o > -300 && o < 2000) { \
        int e = o - (int)org - (int)((osl * cmin(rtab[k], NK)) >> 4) - dtab[k]; if (e < 0) e = 0; \
        int sl_ = ((int)osl * gtab[k]) / 16 + ((e > EBIG ? 0 : e) * btab[k]) / rgap(k); sl_ = sl_ > SLMAX ? SLMAX : sl_; \
        if (STEPST) lorg[16 + k] = (u32)(e & 0xffff) | ((u32)sl_ << 16); \
        __hip_atomic_store(reinterpret_cast<u64*>(lorg), __builtin_bit_cast(u64, u32x2{(u32)((int)org + e + ((((int)osl - sl_) * cmin(n, NK)) >> 4)), (u32)sl_}), __ATOMIC_RELAXED, __HIP_MEMORY_SCOPE_WORKGROUP); } } \
    if constexpr (rnd_at(n) >= 0) { constexpr int k = rnd_at(n); \
      u32 tot = 0; \
      if (ci == k % ACU && (h & ((1 << RSH) - 1)) == (k & ((1 << RSH) - 1))) { const u32 mine = (1u << 24) + ((rt32() - toff - T0) & 0xfffffu); \
        if (lane == 0) tot = __hip_atomic_fetch_add(lorg + 8 + k, mine, __ATOMIC_RELAXED, __HIP_MEMORY_SCOPE_WORKGROUP) + mine; } \
      __builtin_amdgcn_raw_buffer_store_b64(u32x2{T0 + ((tot & 0xffffffu) >> 4), (u32)tag + k}, rs, (tot >> 24) == 16u ? (int)WS_RPT + (h >> RSH) * 4352 : OOB, 128 * k * 35, 16); } \
    if constexpr (rnd_at((n) - RD) >= 0) { constexpr int k = rnd_at((n) - RD); \
      rv = __builtin_amdgcn_raw_buffer_load_b64(rs, (w == ((5 * k + 11) & 15) && lane < (32 >> RSH)) ? (int)WS_RPT + lane * 4352 : OOB, 128 * k * 35, 16); rt = rt32() - T0 - MISS; } \
  } } while (0)
  u32x4 Wr[8], xf;
  u32 pf2 = 0;
  if (BPF == 1) pf2 = __builtin_amdgcn_raw_buffer_load_b32(__builtin_amdgcn_make_buffer_rsrc(const_cast<u16*>(prow < DM ? a.wqB : a.wuk), 0, DM * 4096 * 2, 0x00020000), pfo, 0, 2);
  A_ISSUE(0);
  __builtin_amdgcn_sched_barrier(0);
  if (prod) {
#pragma unroll
    for (int k = 0; k < 8; ++k) Wr[k] = __builtin_amdgcn_raw_buffer_load_b128(rw, wqo, k * 8192, 2);
    xf = __builtin_amdgcn_raw_buffer_load_b128(rx, xo, 0, 2);
  }
  __builtin_amdgcn_sched_barrier(0);
  if (BPF == 2) pf2 = __builtin_amdgcn_raw_buffer_load_b32(__builtin_amdgcn_make_buffer_rsrc(const_cast<u16*>(prow < DM ? a.wqB : a.wuk), 0, DM * 4096 * 2, 0x00020000), pfo, 0, 2);
  if (!K1G) A_ISSUE(1);
  __builtin_amdgcn_raw_buffer_store_b64(u32x2{T0, (u32)tag + 15u}, rs, (ci == 6 && (h & 1) == 0 && tid == 0) ? (int)WS_RPT + (h >> 1) * 4352 : OOB, 128 * 15 * 35, 16);
  __builtin_amdgcn_sched_barrier(0);
  if (tid < 32) lorg[tid] = tid == 0 ? ORG0 : tid == 2 ? T0 : 0u;
  __syncthreads();
  T0 = __builtin_amdgcn_readfirstlane(lorg[2]);
  if (prod) {
    float* qpart = reinterpret_cast<float*>(lds + L_BIG);
#pragma unroll
    for (int hf = 0; hf < 2; ++hf) {
      f32x4 d4[4];
#pragma unroll
      for (int t = 0; t < 4; ++t) d4[t] = mfma16(xf, tcol16(Wr, 4 * hf + t), f32x4{0.f, 0.f, 0.f, 0.f});
      if (g == (j >> 3)) {
#pragma unroll
        for (int r = 0; r < 4; ++r) *reinterpret_cast<f32x4*>(qpart + ((w * 2 + g) * 4 + r) * 64 + 8 * (j & 7) + 4 * hf) = f32x4{d4[0][r], d4[1][r], d4[2][r], d4[3][r]};
      }
    }
    asm volatile("" ::: "memory");
    ST(12);
    u32 arr = 0;
    if (lane == 0) arr = __hip_atomic_fetch_add(lorg + 4, 1u, __ATOMIC_RELAXED, __HIP_MEMORY_SCOPE_WORKGROUP);
    if (__builtin_amdgcn_readfirstlane(arr) == 9 / NP - 1) {
      asm volatile("" ::: "memory");
      f32x4 s0 = {0.f, 0.f, 0.f, 0.f};
#pragma unroll
      for (int k = 0; k < 18 / NP; ++k) s0 += *reinterpret_cast<const f32x4*>(qpart + k * 256 + 4 * lane);
      const int qo = (int)WS_QA + (h * 8 + ci) * 2048 + lane * 16;
      st16_sc1(rs, qo, u32x4{__float_as_uint(s0[0]), tg, __float_as_uint(s0[1]), tg});
      st16_sc1(rs, qo + 1024, u32x4{__float_as_uint(s0[2]), tg, __float_as_uint(s0[3]), tg});
    }
  }
  static_for<RING - 2 + K1G>([&](auto tc) __attribute__((always_inline)) {
    constexpr int t = tc.value + 2 - K1G;
    u32 now = rt32();
    const u32 org = __builtin_amdgcn_readfirstlane(__hip_atomic_load(lorg, __ATOMIC_RELAXED, __HIP_MEMORY_SCOPE_WORKGROUP)), osl = 0;
    GATE(t);
    A_ISSUE(t);
    __builtin_amdgcn_sched_barrier(0);
    if constexpr (t == SP) {
      if (w == 13) {
        const u32x2 sv = __builtin_amdgcn_raw_buffer_load_b64(rs, lane < 16 ? (int)WS_RPT + lane * 4352 : OOB, 128 * 15 * 35, 16);
        const bool ok = sv[1] == (u32)tag + 15u;
        const float sm = row16_sum(ok ? (float)(int)(sv[0] - T0) : 0.f), cn = row16_sum(ok ? 1.f : 0.f);
        const int o = ORG0 + (cn > 0.f ? (int)(sm / cn) : 0);
        if (lane == 0 && o > -300 && o < 1000) lorg[0] = (u32)o;
      }
    }
    if constexpr (t == QP) {
      if (w >= 14) {
        const int qo = (int)WS_QA + (h * 8 + (w & 1)) * 2048 + lane * 16;
        u32x4 v[2 * NP];
        if (tid == 14 * 64) reinterpret_cast<u32*>(lds + L_ST + 3072)[3] = rt32();
#pragma unroll 1
        for (u32 spins = 0; spins < SPIN_CAP; ++spins) {
          bool ok = true;
#pragma unroll
          for (int k = 0; k < 2 * NP; ++k) v[k] = __builtin_amdgcn_raw_buffer_load_b128(rs, qo, (k >> 1) * 4096 + (k & 1) * 1024, 16);
#pragma unroll
          for (int k = 0; k < 2 * NP; ++k) ok = ok && v[k][1] == tg && v[k][3] == tg;
          if (__all(ok)) break;
          __builtin_amdgcn_s_sleep(4);
        }
        f32x4 s0 = {0.f, 0.f, 0.f, 0.f};
#pragma unroll
        for (int k = 0; k < NP; ++k) s0 += f32x4{__uint_as_float(v[2 * k][0]), __uint_as_float(v[2 * k][2]), __uint_as_float(v[2 * k + 1][0]), __uint_as_float(v[2 * k + 1][2])};
        *reinterpret_cast<u32x2*>(lds + L_QS + (g * 128 + (w & 1) * 64 + 4 * j) * 2) = u32x2{pk_bf16(s0[0], s0[1]), pk_bf16(s0[2], s0[3])};
        asm volatile("" ::: "memory");
        if (lane == 0) __hip_atomic_fetch_add(lorg + 5, 1u, __ATOMIC_RELAXED, __HIP_MEMORY_SCOPE_WORKGROUP);
        if (tid == 14 * 64) reinterpret_cast<u32*>(lds + L_ST + 3072)[6] = rt32();
      }
    }
    __builtin_amdgcn_sched_barrier(0);
  });
  while (__hip_atomic_load(lorg + 5, __ATOMIC_RELAXED, __HIP_MEMORY_SCOPE_WORKGROUP) != 2u) __builtin_amdgcn_s_sleep(1);
  asm volatile("" :: "v"(pf), "v"(pf2) : "memory");
  ST(1);
  float* wtab = reinterpret_cast<float*>(lds + L_ST + 1024);
  u32x2 xv = {0u, 0u};
  const int xoff = (w == XW && lane < 8 * ACU) ? (int)WS_MLA + (h * 8 + (lane >> 3)) * 128 + (lane & 7) * 8 : OOB;
  float* pml = reinterpret_cast<float*>(lds + L_ST);
  u32x4 q;
  float acc[8], S[4];
  u32x2 RW[4][8];
  u32x4 WT[4][4];
  float Y[4];
  auto project = [&](int b) __attribute__((always_inline)) {
    f32x4 d4[4];
#pragma unroll
    for (int t = 0; t < 4; ++t) d4[t] = f32x4{0.f, 0.f, 0.f, 0.f};
    const f32x4 wt = *reinterpret_cast<const f32x4*>(wtab + b * 16 + 4 * g);
#pragma unroll
    for (int s = 0; s < 4; ++s) {
      const u32x4 af = *reinterpret_cast<const u32x4*>(lds + L_AB + (b * 16 + j) * 272 + (32 * s + 8 * g) * 2);
#pragma unroll
      for (int t = 0; t < 4; ++t) d4[t] = mfma16(af, WT[s][t], d4[t]);
    }
    float y[4];
#pragma unroll
    for (int t = 0; t < 4; ++t) y[t] = fmaf(d4[t][0], wt[0], d4[t][1] * wt[1]) + fmaf(d4[t][2], wt[2], d4[t][3] * wt[3]);
    Y[b] = sw16(sw32(y[0], y[2]), sw32(y[1], y[3]));
  };
  const __amdgpu_buffer_rsrc_t rwo = __builtin_amdgcn_make_buffer_rsrc(const_cast<u16*>(a.wo + (size_t)h * 128 * DM), 0, w < 9 ? 128 * DM * 2 : 0, 0x00020000);
  const int wvo = (8 * g * DM + 64 * w + 4 * j) * 2;
#define WO_ISSUE() static_for<32>([&](auto kc) __attribute__((always_inline)) { constexpr int k = kc.value; \
    if constexpr (wo_step(k) == i) RW[k >> 3][k & 7] = __builtin_amdgcn_raw_buffer_load_b64(rwo, wvo, (32 * (k >> 3) + (k & 7)) * DM * 2, 2); })
  static_for<NSEQ>([&](auto ic) __attribute__((always_inline)) {
    constexpr int i = ic.value, bb = (i / NS) & 3, pass = i / (4 * NS), sl = i % NS;
    u32 now = 0; if (PACE + STEPST > 0) now = rt32();
    u32 org = 0, osl = 0; if (PACE) { const u32x2 ov = __builtin_bit_cast(u32x2, __hip_atomic_load(reinterpret_cast<u64*>(lorg), __ATOMIC_RELAXED, __HIP_MEMORY_SCOPE_WORKGROUP)); org = __builtin_amdgcn_readfirstlane(ov[0]); osl = __builtin_amdgcn_readfirstlane(ov[1]); }
    if constexpr (i == 10 || i == 20 || i == 40 || i == 60) { if (tid == 0) reinterpret_cast<u32*>(lds + L_ST + 3072)[i == 10 ? 8 : i == 20 ? 9 : i == 40 ? 10 : 11] = now; }
    __builtin_amdgcn_sched_barrier(0);
    const u32x4 r = R[i % RING];
    if constexpr (pass == 0) {
      if constexpr (sl == 0) { q = *reinterpret_cast<const u32x4*>(lds + L_QS + (bb * 128 + 8 * j) * 2); S[bb] = -INFINITY; }
      float d = dot2(r[0], q[0], 0.f); d = dot2(r[1], q[1], d); d = dot2(r[2], q[2], d); d = dot2(r[3], q[3], d);
      asm volatile("" : "+v"(d));
      if constexpr (issued(i + 1) > issued(i)) { GATE(issued(i)); WO_ISSUE(); A_ISSUE(issued(i)); CTRL(issued(i)); if constexpr (issued(i + 1) > issued(i) + 1) { GATE(issued(i) + 1); A_ISSUE(issued(i) + 1); CTRL(issued(i) + 1); } } else { WO_ISSUE(); }
      d = row16_sum(d);
      S[bb] = (j == sl) ? d : S[bb];
      if constexpr (sl == NS - 1) {
        const float sc = (j == bad_sl) ? -INFINITY : S[bb] * C2;
        const float m = xg_max(row16_max(sc));
        S[bb] = __builtin_amdgcn_exp2f(sc - m);
        const float l = xg_sum(row16_sum(S[bb]));
        if (lane == 0) *reinterpret_cast<f32x2*>(pml + (bb * 16 + w) * 2) = f32x2{m, l};
      }
      if constexpr (i == 4 * NS - 1) {
        asm volatile("" ::: "memory");
        u32 arr = 0;
        if (lane == 0) arr = __hip_atomic_fetch_add(lorg + 6, 1u, __ATOMIC_RELAXED, __HIP_MEMORY_SCOPE_WORKGROUP);
        arr = __builtin_amdgcn_readfirstlane(arr);
        asm volatile("" ::: "memory");
        const f32x2 ml = *reinterpret_cast<const f32x2*>(pml + lane * 2);
        const float M = row16_max(ml[0]);
        const float L = row16_sum(__builtin_amdgcn_exp2f(ml[0] - M) * ml[1]);
        __builtin_amdgcn_raw_buffer_store_b64(u32x2{__float_as_uint(j ? L : M), tg}, rs, (arr == 15u && j < 2) ? (int)WS_MLA + (h * 8 + ci) * 128 + g * 16 + j * 8 : OOB, 0, 16);
      }
    } else {
      const float p = dpp<0x150 + sl>(S[bb]);
      if constexpr (sl == 0) {
#pragma unroll
        for (int k = 0; k < 8; ++k) acc[k] = 0.f;
      }
#pragma unroll
      for (int k = 0; k < 4; ++k) { acc[2 * k] = fmaf(p, bf_lo(r[k]), acc[2 * k]); acc[2 * k + 1] = fmaf(p, bf_hi(r[k]), acc[2 * k + 1]); }
      asm volatile("" : "+v"(acc[0]), "+v"(acc[1]), "+v"(acc[2]), "+v"(acc[3]), "+v"(acc[4]), "+v"(acc[5]), "+v"(acc[6]), "+v"(acc[7]));
      if constexpr (issued(i + 1) > issued(i)) { GATE(issued(i)); WO_ISSUE(); A_ISSUE(issued(i)); CTRL(issued(i)); if constexpr (issued(i + 1) > issued(i) + 1) { GATE(issued(i) + 1); A_ISSUE(issued(i) + 1); CTRL(issued(i) + 1); } } else { WO_ISSUE(); }
      if constexpr (i == XR) xv = __builtin_amdgcn_raw_buffer_load_b64(rs, xoff, 0, 16);
      if constexpr (i == XR + 2) {
        if (w == XW) {
          if (__builtin_expect(!__all(lane >= 8 * ACU || xv[1] == tg), 0)) {
#pragma unroll 1
            for (u32 spins = 0; spins < SPIN_CAP; ++spins) {
              __builtin_amdgcn_s_sleep(8); xv = __builtin_amdgcn_raw_buffer_load_b64(rs, xoff, 0, 16);
              if (__all(lane >= 8 * ACU || xv[1] == tg)) break;
            }
          }
          const float x = __uint_as_float(xv[0]);
          const float m = lane < 8 * ACU ? dpp<0xA0>(x) : -INFINITY, l = lane < 8 * ACU ? dpp<0xF5>(x) : 0.f;
          const float M = xg_max(fmaxf(m, dpp<0x128>(m)));
          float t = __builtin_amdgcn_exp2f(m - M) * l; t = xg_sum(t + dpp<0x128>(t));
          if (lane < 8 && !(lane & 1)) *reinterpret_cast<f32x2*>(wtab + 64 + lane) = f32x2{M, 1.f / t};
          asm volatile("" ::: "memory");
          const f32x2 gm = *reinterpret_cast<const f32x2*>(wtab + 64 + 2 * g);
          wtab[lane] = __builtin_amdgcn_exp2f(pml[lane * 2] - gm[0]) * gm[1];
          asm volatile("" ::: "memory");
          if (lane == 0) __hip_atomic_store(lorg + 7, 1u, __ATOMIC_RELAXED, __HIP_MEMORY_SCOPE_WORKGROUP);
        }
      }
      if constexpr (i >= WTS && i < WTS + 4) {
        constexpr int s = i - WTS;
#pragma unroll
        for (int t = 0; t < 4; ++t) WT[s][t] = tcol8(RW[s], t);
        asm volatile("" : "+v"(WT[s][0]), "+v"(WT[s][1]), "+v"(WT[s][2]), "+v"(WT[s][3]));
      }
      if constexpr (i == PJ0 || i == PJ1 || i == PJ2) {
        constexpr int b = i == PJ0 ? 0 : i == PJ1 ? 1 : 2;
        Y[b] = 0.f;
        if (w < 9) {
          while (__hip_atomic_load(lorg + 24 + b, __ATOMIC_RELAXED, __HIP_MEMORY_SCOPE_WORKGROUP) != 16u || __hip_atomic_load(lorg + 7, __ATOMIC_RELAXED, __HIP_MEMORY_SCOPE_WORKGROUP) == 0u) __builtin_amdgcn_s_sleep(1);
          asm volatile("" ::: "memory");
          project(b);
        }
        asm volatile("" : "+v"(Y[b]));
      }
      if constexpr (sl == NS - 1) {
        const float t0 = sw16(sw32(acc[0], acc[4]), sw32(acc[2], acc[6])), t1 = sw16(sw32(acc[1], acc[5]), sw32(acc[3], acc[7]));
        *reinterpret_cast<u32*>(lds + L_AB + (bb * 16 + w) * 272 + 16 * j + 4 * g) = pk_bf16(t0, t1);
        if constexpr (bb < 3) { asm volatile("" ::: "memory"); if (lane == 0) __hip_atomic_fetch_add(lorg + 24 + bb, 1u, __ATOMIC_RELAXED, __HIP_MEMORY_SCOPE_WORKGROUP); }
      }
    }
    __builtin_amdgcn_sched_barrier(0);
  });
#undef A_ISSUE
  if (STEPST) { if (lane == 0) { reinterpret_cast<u32*>(lds + 65536)[w * 96 + NSEQ] = rt32(); reinterpret_cast<u32*>(lds + 65536)[w * 96 + NSEQ + 1] = lorg[0]; for (int k = 0; k < 8; ++k) reinterpret_cast<u32*>(lds + 65536)[w * 96 + NSEQ + 2 + k] = lorg[16 + k]; }
    for (int k = lane; k < 96; k += 64) reinterpret_cast<u32*>(a.ws + (20u << 20))[(blockIdx.x * 16 + w) * 96 + k] = reinterpret_cast<u32*>(lds + 65536)[w * 96 + k]; }
  ST(2);
  __syncthreads();
  if (w < 9) {
    project(3);
    st16_sc1(rs, WS_Y + ((8 * w + (j >> 1)) * 256 + idx) * 128 + ((j & 1) * 4 + g) * 16, u32x4{pk_bf16(Y[0], Y[1]), tg, pk_bf16(Y[2], Y[3]), tg});
  }
  ST(4);
  if (idx < 72) {
    __builtin_amdgcn_sched_barrier(0);
    __builtin_amdgcn_s_barrier();
    __builtin_amdgcn_s_sleep(FDLY);
    const int yo = (int)WS_Y + (idx * 256 + 16 * w) * 128 + lane * 16;
    u32x4 v0, v1;
#pragma unroll 1
    for (u32 spins = 0; spins < SPIN_CAP; ++spins) {
      v0 = __builtin_amdgcn_raw_buffer_load_b128(rs, yo, 0, 16); v1 = __builtin_amdgcn_raw_buffer_load_b128(rs, yo + 1024, 0, 16);
      if (__all(v0[1] == tg && v0[3] == tg && v1[1] == tg && v1[3] == tg)) break;
      __builtin_amdgcn_s_sleep(FSL);
    }
    ST(7);
    float y = sw16(sw32(bf_lo(v0[0]) + bf_lo(v1[0]), bf_lo(v0[2]) + bf_lo(v1[2])), sw32(bf_hi(v0[0]) + bf_hi(v1[0]), bf_hi(v0[2]) + bf_hi(v1[2])));
    y += dpp<0x128>(y);
    float* red = reinterpret_cast<float*>(lds + L_PF);
    if (!(lane & 8)) red[((lane & 7) * 4 + g) * 20 + w] = y;
    __syncthreads();
    if (tid < 32) {
      const f32x4 r0 = *reinterpret_cast<const f32x4*>(red + tid * 20), r1 = *reinterpret_cast<const f32x4*>(red + tid * 20 + 4);
      const f32x4 r2 = *reinterpret_cast<const f32x4*>(red + tid * 20 + 8), r3 = *reinterpret_cast<const f32x4*>(red + tid * 20 + 12);
      const f32x4 r = r0 + r1 + r2;
      const __amdgpu_buffer_rsrc_t ro = __builtin_amdgcn_make_buffer_rsrc(a.out, 0, 4 * 1152 * 2, 0x00020000);
      __builtin_amdgcn_raw_buffer_store_b16(to_bf16(((r[0] + r[1]) + (r[2] + r[3])) + (r3[0] + r3[1])), ro, ((tid & 3) * 1152 + idx * 8 + (tid >> 2)) * 2, 0, OAUX);
      __builtin_amdgcn_raw_buffer_store_b16(to_bf16(r3[2] + r3[3]), ro, ((tid >> 3) * 1152 + 576 + idx * 8 + (tid & 7)) * 2, 0, OAUX);
    }
  }
  ST(5);
}

#define IA 2
#define IB 4
#define IC 6
#define EUV 4
#define YOFF 90
#define WPD 4
template <bool LD> DI void part_b(const Args& a, unsigned char* lds, int wg, u64 tag, u32 pf) {
  const int tid = threadIdx.x, lane = tid & 63, w = __builtin_amdgcn_readfirstlane(tid >> 6);
  const int j = lane & 15, g = lane >> 4;
  const __amdgpu_buffer_rsrc_t rs = __builtin_amdgcn_make_buffer_rsrc(a.ws, 0, (int)WS_END, 0x00020000);
  const u32 tb0 = rt32();
  const int h = BPF ? (wg & 7) * 4 + (wg >> 3) : wg;
  const int h3 = h ^ 2;
  const int b2 = BMAP ? (wg & 7) >> 1 : wg >> 3, sp = BMAP ? (wg >> 3) * 2 + (wg & 1) : wg & 7;
  const u16* cl = a.C + ((size_t)b2 * SK + sp * 512 + 8 * (w - 8)) * DC + lane * 8;
  u32x4 X[16];
  u32x2 RW[4][8];
  u32x4 W8[2];
  u32 tv = 0;
#define BST(n) do { if (STEPST && LD) { __builtin_amdgcn_sched_barrier(0); const u32 t_ = rt32(); tv = (lane == (n)) ? t_ : tv; __builtin_amdgcn_sched_barrier(0); } } while (0)
  u32* cnt = reinterpret_cast<u32*>(lds + L_ST + 4064);
#define C_ISSUE(T) _Pragma("unroll") for (int r = 0; r < 8; ++r) X[8 * ((T) & 1) + r] = ld16(cl + (size_t)(64 * (T) + r) * DC)
#define C_HALF(T, hf) _Pragma("unroll") for (int r = 4 * (hf); r < 4 * (hf) + 4; ++r) X[8 * ((T) & 1) + r] = ld16(cl + (size_t)(64 * (T) + r) * DC)
#define L_PART(T, r0, r1) do { __builtin_amdgcn_sched_barrier(0); \
    if ((T) + 3 < 8) { _Pragma("unroll") for (int r = r0; r < r1; ++r) X[8 * (((T) + 3) & 1) + r] = ld16(cl + (size_t)(64 * ((T) + 3) + r) * DC); } \
    if ((T) == 5 || (T) == 6) { _Pragma("unroll") for (int k = 2 * (r0); k < 2 * (r1); ++k) \
      RW[2 * (((T) - 5) & 1) + (k >> 3)][k & 7] = ld8(a.woB + ((size_t)h3 * 128 + 32 * (2 * (((T) - 5) & 1) + (k >> 3)) + 8 * g + (k & 7)) * DM + 64 * (w - 8) + 4 * j); } \
    __builtin_amdgcn_sched_barrier(0); } while (0)
#define UV_LOAD(n) do { const u32x4 v_ = ld16(a.wuv + ((size_t)(64 * w + 32 * ((n) >> 3) + 8 * g + ((n) & 7)) * H + h3) * 128 + 8 * j); \
    RW[2 * ((n) >> 3)][(n) & 7] = u32x2{v_[0], v_[1]}; RW[2 * ((n) >> 3) + 1][(n) & 7] = u32x2{v_[2], v_[3]}; } while (0)
#define C_WIN(T, r0, r1) _Pragma("unroll") for (int r = r0; r < r1; ++r) { asm volatile("s_waitcnt vmcnt(%0)" :: "n"(WPD - 1) : "memory"); X[8 * ((T) & 1) + r] = ld16(cl + (size_t)(64 * (T) + r) * DC); __builtin_amdgcn_sched_barrier(0); }
  u32 sb0 = L_BIG + 8 * (w - 8) * CRS + lane * 16, sb1 = sb0 + CBUF;
  asm volatile("" : "+v"(sb0), "+v"(sb1));
#define C_STAGE(T) _Pragma("unroll") for (int r = 0; r < 8; ++r) *reinterpret_cast<u32x4*>(lds + (((T) & 1) ? sb1 : sb0) + r * CRS) = X[8 * ((T) & 1) + r]
  {
    const u16* Wq = a.wqB + h * 128;
    u32x4 U[8];
    QPre Q;
    if constexpr (!LD) { ST(0); }
    qproj_load(Q, a.xB, Wq, w, lane);
#pragma unroll
    for (int i = 0; i < 8; ++i) U[i] = ld16(a.wuk + ((size_t)(32 * w + 4 * i + (j & 3)) * H + h) * 128 + 8 * ((j & 12) + g));
    __builtin_amdgcn_sched_barrier(0);
    BST(0);
    if constexpr (!LD) am_zero(lds, tid);
    qproj_mfma(Q, lds, w, lane);
    asm volatile("" :: "v"(pf));
    BST(1);
    __syncthreads();
    BST(2);
    if constexpr (LD) { C_HALF(0, 0); }
    else {
      const float* qpart = reinterpret_cast<const float*>(lds + L_BIG);
      float s = 0.f;
#pragma unroll
      for (int ww = 0; ww < 16; ++ww) s += qpart[ww * 512 + tid];
      reinterpret_cast<u16*>(lds + L_QS)[tid] = to_bf16(s);
    }
    __builtin_amdgcn_sched_barrier(0);
    __syncthreads();
    BST(3);
    const u32x4 af = *reinterpret_cast<const u32x4*>(lds + L_QS + ((j >> 2) * 128 + ((j & 3) * 4 + g) * 8) * 2);
    u16* qa = reinterpret_cast<u16*>(lds + L_PF);
    float v2[2] = {0.f, 0.f};
#pragma unroll
    for (int i = 0; i < 8; ++i) {
      const f32x4 d4 = mfma16(af, U[i], f32x4{0.f, 0.f, 0.f, 0.f});
      float d = (j & 8) ? ((j & 4) ? d4[3] : d4[2]) : ((j & 4) ? d4[1] : d4[0]);
      d += dpp<0x124>(d); d += dpp<0x128>(d);
      v2[i >> 2] = ((j >> 2) == (i & 3)) ? d : v2[i >> 2];
    }
    qa[g * 512 + 32 * w + j] = to_bf16(v2[0]); qa[g * 512 + 32 * w + 16 + j] = to_bf16(v2[1]);
    __builtin_amdgcn_sched_barrier(0);
    BST(4);
    __syncthreads();
    if constexpr (!LD) {
      if (w == 0) {
#pragma unroll
        for (int bb = 0; bb < 4; ++bb) st16_sc1(rs, WS_QABS + ((bb * H + h) * 512 + lane * 8) * 2, *reinterpret_cast<const u32x4*>(qa + bb * 512 + lane * 8));
      }
      __builtin_amdgcn_sched_barrier(0);
      __builtin_amdgcn_s_barrier();
      if (w == 0) {
        asm volatile("s_waitcnt vmcnt(0)" ::: "memory");
        if (lane == 0) { __hip_atomic_store(reinterpret_cast<u64*>(a.ws + WS_F1 + wg * 8), tag, RLX_AGENT); cnt[0] = 0u; cnt[1] = 0u; }
      }
    } else {
      __builtin_amdgcn_s_barrier();
      BST(5);
      C_WIN(0, 4, 8); C_WIN(1, 0, 8);
      BST(6);
      C_STAGE(0);
      __builtin_amdgcn_sched_barrier(0);
      BST(7);
      C_WIN(2, 0, 8);
      BST(8);
    }
    if constexpr (!LD) { ST(1); }
  }
  {
    const int b = b2;
    if constexpr (!LD) wg_wait64(a.ws + WS_F1, tag); else __syncthreads();
    if constexpr (!LD) { ST(2); }
    const int kt = w & 3, hh = (w >> 2) & 1;
    float* stmax = reinterpret_cast<float*>(lds + L_ST);
    float* stsum = stmax + 128;
    float* salpha = stmax + 256;
    unsigned char* Pf = lds + L_PF;
    float M = -INFINITY, lsum = 0.f;
    f32x4 acc[2][2];
#pragma unroll
    for (int ct = 0; ct < 2; ++ct) { acc[ct][0] = f32x4{0.f, 0.f, 0.f, 0.f}; acc[ct][1] = f32x4{0.f, 0.f, 0.f, 0.f}; }
    const int troff = (4 * g + (j >> 2)) * CRS + (32 * w + 4 * (j & 3)) * 2;
    auto pv_step = [&](const unsigned char* cb) __attribute__((always_inline)) {
      const f32x4 al0 = *reinterpret_cast<const f32x4*>(salpha + 4 * g), al1 = *reinterpret_cast<const f32x4*>(salpha + 16 + 4 * g);
#pragma unroll
      for (int ct = 0; ct < 2; ++ct) { acc[ct][0] *= al0; acc[ct][1] *= al1; }
#pragma unroll
      for (int blk = 0; blk < 2; ++blk) {
        const u32x4 p0 = *reinterpret_cast<const u32x4*>(Pf + ((blk * 2 + 0) * 64 + lane) * 16);
        const u32x4 p1 = *reinterpret_cast<const u32x4*>(Pf + ((blk * 2 + 1) * 64 + lane) * 16);
#pragma unroll
        for (int ct = 0; ct < 2; ++ct) {
          const u32x2 lo = trread(cb + troff + (32 * blk) * CRS + 32 * ct);
          const u32x2 hi = trread(cb + troff + (32 * blk + 16) * CRS + 32 * ct);
          const u32x4 bf = {lo[0], lo[1], hi[0], hi[1]};
          acc[ct][0] = mfma16(p0, bf, acc[ct][0]); acc[ct][1] = mfma16(p1, bf, acc[ct][1]);
        }
      }
    };
    unsigned char* Qf = lds + L_BIG + CBUF;
    if constexpr (!LD) {
      u32x4 qv[4];
#pragma unroll
      for (int k = 0; k < 4; ++k) qv[k] = __builtin_amdgcn_raw_buffer_load_b128(rs, (int)WS_QABS + (b * H * 512 + (tid + 512 * k) * 8) * 2, 0, 16);
#pragma unroll
      for (int k = 0; k < 4; ++k) { const int e = tid + 512 * k, head = e >> 6, ch = e & 63;
        *reinterpret_cast<u32x4*>(Qf + ((head >> 4) * 16 + (ch >> 2)) * 1040 + ((ch & 3) * 16 + (head & 15)) * 16) = qv[k]; }
    }
    __syncthreads();
    if constexpr (!LD) {
      u32x4 QF[16];
#pragma unroll
      for (int s = 0; s < 16; ++s) QF[s] = *reinterpret_cast<const u32x4*>(Qf + (hh * 16 + s) * 1040 + lane * 16);
      auto s_tile = [&](const unsigned char* cb, bool last) __attribute__((always_inline)) {
        __syncthreads();
        f32x4 s0 = {0.f, 0.f, 0.f, 0.f}, s1 = {0.f, 0.f, 0.f, 0.f};
#pragma unroll
        for (int s = 0; s < 16; s += 2) {
          s0 = mfma16(*reinterpret_cast<const u32x4*>(cb + (16 * kt + j) * CRS + (32 * s + 8 * g) * 2), QF[s], s0);
          s1 = mfma16(*reinterpret_cast<const u32x4*>(cb + (16 * kt + j) * CRS + (32 * s + 32 + 8 * g) * 2), QF[s + 1], s1);
        }
        const f32x4 sv = (s0 + s1) * C2;
        if (last) { __builtin_amdgcn_sched_barrier(0); _Pragma("unroll") for (int n = 0; n < EUV; ++n) UV_LOAD(n); __builtin_amdgcn_sched_barrier(0); }
        const float v = xg_max(fmaxf(fmaxf(sv[0], sv[1]), fmaxf(sv[2], sv[3])));
        if (g == 0) stmax[kt * 32 + 16 * hh + j] = v;
        __syncthreads();
        const float* sm = stmax + 16 * hh + j;
        const float Mn = fmaxf(fmaxf(M, fmaxf(sm[0], sm[32])), fmaxf(sm[64], sm[96]));
        const float alpha = __builtin_amdgcn_exp2f(M - Mn);
        M = Mn;
        u32x2 pk;
        pk[0] = pk_bf16(__builtin_amdgcn_exp2f(sv[0] - Mn), __builtin_amdgcn_exp2f(sv[1] - Mn));
        pk[1] = pk_bf16(__builtin_amdgcn_exp2f(sv[2] - Mn), __builtin_amdgcn_exp2f(sv[3] - Mn));
        lsum = lsum * alpha + ((bf_lo(pk[0]) + bf_hi(pk[0])) + (bf_lo(pk[1]) + bf_hi(pk[1])));
        *reinterpret_cast<u32x2*>(Pf + (((kt >> 1) * 2 + hh) * 64 + lane) * 16 + (kt & 1) * 8) = pk;
        if (kt == 0 && g == 0) salpha[16 * hh + j] = alpha;
        __syncthreads();
        pv_step(cb);
      };
#pragma unroll 1
      for (int T = 0; T < 6; T += 2) { s_tile(lds + L_BIG, false); s_tile(lds + L_BIG + CBUF, false); }
      s_tile(lds + L_BIG, false); s_tile(lds + L_BIG + CBUF, true);
    } else {
#pragma unroll
      for (int T = 0; T < 8; ++T) {
        __syncthreads();
        BST(10 + 4 * T);
        if (T + 1 < 8) { C_STAGE(T + 1); }
        BST(11 + 4 * T);
        L_PART(T, 0, IA);
        if (T == 7) {
#pragma unroll
          for (int k = 0; k < 2; ++k) W8[k] = ld16(a.woB + ((size_t)h3 * 128 + 16 * (w - 8) + 8 * k + (lane >> 3)) * DM + 512 + (lane & 7) * 8);
        }
        BST(12 + 4 * T);
        __syncthreads();
        L_PART(T, IA, IB);
        __syncthreads();
        BST(13 + 4 * T);
        L_PART(T, IB, IC);
        pv_step(lds + L_BIG + (T & 1) * CBUF);
        L_PART(T, IC, 8);
      }
      BST(42);
      if (STEPST) reinterpret_cast<u32*>(a.ws + (20u << 20))[(blockIdx.x * 16 + w) * 96 + lane] = tv;
    }
#undef C_ISSUE
#undef C_STAGE
    if constexpr (!LD) { ST(3); }
    __syncthreads();
    if constexpr (!LD) { lsum = xg_sum(lsum); if (g == 0) stsum[kt * 32 + 16 * hh + j] = lsum; if (kt == 0 && g == 0) salpha[32 + 16 * hh + j] = M; }
    float* tb = reinterpret_cast<float*>(lds + L_BIG);
#pragma unroll
    for (int ct = 0; ct < 2; ++ct)
#pragma unroll
      for (int h2 = 0; h2 < 2; ++h2)
#pragma unroll
        for (int r = 0; r < 4; ++r) tb[(16 * h2 + 4 * g + r) * 516 + 32 * w + 16 * ct + j] = acc[ct][h2][r];
    if constexpr (!LD) { for (int i = tid; i < 8 * AM2_LD * 2 / 16; i += 512) reinterpret_cast<u32x4*>(lds + L_AM2 + 8 * AM2_LD * 2)[i] = u32x4{0u, 0u, 0u, 0u}; }
    __syncthreads();
    if constexpr (!LD) { ST(4); }
    if constexpr (LD) {
      const int t2 = tid - 512;
#pragma unroll
      for (int k = 0; k < 4; ++k) {
        const int e = t2 + 512 * k, head = e >> 6, c8 = e & 63;
        const f32x4 v0 = *reinterpret_cast<const f32x4*>(tb + head * 516 + 8 * c8), v1 = *reinterpret_cast<const f32x4*>(tb + head * 516 + 8 * c8 + 4);
        __builtin_amdgcn_raw_buffer_store_b128(u32x4{pk_bf16(v0[0], v0[1]), pk_bf16(v0[2], v0[3]), pk_bf16(v1[0], v1[1]), pk_bf16(v1[2], v1[3])}, rs, (int)WS_CTXP + (((b * NSP + sp) * 32 + head) * 512 + 8 * c8) * 2, 0, 16);
      }
      if (t2 < 32) {
        const float L = (stsum[t2] + stsum[32 + t2]) + (stsum[64 + t2] + stsum[96 + t2]);
        st8_sc1(a.ws + WS_MLB + ((size_t)(b * NSP + sp) * 32 + t2) * 8, f32x2{salpha[32 + t2], L});
      }
      __builtin_amdgcn_sched_barrier(0);
      __builtin_amdgcn_s_barrier();
      asm volatile("s_waitcnt vmcnt(0)" ::: "memory");
      if (lane == 0) {
        if (__hip_atomic_fetch_add(cnt, 1u, __ATOMIC_RELAXED, __HIP_MEMORY_SCOPE_WORKGROUP) == 7u) {
          __hip_atomic_store(reinterpret_cast<u64*>(a.ws + WS_F2 + wg * 8), tag, RLX_AGENT);
        }
      }
#pragma unroll
      for (int k = 0; k < 2; ++k) *reinterpret_cast<u32x4*>(lds + L_W8 + (16 * (w - 8) + 8 * k + (lane >> 3)) * 144 + (lane & 7) * 16) = W8[k];
    } else {
      __builtin_amdgcn_s_barrier();
      static_for<16 - EUV>([&](auto ic) __attribute__((always_inline)) {
        asm volatile("s_waitcnt vmcnt(%0)" :: "n"(WPD - 1) : "memory");
        UV_LOAD(EUV + ic.value);
        __builtin_amdgcn_sched_barrier(0);
      });
    }
  }
  {
    int tid = threadIdx.x; asm volatile("" : "+v"(tid));
    const int lane = tid & 63, j = lane & 15, g = lane >> 4;
    u16* Am2 = reinterpret_cast<u16*>(lds + L_AM2);
    float* cpart = reinterpret_cast<float*>(lds + L_CP);
    __builtin_amdgcn_sched_barrier(0);
    if constexpr (LD) {
      if (w == 8) {
        wave_wait(reinterpret_cast<const u64*>(a.ws + WS_F2) + (lane & (NBW - 1)), true, tag);
        asm volatile("s_waitcnt vmcnt(0)" ::: "memory");
        if (lane == 0) __hip_atomic_store(cnt + 1, 1u, __ATOMIC_RELEASE, __HIP_MEMORY_SCOPE_WORKGROUP);
      } else {
        for (u32 spins = 0; spins < 64u * SPIN_CAP; ++spins) { if (__hip_atomic_load(cnt + 1, __ATOMIC_ACQUIRE, __HIP_MEMORY_SCOPE_WORKGROUP) != 0u) break; __builtin_amdgcn_s_sleep(1); }
      }
      __builtin_amdgcn_sched_barrier(0);
      const int b = (tid >> 7) & 3, c4 = tid & 127;
      u32x2 pv[NSP];
      const f32x2 ml = __builtin_bit_cast(f32x2, __builtin_amdgcn_raw_buffer_load_b64(rs, (int)WS_MLB + ((b * NSP + (j & 7)) * 32 + h3) * 8, 0, 16));
#pragma unroll
      for (int s = 0; s < NSP; ++s) pv[s] = __builtin_amdgcn_raw_buffer_load_b64(rs, (int)WS_CTXP + (((b * NSP + s) * 32 + h3) * 512 + 4 * c4) * 2, 0, 16);
      const float M = row16_max(ml[0]);
      float wt = __builtin_amdgcn_exp2f(ml[0] - M);
      wt *= 2.f / row16_sum(wt * ml[1]);
      float c0 = 0.f, c1 = 0.f, c2 = 0.f, c3 = 0.f;
#pragma unroll
      for (int s = 0; s < NSP; ++s) { const float ws_ = rdlane(wt, s); c0 = fmaf(ws_, bf_lo(pv[s][0]), c0); c1 = fmaf(ws_, bf_hi(pv[s][0]), c1); c2 = fmaf(ws_, bf_lo(pv[s][1]), c2); c3 = fmaf(ws_, bf_hi(pv[s][1]), c3); }
      const u32 h0 = pk_bf16(c0, c1), h1 = pk_bf16(c2, c3);
      *reinterpret_cast<u32x2*>(Am2 + (2 * b) * AM2_LD + 4 * c4) = u32x2{h0, h1};
      *reinterpret_cast<u32x2*>(Am2 + (2 * b + 1) * AM2_LD + 4 * c4) = u32x2{pk_bf16(c0 - bf_lo(h0), c1 - bf_hi(h0)), pk_bf16(c2 - bf_lo(h1), c3 - bf_hi(h1))};
      __syncthreads();
      __syncthreads();
      const int t2 = tid - 512;
      float s = 0.f;
#pragma unroll
      for (int ww = 0; ww < 8; ++ww) s += cpart[ww * 512 + t2];
      u16* Am = reinterpret_cast<u16*>(lds + L_AM);
      const int bb = t2 >> 7, d = t2 & 127;
      const u16 hi = to_bf16(s);
      Am[(4 * bb) * AM_LD + d] = hi; Am[(4 * bb + 1) * AM_LD + d] = to_bf16(s - bf2f(hi));
    } else {
      __syncthreads();
      f32x4 acc[8];
#pragma unroll
      for (int t = 0; t < 8; ++t) acc[t] = f32x4{0.f, 0.f, 0.f, 0.f};
#pragma unroll
      for (int k2 = 0; k2 < 2; ++k2) {
        const u32x4 af = *reinterpret_cast<const u32x4*>(Am2 + j * AM2_LD + 64 * w + 32 * k2 + 8 * g);
#pragma unroll
        for (int t = 0; t < 8; ++t) acc[t] = mfma16(af, tcol8(RW[2 * k2 + (t >> 2)], t & 3), acc[t]);
      }
      if (g < 2) {
#pragma unroll
        for (int bb = 0; bb < 2; ++bb) {
          float* p = cpart + (w * 4 + 2 * g + bb) * 128 + 8 * j;
          *reinterpret_cast<f32x4*>(p) = f32x4{acc[0][2 * bb] + acc[0][2 * bb + 1], acc[1][2 * bb] + acc[1][2 * bb + 1], acc[2][2 * bb] + acc[2][2 * bb + 1], acc[3][2 * bb] + acc[3][2 * bb + 1]};
          *reinterpret_cast<f32x4*>(p + 4) = f32x4{acc[4][2 * bb] + acc[4][2 * bb + 1], acc[5][2 * bb] + acc[5][2 * bb + 1], acc[6][2 * bb] + acc[6][2 * bb + 1], acc[7][2 * bb] + acc[7][2 * bb + 1]};
        }
      }
      __builtin_amdgcn_sched_barrier(0);
      __syncthreads();
      if (YDEF == 1 && w == 1) {
        u32 dl = tb0 + 5200u;
#pragma unroll 1
        while ((int)(rt32() - dl) < 0) {
          const u32x2 rv = __builtin_amdgcn_raw_buffer_load_b64(rs, lane < 16 ? (int)WS_RPT + lane * 4352 : OOB, 128 * (NRND - 1) * 35, 16);
          const bool ok = rv[1] == (u32)tag + (NRND - 1);
          const float cn = rdlane(row16_sum(ok ? 1.f : 0.f), 0);
          if (cn >= 8.f) { dl = tb0 + (u32)(int)(rdlane(row16_sum(ok ? (float)(int)(rv[0] - tb0) : 0.f), 0) / cn) + (u32)(due_at(NSEQ - 1) - due_at(rtab[NRND - 1]) + YOFF); break; }
          __builtin_amdgcn_s_sleep(32);
        }
        if (lane == 0) cnt[2] = (int)(dl - tb0) > 5200 ? tb0 : dl;
      }
      if (w == 0) {
#pragma unroll
        for (int s = 0; s < 4; ++s)
#pragma unroll
          for (int jj = 0; jj < 8; ++jj) RW[s][jj] = *reinterpret_cast<const u32x2*>(lds + L_W8 + (32 * s + 8 * g + jj) * 144 + 8 * j);
      }
    }
    __syncthreads();
    if (LD || w == 0) {
      f32x4 ya[4];
      wo_mfma(RW, lds, lane, ya);
      if (YDEF) { const u32 dl = YDEF == 1 ? __builtin_amdgcn_readfirstlane(cnt[2]) : tb0 + YDEF; while ((int)(rt32() - dl) < 0) __builtin_amdgcn_s_sleep(4); }
      st16_sc1(rs, WS_Y + ((8 * (LD ? w - 8 : 8) + (j >> 1)) * 256 + NAW + h3) * 128 + g * 32 + (j & 1) * 16, yrow(ya, (u32)(tag >> 32)));
    }
    if constexpr (!LD) { ST(7); }
  }
}

__global__ void __launch_bounds__(1024, 4) k_main(Args a) {
  __shared__ __attribute__((aligned(16))) unsigned char lds[L_TOTAL];
  const u32 t_entry = (u32)__builtin_amdgcn_s_memrealtime();
  u32 pf;
  { u64 pc; u32 len;
    asm volatile("s_getpc_b64 %0\n.Lpf%=:\n\ts_mov_b32 %1, k_tail_mark-.Lpf%=" : "=s"(pc), "=s"(len));
    pf = __builtin_amdgcn_raw_buffer_load_b32(__builtin_amdgcn_make_buffer_rsrc(reinterpret_cast<void*>(pc), 0, len < 131072u ? len : 0u, 0x00020000), CPFM ? (threadIdx.x < 16 ? ((blockIdx.x >> 3) * 16 + threadIdx.x) * 128 : 0x7ffff000) : threadIdx.x * 128, 0, 0);
    __builtin_amdgcn_sched_barrier(0); }
  u64 tag = (hw_dispatch_id() + 1ull) * 0x9E3779B97F4A7C15ull;
  tag ^= ((u64)__builtin_amdgcn_dispatch_ptr() >> 6) * 0xC2B2AE3D27D4EB4Full;
  tag |= 1ull << 63;
  if (__builtin_expect(blockIdx.x >= NBW, 1)) part_a(a, lds, blockIdx.x - NBW, tag, pf);
  else if (__builtin_amdgcn_readfirstlane(threadIdx.x >> 6) < 8) part_b<false>(a, lds, blockIdx.x, tag, pf);
  else part_b<true>(a, lds, blockIdx.x, tag, pf);

  {
    const u32 tg = (u32)(tag >> 32); const int tid = threadIdx.x;
    u32* sl = reinterpret_cast<u32*>(lds + L_ST + 3072);
    const u32 t_end = (u32)__builtin_amdgcn_s_memrealtime();
    __syncthreads();
    if (tid == 0) { sl[15] = t_entry; sl[14] = t_end; sl[13] = reinterpret_cast<u32*>(lds + L_ST + 2048)[0]; }
    __syncthreads();
    u64* gl = reinterpret_cast<u64*>(a.ws + (16u << 20));
    while ((int)((u32)__builtin_amdgcn_s_memrealtime() - (t_entry + 7000u)) < 0) __builtin_amdgcn_s_sleep(8);
    if (tid < 16) __hip_atomic_store(gl + blockIdx.x * 16 + tid, ((u64)tg << 32) | sl[tid], RLX_AGENT);
    if (blockIdx.x == 0) {
      u32* sh = reinterpret_cast<u32*>(lds);
      u32 v[16];
      if (tid < 256) {
        for (int k = 0; k < 16; ++k) { u64 x; for (int s = 0; s < 1000000; ++s) { x = __hip_atomic_load(gl + tid * 16 + k, RLX_AGENT); if ((u32)(x >> 32) == tg) break; __builtin_amdgcn_s_sleep(4); } v[k] = (u32)x; }
        sh[tid] = v[15] - t_entry + 1000u;
      }
      __syncthreads();
      u32 t0 = ~0u; for (int i = 0; i < 256; ++i) t0 = sh[i] < t0 ? sh[i] : t0;
      t0 = t0 - 1000u + t_entry;
      __syncthreads();
      const bool isB = tid < NBW; u32 x = 0; int mode = 0;
      if (DIAG == 1) x = v[14] - t0;
      if (DIAG == 2) x = v[15] - t0;
      if (DIAG >= 100 && DIAG < 116) { x = isB ? 0 : v[DIAG - 100] - t0; mode = 1; }
      if (DIAG >= 200 && DIAG < 216) { x = isB ? 0 : v[DIAG - 200] - t0; }
      if (DIAG >= 300 && DIAG < 316) { x = isB ? v[DIAG - 300] - t0 : 0; }
      if (DIAG >= 400 && DIAG < 416) { x = isB ? v[DIAG - 400] - t0 : 0; mode = 2; }
      if (DIAG == 4) { x = (isB || tid >= 32 + 192) ? 0 : v[12] - t0; x = x * 224 / 192; mode = 1; }
      if (DIAG == 5) { x = (isB || tid >= 32 + 192) ? 0 : v[12] - t0; }
      if (DIAG == 3) { x = isB ? 0 : v[13]; mode = 1; }
      if (tid < 256) sh[tid] = x;
      __syncthreads();
      u32 X = 0;
      if (mode == 0) { for (int i = 0; i < 256; ++i) X = sh[i] > X ? sh[i] : X; X *= 4; }
      else { for (int i = 0; i < 256; ++i) X += sh[i]; X = X * 4 / (mode == 1 ? 224 : 32); }
      if (X > 40000u) X = 40000u;
      const u32 tgt = t0 + 10000u + X;
      for (int i = 0; i < 4000000; ++i) { if ((int)((u32)__builtin_amdgcn_s_memrealtime() - tgt) >= 0) break; __builtin_amdgcn_s_sleep(1); }
    }
  }
}

extern "C" __global__ void k_tail_mark() {}

static void* g_ws;
extern "C" void kernel_launch(void* const* d_in, const int* in_sizes, int n_in, void* d_out, int out_size, void* d_ws, size_t ws_size,
                              hipStream_t stream) {
  Args a;
  a.xA = (const u16*)d_in[0]; a.K = (const u16*)d_in[1]; a.V = (const u16*)d_in[2]; a.wq = (const u16*)d_in[3]; a.wo = (const u16*)d_in[4];
  a.xB = (const u16*)d_in[5]; a.C = (const u16*)d_in[6]; a.wqB = (const u16*)d_in[7]; a.wuk = (const u16*)d_in[8]; a.wuv = (const u16*)d_in[9]; a.woB = (const u16*)d_in[10];
#define WSTOP 32
  if (WSTOP && ws_size >= ((size_t)WSTOP << 20)) d_ws = (unsigned char*)d_ws + ((ws_size - ((size_t)WSTOP << 20)) & ~(size_t)4095);
  g_ws = d_ws;
  a.ws = (unsigned char*)d_ws;
  a.out = (u16*)d_out;
  k_main<<<NBW + NAW, 1024, 0, stream>>>(a);
}
```

```cpp
#define DIAG 50
#include <hip/hip_runtime.h>
#include <stdint.h>
#include <utility>

typedef unsigned short u16;
typedef unsigned u32;
typedef unsigned long long u64;
typedef u32 u32x4 __attribute__((ext_vector_type(4)));
typedef u32 u32x2 __attribute__((ext_vector_type(2)));
typedef float f32x4 __attribute__((ext_vector_type(4)));
typedef float f32x2 __attribute__((ext_vector_type(2)));
typedef short s16x4 __attribute__((ext_vector_type(4)));
typedef __bf16 bf16x8 __attribute__((ext_vector_type(8)));
typedef __bf16 bf16x2 __attribute__((ext_vector_type(2)));
#define DI __device__ __forceinline__
#define RLX_AGENT __ATOMIC_RELAXED, __HIP_MEMORY_SCOPE_AGENT
extern "C" __device__ u64 hw_dispatch_id() __asm("llvm.amdgcn.dispatch.id");

template <int... Is, class F> DI void static_for_impl(std::integer_sequence<int, Is...>, F&& f) { (f(std::integral_constant<int, Is>{}), ...); }
template <int N, class F> DI void static_for(F&& f) { static_for_impl(std::make_integer_sequence<int, N>{}, f); }

constexpr int H = 32, SK = 4096, DM = 576, DC = 512;
constexpr int ACU = 7;
constexpr int NBW = 32;
constexpr int NAW = H * ACU;
constexpr int NSP = 8;
constexpr float C2 = 0.08838834764831845f * 1.4426950408889634f;

constexpr size_t WS_F1 = 0, WS_F2 = 512;
constexpr size_t WS_QABS = 4096;
constexpr size_t WS_CTXP = WS_QABS + 4 * 32 * 512 * 2;
constexpr size_t WS_MLB = WS_CTXP + (size_t)4 * NSP * 32 * 512 * 2;
constexpr size_t WS_Y = WS_MLB + 4 * NSP * 32 * 2 * 4;
constexpr size_t WS_MLA = WS_Y + (size_t)72 * 256 * 128;
constexpr size_t WS_RPT = WS_MLA + 256 * 128;
constexpr size_t WS_QA = WS_RPT + 64 * 4480;
constexpr size_t WS_END = WS_QA + 256 * 2048;

constexpr int CRS = 1056;
constexpr int CBUF = 64 * CRS;
constexpr int L_BIG = 0;
constexpr int L_AM = 2 * CBUF;
constexpr int L_QS = L_AM + 16 * 272;
constexpr int L_ST = L_QS + 1024;
constexpr int L_PF = L_ST + 4096;
constexpr int L_TOTAL = L_PF + 4096;
constexpr int AM_LD = 136;
constexpr int AM2_LD = 520;
constexpr int L_AM2 = 69632, L_CP = 88064, L_W8 = 106496;

#define ST(i) do { if (threadIdx.x == 0) reinterpret_cast<u32*>(lds + L_ST + 3072)[i] = (u32)__builtin_amdgcn_s_memrealtime(); } while (0)
struct Args {
  const u16 *xA, *K, *V, *wq, *wo, *xB, *C, *wqB, *wuk, *wuv, *woB;
  unsigned char* ws;
  u16* out;
};

DI float bf_lo(u32 x) { return __uint_as_float(x << 16); }
DI float bf_hi(u32 x) { return __uint_as_float(x & 0xffff0000u); }
DI u32 pk_bf16(float lo, float hi) { bf16x2 v = {(__bf16)lo, (__bf16)hi}; return __builtin_bit_cast(u32, v); }
DI u16 to_bf16(float x) { return __builtin_bit_cast(u16, (__bf16)x); }
DI float bf2f(u16 x) { return __uint_as_float(((u32)x) << 16); }
DI float dot2(u32 a, u32 b, float c) { return __builtin_amdgcn_fdot2_f32_bf16(__builtin_bit_cast(bf16x2, a), __builtin_bit_cast(bf16x2, b), c, false); }
template <int CTRL> DI float dpp(float x) { return __builtin_bit_cast(float, __builtin_amdgcn_update_dpp(0, __builtin_bit_cast(int, x), CTRL, 0xf, 0xf, true)); }
DI float row16_sum(float x) { x += dpp<0x128>(x); x += dpp<0x124>(x); x += dpp<0x122>(x); x += dpp<0x121>(x); return x; }
DI float row16_max(float x) { x = fmaxf(x, dpp<0x128>(x)); x = fmaxf(x, dpp<0x124>(x)); x = fmaxf(x, dpp<0x122>(x)); x = fmaxf(x, dpp<0x121>(x)); return x; }
DI float xg_max(float x) {
  auto s = __builtin_amdgcn_permlane16_swap(__float_as_uint(x), __float_as_uint(x), false, false);
  x = fmaxf(__uint_as_float(s[0]), __uint_as_float(s[1]));
  auto t = __builtin_amdgcn_permlane32_swap(__float_as_uint(x), __float_as_uint(x), false, false);
  return fmaxf(__uint_as_float(t[0]), __uint_as_float(t[1]));
}
DI float xg_sum(float x) {
  auto s = __builtin_amdgcn_permlane16_swap(__float_as_uint(x), __float_as_uint(x), false, false);
  x = __uint_as_float(s[0]) + __uint_as_float(s[1]);
  auto t = __builtin_amdgcn_permlane32_swap(__float_as_uint(x), __float_as_uint(x), false, false);
  return __uint_as_float(t[0]) + __uint_as_float(t[1]);
}
DI float sw32(float a, float b) {
  auto s = __builtin_amdgcn_permlane32_swap(__float_as_uint(a), __float_as_uint(b), false, false);
  return __uint_as_float(s[0]) + __uint_as_float(s[1]);
}
DI float sw16(float a, float b) {
  auto s = __builtin_amdgcn_permlane16_swap(__float_as_uint(a), __float_as_uint(b), false, false);
  return __uint_as_float(s[0]) + __uint_as_float(s[1]);
}
DI f32x4 mfma16(u32x4 a, u32x4 b, f32x4 c) { return __builtin_amdgcn_mfma_f32_16x16x32_bf16(__builtin_bit_cast(bf16x8, a), __builtin_bit_cast(bf16x8, b), c, 0, 0, 0); }
DI u32x4 ld16(const u16* p) { return __builtin_nontemporal_load(reinterpret_cast<const u32x4*>(p)); }
DI u32x2 ld8(const u16* p) { return __builtin_nontemporal_load(reinterpret_cast<const u32x2*>(p)); }
DI u32x2 trread(const unsigned char* p) {
  return __builtin_bit_cast(u32x2, __builtin_amdgcn_ds_read_tr16_b64_v4i16((__attribute__((address_space(3))) s16x4*)(p)));
}
DI u32 prm(u32 hi, u32 lo, int odd) { return __builtin_amdgcn_perm(hi, lo, odd ? 0x07060302u : 0x05040100u); }

DI u32x4 tcol16(const u32x4 (&R)[8], int t) {
  u32x4 T;
#pragma unroll
  for (int q = 0; q < 4; ++q) T[q] = prm(R[2 * q + 1][t >> 1], R[2 * q][t >> 1], t & 1);
  return T;
}
DI u32x4 tcol8(const u32x2 (&R)[8], int t) {
  u32x4 T;
#pragma unroll
  for (int q = 0; q < 4; ++q) T[q] = prm(R[2 * q + 1][t >> 1], R[2 * q][t >> 1], t & 1);
  return T;
}

#define SPINLOG 22
#define PSL 2
constexpr u32 SPIN_CAP = 1u << SPINLOG;
DI void wave_wait(const u64* f, bool active, u64 tag) {
  for (u32 spins = 0; spins < SPIN_CAP; ++spins) {
    const u64 v = active ? __hip_atomic_load(f, RLX_AGENT) : tag;
    if (__all(v == tag)) break;
    __builtin_amdgcn_s_sleep(PSL);
  }
}
DI void wg_wait64(const unsigned char* flags, u64 tag, int pw = 0) {
  if ((int)(threadIdx.x >> 6) == pw) {
    wave_wait(reinterpret_cast<const u64*>(flags) + (threadIdx.x & (NBW - 1)), true, tag);
    asm volatile("s_waitcnt vmcnt(0)" ::: "memory");
  }
  __syncthreads();
}
DI void st16_sc1(__amdgpu_buffer_rsrc_t rs, size_t off, u32x4 v) { __builtin_amdgcn_raw_buffer_store_b128(v, rs, (int)off, 0, 16); }
DI void st8_sc1(unsigned char* p, f32x2 v) { __hip_atomic_store(reinterpret_cast<u64*>(p), __builtin_bit_cast(u64, v), RLX_AGENT); }

struct QPre { u32x4 R0[8]; u32x4 R1; u32x2 xa, xb; u32 x1; };
DI void qproj_load(QPre& Q, const u16* x, const u16* Wh, int w, int lane) {
  const int j = lane & 15, g = lane >> 4;
  Q.xa = u32x2{0u, 0u}; Q.xb = u32x2{0u, 0u}; Q.x1 = 0u;
  if (j < 4) { Q.xa = ld8(x + j * DM + 36 * w + 8 * g); Q.xb = ld8(x + j * DM + 36 * w + 8 * g + 4); Q.x1 = x[j * DM + 36 * w + 32 + g]; }
#pragma unroll
  for (int jj = 0; jj < 8; ++jj) Q.R0[jj] = ld16(Wh + (size_t)(36 * w + 8 * g + jj) * 4096 + 8 * j);
  Q.R1 = ld16(Wh + (size_t)(36 * w + 32 + g) * 4096 + 8 * j);
}
DI void qproj_mfma(const QPre& Q, unsigned char* lds, int w, int lane) {
  const int j = lane & 15, g = lane >> 4;
  float* qpart = reinterpret_cast<float*>(lds + L_BIG);
  const u32x4 a = {Q.xa[0], Q.xa[1], Q.xb[0], Q.xb[1]}, a1 = {Q.x1, 0u, 0u, 0u};
#pragma unroll
  for (int hf = 0; hf < 2; ++hf) {
    f32x4 acc[4];
#pragma unroll
    for (int t = 0; t < 4; ++t) acc[t] = mfma16(a, tcol16(Q.R0, 4 * hf + t), f32x4{0.f, 0.f, 0.f, 0.f});
#pragma unroll
    for (int t = 0; t < 4; ++t) { const u32x4 bb = {(t & 1) ? (Q.R1[2 * hf + (t >> 1)] >> 16) : (Q.R1[2 * hf + (t >> 1)] & 0xffffu), 0u, 0u, 0u}; acc[t] = mfma16(a1, bb, acc[t]); }
    if (g == 0) {
#pragma unroll
      for (int r = 0; r < 4; ++r) *reinterpret_cast<f32x4*>(qpart + (w * 4 + r) * 128 + 8 * j + 4 * hf) = f32x4{acc[0][r], acc[1][r], acc[2][r], acc[3][r]};
    }
    __builtin_amdgcn_sched_barrier(0);
  }
}
DI void wo_load(const u16* Wo, u32x2 (&R)[4][8], int nb, int lane, int s0, int s1) {
  const int j = lane & 15, g = lane >> 4;
#pragma unroll
  for (int s = 0; s < 4; ++s) if (s >= s0 && s < s1)
#pragma unroll
    for (int jj = 0; jj < 8; ++jj) R[s][jj] = ld8(Wo + (size_t)(32 * s + 8 * g + jj) * DM + 64 * nb + 4 * j);
}
DI void wo_mfma(const u32x2 (&R)[4][8], const unsigned char* lds, int lane, f32x4 (&acc)[4]) {
  const int j = lane & 15, g = lane >> 4;
#pragma unroll
  for (int t = 0; t < 4; ++t) acc[t] = f32x4{0.f, 0.f, 0.f, 0.f};
#pragma unroll
  for (int s = 0; s < 4; ++s) {
    const u32x4 a = *reinterpret_cast<const u32x4*>(lds + L_AM + j * 272 + (32 * s + 8 * g) * 2);
#pragma unroll
    for (int t = 0; t < 4; ++t) acc[t] = mfma16(a, tcol8(R[s], t), acc[t]);
  }
}
DI u32x4 yrow(const f32x4 (&acc)[4], u32 tg) {
  return u32x4{pk_bf16(acc[0][0] + acc[0][1], acc[1][0] + acc[1][1]), tg, pk_bf16(acc[2][0] + acc[2][1], acc[3][0] + acc[3][1]), tg};
}
DI void am_zero(unsigned char* lds, int tid) {
  if (tid < 136) { const int pr = tid / 34; *reinterpret_cast<u32x4*>(lds + L_AM + (4 * pr + 2) * 272 + (tid - 34 * pr) * 16) = u32x4{0u, 0u, 0u, 0u}; }
}
DI float rdlane(float x, int l) { return __builtin_bit_cast(float, __builtin_amdgcn_readlane(__builtin_bit_cast(int, x), l)); }

constexpr int NS = 10;
constexpr int NSEQ = 8 * NS;
#define RNG 9
#define NP 3
#define QP 5
#define SP 4
#define BPF 2
#define K1G 1
#define PSKIP 0
#define D3FROM 66
#define D4FROM 99
#define ROTM 1
#define OAUX 0
#define DEP 2
constexpr int DEPTH = DEP, RING = RNG, RAMP = 2;
constexpr int issued(int i) {
  int d = RING - (i + RAMP - 1) / RAMP; if (d < DEPTH) d = DEPTH;
  if (i >= D3FROM && d < 3) d = 3;
  if (i >= D4FROM && d < 4) d = 4;
  int e = i + d; if (e < RING) e = RING; return e > NSEQ ? NSEQ : e;
}
#define PACE 1
#define YDEF 1
#define WO0 16
#define WON 36
#define TA1 535
#define TA2 440
#define NK 68
#define ORG0 130
#define SLMAX 80
#define MAXGAP 70
constexpr int cmin(int a, int b) { return a < b ? a : b; }
#define RSH 1
#define RD 4
#define BMAP 1
#define EBIG 9999
#define NJ 54
#define TA3 515
constexpr int due_at(int n) { return (n <= NJ ? n * TA1 : n <= NK ? NJ * TA1 + (n - NJ) * TA3 : NJ * TA1 + (NK - NJ) * TA3 + (n - NK) * TA2) / 10; }
#define RTAB 20, 30, 40, 50, 60, 70
#define GTAB 0, 12, 6, 12, 12, 12
#define BTAB 8, 12, 6, 12, 12, 0
#define MISS 40
#define DB 10
#define DTAB 20, 20, 20, 15, 5, 3, 3, 3
constexpr int rtab[] = {RTAB}, gtab[] = {GTAB}, btab[] = {BTAB}, dtab[] = {DTAB};
constexpr int NRND = sizeof(rtab) / sizeof(int);
constexpr int rgap(int k) { return k ? rtab[k] - rtab[k - 1] : rtab[0] - RING; }
constexpr int rnd_at(int n) { for (int k = 0; k < NRND; ++k) if (rtab[k] == n) return k; return -1; }
static_assert(rtab[0] >= RING + 2 && rtab[NRND - 1] + RD + 2 < NSEQ, "");
constexpr int wo_step(int k) { return WO0 + k * WON / 32; }
#define WTS 54
#define PJ0 58
#define PJ1 63
#define PJ2 72
#define XW 13
#define XR 50
#define FDLY 8
#define FSL 2
#define STEPST 0
#define CPFM 1
#define STAG 16
DI u32 rt32() { return (u32)__builtin_amdgcn_s_memrealtime(); }
constexpr int OOB = 0x7ffff000;
constexpr int L_AB = 32768;
DI void part_a(const Args& a, unsigned char* lds, int idx, u64 tag, u32 pf) {
  const u64 T64 = __builtin_amdgcn_s_memrealtime();
  __builtin_amdgcn_sched_barrier(0);
  const int tid = threadIdx.x, lane = tid & 63, w = __builtin_amdgcn_readfirstlane(tid >> 6);
  const int j = lane & 15, g = lane >> 4;
  const int h = (idx & 7) * 4 + ((idx >> 3) & 3), ci = idx >> 5;
  const int rr = (((h & 3) * 5) >> 1) + (h >> 2), rot = ROTM ? (rr >= NS ? rr - NS : rr) : (h * 5) >> 4, l0 = (ci * 37) >> 4, ln = ((ci * 37 + 37) >> 4) - l0;
  const int bad_sl = (w < ln) ? -1 : NS - 1 - rot;
  int vtab;
  { const int p = lane + rot >= NS ? lane + rot - NS : lane + rot;
    vtab = p == NS - 1 ? (w < ln ? (h << 20) + (1008 + l0 + w) * 1024 : 1 << 30) : (h << 20) + (p * 7 + ci) * 16384 + w * 1024; }
  const bool prod = ci < 2 * NP && w < 9 / NP;
  const int r0 = ((ci >> 1) * (9 / NP) + w) * 64;
  int voff = lane * 16, wqo = ((r0 + 32 * (j >> 3) + 8 * g) * 4096 + h * 128 + (ci & 1) * 64 + 8 * (j & 7)) * 2;
  int xo = j < 8 ? ((j & 3) * DM + r0 + 32 * (j >> 2) + 8 * g) * 2 : OOB;
  const int prow = (idx >> 3) * 40 + (w - 3) * 4;
  int pfo = (w >= 3 && w < 13 && prow < DM + DC && prow >= PSKIP) ? ((prow < DM ? prow : prow - DM) + g) * 8192 + (idx & 7) * 1024 + j * 64 : OOB;
  int so0 = __builtin_amdgcn_readlane(vtab, 0);
  asm volatile("" : "+v"(vtab), "+v"(voff), "+v"(wqo), "+v"(xo), "+v"(pfo), "+s"(so0));
  __builtin_amdgcn_sched_barrier(0);
  asm volatile("" :: "s"(a.xB), "s"(a.C), "s"(a.wqB), "s"(a.wuk), "s"(a.wuv), "s"(a.woB), "s"(T64));
  u32 T0 = (u32)T64;
  const u32 toff = (u32)(w * TA2) / (STAG * 10);
  u32* lorg = reinterpret_cast<u32*>(lds + L_ST + 2048);
  const u32 tg = (u32)(tag >> 32);
  u32 last = T0;
  u32x2 rv = {0u, 0u}; u32 rt = 0;
  const __amdgpu_buffer_rsrc_t rs = __builtin_amdgcn_make_buffer_rsrc(a.ws, 0, (int)WS_END, 0x00020000);
  const __amdgpu_buffer_rsrc_t rk = __builtin_amdgcn_make_buffer_rsrc(const_cast<u16*>(a.K), 0, 1 << 27, 0x00020000);
  const __amdgpu_buffer_rsrc_t rvv = __builtin_amdgcn_make_buffer_rsrc(const_cast<u16*>(a.V), 0, 1 << 27, 0x00020000);
  const __amdgpu_buffer_rsrc_t rw = __builtin_amdgcn_make_buffer_rsrc(const_cast<u16*>(a.wq), 0, DM * 4096 * 2, 0x00020000);
  const __amdgpu_buffer_rsrc_t rx = __builtin_amdgcn_make_buffer_rsrc(const_cast<u16*>(a.xA), 0, 4 * DM * 2, 0x00020000);
  ST(0);
  u32x4 R[RING];
#define A_ISSUE(i) R[(i) % RING] = __builtin_amdgcn_raw_buffer_load_b128((i) >= 4 * NS ? rvv : rk, voff, ((i) == 0 ? so0 : __builtin_amdgcn_readlane(vtab, (i) % NS)) + ((((i) / NS) & 3) << 25), 2)
#define GATE(n) do { if (PACE) { u32 due = T0 + toff + org + ((osl * cmin(n, NK)) >> 4) + (u32)due_at(n); if (MAXGAP && (int)(due - (last + MAXGAP)) > 0) due = last + MAXGAP; \
    while ((int)(now - due) < 0) { __builtin_amdgcn_s_sleep(1); now = rt32(); } last = now; } } while (0)
#define CTRL(n) do { if constexpr (PACE) { \
    if constexpr (rnd_at((n) - RD - 2) >= 0) { constexpr int k = rnd_at((n) - RD - 2); \
      const u32 dt = (rv[1] == (u32)tag + k) ? rv[0] - T0 : rt; \
      const float sm = row16_sum((float)(int)dt); \
      const int o = (int)((RSH ? rdlane(sm, 0) * (1.f / 16.f) : (rdlane(sm, 0) + rdlane(sm, 16)) * (1.f / 32.f))) - due_at(rtab[k]); \
      if (w == ((5 * k + 11) & 15) && lane == 0 && o > -300 && o < 2000) { \
        int e = o - (int)org - (int)((osl * cmin(rtab[k], NK)) >> 4) - dtab[k]; if (e < 0) e = 0; \
        int sl_ = ((int)osl * gtab[k]) / 16 + ((e > EBIG ? 0 : e) * btab[k]) / rgap(k); sl_ = sl_ > SLMAX ? SLMAX : sl_; \
        if (STEPST) lorg[16 + k] = (u32)(e & 0xffff) | ((u32)sl_ << 16); \
        __hip_atomic_store(reinterpret_cast<u64*>(lorg), __builtin_bit_cast(u64, u32x2{(u32)((int)org + e + ((((int)osl - sl_) * cmin(n, NK)) >> 4)), (u32)sl_}), __ATOMIC_RELAXED, __HIP_MEMORY_SCOPE_WORKGROUP); } } \
    if constexpr (rnd_at(n) >= 0) { constexpr int k = rnd_at(n); \
      u32 tot = 0; \
      if (ci == k % ACU && (h & ((1 << RSH) - 1)) == (k & ((1 << RSH) - 1))) { const u32 mine = (1u << 24) + ((rt32() - toff - T0) & 0xfffffu); \
        if (lane == 0) tot = __hip_atomic_fetch_add(lorg + 8 + k, mine, __ATOMIC_RELAXED, __HIP_MEMORY_SCOPE_WORKGROUP) + mine; } \
      __builtin_amdgcn_raw_buffer_store_b64(u32x2{T0 + ((tot & 0xffffffu) >> 4), (u32)tag + k}, rs, (tot >> 24) == 16u ? (int)WS_RPT + (h >> RSH) * 4352 : OOB, 128 * k * 35, 16); } \
    if constexpr (rnd_at((n) - RD) >= 0) { constexpr int k = rnd_at((n) - RD); \
      rv = __builtin_amdgcn_raw_buffer_load_b64(rs, (w == ((5 * k + 11) & 15) && lane < (32 >> RSH)) ? (int)WS_RPT + lane * 4352 : OOB, 128 * k * 35, 16); rt = rt32() - T0 - MISS; } \
  } } while (0)
  u32x4 Wr[8], xf;
  u32 pf2 = 0;
  if (BPF == 1) pf2 = __builtin_amdgcn_raw_buffer_load_b32(__builtin_amdgcn_make_buffer_rsrc(const_cast<u16*>(prow < DM ? a.wqB : a.wuk), 0, DM * 4096 * 2, 0x00020000), pfo, 0, 2);
  A_ISSUE(0);
  __builtin_amdgcn_sched_barrier(0);
  if (prod) {
#pragma unroll
    for (int k = 0; k < 8; ++k) Wr[k] = __builtin_amdgcn_raw_buffer_load_b128(rw, wqo, k * 8192, 2);
    xf = __builtin_amdgcn_raw_buffer_load_b128(rx, xo, 0, 2);
  }
  __builtin_amdgcn_sched_barrier(0);
  if (BPF == 2) pf2 = __builtin_amdgcn_raw_buffer_load_b32(__builtin_amdgcn_make_buffer_rsrc(const_cast<u16*>(prow < DM ? a.wqB : a.wuk), 0, DM * 4096 * 2, 0x00020000), pfo, 0, 2);
  if (!K1G) A_ISSUE(1);
  __builtin_amdgcn_raw_buffer_store_b64(u32x2{T0, (u32)tag + 15u}, rs, (ci == 6 && (h & 1) == 0 && tid == 0) ? (int)WS_RPT + (h >> 1) * 4352 : OOB, 128 * 15 * 35, 16);
  __builtin_amdgcn_sched_barrier(0);
  if (tid < 32) lorg[tid] = tid == 0 ? ORG0 : tid == 2 ? T0 : 0u;
  __syncthreads();
  T0 = __builtin_amdgcn_readfirstlane(lorg[2]);
  if (prod) {
    float* qpart = reinterpret_cast<float*>(lds + L_BIG);
#pragma unroll
    for (int hf = 0; hf < 2; ++hf) {
      f32x4 d4[4];
#pragma unroll
      for (int t = 0; t < 4; ++t) d4[t] = mfma16(xf, tcol16(Wr, 4 * hf + t), f32x4{0.f, 0.f, 0.f, 0.f});
      if (g == (j >> 3)) {
#pragma unroll
        for (int r = 0; r < 4; ++r) *reinterpret_cast<f32x4*>(qpart + ((w * 2 + g) * 4 + r) * 64 + 8 * (j & 7) + 4 * hf) = f32x4{d4[0][r], d4[1][r], d4[2][r], d4[3][r]};
      }
    }
    asm volatile("" ::: "memory");
    ST(12);
    u32 arr = 0;
    if (lane == 0) arr = __hip_atomic_fetch_add(lorg + 4, 1u, __ATOMIC_RELAXED, __HIP_MEMORY_SCOPE_WORKGROUP);
    if (__builtin_amdgcn_readfirstlane(arr) == 9 / NP - 1) {
      asm volatile("" ::: "memory");
      f32x4 s0 = {0.f, 0.f, 0.f, 0.f};
#pragma unroll
      for (int k = 0; k < 18 / NP; ++k) s0 += *reinterpret_cast<const f32x4*>(qpart + k * 256 + 4 * lane);
      const int qo = (int)WS_QA + (h * 8 + ci) * 2048 + lane * 16;
      st16_sc1(rs, qo, u32x4{__float_as_uint(s0[0]), tg, __float_as_uint(s0[1]), tg});
      st16_sc1(rs, qo + 1024, u32x4{__float_as_uint(s0[2]), tg, __float_as_uint(s0[3]), tg});
    }
  }
  static_for<RING - 2 + K1G>([&](auto tc) __attribute__((always_inline)) {
    constexpr int t = tc.value + 2 - K1G;
    u32 now = rt32();
    const u32 org = __builtin_amdgcn_readfirstlane(__hip_atomic_load(lorg, __ATOMIC_RELAXED, __HIP_MEMORY_SCOPE_WORKGROUP)), osl = 0;
    GATE(t);
    A_ISSUE(t);
    __builtin_amdgcn_sched_barrier(0);
    if constexpr (t == SP) {
      if (w == 13) {
        const u32x2 sv = __builtin_amdgcn_raw_buffer_load_b64(rs, lane < 16 ? (int)WS_RPT + lane * 4352 : OOB, 128 * 15 * 35, 16);
        const bool ok = sv[1] == (u32)tag + 15u;
        const float sm = row16_sum(ok ? (float)(int)(sv[0] - T0) : 0.f), cn = row16_sum(ok ? 1.f : 0.f);
        const int o = ORG0 + (cn > 0.f ? (int)(sm / cn) : 0);
        if (lane == 0 && o > -300 && o < 1000) lorg[0] = (u32)o;
      }
    }
    if constexpr (t == QP) {
      if (w >= 14) {
        const int qo = (int)WS_QA + (h * 8 + (w & 1)) * 2048 + lane * 16;
        u32x4 v[2 * NP];
        if (tid == 14 * 64) reinterpret_cast<u32*>(lds + L_ST + 3072)[3] = rt32();
#pragma unroll 1
        for (u32 spins = 0; spins < SPIN_CAP; ++spins) {
          bool ok = true;
#pragma unroll
          for (int k = 0; k < 2 * NP; ++k) v[k] = __builtin_amdgcn_raw_buffer_load_b128(rs, qo, (k >> 1) * 4096 + (k & 1) * 1024, 16);
#pragma unroll
          for (int k = 0; k < 2 * NP; ++k) ok = ok && v[k][1] == tg && v[k][3] == tg;
          if (__all(ok)) break;
          __builtin_amdgcn_s_sleep(4);
        }
        f32x4 s0 = {0.f, 0.f, 0.f, 0.f};
#pragma unroll
        for (int k = 0; k < NP; ++k) s0 += f32x4{__uint_as_float(v[2 * k][0]), __uint_as_float(v[2 * k][2]), __uint_as_float(v[2 * k + 1][0]), __uint_as_float(v[2 * k + 1][2])};
        *reinterpret_cast<u32x2*>(lds + L_QS + (g * 128 + (w & 1) * 64 + 4 * j) * 2) = u32x2{pk_bf16(s0[0], s0[1]), pk_bf16(s0[2], s0[3])};
        asm volatile("" ::: "memory");
        if (lane == 0) __hip_atomic_fetch_add(lorg + 5, 1u, __ATOMIC_RELAXED, __HIP_MEMORY_SCOPE_WORKGROUP);
        if (tid == 14 * 64) reinterpret_cast<u32*>(lds + L_ST + 3072)[6] = rt32();
      }
    }
    __builtin_amdgcn_sched_barrier(0);
  });
  while (__hip_atomic_load(lorg + 5, __ATOMIC_RELAXED, __HIP_MEMORY_SCOPE_WORKGROUP) != 2u) __builtin_amdgcn_s_sleep(1);
  asm volatile("" :: "v"(pf), "v"(pf2) : "memory");
  ST(1);
  float* wtab = reinterpret_cast<float*>(lds + L_ST + 1024);
  u32x2 xv = {0u, 0u};
  const int xoff = (w == XW && lane < 8 * ACU) ? (int)WS_MLA + (h * 8 + (lane >> 3)) * 128 + (lane & 7) * 8 : OOB;
  float* pml = reinterpret_cast<float*>(lds + L_ST);
  u32x4 q;
  float acc[8], S[4];
  u32x2 RW[4][8];
  u32x4 WT[4][4];
  float Y[4];
  auto project = [&](int b) __attribute__((always_inline)) {
    f32x4 d4[4];
#pragma unroll
    for (int t = 0; t < 4; ++t) d4[t] = f32x4{0.f, 0.f, 0.f, 0.f};
    const f32x4 wt = *reinterpret_cast<const f32x4*>(wtab + b * 16 + 4 * g);
#pragma unroll
    for (int s = 0; s < 4; ++s) {
      const u32x4 af = *reinterpret_cast<const u32x4*>(lds + L_AB + (b * 16 + j) * 272 + (32 * s + 8 * g) * 2);
#pragma unroll
      for (int t = 0; t < 4; ++t) d4[t] = mfma16(af, WT[s][t], d4[t]);
    }
    float y[4];
#pragma unroll
    for (int t = 0; t < 4; ++t) y[t] = fmaf(d4[t][0], wt[0], d4[t][1] * wt[1]) + fmaf(d4[t][2], wt[2], d4[t][3] * wt[3]);
    Y[b] = sw16(sw32(y[0], y[2]), sw32(y[1], y[3]));
  };
  const __amdgpu_buffer_rsrc_t rwo = __builtin_amdgcn_make_buffer_rsrc(const_cast<u16*>(a.wo + (size_t)h * 128 * DM), 0, w < 9 ? 128 * DM * 2 : 0, 0x00020000);
  const int wvo = (8 * g * DM + 64 * w + 4 * j) * 2;
#define WO_ISSUE() static_for<32>([&](auto kc) __attribute__((always_inline)) { constexpr int k = kc.value; \
    if constexpr (wo_step(k) == i) RW[k >> 3][k & 7] = __builtin_amdgcn_raw_buffer_load_b64(rwo, wvo, (32 * (k >> 3) + (k & 7)) * DM * 2, 2); })
  static_for<NSEQ>([&](auto ic) __attribute__((always_inline)) {
    constexpr int i = ic.value, bb = (i / NS) & 3, pass = i / (4 * NS), sl = i % NS;
    u32 now = 0; if (PACE + STEPST > 0) now = rt32();
    u32 org = 0, osl = 0; if (PACE) { const u32x2 ov = __builtin_bit_cast(u32x2, __hip_atomic_load(reinterpret_cast<u64*>(lorg), __ATOMIC_RELAXED, __HIP_MEMORY_SCOPE_WORKGROUP)); org = __builtin_amdgcn_readfirstlane(ov[0]); osl = __builtin_amdgcn_readfirstlane(ov[1]); }
    if constexpr (i == 10 || i == 20 || i == 40 || i == 60) { if (tid == 0) reinterpret_cast<u32*>(lds + L_ST + 3072)[i == 10 ? 8 : i == 20 ? 9 : i == 40 ? 10 : 11] = now; }
    __builtin_amdgcn_sched_barrier(0);
    const u32x4 r = R[i % RING];
    if constexpr (pass == 0) {
      if constexpr (sl == 0) { q = *reinterpret_cast<const u32x4*>(lds + L_QS + (bb * 128 + 8 * j) * 2); S[bb] = -INFINITY; }
      float d = dot2(r[0], q[0], 0.f); d = dot2(r[1], q[1], d); d = dot2(r[2], q[2], d); d = dot2(r[3], q[3], d);
      asm volatile("" : "+v"(d));
      if constexpr (issued(i + 1) > issued(i)) { GATE(issued(i)); WO_ISSUE(); A_ISSUE(issued(i)); CTRL(issued(i)); if constexpr (issued(i + 1) > issued(i) + 1) { GATE(issued(i) + 1); A_ISSUE(issued(i) + 1); CTRL(issued(i) + 1); } } else { WO_ISSUE(); }
      d = row16_sum(d);
      S[bb] = (j == sl) ? d : S[bb];
      if constexpr (sl == NS - 1) {
        const float sc = (j == bad_sl) ? -INFINITY : S[bb] * C2;
        const float m = xg_max(row16_max(sc));
        S[bb] = __builtin_amdgcn_exp2f(sc - m);
        const float l = xg_sum(row16_sum(S[bb]));
        if (lane == 0) *reinterpret_cast<f32x2*>(pml + (bb * 16 + w) * 2) = f32x2{m, l};
      }
      if constexpr (i == 4 * NS - 1) {
        asm volatile("" ::: "memory");
        u32 arr = 0;
        if (lane == 0) arr = __hip_atomic_fetch_add(lorg + 6, 1u, __ATOMIC_RELAXED, __HIP_MEMORY_SCOPE_WORKGROUP);
        arr = __builtin_amdgcn_readfirstlane(arr);
        asm volatile("" ::: "memory");
        const f32x2 ml = *reinterpret_cast<const f32x2*>(pml + lane * 2);
        const float M = row16_max(ml[0]);
        const float L = row16_sum(__builtin_amdgcn_exp2f(ml[0] - M) * ml[1]);
        __builtin_amdgcn_raw_buffer_store_b64(u32x2{__float_as_uint(j ? L : M), tg}, rs, (arr == 15u && j < 2) ? (int)WS_MLA + (h * 8 + ci) * 128 + g * 16 + j * 8 : OOB, 0, 16);
      }
    } else {
      const float p = dpp<0x150 + sl>(S[bb]);
      if constexpr (sl == 0) {
#pragma unroll
        for (int k = 0; k < 8; ++k) acc[k] = 0.f;
      }
#pragma unroll
      for (int k = 0; k < 4; ++k) { acc[2 * k] = fmaf(p, bf_lo(r[k]), acc[2 * k]); acc[2 * k + 1] = fmaf(p, bf_hi(r[k]), acc[2 * k + 1]); }
      asm volatile("" : "+v"(acc[0]), "+v"(acc[1]), "+v"(acc[2]), "+v"(acc[3]), "+v"(acc[4]), "+v"(acc[5]), "+v"(acc[6]), "+v"(acc[7]));
      if constexpr (issued(i + 1) > issued(i)) { GATE(issued(i)); WO_ISSUE(); A_ISSUE(issued(i)); CTRL(issued(i)); if constexpr (issued(i + 1) > issued(i) + 1) { GATE(issued(i) + 1); A_ISSUE(issued(i) + 1); CTRL(issued(i) + 1); } } else { WO_ISSUE(); }
      if constexpr (i == XR) xv = __builtin_amdgcn_raw_buffer_load_b64(rs, xoff, 0, 16);
      if constexpr (i == XR + 2) {
        if (w == XW) {
          if (__builtin_expect(!__all(lane >= 8 * ACU || xv[1] == tg), 0)) {
#pragma unroll 1
            for (u32 spins = 0; spins < SPIN_CAP; ++spins) {
              __builtin_amdgcn_s_sleep(8); xv = __builtin_amdgcn_raw_buffer_load_b64(rs, xoff, 0, 16);
              if (__all(lane >= 8 * ACU || xv[1] == tg)) break;
            }
          }
          const float x = __uint_as_float(xv[0]);
          const float m = lane < 8 * ACU ? dpp<0xA0>(x) : -INFINITY, l = lane < 8 * ACU ? dpp<0xF5>(x) : 0.f;
          const float M = xg_max(fmaxf(m, dpp<0x128>(m)));
          float t = __builtin_amdgcn_exp2f(m - M) * l; t = xg_sum(t + dpp<0x128>(t));
          if (lane < 8 && !(lane & 1)) *reinterpret_cast<f32x2*>(wtab + 64 + lane) = f32x2{M, 1.f / t};
          asm volatile("" ::: "memory");
          const f32x2 gm = *reinterpret_cast<const f32x2*>(wtab + 64 + 2 * g);
          wtab[lane] = __builtin_amdgcn_exp2f(pml[lane * 2] - gm[0]) * gm[1];
          asm volatile("" ::: "memory");
          if (lane == 0) __hip_atomic_store(lorg + 7, 1u, __ATOMIC_RELAXED, __HIP_MEMORY_SCOPE_WORKGROUP);
        }
      }
      if constexpr (i >= WTS && i < WTS + 4) {
        constexpr int s = i - WTS;
#pragma unroll
        for (int t = 0; t < 4; ++t) WT[s][t] = tcol8(RW[s], t);
        asm volatile("" : "+v"(WT[s][0]), "+v"(WT[s][1]), "+v"(WT[s][2]), "+v"(WT[s][3]));
      }
      if constexpr (i == PJ0 || i == PJ1 || i == PJ2) {
        constexpr int b = i == PJ0 ? 0 : i == PJ1 ? 1 : 2;
        Y[b] = 0.f;
        if (w < 9) {
          while (__hip_atomic_load(lorg + 24 + b, __ATOMIC_RELAXED, __HIP_MEMORY_SCOPE_WORKGROUP) != 16u || __hip_atomic_load(lorg + 7, __ATOMIC_RELAXED, __HIP_MEMORY_SCOPE_WORKGROUP) == 0u) __builtin_amdgcn_s_sleep(1);
          asm volatile("" ::: "memory");
          project(b);
        }
        asm volatile("" : "+v"(Y[b]));
      }
      if constexpr (sl == NS - 1) {
        const float t0 = sw16(sw32(acc[0], acc[4]), sw32(acc[2], acc[6])), t1 = sw16(sw32(acc[1], acc[5]), sw32(acc[3], acc[7]));
        *reinterpret_cast<u32*>(lds + L_AB + (bb * 16 + w) * 272 + 16 * j + 4 * g) = pk_bf16(t0, t1);
        if constexpr (bb < 3) { asm volatile("" ::: "memory"); if (lane == 0) __hip_atomic_fetch_add(lorg + 24 + bb, 1u, __ATOMIC_RELAXED, __HIP_MEMORY_SCOPE_WORKGROUP); }
      }
    }
    __builtin_amdgcn_sched_barrier(0);
  });
#undef A_ISSUE
  if (STEPST) { if (lane == 0) { reinterpret_cast<u32*>(lds + 65536)[w * 96 + NSEQ] = rt32(); reinterpret_cast<u32*>(lds + 65536)[w * 96 + NSEQ + 1] = lorg[0]; for (int k = 0; k < 8; ++k) reinterpret_cast<u32*>(lds + 65536)[w * 96 + NSEQ + 2 + k] = lorg[16 + k]; }
    for (int k = lane; k < 96; k += 64) reinterpret_cast<u32*>(a.ws + (20u << 20))[(blockIdx.x * 16 + w) * 96 + k] = reinterpret_cast<u32*>(lds + 65536)[w * 96 + k]; }
  ST(2);
  __syncthreads();
  if (w < 9) {
    project(3);
    st16_sc1(rs, WS_Y + ((8 * w + (j >> 1)) * 256 + idx) * 128 + ((j & 1) * 4 + g) * 16, u32x4{pk_bf16(Y[0], Y[1]), tg, pk_bf16(Y[2], Y[3]), tg});
  }
  ST(4);
  if (idx < 72) {
    __builtin_amdgcn_sched_barrier(0);
    __builtin_amdgcn_s_barrier();
    __builtin_amdgcn_s_sleep(FDLY);
    const int yo = (int)WS_Y + (idx * 256 + 16 * w) * 128 + lane * 16;
    u32x4 v0, v1;
#pragma unroll 1
    for (u32 spins = 0; spins < SPIN_CAP; ++spins) {
      v0 = __builtin_amdgcn_raw_buffer_load_b128(rs, yo, 0, 16); v1 = __builtin_amdgcn_raw_buffer_load_b128(rs, yo + 1024, 0, 16);
      if (__all(v0[1] == tg && v0[3] == tg && v1[1] == tg && v1[3] == tg)) break;
      __builtin_amdgcn_s_sleep(FSL);
    }
    ST(7);
    float y = sw16(sw32(bf_lo(v0[0]) + bf_lo(v1[0]), bf_lo(v0[2]) + bf_lo(v1[2])), sw32(bf_hi(v0[0]) + bf_hi(v1[0]), bf_hi(v0[2]) + bf_hi(v1[2])));
    y += dpp<0x128>(y);
    float* red = reinterpret_cast<float*>(lds + L_PF);
    if (!(lane & 8)) red[((lane & 7) * 4 + g) * 20 + w] = y;
    __syncthreads();
    if (tid < 32) {
      const f32x4 r0 = *reinterpret_cast<const f32x4*>(red + tid * 20), r1 = *reinterpret_cast<const f32x4*>(red + tid * 20 + 4);
      const f32x4 r2 = *reinterpret_cast<const f32x4*>(red + tid * 20 + 8), r3 = *reinterpret_cast<const f32x4*>(red + tid * 20 + 12);
      const f32x4 r = r0 + r1 + r2;
      const __amdgpu_buffer_rsrc_t ro = __builtin_amdgcn_make_buffer_rsrc(a.out, 0, 4 * 1152 * 2, 0x00020000);
      __builtin_amdgcn_raw_buffer_store_b16(to_bf16(((r[0] + r[1]) + (r[2] + r[3])) + (r3[0] + r3[1])), ro, ((tid & 3) * 1152 + idx * 8 + (tid >> 2)) * 2, 0, OAUX);
      __builtin_amdgcn_raw_buffer_store_b16(to_bf16(r3[2] + r3[3]), ro, ((tid >> 3) * 1152 + 576 + idx * 8 + (tid & 7)) * 2, 0, OAUX);
    }
  }
  ST(5);
}

#define IA 2
#define IB 4
#define IC 6
#define EUV 4
#define YOFF 90
#define WPD 4
template <bool LD> DI void part_b(const Args& a, unsigned char* lds, int wg, u64 tag, u32 pf) {
  const int tid = threadIdx.x, lane = tid & 63, w = __builtin_amdgcn_readfirstlane(tid >> 6);
  const int j = lane & 15, g = lane >> 4;
  const __amdgpu_buffer_rsrc_t rs = __builtin_amdgcn_make_buffer_rsrc(a.ws, 0, (int)WS_END, 0x00020000);
  const u32 tb0 = rt32();
  const int h = BPF ? (wg & 7) * 4 + (wg >> 3) : wg;
  const int h3 = h ^ 2;
  const int b2 = BMAP ? (wg & 7) >> 1 : wg >> 3, sp = BMAP ? (wg >> 3) * 2 + (wg & 1) : wg & 7;
  const u16* cl = a.C + ((size_t)b2 * SK + sp * 512 + 8 * (w - 8)) * DC + lane * 8;
  u32x4 X[16];
  u32x2 RW[4][8];
  u32x4 W8[2];
  u32 tv = 0;
#define BST(n) do { if (STEPST && LD) { __builtin_amdgcn_sched_barrier(0); const u32 t_ = rt32(); tv = (lane == (n)) ? t_ : tv; __builtin_amdgcn_sched_barrier(0); } } while (0)
  u32* cnt = reinterpret_cast<u32*>(lds + L_ST + 4064);
#define C_ISSUE(T) _Pragma("unroll") for (int r = 0; r < 8; ++r) X[8 * ((T) & 1) + r] = ld16(cl + (size_t)(64 * (T) + r) * DC)
#define C_HALF(T, hf) _Pragma("unroll") for (int r = 4 * (hf); r < 4 * (hf) + 4; ++r) X[8 * ((T) & 1) + r] = ld16(cl + (size_t)(64 * (T) + r) * DC)
#define L_PART(T, r0, r1) do { __builtin_amdgcn_sched_barrier(0); \
    if ((T) + 3 < 8) { _Pragma("unroll") for (int r = r0; r < r1; ++r) X[8 * (((T) + 3) & 1) + r] = ld16(cl + (size_t)(64 * ((T) + 3) + r) * DC); } \
    if ((T) == 5 || (T) == 6) { _Pragma("unroll") for (int k = 2 * (r0); k < 2 * (r1); ++k) \
      RW[2 * (((T) - 5) & 1) + (k >> 3)][k & 7] = ld8(a.woB + ((size_t)h3 * 128 + 32 * (2 * (((T) - 5) & 1) + (k >> 3)) + 8 * g + (k & 7)) * DM + 64 * (w - 8) + 4 * j); } \
    __builtin_amdgcn_sched_barrier(0); } while (0)
#define UV_LOAD(n) do { const u32x4 v_ = ld16(a.wuv + ((size_t)(64 * w + 32 * ((n) >> 3) + 8 * g + ((n) & 7)) * H + h3) * 128 + 8 * j); \
    RW[2 * ((n) >> 3)][(n) & 7] = u32x2{v_[0], v_[1]}; RW[2 * ((n) >> 3) + 1][(n) & 7] = u32x2{v_[2], v_[3]}; } while (0)
#define C_WIN(T, r0, r1) _Pragma("unroll") for (int r = r0; r < r1; ++r) { asm volatile("s_waitcnt vmcnt(%0)" :: "n"(WPD - 1) : "memory"); X[8 * ((T) & 1) + r] = ld16(cl + (size_t)(64 * (T) + r) * DC); __builtin_amdgcn_sched_barrier(0); }
  u32 sb0 = L_BIG + 8 * (w - 8) * CRS + lane * 16, sb1 = sb0 + CBUF;
  asm volatile("" : "+v"(sb0), "+v"(sb1));
#define C_STAGE(T) _Pragma("unroll") for (int r = 0; r < 8; ++r) *reinterpret_cast<u32x4*>(lds + (((T) & 1) ? sb1 : sb0) + r * CRS) = X[8 * ((T) & 1) + r]
  {
    const u16* Wq = a.wqB + h * 128;
    u32x4 U[8];
    QPre Q;
    if constexpr (!LD) { ST(0); }
    qproj_load(Q, a.xB, Wq, w, lane);
#pragma unroll
    for (int i = 0; i < 8; ++i) U[i] = ld16(a.wuk + ((size_t)(32 * w + 4 * i + (j & 3)) * H + h) * 128 + 8 * ((j & 12) + g));
    __builtin_amdgcn_sched_barrier(0);
    BST(0);
    if constexpr (!LD) am_zero(lds, tid);
    qproj_mfma(Q, lds, w, lane);
    asm volatile("" :: "v"(pf));
    BST(1);
    __syncthreads();
    BST(2);
    if constexpr (LD) { C_HALF(0, 0); }
    else {
      const float* qpart = reinterpret_cast<const float*>(lds + L_BIG);
      float s = 0.f;
#pragma unroll
      for (int ww = 0; ww < 16; ++ww) s += qpart[ww * 512 + tid];
      reinterpret_cast<u16*>(lds + L_QS)[tid] = to_bf16(s);
    }
    __builtin_amdgcn_sched_barrier(0);
    __syncthreads();
    BST(3);
    const u32x4 af = *reinterpret_cast<const u32x4*>(lds + L_QS + ((j >> 2) * 128 + ((j & 3) * 4 + g) * 8) * 2);
    u16* qa = reinterpret_cast<u16*>(lds + L_PF);
    float v2[2] = {0.f, 0.f};
#pragma unroll
    for (int i = 0; i < 8; ++i) {
      const f32x4 d4 = mfma16(af, U[i], f32x4{0.f, 0.f, 0.f, 0.f});
      float d = (j & 8) ? ((j & 4) ? d4[3] : d4[2]) : ((j & 4) ? d4[1] : d4[0]);
      d += dpp<0x124>(d); d += dpp<0x128>(d);
      v2[i >> 2] = ((j >> 2) == (i & 3)) ? d : v2[i >> 2];
    }
    qa[g * 512 + 32 * w + j] = to_bf16(v2[0]); qa[g * 512 + 32 * w + 16 + j] = to_bf16(v2[1]);
    __builtin_amdgcn_sched_barrier(0);
    BST(4);
    __syncthreads();
    if constexpr (!LD) {
      if (w == 0) {
#pragma unroll
        for (int bb = 0; bb < 4; ++bb) st16_sc1(rs, WS_QABS + ((bb * H + h) * 512 + lane * 8) * 2, *reinterpret_cast<const u32x4*>(qa + bb * 512 + lane * 8));
      }
      __builtin_amdgcn_sched_barrier(0);
      __builtin_amdgcn_s_barrier();
      if (w == 0) {
        asm volatile("s_waitcnt vmcnt(0)" ::: "memory");
        if (lane == 0) { __hip_atomic_store(reinterpret_cast<u64*>(a.ws + WS_F1 + wg * 8), tag, RLX_AGENT); cnt[0] = 0u; cnt[1] = 0u; }
      }
    } else {
      __builtin_amdgcn_s_barrier();
      BST(5);
      C_WIN(0, 4, 8); C_WIN(1, 0, 8);
      BST(6);
      C_STAGE(0);
      __builtin_amdgcn_sched_barrier(0);
      BST(7);
      C_WIN(2, 0, 8);
      BST(8);
    }
    if constexpr (!LD) { ST(1); }
  }
  {
    const int b = b2;
    if constexpr (!LD) wg_wait64(a.ws + WS_F1, tag); else __syncthreads();
    if constexpr (!LD) { ST(2); }
    const int kt = w & 3, hh = (w >> 2) & 1;
    float* stmax = reinterpret_cast<float*>(lds + L_ST);
    float* stsum = stmax + 128;
    float* salpha = stmax + 256;
    unsigned char* Pf = lds + L_PF;
    float M = -INFINITY, lsum = 0.f;
    f32x4 acc[2][2];
#pragma unroll
    for (int ct = 0; ct < 2; ++ct) { acc[ct][0] = f32x4{0.f, 0.f, 0.f, 0.f}; acc[ct][1] = f32x4{0.f, 0.f, 0.f, 0.f}; }
    const int troff = (4 * g + (j >> 2)) * CRS + (32 * w + 4 * (j & 3)) * 2;
    auto pv_step = [&](const unsigned char* cb) __attribute__((always_inline)) {
      const f32x4 al0 = *reinterpret_cast<const f32x4*>(salpha + 4 * g), al1 = *reinterpret_cast<const f32x4*>(salpha + 16 + 4 * g);
#pragma unroll
      for (int ct = 0; ct < 2; ++ct) { acc[ct][0] *= al0; acc[ct][1] *= al1; }
#pragma unroll
      for (int blk = 0; blk < 2; ++blk) {
        const u32x4 p0 = *reinterpret_cast<const u32x4*>(Pf + ((blk * 2 + 0) * 64 + lane) * 16);
        const u32x4 p1 = *reinterpret_cast<const u32x4*>(Pf + ((blk * 2 + 1) * 64 + lane) * 16);
#pragma unroll
        for (int ct = 0; ct < 2; ++ct) {
          const u32x2 lo = trread(cb + troff + (32 * blk) * CRS + 32 * ct);
          const u32x2 hi = trread(cb + troff + (32 * blk + 16) * CRS + 32 * ct);
          const u32x4 bf = {lo[0], lo[1], hi[0], hi[1]};
          acc[ct][0] = mfma16(p0, bf, acc[ct][0]); acc[ct][1] = mfma16(p1, bf, acc[ct][1]);
        }
      }
    };
    unsigned char* Qf = lds + L_BIG + CBUF;
    if constexpr (!LD) {
      u32x4 qv[4];
#pragma unroll
      for (int k = 0; k < 4; ++k) qv[k] = __builtin_amdgcn_raw_buffer_load_b128(rs, (int)WS_QABS + (b * H * 512 + (tid + 512 * k) * 8) * 2, 0, 16);
#pragma unroll
      for (int k = 0; k < 4; ++k) { const int e = tid + 512 * k, head = e >> 6, ch = e & 63;
        *reinterpret_cast<u32x4*>(Qf + ((head >> 4) * 16 + (ch >> 2)) * 1040 + ((ch & 3) * 16 + (head & 15)) * 16) = qv[k]; }
    }
    __syncthreads();
    if constexpr (!LD) {
      u32x4 QF[16];
#pragma unroll
      for (int s = 0; s < 16; ++s) QF[s] = *reinterpret_cast<const u32x4*>(Qf + (hh * 16 + s) * 1040 + lane * 16);
      auto s_tile = [&](const unsigned char* cb, bool last) __attribute__((always_inline)) {
        __syncthreads();
        f32x4 s0 = {0.f, 0.f, 0.f, 0.f}, s1 = {0.f, 0.f, 0.f, 0.f};
#pragma unroll
        for (int s = 0; s < 16; s += 2) {
          s0 = mfma16(*reinterpret_cast<const u32x4*>(cb + (16 * kt + j) * CRS + (32 * s + 8 * g) * 2), QF[s], s0);
          s1 = mfma16(*reinterpret_cast<const u32x4*>(cb + (16 * kt + j) * CRS + (32 * s + 32 + 8 * g) * 2), QF[s + 1], s1);
        }
        const f32x4 sv = (s0 + s1) * C2;
        if (last) { __builtin_amdgcn_sched_barrier(0); _Pragma("unroll") for (int n = 0; n < EUV; ++n) UV_LOAD(n); __builtin_amdgcn_sched_barrier(0); }
        const float v = xg_max(fmaxf(fmaxf(sv[0], sv[1]), fmaxf(sv[2], sv[3])));
        if (g == 0) stmax[kt * 32 + 16 * hh + j] = v;
        __syncthreads();
        const float* sm = stmax + 16 * hh + j;
        const float Mn = fmaxf(fmaxf(M, fmaxf(sm[0], sm[32])), fmaxf(sm[64], sm[96]));
        const float alpha = __builtin_amdgcn_exp2f(M - Mn);
        M = Mn;
        u32x2 pk;
        pk[0] = pk_bf16(__builtin_amdgcn_exp2f(sv[0] - Mn), __builtin_amdgcn_exp2f(sv[1] - Mn));
        pk[1] = pk_bf16(__builtin_amdgcn_exp2f(sv[2] - Mn), __builtin_amdgcn_exp2f(sv[3] - Mn));
        lsum = lsum * alpha + ((bf_lo(pk[0]) + bf_hi(pk[0])) + (bf_lo(pk[1]) + bf_hi(pk[1])));
        *reinterpret_cast<u32x2*>(Pf + (((kt >> 1) * 2 + hh) * 64 + lane) * 16 + (kt & 1) * 8) = pk;
        if (kt == 0 && g == 0) salpha[16 * hh + j] = alpha;
        __syncthreads();
        pv_step(cb);
      };
#pragma unroll 1
      for (int T = 0; T < 6; T += 2) { s_tile(lds + L_BIG, false); s_tile(lds + L_BIG + CBUF, false); }
      s_tile(lds + L_BIG, false); s_tile(lds + L_BIG + CBUF, true);
    } else {
#pragma unroll
      for (int T = 0; T < 8; ++T) {
        __syncthreads();
        BST(10 + 4 * T);
        if (T + 1 < 8) { C_STAGE(T + 1); }
        BST(11 + 4 * T);
        L_PART(T, 0, IA);
        if (T == 7) {
#pragma unroll
          for (int k = 0; k < 2; ++k) W8[k] = ld16(a.woB + ((size_t)h3 * 128 + 16 * (w - 8) + 8 * k + (lane >> 3)) * DM + 512 + (lane & 7) * 8);
        }
        BST(12 + 4 * T);
        __syncthreads();
        L_PART(T, IA, IB);
        __syncthreads();
        BST(13 + 4 * T);
        L_PART(T, IB, IC);
        pv_step(lds + L_BIG + (T & 1) * CBUF);
        L_PART(T, IC, 8);
      }
      BST(42);
      if (STEPST) reinterpret_cast<u32*>(a.ws + (20u << 20))[(blockIdx.x * 16 + w) * 96 + lane] = tv;
    }
#undef C_ISSUE
#undef C_STAGE
    if constexpr (!LD) { ST(3); }
    __syncthreads();
    if constexpr (!LD) { lsum = xg_sum(lsum); if (g == 0) stsum[kt * 32 + 16 * hh + j] = lsum; if (kt == 0 && g == 0) salpha[32 + 16 * hh + j] = M; }
    float* tb = reinterpret_cast<float*>(lds + L_BIG);
#pragma unroll
    for (int ct = 0; ct < 2; ++ct)
#pragma unroll
      for (int h2 = 0; h2 < 2; ++h2)
#pragma unroll
        for (int r = 0; r < 4; ++r) tb[(16 * h2 + 4 * g + r) * 516 + 32 * w + 16 * ct + j] = acc[ct][h2][r];
    if constexpr (!LD) { for (int i = tid; i < 8 * AM2_LD * 2 / 16; i += 512) reinterpret_cast<u32x4*>(lds + L_AM2 + 8 * AM2_LD * 2)[i] = u32x4{0u, 0u, 0u, 0u}; }
    __syncthreads();
    if constexpr (!LD) { ST(4); }
    if constexpr (LD) {
      const int t2 = tid - 512;
#pragma unroll
      for (int k = 0; k < 4; ++k) {
        const int e = t2 + 512 * k, head = e >> 6, c8 = e & 63;
        const f32x4 v0 = *reinterpret_cast<const f32x4*>(tb + head * 516 + 8 * c8), v1 = *reinterpret_cast<const f32x4*>(tb + head * 516 + 8 * c8 + 4);
        __builtin_amdgcn_raw_buffer_store_b128(u32x4{pk_bf16(v0[0], v0[1]), pk_bf16(v0[2], v0[3]), pk_bf16(v1[0], v1[1]), pk_bf16(v1[2], v1[3])}, rs, (int)WS_CTXP + (((b * NSP + sp) * 32 + head) * 512 + 8 * c8) * 2, 0, 16);
      }
      if (t2 < 32) {
        const float L = (stsum[t2] + stsum[32 + t2]) + (stsum[64 + t2] + stsum[96 + t2]);
        st8_sc1(a.ws + WS_MLB + ((size_t)(b * NSP + sp) * 32 + t2) * 8, f32x2{salpha[32 + t2], L});
      }
      __builtin_amdgcn_sched_barrier(0);
      __builtin_amdgcn_s_barrier();
      asm volatile("s_waitcnt vmcnt(0)" ::: "memory");
      if (lane == 0) {
        if (__hip_atomic_fetch_add(cnt, 1u, __ATOMIC_RELAXED, __HIP_MEMORY_SCOPE_WORKGROUP) == 7u) {
          __hip_atomic_store(reinterpret_cast<u64*>(a.ws + WS_F2 + wg * 8), tag, RLX_AGENT);
        }
      }
#pragma unroll
      for (int k = 0; k < 2; ++k) *reinterpret_cast<u32x4*>(lds + L_W8 + (16 * (w - 8) + 8 * k + (lane >> 3)) * 144 + (lane & 7) * 16) = W8[k];
    } else {
      __builtin_amdgcn_s_barrier();
      static_for<16 - EUV>([&](auto ic) __attribute__((always_inline)) {
        asm volatile("s_waitcnt vmcnt(%0)" :: "n"(WPD - 1) : "memory");
        UV_LOAD(EUV + ic.value);
        __builtin_amdgcn_sched_barrier(0);
      });
    }
  }
  {
    int tid = threadIdx.x; asm volatile("" : "+v"(tid));
    const int lane = tid & 63, j = lane & 15, g = lane >> 4;
    u16* Am2 = reinterpret_cast<u16*>(lds + L_AM2);
    float* cpart = reinterpret_cast<float*>(lds + L_CP);
    __builtin_amdgcn_sched_barrier(0);
    if constexpr (LD) {
      if (w == 8) {
        wave_wait(reinterpret_cast<const u64*>(a.ws + WS_F2) + (lane & (NBW - 1)), true, tag);
        asm volatile("s_waitcnt vmcnt(0)" ::: "memory");
        if (lane == 0) __hip_atomic_store(cnt + 1, 1u, __ATOMIC_RELEASE, __HIP_MEMORY_SCOPE_WORKGROUP);
      } else {
        for (u32 spins = 0; spins < 64u * SPIN_CAP; ++spins) { if (__hip_atomic_load(cnt + 1, __ATOMIC_ACQUIRE, __HIP_MEMORY_SCOPE_WORKGROUP) != 0u) break; __builtin_amdgcn_s_sleep(1); }
      }
      __builtin_amdgcn_sched_barrier(0);
      const int b = (tid >> 7) & 3, c4 = tid & 127;
      u32x2 pv[NSP];
      const f32x2 ml = __builtin_bit_cast(f32x2, __builtin_amdgcn_raw_buffer_load_b64(rs, (int)WS_MLB + ((b * NSP + (j & 7)) * 32 + h3) * 8, 0, 16));
#pragma unroll
      for (int s = 0; s < NSP; ++s) pv[s] = __builtin_amdgcn_raw_buffer_load_b64(rs, (int)WS_CTXP + (((b * NSP + s) * 32 + h3) * 512 + 4 * c4) * 2, 0, 16);
      const float M = row16_max(ml[0]);
      float wt = __builtin_amdgcn_exp2f(ml[0] - M);
      wt *= 2.f / row16_sum(wt * ml[1]);
      float c0 = 0.f, c1 = 0.f, c2 = 0.f, c3 = 0.f;
#pragma unroll
      for (int s = 0; s < NSP; ++s) { const float ws_ = rdlane(wt, s); c0 = fmaf(ws_, bf_lo(pv[s][0]), c0); c1 = fmaf(ws_, bf_hi(pv[s][0]), c1); c2 = fmaf(ws_, bf_lo(pv[s][1]), c2); c3 = fmaf(ws_, bf_hi(pv[s][1]), c3); }
      const u32 h0 = pk_bf16(c0, c1), h1 = pk_bf16(c2, c3);
      *reinterpret_cast<u32x2*>(Am2 + (2 * b) * AM2_LD + 4 * c4) = u32x2{h0, h1};
      *reinterpret_cast<u32x2*>(Am2 + (2 * b + 1) * AM2_LD + 4 * c4) = u32x2{pk_bf16(c0 - bf_lo(h0), c1 - bf_hi(h0)), pk_bf16(c2 - bf_lo(h1), c3 - bf_hi(h1))};
      __syncthreads();
      __syncthreads();
      const int t2 = tid - 512;
      float s = 0.f;
#pragma unroll
      for (int ww = 0; ww < 8; ++ww) s += cpart[ww * 512 + t2];
      u16* Am = reinterpret_cast<u16*>(lds + L_AM);
      const int bb = t2 >> 7, d = t2 & 127;
      const u16 hi = to_bf16(s);
      Am[(4 * bb) * AM_LD + d] = hi; Am[(4 * bb + 1) * AM_LD + d] = to_bf16(s - bf2f(hi));
    } else {
      __syncthreads();
      f32x4 acc[8];
#pragma unroll
      for (int t = 0; t < 8; ++t) acc[t] = f32x4{0.f, 0.f, 0.f, 0.f};
#pragma unroll
      for (int k2 = 0; k2 < 2; ++k2) {
        const u32x4 af = *reinterpret_cast<const u32x4*>(Am2 + j * AM2_LD + 64 * w + 32 * k2 + 8 * g);
#pragma unroll
        for (int t = 0; t < 8; ++t) acc[t] = mfma16(af, tcol8(RW[2 * k2 + (t >> 2)], t & 3), acc[t]);
      }
      if (g < 2) {
#pragma unroll
        for (int bb = 0; bb < 2; ++bb) {
          float* p = cpart + (w * 4 + 2 * g + bb) * 128 + 8 * j;
          *reinterpret_cast<f32x4*>(p) = f32x4{acc[0][2 * bb] + acc[0][2 * bb + 1], acc[1][2 * bb] + acc[1][2 * bb + 1], acc[2][2 * bb] + acc[2][2 * bb + 1], acc[3][2 * bb] + acc[3][2 * bb + 1]};
          *reinterpret_cast<f32x4*>(p + 4) = f32x4{acc[4][2 * bb] + acc[4][2 * bb + 1], acc[5][2 * bb] + acc[5][2 * bb + 1], acc[6][2 * bb] + acc[6][2 * bb + 1], acc[7][2 * bb] + acc[7][2 * bb + 1]};
        }
      }
      __builtin_amdgcn_sched_barrier(0);
      __syncthreads();
      if (YDEF == 1 && w == 1) {
        u32 dl = tb0 + 5200u;
#pragma unroll 1
        while ((int)(rt32() - dl) < 0) {
          const u32x2 rv = __builtin_amdgcn_raw_buffer_load_b64(rs, lane < 16 ? (int)WS_RPT + lane * 4352 : OOB, 128 * (NRND - 1) * 35, 16);
          const bool ok = rv[1] == (u32)tag + (NRND - 1);
          const float cn = rdlane(row16_sum(ok ? 1.f : 0.f), 0);
          if (cn >= 8.f) { dl = tb0 + (u32)(int)(rdlane(row16_sum(ok ? (float)(int)(rv[0] - tb0) : 0.f), 0) / cn) + (u32)(due_at(NSEQ - 1) - due_at(rtab[NRND - 1]) + YOFF); break; }
          __builtin_amdgcn_s_sleep(32);
        }
        if (lane == 0) cnt[2] = (int)(dl - tb0) > 5200 ? tb0 : dl;
      }
      if (w == 0) {
#pragma unroll
        for (int s = 0; s < 4; ++s)
#pragma unroll
          for (int jj = 0; jj < 8; ++jj) RW[s][jj] = *reinterpret_cast<const u32x2*>(lds + L_W8 + (32 * s + 8 * g + jj) * 144 + 8 * j);
      }
    }
    __syncthreads();
    if (LD || w == 0) {
      f32x4 ya[4];
      wo_mfma(RW, lds, lane, ya);
      if (YDEF) { const u32 dl = YDEF == 1 ? __builtin_amdgcn_readfirstlane(cnt[2]) : tb0 + YDEF; while ((int)(rt32() - dl) < 0) __builtin_amdgcn_s_sleep(4); }
      st16_sc1(rs, WS_Y + ((8 * (LD ? w - 8 : 8) + (j >> 1)) * 256 + NAW + h3) * 128 + g * 32 + (j & 1) * 16, yrow(ya, (u32)(tag >> 32)));
    }
    if constexpr (!LD) { ST(7); }
  }
}

__global__ void __launch_bounds__(1024, 4) k_main(Args a) {
  __shared__ __attribute__((aligned(16))) unsigned char lds[L_TOTAL];
  const u32 t_entry = (u32)__builtin_amdgcn_s_memrealtime();
  u32 pf;
  { u64 pc; u32 len;
    asm volatile("s_getpc_b64 %0\n.Lpf%=:\n\ts_mov_b32 %1, k_tail_mark-.Lpf%=" : "=s"(pc), "=s"(len));
    pf = __builtin_amdgcn_raw_buffer_load_b32(__builtin_amdgcn_make_buffer_rsrc(reinterpret_cast<void*>(pc), 0, len < 131072u ? len : 0u, 0x00020000), CPFM ? (threadIdx.x < 16 ? ((blockIdx.x >> 3) * 16 + threadIdx.x) * 128 : 0x7ffff000) : threadIdx.x * 128, 0, 0);
    __builtin_amdgcn_sched_barrier(0); }
  u64 tag = (hw_dispatch_id() + 1ull) * 0x9E3779B97F4A7C15ull;
  tag ^= ((u64)__builtin_amdgcn_dispatch_ptr() >> 6) * 0xC2B2AE3D27D4EB4Full;
  tag |= 1ull << 63;
  if (__builtin_expect(blockIdx.x >= NBW, 1)) part_a(a, lds, blockIdx.x - NBW, tag, pf);
  else if (__builtin_amdgcn_readfirstlane(threadIdx.x >> 6) < 8) part_b<false>(a, lds, blockIdx.x, tag, pf);
  else part_b<true>(a, lds, blockIdx.x, tag, pf);

  {
    const u32 tg = (u32)(tag >> 32); const int tid = threadIdx.x;
    u32* sl = reinterpret_cast<u32*>(lds + L_ST + 3072);
    const u32 t_end = (u32)__builtin_amdgcn_s_memrealtime();
    __syncthreads();
    if (tid == 0) { sl[15] = t_entry; sl[14] = t_end; sl[13] = reinterpret_cast<u32*>(lds + L_ST + 2048)[0]; }
    __syncthreads();
    u64* gl = reinterpret_cast<u64*>(a.ws + (16u << 20));
    while ((int)((u32)__builtin_amdgcn_s_memrealtime() - (t_entry + 7000u)) < 0) __builtin_amdgcn_s_sleep(8);
    if (tid < 16) __hip_atomic_store(gl + blockIdx.x * 16 + tid, ((u64)tg << 32) | sl[tid], RLX_AGENT);
    if (DIAG >= 50 && DIAG < 60) {
      unsigned xcc; asm volatile("s_getreg_b32 %0, hwreg(HW_REG_XCC_ID)" : "=s"(xcc)); xcc &= 15;
      u64 x; for (int s = 0; s < 1000000; ++s) { x = __hip_atomic_load(gl + 15, RLX_AGENT); if ((u32)(x >> 32) == tg) break; __builtin_amdgcn_s_sleep(4); }
      const u32 tgt = (u32)x + ((DIAG == 58 || (int)xcc == DIAG - 50) ? 10000u : 9850u);
      for (int i = 0; i < 4000000; ++i) { if ((int)((u32)__builtin_amdgcn_s_memrealtime() - tgt) >= 0) break; __builtin_amdgcn_s_sleep(1); }
    } else if (DIAG == 60) {
      unsigned xcc; asm volatile("s_getreg_b32 %0, hwreg(HW_REG_XCC_ID)" : "=s"(xcc)); xcc &= 15;
      const u32 tgt = t_entry + 10000u + xcc * 1000u;
      if (blockIdx.x == 0) for (int i = 0; i < 4000000; ++i) { if ((int)((u32)__builtin_amdgcn_s_memrealtime() - tgt) >= 0) break; __builtin_amdgcn_s_sleep(1); }
    } else if (blockIdx.x == 0) {
      u32* sh = reinterpret_cast<u32*>(lds);
      u32 v[16];
      if (tid < 256) {
        for (int k = 0; k < 16; ++k) { u64 x; for (int s = 0; s < 1000000; ++s) { x = __hip_atomic_load(gl + tid * 16 + k, RLX_AGENT); if ((u32)(x >> 32) == tg) break; __builtin_amdgcn_s_sleep(4); } v[k] = (u32)x; }
        sh[tid] = v[15] - t_entry + 1000u;
      }
      __syncthreads();
      u32 t0 = ~0u; for (int i = 0; i < 256; ++i) t0 = sh[i] < t0 ? sh[i] : t0;
      t0 = t0 - 1000u + t_entry;
      __syncthreads();
      const bool isB = tid < NBW; u32 x = 0; int mode = 0;
      if (DIAG == 1) x = v[14] - t0;
      if (DIAG == 2) x = v[15] - t0;
      if (DIAG >= 100 && DIAG < 116) { x = isB ? 0 : v[DIAG - 100] - t0; mode = 1; }
      if (DIAG >= 200 && DIAG < 216) { x = isB ? 0 : v[DIAG - 200] - t0; }
      if (DIAG >= 300 && DIAG < 316) { x = isB ? v[DIAG - 300] - t0 : 0; }
      if (DIAG >= 400 && DIAG < 416) { x = isB ? v[DIAG - 400] - t0 : 0; mode = 2; }
      if (DIAG == 4) { x = (isB || tid >= 32 + 192) ? 0 : v[12] - t0; x = x * 224 / 192; mode = 1; }
      if (DIAG == 5) { x = (isB || tid >= 32 + 192) ? 0 : v[12] - t0; }
      if (DIAG == 3) { x = isB ? 0 : v[13]; mode = 1; }
      if (tid < 256) sh[tid] = x;
      __syncthreads();
      u32 X = 0;
      if (mode == 0) { for (int i = 0; i < 256; ++i) X = sh[i] > X ? sh[i] : X; X *= 4; }
      else { for (int i = 0; i < 256; ++i) X += sh[i]; X = X * 4 / (mode == 1 ? 224 : 32); }
      if (X > 40000u) X = 40000u;
      const u32 tgt = t0 + 10000u + X;
      for (int i = 0; i < 4000000; ++i) { if ((int)((u32)__builtin_amdgcn_s_memrealtime() - tgt) >= 0) break; __builtin_amdgcn_s_sleep(1); }
    }
  }
}

extern "C" __global__ void k_tail_mark() {}

static void* g_ws;
extern "C" void kernel_launch(void* const* d_in, const int* in_sizes, int n_in, void* d_out, int out_size, void* d_ws, size_t ws_size,
                              hipStream_t stream) {
  Args a;
  a.xA = (const u16*)d_in[0]; a.K = (const u16*)d_in[1]; a.V = (const u16*)d_in[2]; a.wq = (const u16*)d_in[3]; a.wo = (const u16*)d_in[4];
  a.xB = (const u16*)d_in[5]; a.C = (const u16*)d_in[6]; a.wqB = (const u16*)d_in[7]; a.wuk = (const u16*)d_in[8]; a.wuv = (const u16*)d_in[9]; a.woB = (const u16*)d_in[10];
#define WSTOP 32
  if (WSTOP && ws_size >= ((size_t)WSTOP << 20)) d_ws = (unsigned char*)d_ws + ((ws_size - ((size_t)WSTOP << 20)) & ~(size_t)4095);
  g_ws = d_ws;
  a.ws = (unsigned char*)d_ws;
  a.out = (u16*)d_out;
  k_main<<<NBW + NAW, 1024, 0, stream>>>(a);
}
```

```cpp
#include <hip/hip_runtime.h>
#include <stdint.h>
#include <utility>

typedef unsigned short u16;
typedef unsigned u32;
typedef unsigned long long u64;
typedef u32 u32x4 __attribute__((ext_vector_type(4)));
typedef u32 u32x2 __attribute__((ext_vector_type(2)));
typedef float f32x4 __attribute__((ext_vector_type(4)));
typedef float f32x2 __attribute__((ext_vector_type(2)));
typedef short s16x4 __attribute__((ext_vector_type(4)));
typedef __bf16 bf16x8 __attribute__((ext_vector_type(8)));
typedef __bf16 bf16x2 __attribute__((ext_vector_type(2)));
#define DI __device__ __forceinline__
#define RLX_AGENT __ATOMIC_RELAXED, __HIP_MEMORY_SCOPE_AGENT
extern "C" __device__ u64 hw_dispatch_id() __asm("llvm.amdgcn.dispatch.id");

template <int... Is, class F> DI void static_for_impl(std::integer_sequence<int, Is...>, F&& f) { (f(std::integral_constant<int, Is>{}), ...); }
template <int N, class F> DI void static_for(F&& f) { static_for_impl(std::make_integer_sequence<int, N>{}, f); }

constexpr int H = 32, SK = 4096, DM = 576, DC = 512;
constexpr int ACU = 7;
constexpr int NBW = 32;
constexpr int NAW = H * ACU;
constexpr int NSP = 8;
constexpr float C2 = 0.08838834764831845f * 1.4426950408889634f;

constexpr size_t WS_F1 = 0, WS_F2 = 512;
constexpr size_t WS_QABS = 4096;
constexpr size_t WS_CTXP = WS_QABS + 4 * 32 * 512 * 2;
constexpr size_t WS_MLB = WS_CTXP + (size_t)4 * NSP * 32 * 512 * 2;
constexpr size_t WS_Y = WS_MLB + 4 * NSP * 32 * 2 * 4;
constexpr size_t WS_MLA = WS_Y + (size_t)72 * 256 * 128;
constexpr size_t WS_RPT = WS_MLA + 256 * 128;
constexpr size_t WS_QA = WS_RPT + 64 * 4480;
constexpr size_t WS_END = WS_QA + 256 * 2048;

constexpr int CRS = 1056;
constexpr int CBUF = 64 * CRS;
constexpr int L_BIG = 0;
constexpr int L_AM = 2 * CBUF;
constexpr int L_QS = L_AM + 16 * 272;
constexpr int L_ST = L_QS + 1024;
constexpr int L_PF = L_ST + 4096;
constexpr int L_TOTAL = L_PF + 4096;
constexpr int AM_LD = 136;
constexpr int AM2_LD = 520;
constexpr int L_AM2 = 69632, L_CP = 88064, L_W8 = 106496;

struct Args {
  const u16 *xA, *K, *V, *wq, *wo, *xB, *C, *wqB, *wuk, *wuv, *woB;
  unsigned char* ws;
  u16* out;
};

DI float bf_lo(u32 x) { return __uint_as_float(x << 16); }
DI float bf_hi(u32 x) { return __uint_as_float(x & 0xffff0000u); }
DI u32 pk_bf16(float lo, float hi) { bf16x2 v = {(__bf16)lo, (__bf16)hi}; return __builtin_bit_cast(u32, v); }
DI u16 to_bf16(float x) { return __builtin_bit_cast(u16, (__bf16)x); }
DI float bf2f(u16 x) { return __uint_as_float(((u32)x) << 16); }
DI float dot2(u32 a, u32 b, float c) { return __builtin_amdgcn_fdot2_f32_bf16(__builtin_bit_cast(bf16x2, a), __builtin_bit_cast(bf16x2, b), c, false); }
template <int CTRL> DI float dpp(float x) { return __builtin_bit_cast(float, __builtin_amdgcn_update_dpp(0, __builtin_bit_cast(int, x), CTRL, 0xf, 0xf, true)); }
DI float row16_sum(float x) { x += dpp<0x128>(x); x += dpp<0x124>(x); x += dpp<0x122>(x); x += dpp<0x121>(x); return x; }
DI float row16_max(float x) { x = fmaxf(x, dpp<0x128>(x)); x = fmaxf(x, dpp<0x124>(x)); x = fmaxf(x, dpp<0x122>(x)); x = fmaxf(x, dpp<0x121>(x)); return x; }
DI float xg_max(float x) {
  auto s = __builtin_amdgcn_permlane16_swap(__float_as_uint(x), __float_as_uint(x), false, false);
  x = fmaxf(__uint_as_float(s[0]), __uint_as_float(s[1]));
  auto t = __builtin_amdgcn_permlane32_swap(__float_as_uint(x), __float_as_uint(x), false, false);
  return fmaxf(__uint_as_float(t[0]), __uint_as_float(t[1]));
}
DI float xg_sum(float x) {
  auto s = __builtin_amdgcn_permlane16_swap(__float_as_uint(x), __float_as_uint(x), false, false);
  x = __uint_as_float(s[0]) + __uint_as_float(s[1]);
  auto t = __builtin_amdgcn_permlane32_swap(__float_as_uint(x), __float_as_uint(x), false, false);
  return __uint_as_float(t[0]) + __uint_as_float(t[1]);
}
DI float sw32(float a, float b) {
  auto s = __builtin_amdgcn_permlane32_swap(__float_as_uint(a), __float_as_uint(b), false, false);
  return __uint_as_float(s[0]) + __uint_as_float(s[1]);
}
DI float sw16(float a, float b) {
  auto s = __builtin_amdgcn_permlane16_swap(__float_as_uint(a), __float_as_uint(b), false, false);
  return __uint_as_float(s[0]) + __uint_as_float(s[1]);
}
DI f32x4 mfma16(u32x4 a, u32x4 b, f32x4 c) { return __builtin_amdgcn_mfma_f32_16x16x32_bf16(__builtin_bit_cast(bf16x8, a), __builtin_bit_cast(bf16x8, b), c, 0, 0, 0); }
DI u32x4 ld16(const u16* p) { return __builtin_nontemporal_load(reinterpret_cast<const u32x4*>(p)); }
DI u32x2 ld8(const u16* p) { return __builtin_nontemporal_load(reinterpret_cast<const u32x2*>(p)); }
DI u32x2 trread(const unsigned char* p) {
  return __builtin_bit_cast(u32x2, __builtin_amdgcn_ds_read_tr16_b64_v4i16((__attribute__((address_space(3))) s16x4*)(p)));
}
DI u32 prm(u32 hi, u32 lo, int odd) { return __builtin_amdgcn_perm(hi, lo, odd ? 0x07060302u : 0x05040100u); }

DI u32x4 tcol16(const u32x4 (&R)[8], int t) {
  u32x4 T;
#pragma unroll
  for (int q = 0; q < 4; ++q) T[q] = prm(R[2 * q + 1][t >> 1], R[2 * q][t >> 1], t & 1);
  return T;
}
DI u32x4 tcol8(const u32x2 (&R)[8], int t) {
  u32x4 T;
#pragma unroll
  for (int q = 0; q < 4; ++q) T[q] = prm(R[2 * q + 1][t >> 1], R[2 * q][t >> 1], t & 1);
  return T;
}

#define SPINLOG 22
#define PSL 2
constexpr u32 SPIN_CAP = 1u << SPINLOG;
DI void wave_wait(const u64* f, bool active, u64 tag) {
  for (u32 spins = 0; spins < SPIN_CAP; ++spins) {
    const u64 v = active ? __hip_atomic_load(f, RLX_AGENT) : tag;
    if (__all(v == tag)) break;
    __builtin_amdgcn_s_sleep(PSL);
  }
}
DI void wg_wait64(const unsigned char* flags, u64 tag, int pw = 0) {
  if ((int)(threadIdx.x >> 6) == pw) {
    wave_wait(reinterpret_cast<const u64*>(flags) + (threadIdx.x & (NBW - 1)), true, tag);
    asm volatile("s_waitcnt vmcnt(0)" ::: "memory");
  }
  __syncthreads();
}
DI void st16_sc1(__amdgpu_buffer_rsrc_t rs, size_t off, u32x4 v) { __builtin_amdgcn_raw_buffer_store_b128(v, rs, (int)off, 0, 16); }
DI void st8_sc1(unsigned char* p, f32x2 v) { __hip_atomic_store(reinterpret_cast<u64*>(p), __builtin_bit_cast(u64, v), RLX_AGENT); }

struct QPre { u32x4 R0[8]; u32x4 R1; u32x2 xa, xb; u32 x1; };
DI void qproj_load(QPre& Q, const u16* x, const u16* Wh, int w, int lane) {
  const int j = lane & 15, g = lane >> 4;
  Q.xa = u32x2{0u, 0u}; Q.xb = u32x2{0u, 0u}; Q.x1 = 0u;
  if (j < 4) { Q.xa = ld8(x + j * DM + 36 * w + 8 * g); Q.xb = ld8(x + j * DM + 36 * w + 8 * g + 4); Q.x1 = x[j * DM + 36 * w + 32 + g]; }
#pragma unroll
  for (int jj = 0; jj < 8; ++jj) Q.R0[jj] = ld16(Wh + (size_t)(36 * w + 8 * g + jj) * 4096 + 8 * j);
  Q.R1 = ld16(Wh + (size_t)(36 * w + 32 + g) * 4096 + 8 * j);
}
DI void qproj_mfma(const QPre& Q, unsigned char* lds, int w, int lane) {
  const int j = lane & 15, g = lane >> 4;
  float* qpart = reinterpret_cast<float*>(lds + L_BIG);
  const u32x4 a = {Q.xa[0], Q.xa[1], Q.xb[0], Q.xb[1]}, a1 = {Q.x1, 0u, 0u, 0u};
#pragma unroll
  for (int hf = 0; hf < 2; ++hf) {
    f32x4 acc[4];
#pragma unroll
    for (int t = 0; t < 4; ++t) acc[t] = mfma16(a, tcol16(Q.R0, 4 * hf + t), f32x4{0.f, 0.f, 0.f, 0.f});
#pragma unroll
    for (int t = 0; t < 4; ++t) { const u32x4 bb = {(t & 1) ? (Q.R1[2 * hf + (t >> 1)] >> 16) : (Q.R1[2 * hf + (t >> 1)] & 0xffffu), 0u, 0u, 0u}; acc[t] = mfma16(a1, bb, acc[t]); }
    if (g == 0) {
#pragma unroll
      for (int r = 0; r < 4; ++r) *reinterpret_cast<f32x4*>(qpart + (w * 4 + r) * 128 + 8 * j + 4 * hf) = f32x4{acc[0][r], acc[1][r], acc[2][r], acc[3][r]};
    }
    __builtin_amdgcn_sched_barrier(0);
  }
}
DI void wo_load(const u16* Wo, u32x2 (&R)[4][8], int nb, int lane, int s0, int s1) {
  const int j = lane & 15, g = lane >> 4;
#pragma unroll
  for (int s = 0; s < 4; ++s) if (s >= s0 && s < s1)
#pragma unroll
    for (int jj = 0; jj < 8; ++jj) R[s][jj] = ld8(Wo + (size_t)(32 * s + 8 * g + jj) * DM + 64 * nb + 4 * j);
}
DI void wo_mfma(const u32x2 (&R)[4][8], const unsigned char* lds, int lane, f32x4 (&acc)[4]) {
  const int j = lane & 15, g = lane >> 4;
#pragma unroll
  for (int t = 0; t < 4; ++t) acc[t] = f32x4{0.f, 0.f, 0.f, 0.f};
#pragma unroll
  for (int s = 0; s < 4; ++s) {
    const u32x4 a = *reinterpret_cast<const u32x4*>(lds + L_AM + j * 272 + (32 * s + 8 * g) * 2);
#pragma unroll
    for (int t = 0; t < 4; ++t) acc[t] = mfma16(a, tcol8(R[s], t), acc[t]);
  }
}
DI u32x4 yrow(const f32x4 (&acc)[4], u32 tg) {
  return u32x4{pk_bf16(acc[0][0] + acc[0][1], acc[1][0] + acc[1][1]), tg, pk_bf16(acc[2][0] + acc[2][1], acc[3][0] + acc[3][1]), tg};
}
DI void am_zero(unsigned char* lds, int tid) {
  if (tid < 136) { const int pr = tid / 34; *reinterpret_cast<u32x4*>(lds + L_AM + (4 * pr + 2) * 272 + (tid - 34 * pr) * 16) = u32x4{0u, 0u, 0u, 0u}; }
}
DI float rdlane(float x, int l) { return __builtin_bit_cast(float, __builtin_amdgcn_readlane(__builtin_bit_cast(int, x), l)); }

constexpr int NS = 10;
constexpr int NSEQ = 8 * NS;
#define RNG 9
#define NP 3
#define QP 4
#define SP 4
#define BPF 2
#define TPS 3
#define KD0 60
#define KDP 60
#define KDS 60
#define NOWO 0
#define NOB 0
#define GF0 12
#define GF1 99
#define WOF 1
#define RAUX 16
#define SAUX 16
#define NOACT 0
#define NOPOST 0
#define NOREAD 0
#define PFULL 0
#define PAUX 16
#define RSPR 0
#define QTRY 0
#define QAUX 3
#define QSA 16
#define LATE 0
#define EXQ 1
#define PF2S 0
#define TPN 8
#define TPC 12
#define TPW 10
#define K1G 1
#define PSKIP 9999
#define D3FROM 66
#define D4FROM 99
#define ROTM 1
#define OAUX 0
#define DEP 2
constexpr int DEPTH = DEP, RING = RNG, RAMP = 2;
constexpr int issued(int i) {
  int d = RING - (i + RAMP - 1) / RAMP; if (d < DEPTH) d = DEPTH;
  if (i >= D3FROM && d < 3) d = 3;
  if (i >= D4FROM && d < 4) d = 4;
  int e = i + d; if (e < RING) e = RING; return e > NSEQ ? NSEQ : e;
}
#define PACE 1
#define YDEF 1
#define WO0 16
#define WON 36
#define TA1 535
#define TA2 440
#define NK 68
#define ORG0 130
#define SLMAX 80
#define MAXGAP 70
constexpr int cmin(int a, int b) { return a < b ? a : b; }
#define RSH 1
#define RD 4
#define BMAP 1
#define EBIG 9999
#define NJ 54
#define TA3 515
constexpr int due_at(int n) { return (n <= NJ ? n * TA1 : n <= NK ? NJ * TA1 + (n - NJ) * TA3 : NJ * TA1 + (NK - NJ) * TA3 + (n - NK) * TA2) / 10; }
#define RTAB 20, 30, 40, 50, 60, 70
#define GTAB 0, 12, 6, 12, 12, 12
#define BTAB 8, 12, 6, 12, 12, 0
#define MISS 40
#define DB 10
#define DTAB 20, 20, 20, 15, 5, 3, 3, 3
constexpr int rtab[] = {RTAB}, gtab[] = {GTAB}, btab[] = {BTAB}, dtab[] = {DTAB};
constexpr int NRND = sizeof(rtab) / sizeof(int);
constexpr int rgap(int k) { return k ? rtab[k] - rtab[k - 1] : rtab[0] - RING; }
constexpr int rnd_at(int n) { for (int k = 0; k < NRND; ++k) if (rtab[k] == n) return k; return -1; }
static_assert(rtab[0] >= RING + 2 && rtab[NRND - 1] + RD + 2 < NSEQ, "");
constexpr int wo_step(int k) { return WO0 + k * WON / 32; }
#define WTS 54
#define PJ0 58
#define PJ1 63
#define PJ2 72
#define XW 13
#define XR 50
#define FDLY 8
#define FSL 2
#define CPFM 1
#define STAG 16
#define EXM(m, ...) do { if (EXQ) { __builtin_amdgcn_sched_barrier(0); asm volatile("s_mov_b64 exec, %0" :: "s"((u64)(m))); __builtin_amdgcn_sched_barrier(0); } __VA_ARGS__; \
    if (EXQ) { __builtin_amdgcn_sched_barrier(0); asm volatile("s_mov_b64 exec, -1"); __builtin_amdgcn_sched_barrier(0); } } while (0)
DI u32 rt32() { return (u32)__builtin_amdgcn_s_memrealtime(); }
constexpr int OOB = 0x7ffff000;
constexpr int L_AB = 32768;
#define PERM 0
DI int uoff(int c, int u, int x) {
  if (PERM == 0) return c * 16384 + u * 1024;
  if (PERM == 1) return ((u >> 2) << 10) | ((u & 3) << 15) | ((c & 7) << 12) | ((c >> 3) << 17);
  if (PERM == 2) return ((c & 3) << 10) | ((u & 8) << 9) | ((u & 4) << 11) | ((c & 4) << 12) | ((u & 3) << 15) | ((c >> 3) << 17);
  if (PERM == 3) return ((((u >> 2) ^ x) & 3) << 10) | ((u & 3) << 15) | ((c & 7) << 12) | ((c >> 3) << 17);
  if (PERM == 4) return ((c & 3) << 10) | ((u & 3) << 12) | ((c & 4) << 12) | ((u >> 2) << 15) | ((c >> 3) << 17);
  if (PERM == 12) return c * 16384 + ((u ^ ((c >> 1) & 3)) * 1024);
  if (PERM == 13) return c * 16384 + ((u ^ ((c >> 1) & 3) ^ ((c >> 3) & 3)) * 1024);
  if (PERM == 5) return (((u ^ x) & 3) << 10) | ((u >> 2) << 15) | ((c & 7) << 12) | ((c >> 3) << 17);
  return 0;
}
DI void part_a(const Args& a, unsigned char* lds, int idx, u64 tag, u32 pf) {
  const u64 T64 = __builtin_amdgcn_s_memrealtime();
  __builtin_amdgcn_sched_barrier(0);
  const int tid = threadIdx.x, lane = tid & 63, w = __builtin_amdgcn_readfirstlane(tid >> 6);
  const int j = lane & 15, g = lane >> 4;
  const int h = (idx & 7) * 4 + ((idx >> 3) & 3), ci = idx >> 5;
  const int rr = (((h & 3) * 5) >> 1) + (h >> 2), rot = ROTM ? (rr >= NS ? rr - NS : rr) : (h * 5) >> 4, l0 = (ci * 37) >> 4, ln = ((ci * 37 + 37) >> 4) - l0;
  const int bad_sl = (w < ln) ? -1 : NS - 1 - rot;
  int vtab;
  { const int p = lane + rot >= NS ? lane + rot - NS : lane + rot;
    const bool last = p == NS - 1;
    vtab = uoff(last ? 63 : p * 7 + ci, last ? (l0 + w) & 15 : w, last ? 0 : ci) + ((last && w >= ln) ? 1 << 30 : 0) + (h << 20); }
  const bool prod = ci < 2 * NP && w < 9 / NP;
  const int r0 = ((ci >> 1) * (9 / NP) + w) * 64;
  int voff = lane * 16, wqo = ((r0 + 32 * (j >> 3) + 8 * g) * 4096 + h * 128 + (ci & 1) * 64 + 8 * (j & 7)) * 2;
  int xo = j < 8 ? ((j & 3) * DM + r0 + 32 * (j >> 2) + 8 * g) * 2 : OOB;
  const int prow = (idx >> 3) * 40 + (w - 3) * 4;
  int pfo = (w >= 3 && w < 13 && prow < DM + DC && prow >= PSKIP) ? ((prow < DM ? prow : prow - DM) + g) * 8192 + (idx & 7) * 1024 + j * 64 : OOB;
  int so0 = __builtin_amdgcn_readlane(vtab, 0);
  asm volatile("" : "+v"(vtab), "+v"(voff), "+v"(wqo), "+v"(xo), "+v"(pfo), "+s"(so0));
  __builtin_amdgcn_sched_barrier(0);
  asm volatile("" :: "s"(a.xB), "s"(a.C), "s"(a.wqB), "s"(a.wuk), "s"(a.wuv), "s"(a.woB), "s"(T64));
  u32 T0 = (u32)T64;
  const u32 toff = (u32)(w * TA2) / (STAG * 10);
  u32* lorg = reinterpret_cast<u32*>(lds + L_ST + 2048);
  const u32 tg = (u32)(tag >> 32);
  u32 last = T0; int lag = 0;
  u32x2 rv = {0u, 0u}; u32 rt = 0;
  const __amdgpu_buffer_rsrc_t rs = __builtin_amdgcn_make_buffer_rsrc(a.ws, 0, (int)WS_END, 0x00020000);
  const __amdgpu_buffer_rsrc_t rk = __builtin_amdgcn_make_buffer_rsrc(const_cast<u16*>(a.K), 0, 1 << 27, 0x00020000);
  const __amdgpu_buffer_rsrc_t rvv = __builtin_amdgcn_make_buffer_rsrc(const_cast<u16*>(a.V), 0, 1 << 27, 0x00020000);
  const __amdgpu_buffer_rsrc_t rw = __builtin_amdgcn_make_buffer_rsrc(const_cast<u16*>(a.wq), 0, DM * 4096 * 2, 0x00020000);
  const __amdgpu_buffer_rsrc_t rx = __builtin_amdgcn_make_buffer_rsrc(const_cast<u16*>(a.xA), 0, 4 * DM * 2, 0x00020000);
  u32x4 R[RING];
#define A_ISSUE(i) R[(i) % RING] = __builtin_amdgcn_raw_buffer_load_b128((i) >= 4 * NS ? rvv : rk, voff, ((i) == 0 ? so0 : __builtin_amdgcn_readlane(vtab, (i) % NS)) + ((((i) / NS) & 3) << 25), 2)
#define GATE(n) do { if (PACE) { u32 due = T0 + toff + org + ((osl * cmin(n, NK)) >> 4) + (u32)due_at(n); lag = (int)(now - due); if (MAXGAP && (int)(due - (last + MAXGAP)) > 0) due = last + MAXGAP; \
    while ((int)(now - due) < 0) { __builtin_amdgcn_s_sleep(1); now = rt32(); } last = now; } } while (0)
#define CTRL(n) do { if constexpr (PACE) { \
    if constexpr (rnd_at((n) - RD - 2) >= 0) { constexpr int k = rnd_at((n) - RD - 2); \
      if (k < NOACT) asm volatile("" :: "v"(rv)); \
      const u32 dt = (rv[1] == (u32)tag + k) ? rv[0] - T0 : rt; \
      const float sm = row16_sum((float)(int)dt); \
      const int o = (int)((RSH ? rdlane(sm, 0) * (1.f / 16.f) : (rdlane(sm, 0) + rdlane(sm, 16)) * (1.f / 32.f))) - due_at(rtab[k]); \
      if (w == ((5 * k + 11) & 15) && lane == 0 && o > -300 && o < (k < NOACT ? -5000 : 2000)) { \
        int e = o - (int)org - (int)((osl * cmin(rtab[k], NK)) >> 4) - dtab[k]; if (e < 0) e = 0; \
        int sl_ = ((int)osl * gtab[k]) / 16 + ((e > EBIG ? 0 : e) * btab[k]) / rgap(k); sl_ = sl_ > SLMAX ? SLMAX : sl_; \
        __hip_atomic_store(reinterpret_cast<u64*>(lorg), __builtin_bit_cast(u64, u32x2{(u32)((int)org + e + ((((int)osl - sl_) * cmin(n, NK)) >> 4)), (u32)sl_}), __ATOMIC_RELAXED, __HIP_MEMORY_SCOPE_WORKGROUP); } } \
    if constexpr (rnd_at(n) >= 0) { constexpr int k = rnd_at(n); \
      u32 tot = 0; \
      if (ci == k % ACU && (h & ((1 << RSH) - 1)) == (k & ((1 << RSH) - 1))) { const u32 mine = (1u << 24) + ((rt32() - toff - T0) & 0xfffffu); \
        if (lane == 0) tot = __hip_atomic_fetch_add(lorg + 8 + k, mine, __ATOMIC_RELAXED, __HIP_MEMORY_SCOPE_WORKGROUP) + mine; } \
      if (PFULL) tot = lane < PFULL ? __builtin_amdgcn_readfirstlane(tot) : 0u; \
      u32x2 pv_ = {T0 + ((tot & 0xffffffu) >> 4), (u32)tag + k}; int po_ = (tot >> 24) == (k < NOPOST ? 99u : 16u) ? (int)WS_RPT + (h >> RSH) * 4352 : OOB; asm volatile("" : "+v"(pv_), "+v"(po_)); \
      EXM(1, __builtin_amdgcn_raw_buffer_store_b64(pv_, rs, po_, 128 * k * 35, PAUX)); } \
    if constexpr (rnd_at((n) - RD) >= 0) { constexpr int k = rnd_at((n) - RD); \
      int ro_ = (w == ((5 * k + 11) & 15) && lane < (k < NOREAD ? 0 : 16)) ? (int)WS_RPT + lane * 4352 : OOB; asm volatile("" : "+v"(ro_)); \
      EXM(w == ((5 * k + 11) & 15) ? 0xffffu : 1u, rv = __builtin_amdgcn_raw_buffer_load_b64(rs, ro_, 128 * k * 35, RAUX)); rt = rt32() - T0 - MISS; } \
  } } while (0)
  u32x4 Wr[8], xf;
  u32 pf2 = 0, pf3 = 0, pf4 = 0;
  auto touch = [&]() __attribute__((always_inline)) {
    const int k = ci < 2 ? ci : (ci - 2) * 4 + (h & 3) + 2;
    const __attribute__((address_space(4))) u64* ap = (const __attribute__((address_space(4))) u64*)__builtin_amdgcn_kernarg_segment_ptr();
    const u64 p = ap[(0xCBA96421u >> (4 * (k & 7))) & 15u];
    const int len = k < 2 ? 1 << 27 : k == 3 ? 1 << 24 : k == 4 ? DC * 8192 : k == 6 ? (int)WS_END : k == 7 ? 9216 : DM * 8192;
    const int o = (k < 2 ? (lane << 25) + (h << 20) + lane * 4352 : (lane << 21)) + (idx & 31) * 256;
    pf3 = __builtin_amdgcn_raw_buffer_load_b32(__builtin_amdgcn_make_buffer_rsrc(reinterpret_cast<void*>(p), 0, (w == TPW && k < TPN) ? len : 0, 0x00020000), lane < 8 ? o : OOB, 0, 2);
  };
  if (BPF == 1) pf2 = __builtin_amdgcn_raw_buffer_load_b32(__builtin_amdgcn_make_buffer_rsrc(const_cast<u16*>(prow < DM ? a.wqB : a.wuk), 0, DM * 4096 * 2, 0x00020000), pfo, 0, 2);
  if (!KD0) A_ISSUE(0);
  __builtin_amdgcn_sched_barrier(0);
  if (prod) {
#pragma unroll
    for (int k = 0; k < 8; ++k) Wr[k] = __builtin_amdgcn_raw_buffer_load_b128(rw, wqo, k * 8192, 2);
    xf = __builtin_amdgcn_raw_buffer_load_b128(rx, xo, 0, 2);
  }
  __builtin_amdgcn_sched_barrier(0);
#define PF2() do { const __amdgpu_buffer_rsrc_t rp_ = __builtin_amdgcn_make_buffer_rsrc(const_cast<u16*>(prow < DM ? a.wqB : a.wuk), 0, DM * 4096 * 2, 0x00020000); pf2 = __builtin_amdgcn_raw_buffer_load_b32(rp_, pfo, 0, 2); \
    if (PF2S) { __builtin_amdgcn_sched_barrier(0); pf4 = __builtin_amdgcn_raw_buffer_load_b32(rp_, pfo, 0, 0); } } while (0)
#define TWAIT(d) do { while ((int)(rt32() - T0 - (d)) < 0) __builtin_amdgcn_s_sleep(1); __builtin_amdgcn_sched_barrier(0); } while (0)
  if (BPF == 2 && !KDP) pf2 = __builtin_amdgcn_raw_buffer_load_b32(__builtin_amdgcn_make_buffer_rsrc(const_cast<u16*>(prow < DM ? a.wqB : a.wuk), 0, DM * 4096 * 2, 0x00020000), pfo, 0, 2);
  if (TPS == 0) touch();
  if (!K1G) A_ISSUE(1);
  __builtin_amdgcn_raw_buffer_store_b64(u32x2{T0, (u32)tag + 15u}, rs, (ci == 6 && (h & 1) == 0 && tid == 0) ? (int)WS_RPT + (h >> 1) * 4352 : OOB, 128 * 15 * 35, 16);
  __builtin_amdgcn_sched_barrier(0);
  if (tid < 32) lorg[tid] = tid == 0 ? ORG0 : tid == 2 ? T0 : 0u;
  __syncthreads();
  T0 = __builtin_amdgcn_readfirstlane(lorg[2]);
  if (KDP && KDP <= KD0) { TWAIT(KDP + ((w * KDS) >> 4)); PF2(); }
  if (KD0) { TWAIT(KD0 + ((w * KDS) >> 4)); A_ISSUE(0); last = rt32(); }
  if (KDP > KD0) { TWAIT(KDP + ((w * KDS) >> 4)); PF2(); }
  __builtin_amdgcn_sched_barrier(0);
  if (prod) {
    float* qpart = reinterpret_cast<float*>(lds + L_BIG);
#pragma unroll
    for (int hf = 0; hf < 2; ++hf) {
      f32x4 d4[4];
#pragma unroll
      for (int t = 0; t < 4; ++t) d4[t] = mfma16(xf, tcol16(Wr, 4 * hf + t), f32x4{0.f, 0.f, 0.f, 0.f});
      if (g == (j >> 3)) {
#pragma unroll
        for (int r = 0; r < 4; ++r) *reinterpret_cast<f32x4*>(qpart + ((w * 2 + g) * 4 + r) * 64 + 8 * (j & 7) + 4 * hf) = f32x4{d4[0][r], d4[1][r], d4[2][r], d4[3][r]};
      }
    }
    asm volatile("" ::: "memory");
    u32 arr = 0;
    if (lane == 0) arr = __hip_atomic_fetch_add(lorg + 4, 1u, __ATOMIC_RELAXED, __HIP_MEMORY_SCOPE_WORKGROUP);
    if (__builtin_amdgcn_readfirstlane(arr) == 9 / NP - 1) {
      asm volatile("" ::: "memory");
      f32x4 s0 = {0.f, 0.f, 0.f, 0.f};
#pragma unroll
      for (int k = 0; k < 18 / NP; ++k) s0 += *reinterpret_cast<const f32x4*>(qpart + k * 256 + 4 * lane);
      const int qo = (int)WS_QA + (h * 8 + ci) * 2048 + lane * 16;
      __builtin_amdgcn_raw_buffer_store_b128(u32x4{__float_as_uint(s0[0]), tg, __float_as_uint(s0[1]), tg}, rs, qo, 0, QSA);
      __builtin_amdgcn_raw_buffer_store_b128(u32x4{__float_as_uint(s0[2]), tg, __float_as_uint(s0[3]), tg}, rs, qo + 1024, 0, QSA);
    }
  }
  static_for<RING - 2 + K1G>([&](auto tc) __attribute__((always_inline)) {
    constexpr int t = tc.value + 2 - K1G;
    u32 now = rt32();
    const u32 org = __builtin_amdgcn_readfirstlane(__hip_atomic_load(lorg, __ATOMIC_RELAXED, __HIP_MEMORY_SCOPE_WORKGROUP)), osl = 0;
    GATE(t);
    A_ISSUE(t);
    if constexpr (t == TPS) touch();
    __builtin_amdgcn_sched_barrier(0);
    if constexpr (t == SP) {
      if (w == 13) {
        const u32x2 sv = __builtin_amdgcn_raw_buffer_load_b64(rs, lane < 16 ? (int)WS_RPT + lane * 4352 : OOB, 128 * 15 * 35, SAUX);
        const bool ok = sv[1] == (u32)tag + 15u;
        const float sm = row16_sum(ok ? (float)(int)(sv[0] - T0) : 0.f), cn = row16_sum(ok ? 1.f : 0.f);
        const int o = ORG0 + (cn > 0.f ? (int)(sm / cn) : 0);
        if (lane == 0 && o > -300 && o < 1000) lorg[0] = (u32)o;
      }
    }
    if constexpr (t == QP) {
      if (w >= 14) {
        const int qo = (int)WS_QA + (h * 8 + (w & 1)) * 2048 + lane * 16;
        u32x4 v[2 * NP];
#pragma unroll 1
        for (u32 spins = 0; spins < SPIN_CAP; ++spins) {
          bool ok = true;
          if (spins < QTRY) {
#pragma unroll
            for (int k = 0; k < 2 * NP; ++k) v[k] = __builtin_amdgcn_raw_buffer_load_b128(rs, qo, (k >> 1) * 4096 + (k & 1) * 1024, QAUX);
          } else {
#pragma unroll
            for (int k = 0; k < 2 * NP; ++k) v[k] = __builtin_amdgcn_raw_buffer_load_b128(rs, qo, (k >> 1) * 4096 + (k & 1) * 1024, 16);
          }
#pragma unroll
          for (int k = 0; k < 2 * NP; ++k) ok = ok && v[k][1] == tg && v[k][3] == tg;
          if (__all(ok)) break;
          __builtin_amdgcn_s_sleep(4);
        }
        f32x4 s0 = {0.f, 0.f, 0.f, 0.f};
#pragma unroll
        for (int k = 0; k < NP; ++k) s0 += f32x4{__uint_as_float(v[2 * k][0]), __uint_as_float(v[2 * k][2]), __uint_as_float(v[2 * k + 1][0]), __uint_as_float(v[2 * k + 1][2])};
        *reinterpret_cast<u32x2*>(lds + L_QS + (g * 128 + (w & 1) * 64 + 4 * j) * 2) = u32x2{pk_bf16(s0[0], s0[1]), pk_bf16(s0[2], s0[3])};
        asm volatile("" ::: "memory");
        if (lane == 0) __hip_atomic_fetch_add(lorg + 5, 1u, __ATOMIC_RELAXED, __HIP_MEMORY_SCOPE_WORKGROUP);
      }
    }
    __builtin_amdgcn_sched_barrier(0);
  });
  while (__hip_atomic_load(lorg + 5, __ATOMIC_RELAXED, __HIP_MEMORY_SCOPE_WORKGROUP) != 2u) __builtin_amdgcn_s_sleep(1);
  asm volatile("" :: "v"(pf), "v"(pf2), "v"(pf4) : "memory");
  float* wtab = reinterpret_cast<float*>(lds + L_ST + 1024);
  u32x2 xv = {0u, 0u};
  const int xoff = (w == XW && lane < 8 * ACU) ? (int)WS_MLA + (h * 8 + (lane >> 3)) * 128 + (lane & 7) * 8 : OOB;
  float* pml = reinterpret_cast<float*>(lds + L_ST);
  u32x4 q;
  float acc[8], S[4];
  u32x2 RW[4][8];
  u32x4 WT[4][4];
  float Y[4];
  auto project = [&](int b) __attribute__((always_inline)) {
    f32x4 d4[4];
#pragma unroll
    for (int t = 0; t < 4; ++t) d4[t] = f32x4{0.f, 0.f, 0.f, 0.f};
    const f32x4 wt = *reinterpret_cast<const f32x4*>(wtab + b * 16 + 4 * g);
#pragma unroll
    for (int s = 0; s < 4; ++s) {
      const u32x4 af = *reinterpret_cast<const u32x4*>(lds + L_AB + (b * 16 + j) * 272 + (32 * s + 8 * g) * 2);
#pragma unroll
      for (int t = 0; t < 4; ++t) d4[t] = mfma16(af, WT[s][t], d4[t]);
    }
    float y[4];
#pragma unroll
    for (int t = 0; t < 4; ++t) y[t] = fmaf(d4[t][0], wt[0], d4[t][1] * wt[1]) + fmaf(d4[t][2], wt[2], d4[t][3] * wt[3]);
    Y[b] = sw16(sw32(y[0], y[2]), sw32(y[1], y[3]));
  };
  const __amdgpu_buffer_rsrc_t rwo = __builtin_amdgcn_make_buffer_rsrc(const_cast<u16*>(a.wo + (size_t)h * 128 * DM), 0, (w < 9 && (NOWO == 0 || NOWO == 4 || (NOWO == 2 && ci == 0))) ? 128 * DM * 2 : 0, 0x00020000);
  int wvo = (8 * g * DM + 64 * w + 4 * j) * 2;
  asm volatile("" : "+v"(wvo));
  int wvo4 = ((32 * (j >> 3) + 8 * g) * DM + 64 * w + 8 * (j & 7)) * 2;
  asm volatile("" : "+v"(wvo4));
  const u64 wmask = w < 9 ? ~0ull : 1ull;
#define WO_ISSUE() static_for<32>([&](auto kc) __attribute__((always_inline)) { constexpr int k = kc.value; \
    if constexpr (NOWO == 3 && wo_step(k) == i) { RW[k >> 3][k & 7] = u32x2{0u, 0u}; asm volatile("" : "+v"(RW[k >> 3][k & 7])); } \
    if constexpr (NOWO == 4 && wo_step(k) == i && !(k & 1)) { u32x4 t_; EXM(wmask, t_ = __builtin_amdgcn_raw_buffer_load_b128(rwo, wvo4, (64 * (k >> 4) + ((k >> 1) & 7)) * DM * 2, 2)); RW[k >> 3][k & 7] = u32x2{t_[0], t_[1]}; RW[k >> 3][(k & 7) + 1] = u32x2{t_[2], t_[3]}; } \
    if constexpr (wo_step(k) == (NOWO >= 3 ? -i - 1 : i)) EXM(wmask, RW[k >> 3][k & 7] = __builtin_amdgcn_raw_buffer_load_b64(rwo, wvo, (32 * (k >> 3) + (k & 7)) * DM * 2, 2)); })
#define ISSUE_STEP() do { if constexpr (issued(i + 1) > issued(i)) { GATE(issued(i)); if constexpr (gf) { if (LATE == 0 || i >= D3FROM - 1 || lag < LATE) asm volatile("" :: "v"(r)); __builtin_amdgcn_sched_barrier(0); } \
    if (WOF) WO_ISSUE(); A_ISSUE(issued(i)); if (!WOF) WO_ISSUE(); if constexpr (gf) __builtin_amdgcn_sched_barrier(0); CTRL(issued(i)); \
    if constexpr (issued(i + 1) > issued(i) + 1) { GATE(issued(i) + 1); A_ISSUE(issued(i) + 1); CTRL(issued(i) + 1); } } else { WO_ISSUE(); } } while (0)
  static_for<NSEQ>([&](auto ic) __attribute__((always_inline)) {
    constexpr int i = ic.value, bb = (i / NS) & 3, pass = i / (4 * NS), sl = i % NS;
    u32 now = 0; if (PACE) now = rt32();
    u32 org = 0, osl = 0; if (PACE) { const u32x2 ov = __builtin_bit_cast(u32x2, __hip_atomic_load(reinterpret_cast<u64*>(lorg), __ATOMIC_RELAXED, __HIP_MEMORY_SCOPE_WORKGROUP)); org = __builtin_amdgcn_readfirstlane(ov[0]); osl = __builtin_amdgcn_readfirstlane(ov[1]); }
    __builtin_amdgcn_sched_barrier(0);
    const u32x4 r = R[i % RING];
    constexpr bool gf = i >= GF0 && i < GF1;
    if constexpr (gf) { ISSUE_STEP(); }
    if constexpr (i == TPC) asm volatile("" :: "v"(pf3));
    if constexpr (pass == 0) {
      if constexpr (sl == 0) { q = *reinterpret_cast<const u32x4*>(lds + L_QS + (bb * 128 + 8 * j) * 2); S[bb] = -INFINITY; }
      float d = dot2(r[0], q[0], 0.f); d = dot2(r[1], q[1], d); d = dot2(r[2], q[2], d); d = dot2(r[3], q[3], d);
      asm volatile("" : "+v"(d));
      if constexpr (!gf) { ISSUE_STEP(); }
      d = row16_sum(d);
      S[bb] = (j == sl) ? d : S[bb];
      if constexpr (sl == NS - 1) {
        const float sc = (j == bad_sl) ? -INFINITY : S[bb] * C2;
        const float m = xg_max(row16_max(sc));
        S[bb] = __builtin_amdgcn_exp2f(sc - m);
        const float l = xg_sum(row16_sum(S[bb]));
        if (lane == 0) *reinterpret_cast<f32x2*>(pml + (bb * 16 + w) * 2) = f32x2{m, l};
      }
      if constexpr (i == 4 * NS - 1) {
        asm volatile("" ::: "memory");
        u32 arr = 0;
        if (lane == 0) arr = __hip_atomic_fetch_add(lorg + 6, 1u, __ATOMIC_RELAXED, __HIP_MEMORY_SCOPE_WORKGROUP);
        arr = __builtin_amdgcn_readfirstlane(arr);
        asm volatile("" ::: "memory");
        const f32x2 ml = *reinterpret_cast<const f32x2*>(pml + lane * 2);
        const float M = row16_max(ml[0]);
        const float L = row16_sum(__builtin_amdgcn_exp2f(ml[0] - M) * ml[1]);
        __builtin_amdgcn_raw_buffer_store_b64(u32x2{__float_as_uint(j ? L : M), tg}, rs, (arr == 15u && j < 2) ? (int)WS_MLA + (h * 8 + ci) * 128 + g * 16 + j * 8 : OOB, 0, 16);
      }
    } else {
      const float p = dpp<0x150 + sl>(S[bb]);
      if constexpr (sl == 0) {
#pragma unroll
        for (int k = 0; k < 8; ++k) acc[k] = 0.f;
      }
#pragma unroll
      for (int k = 0; k < 4; ++k) { acc[2 * k] = fmaf(p, bf_lo(r[k]), acc[2 * k]); acc[2 * k + 1] = fmaf(p, bf_hi(r[k]), acc[2 * k + 1]); }
      asm volatile("" : "+v"(acc[0]), "+v"(acc[1]), "+v"(acc[2]), "+v"(acc[3]), "+v"(acc[4]), "+v"(acc[5]), "+v"(acc[6]), "+v"(acc[7]));
      if constexpr (!gf) { ISSUE_STEP(); }
      if constexpr (i == XR) xv = __builtin_amdgcn_raw_buffer_load_b64(rs, xoff, 0, 16);
      if constexpr (i == XR + 2) {
        if (w == XW) {
          if (__builtin_expect(!__all(lane >= 8 * ACU || xv[1] == tg), 0)) {
#pragma unroll 1
            for (u32 spins = 0; spins < SPIN_CAP; ++spins) {
              __builtin_amdgcn_s_sleep(8); xv = __builtin_amdgcn_raw_buffer_load_b64(rs, xoff, 0, 16);
              if (__all(lane >= 8 * ACU || xv[1] == tg)) break;
            }
          }
          const float x = __uint_as_float(xv[0]);
          const float m = lane < 8 * ACU ? dpp<0xA0>(x) : -INFINITY, l = lane < 8 * ACU ? dpp<0xF5>(x) : 0.f;
          const float M = xg_max(fmaxf(m, dpp<0x128>(m)));
          float t = __builtin_amdgcn_exp2f(m - M) * l; t = xg_sum(t + dpp<0x128>(t));
          if (lane < 8 && !(lane & 1)) *reinterpret_cast<f32x2*>(wtab + 64 + lane) = f32x2{M, 1.f / t};
          asm volatile("" ::: "memory");
          const f32x2 gm = *reinterpret_cast<const f32x2*>(wtab + 64 + 2 * g);
          wtab[lane] = __builtin_amdgcn_exp2f(pml[lane * 2] - gm[0]) * gm[1];
          asm volatile("" ::: "memory");
          if (lane == 0) __hip_atomic_store(lorg + 7, 1u, __ATOMIC_RELAXED, __HIP_MEMORY_SCOPE_WORKGROUP);
        }
      }
      if constexpr (i >= WTS && i < WTS + 4) {
        constexpr int s = i - WTS;
#pragma unroll
        for (int t = 0; t < 4; ++t) WT[s][t] = tcol8(RW[s], t);
        asm volatile("" : "+v"(WT[s][0]), "+v"(WT[s][1]), "+v"(WT[s][2]), "+v"(WT[s][3]));
      }
      if constexpr (i == PJ0 || i == PJ1 || i == PJ2) {
        constexpr int b = i == PJ0 ? 0 : i == PJ1 ? 1 : 2;
        Y[b] = 0.f;
        if (w < 9) {
          while (__hip_atomic_load(lorg + 24 + b, __ATOMIC_RELAXED, __HIP_MEMORY_SCOPE_WORKGROUP) != 16u || __hip_atomic_load(lorg + 7, __ATOMIC_RELAXED, __HIP_MEMORY_SCOPE_WORKGROUP) == 0u) __builtin_amdgcn_s_sleep(1);
          asm volatile("" ::: "memory");
          project(b);
        }
        asm volatile("" : "+v"(Y[b]));
      }
      if constexpr (sl == NS - 1) {
        const float t0 = sw16(sw32(acc[0], acc[4]), sw32(acc[2], acc[6])), t1 = sw16(sw32(acc[1], acc[5]), sw32(acc[3], acc[7]));
        *reinterpret_cast<u32*>(lds + L_AB + (bb * 16 + w) * 272 + 16 * j + 4 * g) = pk_bf16(t0, t1);
        if constexpr (bb < 3) { asm volatile("" ::: "memory"); if (lane == 0) __hip_atomic_fetch_add(lorg + 24 + bb, 1u, __ATOMIC_RELAXED, __HIP_MEMORY_SCOPE_WORKGROUP); }
      }
    }
    __builtin_amdgcn_sched_barrier(0);
  });
#undef A_ISSUE
  __syncthreads();
  if (w < 9) {
    project(3);
    st16_sc1(rs, WS_Y + ((8 * w + (j >> 1)) * 256 + idx) * 128 + ((j & 1) * 4 + g) * 16, u32x4{pk_bf16(Y[0], Y[1]), tg, pk_bf16(Y[2], Y[3]), tg});
  }
  if (idx < 72) {
    __builtin_amdgcn_sched_barrier(0);
    __builtin_amdgcn_s_barrier();
    __builtin_amdgcn_s_sleep(FDLY);
    const int yo = (int)WS_Y + (idx * 256 + 16 * w) * 128 + lane * 16;
    u32x4 v0, v1;
#pragma unroll 1
    for (u32 spins = 0; spins < SPIN_CAP; ++spins) {
      v0 = __builtin_amdgcn_raw_buffer_load_b128(rs, yo, 0, 16); v1 = __builtin_amdgcn_raw_buffer_load_b128(rs, yo + 1024, 0, 16);
      if (__all(v0[1] == tg && v0[3] == tg && v1[1] == tg && v1[3] == tg)) break;
      __builtin_amdgcn_s_sleep(FSL);
    }
    float y = sw16(sw32(bf_lo(v0[0]) + bf_lo(v1[0]), bf_lo(v0[2]) + bf_lo(v1[2])), sw32(bf_hi(v0[0]) + bf_hi(v1[0]), bf_hi(v0[2]) + bf_hi(v1[2])));
    y += dpp<0x128>(y);
    float* red = reinterpret_cast<float*>(lds + L_PF);
    if (!(lane & 8)) red[((lane & 7) * 4 + g) * 20 + w] = y;
    __syncthreads();
    if (tid < 32) {
      const f32x4 r0 = *reinterpret_cast<const f32x4*>(red + tid * 20), r1 = *reinterpret_cast<const f32x4*>(red + tid * 20 + 4);
      const f32x4 r2 = *reinterpret_cast<const f32x4*>(red + tid * 20 + 8), r3 = *reinterpret_cast<const f32x4*>(red + tid * 20 + 12);
      const f32x4 r = r0 + r1 + r2;
      const __amdgpu_buffer_rsrc_t ro = __builtin_amdgcn_make_buffer_rsrc(a.out, 0, 4 * 1152 * 2, 0x00020000);
      __builtin_amdgcn_raw_buffer_store_b16(to_bf16(((r[0] + r[1]) + (r[2] + r[3])) + (r3[0] + r3[1])), ro, ((tid & 3) * 1152 + idx * 8 + (tid >> 2)) * 2, 0, OAUX);
      __builtin_amdgcn_raw_buffer_store_b16(to_bf16(r3[2] + r3[3]), ro, ((tid >> 3) * 1152 + 576 + idx * 8 + (tid & 7)) * 2, 0, OAUX);
    }
  }
}

#define CROW 1
constexpr int CW = CROW ? 1 : 8, CR = CROW ? 8 : 1;
#define IA 2
#define IB 4
#define IC 6
#define EUV 4
#define YOFF 90
#define WPD 4
template <bool LD> DI void part_b(const Args& a, unsigned char* lds, int wg, u64 tag, u32 pf) {
  const int tid = threadIdx.x, lane = tid & 63, w = __builtin_amdgcn_readfirstlane(tid >> 6);
  const int j = lane & 15, g = lane >> 4;
  const __amdgpu_buffer_rsrc_t rs = __builtin_amdgcn_make_buffer_rsrc(a.ws, 0, (int)WS_END, 0x00020000);
  const u32 tb0 = rt32();
  const int h = BPF ? (wg & 7) * 4 + (wg >> 3) : wg;
  const int h3 = h ^ 2;
  const int b2 = BMAP ? (wg & 7) >> 1 : wg >> 3, sp = BMAP ? (wg >> 3) * 2 + (wg & 1) : wg & 7;
  const u16* cl = a.C + ((size_t)b2 * SK + sp * 512 + CW * (w - 8)) * DC + lane * 8;
  u32x4 X[16];
  u32x2 RW[4][8];
  u32x4 W8[2];
  u32* cnt = reinterpret_cast<u32*>(lds + L_ST + 4064);
#define C_ISSUE(T) _Pragma("unroll") for (int r = 0; r < 8; ++r) X[8 * ((T) & 1) + r] = ld16(cl + (size_t)(64 * (T) + CR * r) * DC)
#define C_HALF(T, hf) _Pragma("unroll") for (int r = 4 * (hf); r < 4 * (hf) + 4; ++r) X[8 * ((T) & 1) + r] = ld16(cl + (size_t)(64 * (T) + CR * r) * DC)
#define L_PART(T, r0, r1) do { __builtin_amdgcn_sched_barrier(0); \
    if ((T) + 3 < 8) { _Pragma("unroll") for (int r = r0; r < r1; ++r) X[8 * (((T) + 3) & 1) + r] = ld16(cl + (size_t)(64 * ((T) + 3) + CR * r) * DC); } \
    if ((T) == 5 || (T) == 6) { _Pragma("unroll") for (int k = 2 * (r0); k < 2 * (r1); ++k) \
      RW[2 * (((T) - 5) & 1) + (k >> 3)][k & 7] = ld8(a.woB + ((size_t)h3 * 128 + 32 * (2 * (((T) - 5) & 1) + (k >> 3)) + 8 * g + (k & 7)) * DM + 64 * (w - 8) + 4 * j); } \
    __builtin_amdgcn_sched_barrier(0); } while (0)
#define UV_LOAD(n) do { const u32x4 v_ = ld16(a.wuv + ((size_t)(64 * w + 32 * ((n) >> 3) + 8 * g + ((n) & 7)) * H + h3) * 128 + 8 * j); \
    RW[2 * ((n) >> 3)][(n) & 7] = u32x2{v_[0], v_[1]}; RW[2 * ((n) >> 3) + 1][(n) & 7] = u32x2{v_[2], v_[3]}; } while (0)
#define C_WIN(T, r0, r1) _Pragma("unroll") for (int r = r0; r < r1; ++r) { asm volatile("s_waitcnt vmcnt(%0)" :: "n"(WPD - 1) : "memory"); X[8 * ((T) & 1) + r] = ld16(cl + (size_t)(64 * (T) + CR * r) * DC); __builtin_amdgcn_sched_barrier(0); }
  u32 sb0 = L_BIG + CW * (w - 8) * CRS + lane * 16, sb1 = sb0 + CBUF;
  asm volatile("" : "+v"(sb0), "+v"(sb1));
#define C_STAGE(T) _Pragma("unroll") for (int r = 0; r < 8; ++r) *reinterpret_cast<u32x4*>(lds + (((T) & 1) ? sb1 : sb0) + CR * r * CRS) = X[8 * ((T) & 1) + r]
  {
    const u16* Wq = a.wqB + h * 128;
    u32x4 U[8];
    QPre Q;
    qproj_load(Q, a.xB, Wq, w, lane);
#pragma unroll
    for (int i = 0; i < 8; ++i) U[i] = ld16(a.wuk + ((size_t)(32 * w + 4 * i + (j & 3)) * H + h) * 128 + 8 * ((j & 12) + g));
    __builtin_amdgcn_sched_barrier(0);
    if constexpr (!LD) am_zero(lds, tid);
    qproj_mfma(Q, lds, w, lane);
    asm volatile("" :: "v"(pf));
    __syncthreads();
    if constexpr (LD) { C_HALF(0, 0); }
    else {
      const float* qpart = reinterpret_cast<const float*>(lds + L_BIG);
      float s = 0.f;
#pragma unroll
      for (int ww = 0; ww < 16; ++ww) s += qpart[ww * 512 + tid];
      reinterpret_cast<u16*>(lds + L_QS)[tid] = to_bf16(s);
    }
    __builtin_amdgcn_sched_barrier(0);
    __syncthreads();
    const u32x4 af = *reinterpret_cast<const u32x4*>(lds + L_QS + ((j >> 2) * 128 + ((j & 3) * 4 + g) * 8) * 2);
    u16* qa = reinterpret_cast<u16*>(lds + L_PF);
    float v2[2] = {0.f, 0.f};
#pragma unroll
    for (int i = 0; i < 8; ++i) {
      const f32x4 d4 = mfma16(af, U[i], f32x4{0.f, 0.f, 0.f, 0.f});
      float d = (j & 8) ? ((j & 4) ? d4[3] : d4[2]) : ((j & 4) ? d4[1] : d4[0]);
      d += dpp<0x124>(d); d += dpp<0x128>(d);
      v2[i >> 2] = ((j >> 2) == (i & 3)) ? d : v2[i >> 2];
    }
    qa[g * 512 + 32 * w + j] = to_bf16(v2[0]); qa[g * 512 + 32 * w + 16 + j] = to_bf16(v2[1]);
    __builtin_amdgcn_sched_barrier(0);
    __syncthreads();
    if constexpr (!LD) {
      if (w == 0) {
#pragma unroll
        for (int bb = 0; bb < 4; ++bb) st16_sc1(rs, WS_QABS + ((bb * H + h) * 512 + lane * 8) * 2, *reinterpret_cast<const u32x4*>(qa + bb * 512 + lane * 8));
      }
      __builtin_amdgcn_sched_barrier(0);
      __builtin_amdgcn_s_barrier();
      if (w == 0) {
        asm volatile("s_waitcnt vmcnt(0)" ::: "memory");
        if (lane == 0) { __hip_atomic_store(reinterpret_cast<u64*>(a.ws + WS_F1 + wg * 8), tag, RLX_AGENT); cnt[0] = 0u; cnt[1] = 0u; }
      }
    } else {
      __builtin_amdgcn_s_barrier();
      C_WIN(0, 4, 8); C_WIN(1, 0, 8);
      C_STAGE(0);
      __builtin_amdgcn_sched_barrier(0);
      C_WIN(2, 0, 8);
    }
  }
  {
    const int b = b2;
    if constexpr (!LD) wg_wait64(a.ws + WS_F1, tag); else __syncthreads();
    const int kt = w & 3, hh = (w >> 2) & 1;
    float* stmax = reinterpret_cast<float*>(lds + L_ST);
    float* stsum = stmax + 128;
    float* salpha = stmax + 256;
    unsigned char* Pf = lds + L_PF;
    float M = -INFINITY, lsum = 0.f;
    f32x4 acc[2][2];
#pragma unroll
    for (int ct = 0; ct < 2; ++ct) { acc[ct][0] = f32x4{0.f, 0.f, 0.f, 0.f}; acc[ct][1] = f32x4{0.f, 0.f, 0.f, 0.f}; }
    const int troff = (4 * g + (j >> 2)) * CRS + (32 * w + 4 * (j & 3)) * 2;
    auto pv_step = [&](const unsigned char* cb) __attribute__((always_inline)) {
      const f32x4 al0 = *reinterpret_cast<const f32x4*>(salpha + 4 * g), al1 = *reinterpret_cast<const f32x4*>(salpha + 16 + 4 * g);
#pragma unroll
      for (int ct = 0; ct < 2; ++ct) { acc[ct][0] *= al0; acc[ct][1] *= al1; }
#pragma unroll
      for (int blk = 0; blk < 2; ++blk) {
        const u32x4 p0 = *reinterpret_cast<const u32x4*>(Pf + ((blk * 2 + 0) * 64 + lane) * 16);
        const u32x4 p1 = *reinterpret_cast<const u32x4*>(Pf + ((blk * 2 + 1) * 64 + lane) * 16);
#pragma unroll
        for (int ct = 0; ct < 2; ++ct) {
          const u32x2 lo = trread(cb + troff + (32 * blk) * CRS + 32 * ct);
          const u32x2 hi = trread(cb + troff + (32 * blk + 16) * CRS + 32 * ct);
          const u32x4 bf = {lo[0], lo[1], hi[0], hi[1]};
          acc[ct][0] = mfma16(p0, bf, acc[ct][0]); acc[ct][1] = mfma16(p1, bf, acc[ct][1]);
        }
      }
    };
    unsigned char* Qf = lds + L_BIG + CBUF;
    if constexpr (!LD) {
      u32x4 qv[4];
#pragma unroll
      for (int k = 0; k < 4; ++k) qv[k] = __builtin_amdgcn_raw_buffer_load_b128(rs, (int)WS_QABS + (b * H * 512 + (tid + 512 * k) * 8) * 2, 0, 16);
#pragma unroll
      for (int k = 0; k < 4; ++k) { const int e = tid + 512 * k, head = e >> 6, ch = e & 63;
        *reinterpret_cast<u32x4*>(Qf + ((head >> 4) * 16 + (ch >> 2)) * 1040 + ((ch & 3) * 16 + (head & 15)) * 16) = qv[k]; }
    }
    __syncthreads();
    if constexpr (!LD) {
      u32x4 QF[16];
#pragma unroll
      for (int s = 0; s < 16; ++s) QF[s] = *reinterpret_cast<const u32x4*>(Qf + (hh * 16 + s) * 1040 + lane * 16);
      auto s_tile = [&](const unsigned char* cb, bool last) __attribute__((always_inline)) {
        __syncthreads();
        f32x4 s0 = {0.f, 0.f, 0.f, 0.f}, s1 = {0.f, 0.f, 0.f, 0.f};
#pragma unroll
        for (int s = 0; s < 16; s += 2) {
          s0 = mfma16(*reinterpret_cast<const u32x4*>(cb + (16 * kt + j) * CRS + (32 * s + 8 * g) * 2), QF[s], s0);
          s1 = mfma16(*reinterpret_cast<const u32x4*>(cb + (16 * kt + j) * CRS + (32 * s + 32 + 8 * g) * 2), QF[s + 1], s1);
        }
        const f32x4 sv = (s0 + s1) * C2;
        if (last) { __builtin_amdgcn_sched_barrier(0); _Pragma("unroll") for (int n = 0; n < EUV; ++n) UV_LOAD(n); __builtin_amdgcn_sched_barrier(0); }
        const float v = xg_max(fmaxf(fmaxf(sv[0], sv[1]), fmaxf(sv[2], sv[3])));
        if (g == 0) stmax[kt * 32 + 16 * hh + j] = v;
        __syncthreads();
        const float* sm = stmax + 16 * hh + j;
        const float Mn = fmaxf(fmaxf(M, fmaxf(sm[0], sm[32])), fmaxf(sm[64], sm[96]));
        const float alpha = __builtin_amdgcn_exp2f(M - Mn);
        M = Mn;
        u32x2 pk;
        pk[0] = pk_bf16(__builtin_amdgcn_exp2f(sv[0] - Mn), __builtin_amdgcn_exp2f(sv[1] - Mn));
        pk[1] = pk_bf16(__builtin_amdgcn_exp2f(sv[2] - Mn), __builtin_amdgcn_exp2f(sv[3] - Mn));
        lsum = lsum * alpha + ((bf_lo(pk[0]) + bf_hi(pk[0])) + (bf_lo(pk[1]) + bf_hi(pk[1])));
        *reinterpret_cast<u32x2*>(Pf + (((kt >> 1) * 2 + hh) * 64 + lane) * 16 + (kt & 1) * 8) = pk;
        if (kt == 0 && g == 0) salpha[16 * hh + j] = alpha;
        __syncthreads();
        pv_step(cb);
      };
#pragma unroll 1
      for (int T = 0; T < 6; T += 2) { s_tile(lds + L_BIG, false); s_tile(lds + L_BIG + CBUF, false); }
      s_tile(lds + L_BIG, false); s_tile(lds + L_BIG + CBUF, true);
    } else {
#pragma unroll
      for (int T = 0; T < 8; ++T) {
        __syncthreads();
        if (T + 1 < 8) { C_STAGE(T + 1); }
        L_PART(T, 0, IA);
        if (T == 7) {
#pragma unroll
          for (int k = 0; k < 2; ++k) W8[k] = ld16(a.woB + ((size_t)h3 * 128 + 16 * (w - 8) + 8 * k + (lane >> 3)) * DM + 512 + (lane & 7) * 8);
        }
        __syncthreads();
        L_PART(T, IA, IB);
        __syncthreads();
        L_PART(T, IB, IC);
        pv_step(lds + L_BIG + (T & 1) * CBUF);
        L_PART(T, IC, 8);
      }
    }
#undef C_ISSUE
#undef C_STAGE
    __syncthreads();
    if constexpr (!LD) { lsum = xg_sum(lsum); if (g == 0) stsum[kt * 32 + 16 * hh + j] = lsum; if (kt == 0 && g == 0) salpha[32 + 16 * hh + j] = M; }
    float* tb = reinterpret_cast<float*>(lds + L_BIG);
#pragma unroll
    for (int ct = 0; ct < 2; ++ct)
#pragma unroll
      for (int h2 = 0; h2 < 2; ++h2)
#pragma unroll
        for (int r = 0; r < 4; ++r) tb[(16 * h2 + 4 * g + r) * 516 + 32 * w + 16 * ct + j] = acc[ct][h2][r];
    if constexpr (!LD) { for (int i = tid; i < 8 * AM2_LD * 2 / 16; i += 512) reinterpret_cast<u32x4*>(lds + L_AM2 + 8 * AM2_LD * 2)[i] = u32x4{0u, 0u, 0u, 0u}; }
    __syncthreads();
    if constexpr (LD) {
      const int t2 = tid - 512;
#pragma unroll
      for (int k = 0; k < 4; ++k) {
        const int e = t2 + 512 * k, head = e >> 6, c8 = e & 63;
        const f32x4 v0 = *reinterpret_cast<const f32x4*>(tb + head * 516 + 8 * c8), v1 = *reinterpret_cast<const f32x4*>(tb + head * 516 + 8 * c8 + 4);
        __builtin_amdgcn_raw_buffer_store_b128(u32x4{pk_bf16(v0[0], v0[1]), pk_bf16(v0[2], v0[3]), pk_bf16(v1[0], v1[1]), pk_bf16(v1[2], v1[3])}, rs, (int)WS_CTXP + (((b * NSP + sp) * 32 + head) * 512 + 8 * c8) * 2, 0, 16);
      }
      if (t2 < 32) {
        const float L = (stsum[t2] + stsum[32 + t2]) + (stsum[64 + t2] + stsum[96 + t2]);
        st8_sc1(a.ws + WS_MLB + ((size_t)(b * NSP + sp) * 32 + t2) * 8, f32x2{salpha[32 + t2], L});
      }
      __builtin_amdgcn_sched_barrier(0);
      __builtin_amdgcn_s_barrier();
      asm volatile("s_waitcnt vmcnt(0)" ::: "memory");
      if (lane == 0) {
        if (__hip_atomic_fetch_add(cnt, 1u, __ATOMIC_RELAXED, __HIP_MEMORY_SCOPE_WORKGROUP) == 7u) {
          __hip_atomic_store(reinterpret_cast<u64*>(a.ws + WS_F2 + wg * 8), tag, RLX_AGENT);
        }
      }
#pragma unroll
      for (int k = 0; k < 2; ++k) *reinterpret_cast<u32x4*>(lds + L_W8 + (16 * (w - 8) + 8 * k + (lane >> 3)) * 144 + (lane & 7) * 16) = W8[k];
    } else {
      __builtin_amdgcn_s_barrier();
      static_for<16 - EUV>([&](auto ic) __attribute__((always_inline)) {
        asm volatile("s_waitcnt vmcnt(%0)" :: "n"(WPD - 1) : "memory");
        UV_LOAD(EUV + ic.value);
        __builtin_amdgcn_sched_barrier(0);
      });
    }
  }
  {
    int tid = threadIdx.x; asm volatile("" : "+v"(tid));
    const int lane = tid & 63, j = lane & 15, g = lane >> 4;
    u16* Am2 = reinterpret_cast<u16*>(lds + L_AM2);
    float* cpart = reinterpret_cast<float*>(lds + L_CP);
    __builtin_amdgcn_sched_barrier(0);
    if constexpr (LD) {
      if (w == 8) {
        wave_wait(reinterpret_cast<const u64*>(a.ws + WS_F2) + (lane & (NBW - 1)), true, tag);
        asm volatile("s_waitcnt vmcnt(0)" ::: "memory");
        if (lane == 0) __hip_atomic_store(cnt + 1, 1u, __ATOMIC_RELEASE, __HIP_MEMORY_SCOPE_WORKGROUP);
      } else {
        for (u32 spins = 0; spins < 64u * SPIN_CAP; ++spins) { if (__hip_atomic_load(cnt + 1, __ATOMIC_ACQUIRE, __HIP_MEMORY_SCOPE_WORKGROUP) != 0u) break; __builtin_amdgcn_s_sleep(1); }
      }
      __builtin_amdgcn_sched_barrier(0);
      const int b = (tid >> 7) & 3, c4 = tid & 127;
      u32x2 pv[NSP];
      const f32x2 ml = __builtin_bit_cast(f32x2, __builtin_amdgcn_raw_buffer_load_b64(rs, (int)WS_MLB + ((b * NSP + (j & 7)) * 32 + h3) * 8, 0, 16));
#pragma unroll
      for (int s = 0; s < NSP; ++s) pv[s] = __builtin_amdgcn_raw_buffer_load_b64(rs, (int)WS_CTXP + (((b * NSP + s) * 32 + h3) * 512 + 4 * c4) * 2, 0, 16);
      const float M = row16_max(ml[0]);
      float wt = __builtin_amdgcn_exp2f(ml[0] - M);
      wt *= 2.f / row16_sum(wt * ml[1]);
      float c0 = 0.f, c1 = 0.f, c2 = 0.f, c3 = 0.f;
#pragma unroll
      for (int s = 0; s < NSP; ++s) { const float ws_ = rdlane(wt, s); c0 = fmaf(ws_, bf_lo(pv[s][0]), c0); c1 = fmaf(ws_, bf_hi(pv[s][0]), c1); c2 = fmaf(ws_, bf_lo(pv[s][1]), c2); c3 = fmaf(ws_, bf_hi(pv[s][1]), c3); }
      const u32 h0 = pk_bf16(c0, c1), h1 = pk_bf16(c2, c3);
      *reinterpret_cast<u32x2*>(Am2 + (2 * b) * AM2_LD + 4 * c4) = u32x2{h0, h1};
      *reinterpret_cast<u32x2*>(Am2 + (2 * b + 1) * AM2_LD + 4 * c4) = u32x2{pk_bf16(c0 - bf_lo(h0), c1 - bf_hi(h0)), pk_bf16(c2 - bf_lo(h1), c3 - bf_hi(h1))};
      __syncthreads();
      __syncthreads();
      const int t2 = tid - 512;
      float s = 0.f;
#pragma unroll
      for (int ww = 0; ww < 8; ++ww) s += cpart[ww * 512 + t2];
      u16* Am = reinterpret_cast<u16*>(lds + L_AM);
      const int bb = t2 >> 7, d = t2 & 127;
      const u16 hi = to_bf16(s);
      Am[(4 * bb) * AM_LD + d] = hi; Am[(4 * bb + 1) * AM_LD + d] = to_bf16(s - bf2f(hi));
    } else {
      __syncthreads();
      f32x4 acc[8];
#pragma unroll
      for (int t = 0; t < 8; ++t) acc[t] = f32x4{0.f, 0.f, 0.f, 0.f};
#pragma unroll
      for (int k2 = 0; k2 < 2; ++k2) {
        const u32x4 af = *reinterpret_cast<const u32x4*>(Am2 + j * AM2_LD + 64 * w + 32 * k2 + 8 * g);
#pragma unroll
        for (int t = 0; t < 8; ++t) acc[t] = mfma16(af, tcol8(RW[2 * k2 + (t >> 2)], t & 3), acc[t]);
      }
      if (g < 2) {
#pragma unroll
        for (int bb = 0; bb < 2; ++bb) {
          float* p = cpart + (w * 4 + 2 * g + bb) * 128 + 8 * j;
          *reinterpret_cast<f32x4*>(p) = f32x4{acc[0][2 * bb] + acc[0][2 * bb + 1], acc[1][2 * bb] + acc[1][2 * bb + 1], acc[2][2 * bb] + acc[2][2 * bb + 1], acc[3][2 * bb] + acc[3][2 * bb + 1]};
          *reinterpret_cast<f32x4*>(p + 4) = f32x4{acc[4][2 * bb] + acc[4][2 * bb + 1], acc[5][2 * bb] + acc[5][2 * bb + 1], acc[6][2 * bb] + acc[6][2 * bb + 1], acc[7][2 * bb] + acc[7][2 * bb + 1]};
        }
      }
      __builtin_amdgcn_sched_barrier(0);
      __syncthreads();
      if (YDEF == 1 && w == 1) {
        u32 dl = tb0 + 5200u;
#pragma unroll 1
        while ((int)(rt32() - dl) < 0) {
          const u32x2 rv = __builtin_amdgcn_raw_buffer_load_b64(rs, lane < 16 ? (int)WS_RPT + lane * 4352 : OOB, 128 * (NRND - 1) * 35, 16);
          const bool ok = rv[1] == (u32)tag + (NRND - 1);
          const float cn = rdlane(row16_sum(ok ? 1.f : 0.f), 0);
          if (cn >= 8.f) { dl = tb0 + (u32)(int)(rdlane(row16_sum(ok ? (float)(int)(rv[0] - tb0) : 0.f), 0) / cn) + (u32)(due_at(NSEQ - 1) - due_at(rtab[NRND - 1]) + YOFF); break; }
          __builtin_amdgcn_s_sleep(32);
        }
        if (lane == 0) cnt[2] = (int)(dl - tb0) > 5200 ? tb0 : dl;
      }
      if (w == 0) {
#pragma unroll
        for (int s = 0; s < 4; ++s)
#pragma unroll
          for (int jj = 0; jj < 8; ++jj) RW[s][jj] = *reinterpret_cast<const u32x2*>(lds + L_W8 + (32 * s + 8 * g + jj) * 144 + 8 * j);
      }
    }
    __syncthreads();
    if (LD || w == 0) {
      f32x4 ya[4];
      wo_mfma(RW, lds, lane, ya);
      if (YDEF) { const u32 dl = YDEF == 1 ? __builtin_amdgcn_readfirstlane(cnt[2]) : tb0 + YDEF; while ((int)(rt32() - dl) < 0) __builtin_amdgcn_s_sleep(4); }
      st16_sc1(rs, WS_Y + ((8 * (LD ? w - 8 : 8) + (j >> 1)) * 256 + NAW + h3) * 128 + g * 32 + (j & 1) * 16, yrow(ya, (u32)(tag >> 32)));
    }
  }
}

__global__ void __launch_bounds__(1024, 4) k_main(Args a) {
  __shared__ __attribute__((aligned(16))) unsigned char lds[L_TOTAL];
  u32 pf;
  { u64 pc; u32 len;
    asm volatile("s_getpc_b64 %0\n.Lpf%=:\n\ts_mov_b32 %1, k_tail_mark-.Lpf%=" : "=s"(pc), "=s"(len));
    pf = __builtin_amdgcn_raw_buffer_load_b32(__builtin_amdgcn_make_buffer_rsrc(reinterpret_cast<void*>(pc), 0, len < 131072u ? len : 0u, 0x00020000), CPFM ? (threadIdx.x < 16 ? ((blockIdx.x >> 3) * 16 + threadIdx.x) * 128 : 0x7ffff000) : threadIdx.x * 128, 0, 0);
    __builtin_amdgcn_sched_barrier(0); }
  u64 tag = (hw_dispatch_id() + 1ull) * 0x9E3779B97F4A7C15ull;
  tag ^= ((u64)__builtin_amdgcn_dispatch_ptr() >> 6) * 0xC2B2AE3D27D4EB4Full;
  tag |= 1ull << 63;
  if (__builtin_expect(blockIdx.x >= NBW, 1)) part_a(a, lds, blockIdx.x - NBW, tag, pf);
  else if (NOB) { const int w = threadIdx.x >> 6, lane = threadIdx.x & 63, j = lane & 15, g = lane >> 4; const u32 tg = (u32)(tag >> 32);
    if (w < 9) st16_sc1(__builtin_amdgcn_make_buffer_rsrc(a.ws, 0, (int)WS_END, 0x00020000), WS_Y + ((8 * w + (j >> 1)) * 256 + NAW + blockIdx.x) * 128 + g * 32 + (j & 1) * 16, u32x4{0u, tg, 0u, tg}); }
  else if (__builtin_amdgcn_readfirstlane(threadIdx.x >> 6) < 8) part_b<false>(a, lds, blockIdx.x, tag, pf);
  else part_b<true>(a, lds, blockIdx.x, tag, pf);
}

extern "C" __global__ void k_tail_mark() {}

extern "C" void kernel_launch(void* const* d_in, const int* in_sizes, int n_in, void* d_out, int out_size, void* d_ws, size_t ws_size,
                              hipStream_t stream) {
  Args a;
  a.xA = (const u16*)d_in[0]; a.K = (const u16*)d_in[1]; a.V = (const u16*)d_in[2]; a.wq = (const u16*)d_in[3]; a.wo = (const u16*)d_in[4];
  a.xB = (const u16*)d_in[5]; a.C = (const u16*)d_in[6]; a.wqB = (const u16*)d_in[7]; a.wuk = (const u16*)d_in[8]; a.wuv = (const u16*)d_in[9]; a.woB = (const u16*)d_in[10];
#define WSTOP 32
  if (WSTOP && ws_size >= ((size_t)WSTOP << 20)) d_ws = (unsigned char*)d_ws + ((ws_size - ((size_t)WSTOP << 20)) & ~(size_t)4095);
  a.ws = (unsigned char*)d_ws;
  a.out = (u16*)d_out;
  k_main<<<NBW + NAW, 1024, 0, stream>>>(a);
}
```

```cpp
#include <hip/hip_runtime.h>
#include <stdint.h>
#include <utility>

typedef unsigned short u16;
typedef unsigned u32;
typedef unsigned long long u64;
typedef u32 u32x4 __attribute__((ext_vector_type(4)));
typedef u32 u32x2 __attribute__((ext_vector_type(2)));
typedef float f32x4 __attribute__((ext_vector_type(4)));
typedef float f32x2 __attribute__((ext_vector_type(2)));
typedef short s16x4 __attribute__((ext_vector_type(4)));
typedef __bf16 bf16x8 __attribute__((ext_vector_type(8)));
typedef __bf16 bf16x2 __attribute__((ext_vector_type(2)));
#define DI __device__ __forceinline__
#define RLX_AGENT __ATOMIC_RELAXED, __HIP_MEMORY_SCOPE_AGENT
extern "C" __device__ u64 hw_dispatch_id() __asm("llvm.amdgcn.dispatch.id");

template <int... Is, class F> DI void static_for_impl(std::integer_sequence<int, Is...>, F&& f) { (f(std::integral_constant<int, Is>{}), ...); }
template <int N, class F> DI void static_for(F&& f) { static_for_impl(std::make_integer_sequence<int, N>{}, f); }

constexpr int H = 32, SK = 4096, DM = 576, DC = 512;
constexpr int ACU = 7;
constexpr int NBW = 32;
constexpr int NAW = H * ACU;
constexpr int NSP = 8;
constexpr float C2 = 0.08838834764831845f * 1.4426950408889634f;

#define FST 4352
constexpr size_t WS_QABS = 4096;
constexpr size_t WS_CTXP = WS_QABS + 4 * 32 * 512 * 2;
constexpr size_t WS_MLB = WS_CTXP + (size_t)4 * NSP * 32 * 512 * 2;
constexpr size_t WS_Y = WS_MLB + 4 * NSP * 32 * 2 * 4;
constexpr size_t WS_MLA = WS_Y + (size_t)72 * 256 * 128;
constexpr size_t WS_RPT = WS_MLA + 256 * 128;
constexpr size_t WS_QA = WS_RPT + 64 * 4480;
constexpr size_t WS_F1 = WS_QA + 256 * 2048, WS_F2 = WS_F1 + 512;
constexpr size_t WS_END = WS_F1 + 32 * 4352 + 1024;

constexpr int CRS = 1056;
constexpr int CBUF = 64 * CRS;
constexpr int L_BIG = 0;
constexpr int L_AM = 2 * CBUF;
constexpr int L_QS = L_AM + 16 * 272;
constexpr int L_ST = L_QS + 1024;
constexpr int L_PF = L_ST + 4096;
constexpr int L_TOTAL = L_PF + 4096;
constexpr int AM_LD = 136;
constexpr int AM2_LD = 520;
constexpr int L_AM2 = 69632, L_CP = 88064, L_W8 = 106496;

struct Args {
  const u16 *xA, *K, *V, *wq, *wo, *xB, *C, *wqB, *wuk, *wuv, *woB;
  unsigned char* ws;
  u16* out;
};

DI float bf_lo(u32 x) { return __uint_as_float(x << 16); }
DI float bf_hi(u32 x) { return __uint_as_float(x & 0xffff0000u); }
DI u32 pk_bf16(float lo, float hi) { bf16x2 v = {(__bf16)lo, (__bf16)hi}; return __builtin_bit_cast(u32, v); }
DI u16 to_bf16(float x) { return __builtin_bit_cast(u16, (__bf16)x); }
DI float bf2f(u16 x) { return __uint_as_float(((u32)x) << 16); }
DI float dot2(u32 a, u32 b, float c) { return __builtin_amdgcn_fdot2_f32_bf16(__builtin_bit_cast(bf16x2, a), __builtin_bit_cast(bf16x2, b), c, false); }
template <int CTRL> DI float dpp(float x) { return __builtin_bit_cast(float, __builtin_amdgcn_update_dpp(0, __builtin_bit_cast(int, x), CTRL, 0xf, 0xf, true)); }
DI float row16_sum(float x) { x += dpp<0x128>(x); x += dpp<0x124>(x); x += dpp<0x122>(x); x += dpp<0x121>(x); return x; }
DI float row16_max(float x) { x = fmaxf(x, dpp<0x128>(x)); x = fmaxf(x, dpp<0x124>(x)); x = fmaxf(x, dpp<0x122>(x)); x = fmaxf(x, dpp<0x121>(x)); return x; }
DI float xg_max(float x) {
  auto s = __builtin_amdgcn_permlane16_swap(__float_as_uint(x), __float_as_uint(x), false, false);
  x = fmaxf(__uint_as_float(s[0]), __uint_as_float(s[1]));
  auto t = __builtin_amdgcn_permlane32_swap(__float_as_uint(x), __float_as_uint(x), false, false);
  return fmaxf(__uint_as_float(t[0]), __uint_as_float(t[1]));
}
DI float xg_sum(float x) {
  auto s = __builtin_amdgcn_permlane16_swap(__float_as_uint(x), __float_as_uint(x), false, false);
  x = __uint_as_float(s[0]) + __uint_as_float(s[1]);
  auto t = __builtin_amdgcn_permlane32_swap(__float_as_uint(x), __float_as_uint(x), false, false);
  return __uint_as_float(t[0]) + __uint_as_float(t[1]);
}
DI float sw32(float a, float b) {
  auto s = __builtin_amdgcn_permlane32_swap(__float_as_uint(a), __float_as_uint(b), false, false);
  return __uint_as_float(s[0]) + __uint_as_float(s[1]);
}
DI float sw16(float a, float b) {
  auto s = __builtin_amdgcn_permlane16_swap(__float_as_uint(a), __float_as_uint(b), false, false);
  return __uint_as_float(s[0]) + __uint_as_float(s[1]);
}
DI f32x4 mfma16(u32x4 a, u32x4 b, f32x4 c) { return __builtin_amdgcn_mfma_f32_16x16x32_bf16(__builtin_bit_cast(bf16x8, a), __builtin_bit_cast(bf16x8, b), c, 0, 0, 0); }
DI u32x4 ld16(const u16* p) { return __builtin_nontemporal_load(reinterpret_cast<const u32x4*>(p)); }
DI u32x2 ld8(const u16* p) { return __builtin_nontemporal_load(reinterpret_cast<const u32x2*>(p)); }
DI u32x2 trread(const unsigned char* p) {
  return __builtin_bit_cast(u32x2, __builtin_amdgcn_ds_read_tr16_b64_v4i16((__attribute__((address_space(3))) s16x4*)(p)));
}
DI u32 prm(u32 hi, u32 lo, int odd) { return __builtin_amdgcn_perm(hi, lo, odd ? 0x07060302u : 0x05040100u); }

DI u32x4 tcol16(const u32x4 (&R)[8], int t) {
  u32x4 T;
#pragma unroll
  for (int q = 0; q < 4; ++q) T[q] = prm(R[2 * q + 1][t >> 1], R[2 * q][t >> 1], t & 1);
  return T;
}
DI u32x4 tcol8(const u32x2 (&R)[8], int t) {
  u32x4 T;
#pragma unroll
  for (int q = 0; q < 4; ++q) T[q] = prm(R[2 * q + 1][t >> 1], R[2 * q][t >> 1], t & 1);
  return T;
}

#define SPINLOG 22
#define PSL 4
constexpr u32 SPIN_CAP = 1u << SPINLOG;
DI void wave_wait(const u64* f, bool active, u64 tag) {
#pragma unroll 1
  for (u32 spins = 0; spins < SPIN_CAP; ++spins) {
    if (active) active = __hip_atomic_load(f, RLX_AGENT) != tag;
    if (!__any(active)) break;
    __builtin_amdgcn_s_sleep(PSL);
  }
}
DI void wg_wait64(const unsigned char* flags, u64 tag, int pw = 0) {
  if ((int)(threadIdx.x >> 6) == pw) {
    wave_wait(reinterpret_cast<const u64*>(flags) + (threadIdx.x & (NBW - 1)) * (FST / 8), (threadIdx.x & 63) < NBW, tag);
    asm volatile("s_waitcnt vmcnt(0)" ::: "memory");
  }
  __syncthreads();
}
DI void st16_sc1(__amdgpu_buffer_rsrc_t rs, size_t off, u32x4 v) { __builtin_amdgcn_raw_buffer_store_b128(v, rs, (int)off, 0, 16); }
DI void st8_sc1(unsigned char* p, f32x2 v) { __hip_atomic_store(reinterpret_cast<u64*>(p), __builtin_bit_cast(u64, v), RLX_AGENT); }

struct QPre { u32x4 R0[8]; u32x4 R1; u32x2 xa, xb; u32 x1; };
DI void qproj_load(QPre& Q, const u16* x, const u16* Wh, int w, int lane) {
  const int j = lane & 15, g = lane >> 4;
  Q.xa = u32x2{0u, 0u}; Q.xb = u32x2{0u, 0u}; Q.x1 = 0u;
  if (j < 4) { Q.xa = ld8(x + j * DM + 36 * w + 8 * g); Q.xb = ld8(x + j * DM + 36 * w + 8 * g + 4); Q.x1 = x[j * DM + 36 * w + 32 + g]; }
#pragma unroll
  for (int jj = 0; jj < 8; ++jj) Q.R0[jj] = ld16(Wh + (size_t)(36 * w + 8 * g + jj) * 4096 + 8 * j);
  Q.R1 = ld16(Wh + (size_t)(36 * w + 32 + g) * 4096 + 8 * j);
}
DI void qproj_mfma(const QPre& Q, unsigned char* lds, int w, int lane) {
  const int j = lane & 15, g = lane >> 4;
  float* qpart = reinterpret_cast<float*>(lds + L_BIG);
  const u32x4 a = {Q.xa[0], Q.xa[1], Q.xb[0], Q.xb[1]}, a1 = {Q.x1, 0u, 0u, 0u};
#pragma unroll
  for (int hf = 0; hf < 2; ++hf) {
    f32x4 acc[4];
#pragma unroll
    for (int t = 0; t < 4; ++t) acc[t] = mfma16(a, tcol16(Q.R0, 4 * hf + t), f32x4{0.f, 0.f, 0.f, 0.f});
#pragma unroll
    for (int t = 0; t < 4; ++t) { const u32x4 bb = {(t & 1) ? (Q.R1[2 * hf + (t >> 1)] >> 16) : (Q.R1[2 * hf + (t >> 1)] & 0xffffu), 0u, 0u, 0u}; acc[t] = mfma16(a1, bb, acc[t]); }
    if (g == 0) {
#pragma unroll
      for (int r = 0; r < 4; ++r) *reinterpret_cast<f32x4*>(qpart + (w * 4 + r) * 128 + 8 * j + 4 * hf) = f32x4{acc[0][r], acc[1][r], acc[2][r], acc[3][r]};
    }
    __builtin_amdgcn_sched_barrier(0);
  }
}
DI void wo_load(const u16* Wo, u32x2 (&R)[4][8], int nb, int lane, int s0, int s1) {
  const int j = lane & 15, g = lane >> 4;
#pragma unroll
  for (int s = 0; s < 4; ++s) if (s >= s0 && s < s1)
#pragma unroll
    for (int jj = 0; jj < 8; ++jj) R[s][jj] = ld8(Wo + (size_t)(32 * s + 8 * g + jj) * DM + 64 * nb + 4 * j);
}
DI void wo_mfma(const u32x2 (&R)[4][8], const unsigned char* lds, int lane, f32x4 (&acc)[4]) {
  const int j = lane & 15, g = lane >> 4;
#pragma unroll
  for (int t = 0; t < 4; ++t) acc[t] = f32x4{0.f, 0.f, 0.f, 0.f};
#pragma unroll
  for (int s = 0; s < 4; ++s) {
    const u32x4 a = *reinterpret_cast<const u32x4*>(lds + L_AM + j * 272 + (32 * s + 8 * g) * 2);
#pragma unroll
    for (int t = 0; t < 4; ++t) acc[t] = mfma16(a, tcol8(R[s], t), acc[t]);
  }
}
DI u32x4 yrow(const f32x4 (&acc)[4], u32 tg) {
  return u32x4{pk_bf16(acc[0][0] + acc[0][1], acc[1][0] + acc[1][1]), tg, pk_bf16(acc[2][0] + acc[2][1], acc[3][0] + acc[3][1]), tg};
}
DI void am_zero(unsigned char* lds, int tid) {
  if (tid < 136) { const int pr = tid / 34; *reinterpret_cast<u32x4*>(lds + L_AM + (4 * pr + 2) * 272 + (tid - 34 * pr) * 16) = u32x4{0u, 0u, 0u, 0u}; }
}
DI float rdlane(float x, int l) { return __builtin_bit_cast(float, __builtin_amdgcn_readlane(__builtin_bit_cast(int, x), l)); }

constexpr int NS = 10;
constexpr int NSEQ = 8 * NS;
#define RNG 9
#define NP 3
#define QP 4
#define SP 4
#define BPF 2
#define TPS 3
#define KD0 60
#define KDP 60
#define KDS 60
#define NOWO 0
#define NOB 0
#define GF0 12
#define GF1 99
#define WOF 1
#define RAUX 16
#define SAUX 16
#define NOACT 0
#define NOPOST 0
#define NOREAD 0
#define PFULL 0
#define PAUX 16
#define RSPR 0
#define QTRY 0
#define QAUX 3
#define QSA 16
#define LATE 0
#define EXQ 1
#define PF2S 0
#define TPN 8
#define TPC 12
#define TPW 10
#define K1G 1
#define PSKIP 9999
#define D3FROM 66
#define D4FROM 99
#define ROTM 1
#define OAUX 0
#define DEP 2
constexpr int DEPTH = DEP, RING = RNG, RAMP = 2;
constexpr int issued(int i) {
  int d = RING - (i + RAMP - 1) / RAMP; if (d < DEPTH) d = DEPTH;
  if (i >= D3FROM && d < 3) d = 3;
  if (i >= D4FROM && d < 4) d = 4;
  int e = i + d; if (e < RING) e = RING; return e > NSEQ ? NSEQ : e;
}
#define PACE 1
#define YDEF 1
#define WO0 16
#define WON 36
#define TA1 535
#define TA2 440
#define NK 68
#define ORG0 130
#define SLMAX 80
#define MAXGAP 70
constexpr int cmin(int a, int b) { return a < b ? a : b; }
#define RSH 1
#define RD 4
#define BMAP 1
#define EBIG 9999
#define NJ 54
#define TA3 515
constexpr int due_at(int n) { return (n <= NJ ? n * TA1 : n <= NK ? NJ * TA1 + (n - NJ) * TA3 : NJ * TA1 + (NK - NJ) * TA3 + (n - NK) * TA2) / 10; }
#define RTAB 20, 30, 40, 50, 60, 70
#define GTAB 0, 12, 6, 12, 12, 12
#define BTAB 8, 12, 6, 12, 12, 0
#define MISS 40
#define DB 10
#define DTAB 20, 20, 20, 15, 5, 3, 3, 3
constexpr int rtab[] = {RTAB}, gtab[] = {GTAB}, btab[] = {BTAB}, dtab[] = {DTAB};
constexpr int NRND = sizeof(rtab) / sizeof(int);
constexpr int rgap(int k) { return k ? rtab[k] - rtab[k - 1] : rtab[0] - RING; }
constexpr int rnd_at(int n) { for (int k = 0; k < NRND; ++k) if (rtab[k] == n) return k; return -1; }
static_assert(rtab[0] >= RING + 2 && rtab[NRND - 1] + RD + 2 < NSEQ, "");
constexpr int wo_step(int k) { return WO0 + k * WON / 32; }
#define WTS 54
#define PJ0 58
#define PJ1 63
#define PJ2 72
#define XW 13
#define XR 50
#define FDLY 8
#define FSL 2
#define CPFM 1
#define STAG 16
#define EXM(m, ...) do { if (EXQ) { __builtin_amdgcn_sched_barrier(0); asm volatile("s_mov_b64 exec, %0" :: "s"((u64)(m))); __builtin_amdgcn_sched_barrier(0); } __VA_ARGS__; \
    if (EXQ) { __builtin_amdgcn_sched_barrier(0); asm volatile("s_mov_b64 exec, -1"); __builtin_amdgcn_sched_barrier(0); } } while (0)
DI u32 rt32() { return (u32)__builtin_amdgcn_s_memrealtime(); }
constexpr int OOB = 0x7ffff000;
constexpr int L_AB = 32768;
#define PERM 0
DI int uoff(int c, int u, int x) {
  if (PERM == 0) return c * 16384 + u * 1024;
  if (PERM == 1) return ((u >> 2) << 10) | ((u & 3) << 15) | ((c & 7) << 12) | ((c >> 3) << 17);
  if (PERM == 2) return ((c & 3) << 10) | ((u & 8) << 9) | ((u & 4) << 11) | ((c & 4) << 12) | ((u & 3) << 15) | ((c >> 3) << 17);
  if (PERM == 3) return ((((u >> 2) ^ x) & 3) << 10) | ((u & 3) << 15) | ((c & 7) << 12) | ((c >> 3) << 17);
  if (PERM == 4) return ((c & 3) << 10) | ((u & 3) << 12) | ((c & 4) << 12) | ((u >> 2) << 15) | ((c >> 3) << 17);
  if (PERM == 12) return c * 16384 + ((u ^ ((c >> 1) & 3)) * 1024);
  if (PERM == 13) return c * 16384 + ((u ^ ((c >> 1) & 3) ^ ((c >> 3) & 3)) * 1024);
  if (PERM == 5) return (((u ^ x) & 3) << 10) | ((u >> 2) << 15) | ((c & 7) << 12) | ((c >> 3) << 17);
  return 0;
}
DI void part_a(const Args& a, unsigned char* lds, int idx, u64 tag, u32 pf) {
  const u64 T64 = __builtin_amdgcn_s_memrealtime();
  __builtin_amdgcn_sched_barrier(0);
  const int tid = threadIdx.x, lane = tid & 63, w = __builtin_amdgcn_readfirstlane(tid >> 6);
  const int j = lane & 15, g = lane >> 4;
  const int h = (idx & 7) * 4 + ((idx >> 3) & 3), ci = idx >> 5;
  const int rr = (((h & 3) * 5) >> 1) + (h >> 2), rot = ROTM ? (rr >= NS ? rr - NS : rr) : (h * 5) >> 4, l0 = (ci * 37) >> 4, ln = ((ci * 37 + 37) >> 4) - l0;
  const int bad_sl = (w < ln) ? -1 : NS - 1 - rot;
  int vtab;
  { const int p = lane + rot >= NS ? lane + rot - NS : lane + rot;
    const bool last = p == NS - 1;
    vtab = uoff(last ? 63 : p * 7 + ci, last ? (l0 + w) & 15 : w, last ? 0 : ci) + ((last && w >= ln) ? 1 << 30 : 0) + (h << 20); }
  const bool prod = ci < 2 * NP && w < 9 / NP;
  const int r0 = ((ci >> 1) * (9 / NP) + w) * 64;
  int voff = lane * 16, wqo = ((r0 + 32 * (j >> 3) + 8 * g) * 4096 + h * 128 + (ci & 1) * 64 + 8 * (j & 7)) * 2;
  int xo = j < 8 ? ((j & 3) * DM + r0 + 32 * (j >> 2) + 8 * g) * 2 : OOB;
  const int prow = (idx >> 3) * 40 + (w - 3) * 4;
  int pfo = (w >= 3 && w < 13 && prow < DM + DC && prow >= PSKIP) ? ((prow < DM ? prow : prow - DM) + g) * 8192 + (idx & 7) * 1024 + j * 64 : OOB;
  int so0 = __builtin_amdgcn_readlane(vtab, 0);
  asm volatile("" : "+v"(vtab), "+v"(voff), "+v"(wqo), "+v"(xo), "+v"(pfo), "+s"(so0));
  __builtin_amdgcn_sched_barrier(0);
  asm volatile("" :: "s"(a.xB), "s"(a.C), "s"(a.wqB), "s"(a.wuk), "s"(a.wuv), "s"(a.woB), "s"(T64));
  u32 T0 = (u32)T64;
  const u32 toff = (u32)(w * TA2) / (STAG * 10);
  u32* lorg = reinterpret_cast<u32*>(lds + L_ST + 2048);
  const u32 tg = (u32)(tag >> 32);
  u32 last = T0; int lag = 0;
  u32x2 rv = {0u, 0u}; u32 rt = 0;
  const __amdgpu_buffer_rsrc_t rs = __builtin_amdgcn_make_buffer_rsrc(a.ws, 0, (int)WS_END, 0x00020000);
  const __amdgpu_buffer_rsrc_t rk = __builtin_amdgcn_make_buffer_rsrc(const_cast<u16*>(a.K), 0, 1 << 27, 0x00020000);
  const __amdgpu_buffer_rsrc_t rvv = __builtin_amdgcn_make_buffer_rsrc(const_cast<u16*>(a.V), 0, 1 << 27, 0x00020000);
  const __amdgpu_buffer_rsrc_t rw = __builtin_amdgcn_make_buffer_rsrc(const_cast<u16*>(a.wq), 0, DM * 4096 * 2, 0x00020000);
  const __amdgpu_buffer_rsrc_t rx = __builtin_amdgcn_make_buffer_rsrc(const_cast<u16*>(a.xA), 0, 4 * DM * 2, 0x00020000);
  u32x4 R[RING];
#define A_ISSUE(i) R[(i) % RING] = __builtin_amdgcn_raw_buffer_load_b128((i) >= 4 * NS ? rvv : rk, voff, ((i) == 0 ? so0 : __builtin_amdgcn_readlane(vtab, (i) % NS)) + ((((i) / NS) & 3) << 25), 2)
#define GATE(n) do { if (PACE) { u32 due = T0 + toff + org + ((osl * cmin(n, NK)) >> 4) + (u32)due_at(n); lag = (int)(now - due); if (MAXGAP && (int)(due - (last + MAXGAP)) > 0) due = last + MAXGAP; \
    while ((int)(now - due) < 0) { __builtin_amdgcn_s_sleep(1); now = rt32(); } last = now; } } while (0)
#define CTRL(n) do { if constexpr (PACE) { \
    if constexpr (rnd_at((n) - RD - 2) >= 0) { constexpr int k = rnd_at((n) - RD - 2); \
      if (k < NOACT) asm volatile("" :: "v"(rv)); \
      const u32 dt = (rv[1] == (u32)tag + k) ? rv[0] - T0 : rt; \
      const float sm = row16_sum((float)(int)dt); \
      const int o = (int)((RSH ? rdlane(sm, 0) * (1.f / 16.f) : (rdlane(sm, 0) + rdlane(sm, 16)) * (1.f / 32.f))) - due_at(rtab[k]); \
      if (w == ((5 * k + 11) & 15) && lane == 0 && o > -300 && o < (k < NOACT ? -5000 : 2000)) { \
        int e = o - (int)org - (int)((osl * cmin(rtab[k], NK)) >> 4) - dtab[k]; if (e < 0) e = 0; \
        int sl_ = ((int)osl * gtab[k]) / 16 + ((e > EBIG ? 0 : e) * btab[k]) / rgap(k); sl_ = sl_ > SLMAX ? SLMAX : sl_; \
        __hip_atomic_store(reinterpret_cast<u64*>(lorg), __builtin_bit_cast(u64, u32x2{(u32)((int)org + e + ((((int)osl - sl_) * cmin(n, NK)) >> 4)), (u32)sl_}), __ATOMIC_RELAXED, __HIP_MEMORY_SCOPE_WORKGROUP); } } \
    if constexpr (rnd_at(n) >= 0) { constexpr int k = rnd_at(n); \
      u32 tot = 0; \
      if (ci == k % ACU && (h & ((1 << RSH) - 1)) == (k & ((1 << RSH) - 1))) { const u32 mine = (1u << 24) + ((rt32() - toff - T0) & 0xfffffu); \
        if (lane == 0) tot = __hip_atomic_fetch_add(lorg + 8 + k, mine, __ATOMIC_RELAXED, __HIP_MEMORY_SCOPE_WORKGROUP) + mine; } \
      if (PFULL) tot = lane < PFULL ? __builtin_amdgcn_readfirstlane(tot) : 0u; \
      u32x2 pv_ = {T0 + ((tot & 0xffffffu) >> 4), (u32)tag + k}; int po_ = (tot >> 24) == (k < NOPOST ? 99u : 16u) ? (int)WS_RPT + (h >> RSH) * 4352 : OOB; asm volatile("" : "+v"(pv_), "+v"(po_)); \
      EXM(1, __builtin_amdgcn_raw_buffer_store_b64(pv_, rs, po_, 128 * k * 35, PAUX)); } \
    if constexpr (rnd_at((n) - RD) >= 0) { constexpr int k = rnd_at((n) - RD); \
      int ro_ = (w == ((5 * k + 11) & 15) && lane < (k < NOREAD ? 0 : 16)) ? (int)WS_RPT + lane * 4352 : OOB; asm volatile("" : "+v"(ro_)); \
      EXM(w == ((5 * k + 11) & 15) ? 0xffffu : 1u, rv = __builtin_amdgcn_raw_buffer_load_b64(rs, ro_, 128 * k * 35, RAUX)); rt = rt32() - T0 - MISS; } \
  } } while (0)
  u32x4 Wr[8], xf;
  u32 pf2 = 0, pf3 = 0, pf4 = 0;
  auto touch = [&]() __attribute__((always_inline)) {
    const int k = ci < 2 ? ci : (ci - 2) * 4 + (h & 3) + 2;
    const __attribute__((address_space(4))) u64* ap = (const __attribute__((address_space(4))) u64*)__builtin_amdgcn_kernarg_segment_ptr();
    const u64 p = ap[(0xCBA96421u >> (4 * (k & 7))) & 15u];
    const int len = k < 2 ? 1 << 27 : k == 3 ? 1 << 24 : k == 4 ? DC * 8192 : k == 6 ? (int)WS_END : k == 7 ? 9216 : DM * 8192;
    const int o = (k < 2 ? (lane << 25) + (h << 20) + lane * 4352 : (lane << 21)) + (idx & 31) * 256;
    pf3 = __builtin_amdgcn_raw_buffer_load_b32(__builtin_amdgcn_make_buffer_rsrc(reinterpret_cast<void*>(p), 0, (w == TPW && k < TPN) ? len : 0, 0x00020000), lane < 8 ? o : OOB, 0, 2);
  };
  if (BPF == 1) pf2 = __builtin_amdgcn_raw_buffer_load_b32(__builtin_amdgcn_make_buffer_rsrc(const_cast<u16*>(prow < DM ? a.wqB : a.wuk), 0, DM * 4096 * 2, 0x00020000), pfo, 0, 2);
  if (!KD0) A_ISSUE(0);
  __builtin_amdgcn_sched_barrier(0);
  if (prod) {
#pragma unroll
    for (int k = 0; k < 8; ++k) Wr[k] = __builtin_amdgcn_raw_buffer_load_b128(rw, wqo, k * 8192, 2);
    xf = __builtin_amdgcn_raw_buffer_load_b128(rx, xo, 0, 2);
  }
  __builtin_amdgcn_sched_barrier(0);
#define PF2() do { const __amdgpu_buffer_rsrc_t rp_ = __builtin_amdgcn_make_buffer_rsrc(const_cast<u16*>(prow < DM ? a.wqB : a.wuk), 0, DM * 4096 * 2, 0x00020000); pf2 = __builtin_amdgcn_raw_buffer_load_b32(rp_, pfo, 0, 2); \
    if (PF2S) { __builtin_amdgcn_sched_barrier(0); pf4 = __builtin_amdgcn_raw_buffer_load_b32(rp_, pfo, 0, 0); } } while (0)
#define TWAIT(d) do { while ((int)(rt32() - T0 - (d)) < 0) __builtin_amdgcn_s_sleep(1); __builtin_amdgcn_sched_barrier(0); } while (0)
  if (BPF == 2 && !KDP) pf2 = __builtin_amdgcn_raw_buffer_load_b32(__builtin_amdgcn_make_buffer_rsrc(const_cast<u16*>(prow < DM ? a.wqB : a.wuk), 0, DM * 4096 * 2, 0x00020000), pfo, 0, 2);
  if (TPS == 0) touch();
  if (!K1G) A_ISSUE(1);
  __builtin_amdgcn_raw_buffer_store_b64(u32x2{T0, (u32)tag + 15u}, rs, (ci == 6 && (h & 1) == 0 && tid == 0) ? (int)WS_RPT + (h >> 1) * 4352 : OOB, 128 * 15 * 35, 16);
  __builtin_amdgcn_sched_barrier(0);
  if (tid < 32) lorg[tid] = tid == 0 ? ORG0 : tid == 2 ? T0 : 0u;
  __syncthreads();
  T0 = __builtin_amdgcn_readfirstlane(lorg[2]);
  if (KDP && KDP <= KD0) { TWAIT(KDP + ((w * KDS) >> 4)); PF2(); }
  if (KD0) { TWAIT(KD0 + ((w * KDS) >> 4)); A_ISSUE(0); last = rt32(); }
  if (KDP > KD0) { TWAIT(KDP + ((w * KDS) >> 4)); PF2(); }
  __builtin_amdgcn_sched_barrier(0);
  if (prod) {
    float* qpart = reinterpret_cast<float*>(lds + L_BIG);
#pragma unroll
    for (int hf = 0; hf < 2; ++hf) {
      f32x4 d4[4];
#pragma unroll
      for (int t = 0; t < 4; ++t) d4[t] = mfma16(xf, tcol16(Wr, 4 * hf + t), f32x4{0.f, 0.f, 0.f, 0.f});
      if (g == (j >> 3)) {
#pragma unroll
        for (int r = 0; r < 4; ++r) *reinterpret_cast<f32x4*>(qpart + ((w * 2 + g) * 4 + r) * 64 + 8 * (j & 7) + 4 * hf) = f32x4{d4[0][r], d4[1][r], d4[2][r], d4[3][r]};
      }
    }
    asm volatile("" ::: "memory");
    u32 arr = 0;
    if (lane == 0) arr = __hip_atomic_fetch_add(lorg + 4, 1u, __ATOMIC_RELAXED, __HIP_MEMORY_SCOPE_WORKGROUP);
    if (__builtin_amdgcn_readfirstlane(arr) == 9 / NP - 1) {
      asm volatile("" ::: "memory");
      f32x4 s0 = {0.f, 0.f, 0.f, 0.f};
#pragma unroll
      for (int k = 0; k < 18 / NP; ++k) s0 += *reinterpret_cast<const f32x4*>(qpart + k * 256 + 4 * lane);
      const int qo = (int)WS_QA + (h * 8 + ci) * 2048 + lane * 16;
      __builtin_amdgcn_raw_buffer_store_b128(u32x4{__float_as_uint(s0[0]), tg, __float_as_uint(s0[1]), tg}, rs, qo, 0, QSA);
      __builtin_amdgcn_raw_buffer_store_b128(u32x4{__float_as_uint(s0[2]), tg, __float_as_uint(s0[3]), tg}, rs, qo + 1024, 0, QSA);
    }
  }
  static_for<RING - 2 + K1G>([&](auto tc) __attribute__((always_inline)) {
    constexpr int t = tc.value + 2 - K1G;
    u32 now = rt32();
    const u32 org = __builtin_amdgcn_readfirstlane(__hip_atomic_load(lorg, __ATOMIC_RELAXED, __HIP_MEMORY_SCOPE_WORKGROUP)), osl = 0;
    GATE(t);
    A_ISSUE(t);
    if constexpr (t == TPS) touch();
    __builtin_amdgcn_sched_barrier(0);
    if constexpr (t == SP) {
      if (w == 13) {
        const u32x2 sv = __builtin_amdgcn_raw_buffer_load_b64(rs, lane < 16 ? (int)WS_RPT + lane * 4352 : OOB, 128 * 15 * 35, SAUX);
        const bool ok = sv[1] == (u32)tag + 15u;
        const float sm = row16_sum(ok ? (float)(int)(sv[0] - T0) : 0.f), cn = row16_sum(ok ? 1.f : 0.f);
        const int o = ORG0 + (cn > 0.f ? (int)(sm / cn) : 0);
        if (lane == 0 && o > -300 && o < 1000) lorg[0] = (u32)o;
      }
    }
    if constexpr (t == QP) {
      if (w >= 14) {
        const int qo = (int)WS_QA + (h * 8 + (w & 1)) * 2048 + lane * 16;
        u32x4 v[2 * NP];
#pragma unroll 1
        for (u32 spins = 0; spins < SPIN_CAP; ++spins) {
          bool ok = true;
          if (spins < QTRY) {
#pragma unroll
            for (int k = 0; k < 2 * NP; ++k) v[k] = __builtin_amdgcn_raw_buffer_load_b128(rs, qo, (k >> 1) * 4096 + (k & 1) * 1024, QAUX);
          } else {
#pragma unroll
            for (int k = 0; k < 2 * NP; ++k) v[k] = __builtin_amdgcn_raw_buffer_load_b128(rs, qo, (k >> 1) * 4096 + (k & 1) * 1024, 16);
          }
#pragma unroll
          for (int k = 0; k < 2 * NP; ++k) ok = ok && v[k][1] == tg && v[k][3] == tg;
          if (__all(ok)) break;
          __builtin_amdgcn_s_sleep(4);
        }
        f32x4 s0 = {0.f, 0.f, 0.f, 0.f};
#pragma unroll
        for (int k = 0; k < NP; ++k) s0 += f32x4{__uint_as_float(v[2 * k][0]), __uint_as_float(v[2 * k][2]), __uint_as_float(v[2 * k + 1][0]), __uint_as_float(v[2 * k + 1][2])};
        *reinterpret_cast<u32x2*>(lds + L_QS + (g * 128 + (w & 1) * 64 + 4 * j) * 2) = u32x2{pk_bf16(s0[0], s0[1]), pk_bf16(s0[2], s0[3])};
        asm volatile("" ::: "memory");
        if (lane == 0) __hip_atomic_fetch_add(lorg + 5, 1u, __ATOMIC_RELAXED, __HIP_MEMORY_SCOPE_WORKGROUP);
      }
    }
    __builtin_amdgcn_sched_barrier(0);
  });
  while (__hip_atomic_load(lorg + 5, __ATOMIC_RELAXED, __HIP_MEMORY_SCOPE_WORKGROUP) != 2u) __builtin_amdgcn_s_sleep(1);
  asm volatile("" :: "v"(pf), "v"(pf2), "v"(pf4) : "memory");
  float* wtab = reinterpret_cast<float*>(lds + L_ST + 1024);
  u32x2 xv = {0u, 0u};
  const int xoff = (w == XW && lane < 8 * ACU) ? (int)WS_MLA + (h * 8 + (lane >> 3)) * 128 + (lane & 7) * 8 : OOB;
  float* pml = reinterpret_cast<float*>(lds + L_ST);
  u32x4 q;
  float acc[8], S[4];
  u32x2 RW[4][8];
  u32x4 WT[4][4];
  float Y[4];
  auto project = [&](int b) __attribute__((always_inline)) {
    f32x4 d4[4];
#pragma unroll
    for (int t = 0; t < 4; ++t) d4[t] = f32x4{0.f, 0.f, 0.f, 0.f};
    const f32x4 wt = *reinterpret_cast<const f32x4*>(wtab + b * 16 + 4 * g);
#pragma unroll
    for (int s = 0; s < 4; ++s) {
      const u32x4 af = *reinterpret_cast<const u32x4*>(lds + L_AB + (b * 16 + j) * 272 + (32 * s + 8 * g) * 2);
#pragma unroll
      for (int t = 0; t < 4; ++t) d4[t] = mfma16(af, WT[s][t], d4[t]);
    }
    float y[4];
#pragma unroll
    for (int t = 0; t < 4; ++t) y[t] = fmaf(d4[t][0], wt[0], d4[t][1] * wt[1]) + fmaf(d4[t][2], wt[2], d4[t][3] * wt[3]);
    Y[b] = sw16(sw32(y[0], y[2]), sw32(y[1], y[3]));
  };
  const __amdgpu_buffer_rsrc_t rwo = __builtin_amdgcn_make_buffer_rsrc(const_cast<u16*>(a.wo + (size_t)h * 128 * DM), 0, (w < 9 && (NOWO == 0 || NOWO == 4 || (NOWO == 2 && ci == 0))) ? 128 * DM * 2 : 0, 0x00020000);
  int wvo = (8 * g * DM + 64 * w + 4 * j) * 2;
  asm volatile("" : "+v"(wvo));
  int wvo4 = ((32 * (j >> 3) + 8 * g) * DM + 64 * w + 8 * (j & 7)) * 2;
  asm volatile("" : "+v"(wvo4));
  const u64 wmask = w < 9 ? ~0ull : 1ull;
#define WO_ISSUE() static_for<32>([&](auto kc) __attribute__((always_inline)) { constexpr int k = kc.value; \
    if constexpr (NOWO == 3 && wo_step(k) == i) { RW[k >> 3][k & 7] = u32x2{0u, 0u}; asm volatile("" : "+v"(RW[k >> 3][k & 7])); } \
    if constexpr (NOWO == 4 && wo_step(k) == i && !(k & 1)) { u32x4 t_; EXM(wmask, t_ = __builtin_amdgcn_raw_buffer_load_b128(rwo, wvo4, (64 * (k >> 4) + ((k >> 1) & 7)) * DM * 2, 2)); RW[k >> 3][k & 7] = u32x2{t_[0], t_[1]}; RW[k >> 3][(k & 7) + 1] = u32x2{t_[2], t_[3]}; } \
    if constexpr (wo_step(k) == (NOWO >= 3 ? -i - 1 : i)) EXM(wmask, RW[k >> 3][k & 7] = __builtin_amdgcn_raw_buffer_load_b64(rwo, wvo, (32 * (k >> 3) + (k & 7)) * DM * 2, 2)); })
#define ISSUE_STEP() do { if constexpr (issued(i + 1) > issued(i)) { GATE(issued(i)); if constexpr (gf) { if (LATE == 0 || i >= D3FROM - 1 || lag < LATE) asm volatile("" :: "v"(r)); __builtin_amdgcn_sched_barrier(0); } \
    if (WOF) WO_ISSUE(); A_ISSUE(issued(i)); if (!WOF) WO_ISSUE(); if constexpr (gf) __builtin_amdgcn_sched_barrier(0); CTRL(issued(i)); \
    if constexpr (issued(i + 1) > issued(i) + 1) { GATE(issued(i) + 1); A_ISSUE(issued(i) + 1); CTRL(issued(i) + 1); } } else { WO_ISSUE(); } } while (0)
  static_for<NSEQ>([&](auto ic) __attribute__((always_inline)) {
    constexpr int i = ic.value, bb = (i / NS) & 3, pass = i / (4 * NS), sl = i % NS;
    u32 now = 0; if (PACE) now = rt32();
    u32 org = 0, osl = 0; if (PACE) { const u32x2 ov = __builtin_bit_cast(u32x2, __hip_atomic_load(reinterpret_cast<u64*>(lorg), __ATOMIC_RELAXED, __HIP_MEMORY_SCOPE_WORKGROUP)); org = __builtin_amdgcn_readfirstlane(ov[0]); osl = __builtin_amdgcn_readfirstlane(ov[1]); }
    __builtin_amdgcn_sched_barrier(0);
    const u32x4 r = R[i % RING];
    constexpr bool gf = i >= GF0 && i < GF1;
    if constexpr (gf) { ISSUE_STEP(); }
    if constexpr (i == TPC) asm volatile("" :: "v"(pf3));
    if constexpr (pass == 0) {
      if constexpr (sl == 0) { q = *reinterpret_cast<const u32x4*>(lds + L_QS + (bb * 128 + 8 * j) * 2); S[bb] = -INFINITY; }
      float d = dot2(r[0], q[0], 0.f); d = dot2(r[1], q[1], d); d = dot2(r[2], q[2], d); d = dot2(r[3], q[3], d);
      asm volatile("" : "+v"(d));
      if constexpr (!gf) { ISSUE_STEP(); }
      d = row16_sum(d);
      S[bb] = (j == sl) ? d : S[bb];
      if constexpr (sl == NS - 1) {
        const float sc = (j == bad_sl) ? -INFINITY : S[bb] * C2;
        const float m = xg_max(row16_max(sc));
        S[bb] = __builtin_amdgcn_exp2f(sc - m);
        const float l = xg_sum(row16_sum(S[bb]));
        if (lane == 0) *reinterpret_cast<f32x2*>(pml + (bb * 16 + w) * 2) = f32x2{m, l};
      }
      if constexpr (i == 4 * NS - 1) {
        asm volatile("" ::: "memory");
        u32 arr = 0;
        if (lane == 0) arr = __hip_atomic_fetch_add(lorg + 6, 1u, __ATOMIC_RELAXED, __HIP_MEMORY_SCOPE_WORKGROUP);
        arr = __builtin_amdgcn_readfirstlane(arr);
        asm volatile("" ::: "memory");
        const f32x2 ml = *reinterpret_cast<const f32x2*>(pml + lane * 2);
        const float M = row16_max(ml[0]);
        const float L = row16_sum(__builtin_amdgcn_exp2f(ml[0] - M) * ml[1]);
        __builtin_amdgcn_raw_buffer_store_b64(u32x2{__float_as_uint(j ? L : M), tg}, rs, (arr == 15u && j < 2) ? (int)WS_MLA + (h * 8 + ci) * 128 + g * 16 + j * 8 : OOB, 0, 16);
      }
    } else {
      const float p = dpp<0x150 + sl>(S[bb]);
      if constexpr (sl == 0) {
#pragma unroll
        for (int k = 0; k < 8; ++k) acc[k] = 0.f;
      }
#pragma unroll
      for (int k = 0; k < 4; ++k) { acc[2 * k] = fmaf(p, bf_lo(r[k]), acc[2 * k]); acc[2 * k + 1] = fmaf(p, bf_hi(r[k]), acc[2 * k + 1]); }
      asm volatile("" : "+v"(acc[0]), "+v"(acc[1]), "+v"(acc[2]), "+v"(acc[3]), "+v"(acc[4]), "+v"(acc[5]), "+v"(acc[6]), "+v"(acc[7]));
      if constexpr (!gf) { ISSUE_STEP(); }
      if constexpr (i == XR) xv = __builtin_amdgcn_raw_buffer_load_b64(rs, xoff, 0, 16);
      if constexpr (i == XR + 2) {
        if (w == XW) {
          if (__builtin_expect(!__all(lane >= 8 * ACU || xv[1] == tg), 0)) {
#pragma unroll 1
            for (u32 spins = 0; spins < SPIN_CAP; ++spins) {
              __builtin_amdgcn_s_sleep(8); xv = __builtin_amdgcn_raw_buffer_load_b64(rs, xoff, 0, 16);
              if (__all(lane >= 8 * ACU || xv[1] == tg)) break;
            }
          }
          const float x = __uint_as_float(xv[0]);
          const float m = lane < 8 * ACU ? dpp<0xA0>(x) : -INFINITY, l = lane < 8 * ACU ? dpp<0xF5>(x) : 0.f;
          const float M = xg_max(fmaxf(m, dpp<0x128>(m)));
          float t = __builtin_amdgcn_exp2f(m - M) * l; t = xg_sum(t + dpp<0x128>(t));
          if (lane < 8 && !(lane & 1)) *reinterpret_cast<f32x2*>(wtab + 64 + lane) = f32x2{M, 1.f / t};
          asm volatile("" ::: "memory");
          const f32x2 gm = *reinterpret_cast<const f32x2*>(wtab + 64 + 2 * g);
          wtab[lane] = __builtin_amdgcn_exp2f(pml[lane * 2] - gm[0]) * gm[1];
          asm volatile("" ::: "memory");
          if (lane == 0) __hip_atomic_store(lorg + 7, 1u, __ATOMIC_RELAXED, __HIP_MEMORY_SCOPE_WORKGROUP);
        }
      }
      if constexpr (i >= WTS && i < WTS + 4) {
        constexpr int s = i - WTS;
#pragma unroll
        for (int t = 0; t < 4; ++t) WT[s][t] = tcol8(RW[s], t);
        asm volatile("" : "+v"(WT[s][0]), "+v"(WT[s][1]), "+v"(WT[s][2]), "+v"(WT[s][3]));
      }
      if constexpr (i == PJ0 || i == PJ1 || i == PJ2) {
        constexpr int b = i == PJ0 ? 0 : i == PJ1 ? 1 : 2;
        Y[b] = 0.f;
        if (w < 9) {
          while (__hip_atomic_load(lorg + 24 + b, __ATOMIC_RELAXED, __HIP_MEMORY_SCOPE_WORKGROUP) != 16u || __hip_atomic_load(lorg + 7, __ATOMIC_RELAXED, __HIP_MEMORY_SCOPE_WORKGROUP) == 0u) __builtin_amdgcn_s_sleep(1);
          asm volatile("" ::: "memory");
          project(b);
        }
        asm volatile("" : "+v"(Y[b]));
      }
      if constexpr (sl == NS - 1) {
        const float t0 = sw16(sw32(acc[0], acc[4]), sw32(acc[2], acc[6])), t1 = sw16(sw32(acc[1], acc[5]), sw32(acc[3], acc[7]));
        *reinterpret_cast<u32*>(lds + L_AB + (bb * 16 + w) * 272 + 16 * j + 4 * g) = pk_bf16(t0, t1);
        if constexpr (bb < 3) { asm volatile("" ::: "memory"); if (lane == 0) __hip_atomic_fetch_add(lorg + 24 + bb, 1u, __ATOMIC_RELAXED, __HIP_MEMORY_SCOPE_WORKGROUP); }
      }
    }
    __builtin_amdgcn_sched_barrier(0);
  });
#undef A_ISSUE
  __syncthreads();
  if (w < 9) {
    project(3);
    st16_sc1(rs, WS_Y + ((8 * w + (j >> 1)) * 256 + idx) * 128 + ((j & 1) * 4 + g) * 16, u32x4{pk_bf16(Y[0], Y[1]), tg, pk_bf16(Y[2], Y[3]), tg});
  }
  if (idx < 72) {
    __builtin_amdgcn_sched_barrier(0);
    __builtin_amdgcn_s_barrier();
    __builtin_amdgcn_s_sleep(FDLY);
    const int yo = (int)WS_Y + (idx * 256 + 16 * w) * 128 + lane * 16;
    u32x4 v0, v1;
#pragma unroll 1
    for (u32 spins = 0; spins < SPIN_CAP; ++spins) {
      v0 = __builtin_amdgcn_raw_buffer_load_b128(rs, yo, 0, 16); v1 = __builtin_amdgcn_raw_buffer_load_b128(rs, yo + 1024, 0, 16);
      if (__all(v0[1] == tg && v0[3] == tg && v1[1] == tg && v1[3] == tg)) break;
      __builtin_amdgcn_s_sleep(FSL);
    }
    float y = sw16(sw32(bf_lo(v0[0]) + bf_lo(v1[0]), bf_lo(v0[2]) + bf_lo(v1[2])), sw32(bf_hi(v0[0]) + bf_hi(v1[0]), bf_hi(v0[2]) + bf_hi(v1[2])));
    y += dpp<0x128>(y);
    float* red = reinterpret_cast<float*>(lds + L_PF);
    if (!(lane & 8)) red[((lane & 7) * 4 + g) * 20 + w] = y;
    __syncthreads();
    if (tid < 32) {
      const f32x4 r0 = *reinterpret_cast<const f32x4*>(red + tid * 20), r1 = *reinterpret_cast<const f32x4*>(red + tid * 20 + 4);
      const f32x4 r2 = *reinterpret_cast<const f32x4*>(red + tid * 20 + 8), r3 = *reinterpret_cast<const f32x4*>(red + tid * 20 + 12);
      const f32x4 r = r0 + r1 + r2;
      const __amdgpu_buffer_rsrc_t ro = __builtin_amdgcn_make_buffer_rsrc(a.out, 0, 4 * 1152 * 2, 0x00020000);
      __builtin_amdgcn_raw_buffer_store_b16(to_bf16(((r[0] + r[1]) + (r[2] + r[3])) + (r3[0] + r3[1])), ro, ((tid & 3) * 1152 + idx * 8 + (tid >> 2)) * 2, 0, OAUX);
      __builtin_amdgcn_raw_buffer_store_b16(to_bf16(r3[2] + r3[3]), ro, ((tid >> 3) * 1152 + 576 + idx * 8 + (tid & 7)) * 2, 0, OAUX);
    }
  }
}

#define CROW 1
constexpr int CW = CROW ? 1 : 8, CR = CROW ? 8 : 1;
#define IA 2
#define IB 4
#define IC 6
#define EUV 4
#define YOFF 90
#define WPD 4
template <bool LD> DI void part_b(const Args& a, unsigned char* lds, int wg, u64 tag, u32 pf) {
  const int tid = threadIdx.x, lane = tid & 63, w = __builtin_amdgcn_readfirstlane(tid >> 6);
  const int j = lane & 15, g = lane >> 4;
  const __amdgpu_buffer_rsrc_t rs = __builtin_amdgcn_make_buffer_rsrc(a.ws, 0, (int)WS_END, 0x00020000);
  const u32 tb0 = rt32();
  const int h = BPF ? (wg & 7) * 4 + (wg >> 3) : wg;
  const int h3 = h ^ 2;
  const int b2 = BMAP ? (wg & 7) >> 1 : wg >> 3, sp = BMAP ? (wg >> 3) * 2 + (wg & 1) : wg & 7;
  const u16* cl = a.C + ((size_t)b2 * SK + sp * 512 + CW * (w - 8)) * DC + lane * 8;
  u32x4 X[16];
  u32x2 RW[4][8];
  u32x4 W8[2];
  u32* cnt = reinterpret_cast<u32*>(lds + L_ST + 4064);
#define C_ISSUE(T) _Pragma("unroll") for (int r = 0; r < 8; ++r) X[8 * ((T) & 1) + r] = ld16(cl + (size_t)(64 * (T) + CR * r) * DC)
#define C_HALF(T, hf) _Pragma("unroll") for (int r = 4 * (hf); r < 4 * (hf) + 4; ++r) X[8 * ((T) & 1) + r] = ld16(cl + (size_t)(64 * (T) + CR * r) * DC)
#define L_PART(T, r0, r1) do { __builtin_amdgcn_sched_barrier(0); \
    if ((T) + 3 < 8) { _Pragma("unroll") for (int r = r0; r < r1; ++r) X[8 * (((T) + 3) & 1) + r] = ld16(cl + (size_t)(64 * ((T) + 3) + CR * r) * DC); } \
    if ((T) == 5 || (T) == 6) { _Pragma("unroll") for (int k = 2 * (r0); k < 2 * (r1); ++k) \
      RW[2 * (((T) - 5) & 1) + (k >> 3)][k & 7] = ld8(a.woB + ((size_t)h3 * 128 + 32 * (2 * (((T) - 5) & 1) + (k >> 3)) + 8 * g + (k & 7)) * DM + 64 * (w - 8) + 4 * j); } \
    __builtin_amdgcn_sched_barrier(0); } while (0)
#define UV_LOAD(n) do { const u32x4 v_ = ld16(a.wuv + ((size_t)(64 * w + 32 * ((n) >> 3) + 8 * g + ((n) & 7)) * H + h3) * 128 + 8 * j); \
    RW[2 * ((n) >> 3)][(n) & 7] = u32x2{v_[0], v_[1]}; RW[2 * ((n) >> 3) + 1][(n) & 7] = u32x2{v_[2], v_[3]}; } while (0)
#define C_WIN(T, r0, r1) _Pragma("unroll") for (int r = r0; r < r1; ++r) { asm volatile("s_waitcnt vmcnt(%0)" :: "n"(WPD - 1) : "memory"); X[8 * ((T) & 1) + r] = ld16(cl + (size_t)(64 * (T) + CR * r) * DC); __builtin_amdgcn_sched_barrier(0); }
  u32 sb0 = L_BIG + CW * (w - 8) * CRS + lane * 16, sb1 = sb0 + CBUF;
  asm volatile("" : "+v"(sb0), "+v"(sb1));
#define C_STAGE(T) _Pragma("unroll") for (int r = 0; r < 8; ++r) *reinterpret_cast<u32x4*>(lds + (((T) & 1) ? sb1 : sb0) + CR * r * CRS) = X[8 * ((T) & 1) + r]
  {
    const u16* Wq = a.wqB + h * 128;
    u32x4 U[8];
    QPre Q;
    qproj_load(Q, a.xB, Wq, w, lane);
#pragma unroll
    for (int i = 0; i < 8; ++i) U[i] = ld16(a.wuk + ((size_t)(32 * w + 4 * i + (j >> 2)) * H + h) * 128 + 8 * (4 * g + (j & 3)));
    __builtin_amdgcn_sched_barrier(0);
    if constexpr (!LD) am_zero(lds, tid);
    qproj_mfma(Q, lds, w, lane);
    asm volatile("" :: "v"(pf));
    __syncthreads();
    if constexpr (LD) { C_HALF(0, 0); }
    else {
      const float* qpart = reinterpret_cast<const float*>(lds + L_BIG);
      float s = 0.f;
#pragma unroll
      for (int ww = 0; ww < 16; ++ww) s += qpart[ww * 512 + tid];
      reinterpret_cast<u16*>(lds + L_QS)[tid] = to_bf16(s);
    }
    __builtin_amdgcn_sched_barrier(0);
    __syncthreads();
    const u32x4 af = *reinterpret_cast<const u32x4*>(lds + L_QS + ((j >> 2) * 128 + (4 * g + (j & 3)) * 8) * 2);
    u16* qa = reinterpret_cast<u16*>(lds + L_PF);
    float v2[2] = {0.f, 0.f};
#pragma unroll
    for (int i = 0; i < 8; ++i) {
      const f32x4 d4 = mfma16(af, U[i], f32x4{0.f, 0.f, 0.f, 0.f});
      float d = (j & 2) ? ((j & 1) ? d4[3] : d4[2]) : ((j & 1) ? d4[1] : d4[0]);
      d += dpp<0xB1>(d); d += dpp<0x4E>(d);
      v2[i >> 2] = ((j & 3) == (i & 3)) ? d : v2[i >> 2];
    }
    qa[g * 512 + 32 * w + 4 * (j & 3) + (j >> 2)] = to_bf16(v2[0]); qa[g * 512 + 32 * w + 16 + 4 * (j & 3) + (j >> 2)] = to_bf16(v2[1]);
    __builtin_amdgcn_sched_barrier(0);
    __syncthreads();
    if constexpr (!LD) {
      if (w == 0) {
#pragma unroll
        for (int bb = 0; bb < 4; ++bb) st16_sc1(rs, WS_QABS + ((bb * H + h) * 512 + lane * 8) * 2, *reinterpret_cast<const u32x4*>(qa + bb * 512 + lane * 8));
      }
      __builtin_amdgcn_sched_barrier(0);
      __builtin_amdgcn_s_barrier();
      if (w == 0) {
        asm volatile("s_waitcnt vmcnt(0)" ::: "memory");
        if (lane == 0) { __hip_atomic_store(reinterpret_cast<u64*>(a.ws + WS_F1 + wg * FST), tag, RLX_AGENT); cnt[0] = 0u; cnt[1] = 0u; }
      }
    } else {
      __builtin_amdgcn_s_barrier();
      C_WIN(0, 4, 8); C_WIN(1, 0, 8);
      C_STAGE(0);
      __builtin_amdgcn_sched_barrier(0);
      C_WIN(2, 0, 8);
    }
  }
  {
    const int b = b2;
    if constexpr (!LD) wg_wait64(a.ws + WS_F1, tag); else __syncthreads();
    const int kt = w & 3, hh = (w >> 2) & 1;
    float* stmax = reinterpret_cast<float*>(lds + L_ST);
    float* stsum = stmax + 128;
    float* salpha = stmax + 256;
    unsigned char* Pf = lds + L_PF;
    float M = -INFINITY, lsum = 0.f;
    f32x4 acc[2][2];
#pragma unroll
    for (int ct = 0; ct < 2; ++ct) { acc[ct][0] = f32x4{0.f, 0.f, 0.f, 0.f}; acc[ct][1] = f32x4{0.f, 0.f, 0.f, 0.f}; }
    const int troff = (4 * g + (j >> 2)) * CRS + (32 * w + 4 * (j & 3)) * 2;
    auto pv_step = [&](const unsigned char* cb) __attribute__((always_inline)) {
      const f32x4 al0 = *reinterpret_cast<const f32x4*>(salpha + 4 * g), al1 = *reinterpret_cast<const f32x4*>(salpha + 16 + 4 * g);
#pragma unroll
      for (int ct = 0; ct < 2; ++ct) { acc[ct][0] *= al0; acc[ct][1] *= al1; }
#pragma unroll
      for (int blk = 0; blk < 2; ++blk) {
        const u32x4 p0 = *reinterpret_cast<const u32x4*>(Pf + ((blk * 2 + 0) * 64 + lane) * 16);
        const u32x4 p1 = *reinterpret_cast<const u32x4*>(Pf + ((blk * 2 + 1) * 64 + lane) * 16);
#pragma unroll
        for (int ct = 0; ct < 2; ++ct) {
          const u32x2 lo = trread(cb + troff + (32 * blk) * CRS + 32 * ct);
          const u32x2 hi = trread(cb + troff + (32 * blk + 16) * CRS + 32 * ct);
          const u32x4 bf = {lo[0], lo[1], hi[0], hi[1]};
          acc[ct][0] = mfma16(p0, bf, acc[ct][0]); acc[ct][1] = mfma16(p1, bf, acc[ct][1]);
        }
      }
    };
    unsigned char* Qf = lds + L_BIG + CBUF;
    if constexpr (!LD) {
      u32x4 qv[4];
#pragma unroll
      for (int k = 0; k < 4; ++k) qv[k] = __builtin_amdgcn_raw_buffer_load_b128(rs, (int)WS_QABS + (b * H * 512 + (tid + 512 * k) * 8) * 2, 0, 16);
#pragma unroll
      for (int k = 0; k < 4; ++k) { const int e = tid + 512 * k, head = e >> 6, ch = e & 63;
        *reinterpret_cast<u32x4*>(Qf + ((head >> 4) * 16 + (ch >> 2)) * 1040 + ((ch & 3) * 16 + (head & 15)) * 16) = qv[k]; }
    }
    __syncthreads();
    if constexpr (!LD) {
      u32x4 QF[16];
#pragma unroll
      for (int s = 0; s < 16; ++s) QF[s] = *reinterpret_cast<const u32x4*>(Qf + (hh * 16 + s) * 1040 + lane * 16);
      auto s_tile = [&](const unsigned char* cb, bool last) __attribute__((always_inline)) {
        __syncthreads();
        f32x4 s0 = {0.f, 0.f, 0.f, 0.f}, s1 = {0.f, 0.f, 0.f, 0.f};
#pragma unroll
        for (int s = 0; s < 16; s += 2) {
          s0 = mfma16(*reinterpret_cast<const u32x4*>(cb + (16 * kt + j) * CRS + (32 * s + 8 * g) * 2), QF[s], s0);
          s1 = mfma16(*reinterpret_cast<const u32x4*>(cb + (16 * kt + j) * CRS + (32 * s + 32 + 8 * g) * 2), QF[s + 1], s1);
        }
        const f32x4 sv = (s0 + s1) * C2;
        if (last) { __builtin_amdgcn_sched_barrier(0); _Pragma("unroll") for (int n = 0; n < EUV; ++n) UV_LOAD(n); __builtin_amdgcn_sched_barrier(0); }
        const float v = xg_max(fmaxf(fmaxf(sv[0], sv[1]), fmaxf(sv[2], sv[3])));
        if (g == 0) stmax[kt * 32 + 16 * hh + j] = v;
        __syncthreads();
        const float* sm = stmax + 16 * hh + j;
        const float Mn = fmaxf(fmaxf(M, fmaxf(sm[0], sm[32])), fmaxf(sm[64], sm[96]));
        const float alpha = __builtin_amdgcn_exp2f(M - Mn);
        M = Mn;
        u32x2 pk;
        pk[0] = pk_bf16(__builtin_amdgcn_exp2f(sv[0] - Mn), __builtin_amdgcn_exp2f(sv[1] - Mn));
        pk[1] = pk_bf16(__builtin_amdgcn_exp2f(sv[2] - Mn), __builtin_amdgcn_exp2f(sv[3] - Mn));
        lsum = lsum * alpha + ((bf_lo(pk[0]) + bf_hi(pk[0])) + (bf_lo(pk[1]) + bf_hi(pk[1])));
        *reinterpret_cast<u32x2*>(Pf + (((kt >> 1) * 2 + hh) * 64 + lane) * 16 + (kt & 1) * 8) = pk;
        if (kt == 0 && g == 0) salpha[16 * hh + j] = alpha;
        __syncthreads();
        pv_step(cb);
      };
#pragma unroll 1
      for (int T = 0; T < 6; T += 2) { s_tile(lds + L_BIG, false); s_tile(lds + L_BIG + CBUF, false); }
      s_tile(lds + L_BIG, false); s_tile(lds + L_BIG + CBUF, true);
    } else {
#pragma unroll
      for (int T = 0; T < 8; ++T) {
        __syncthreads();
        if (T + 1 < 8) { C_STAGE(T + 1); }
        L_PART(T, 0, IA);
        if (T == 7) {
#pragma unroll
          for (int k = 0; k < 2; ++k) W8[k] = ld16(a.woB + ((size_t)h3 * 128 + 16 * (w - 8) + 8 * k + (lane >> 3)) * DM + 512 + (lane & 7) * 8);
        }
        __syncthreads();
        L_PART(T, IA, IB);
        __syncthreads();
        L_PART(T, IB, IC);
        pv_step(lds + L_BIG + (T & 1) * CBUF);
        L_PART(T, IC, 8);
      }
    }
#undef C_ISSUE
#undef C_STAGE
    __syncthreads();
    if constexpr (!LD) { lsum = xg_sum(lsum); if (g == 0) stsum[kt * 32 + 16 * hh + j] = lsum; if (kt == 0 && g == 0) salpha[32 + 16 * hh + j] = M; }
    float* tb = reinterpret_cast<float*>(lds + L_BIG);
#pragma unroll
    for (int ct = 0; ct < 2; ++ct)
#pragma unroll
      for (int h2 = 0; h2 < 2; ++h2)
#pragma unroll
        for (int r = 0; r < 4; ++r) tb[(16 * h2 + 4 * g + r) * 516 + 32 * w + 16 * ct + j] = acc[ct][h2][r];
    if constexpr (!LD) { for (int i = tid; i < 8 * AM2_LD * 2 / 16; i += 512) reinterpret_cast<u32x4*>(lds + L_AM2 + 8 * AM2_LD * 2)[i] = u32x4{0u, 0u, 0u, 0u}; }
    __syncthreads();
    if constexpr (LD) {
      const int t2 = tid - 512;
#pragma unroll
      for (int k = 0; k < 4; ++k) {
        const int e = t2 + 512 * k, head = e >> 6, c8 = e & 63;
        const f32x4 v0 = *reinterpret_cast<const f32x4*>(tb + head * 516 + 8 * c8), v1 = *reinterpret_cast<const f32x4*>(tb + head * 516 + 8 * c8 + 4);
        __builtin_amdgcn_raw_buffer_store_b128(u32x4{pk_bf16(v0[0], v0[1]), pk_bf16(v0[2], v0[3]), pk_bf16(v1[0], v1[1]), pk_bf16(v1[2], v1[3])}, rs, (int)WS_CTXP + (((b * NSP + sp) * 32 + head) * 512 + 8 * c8) * 2, 0, 16);
      }
      if (t2 < 32) {
        const float L = (stsum[t2] + stsum[32 + t2]) + (stsum[64 + t2] + stsum[96 + t2]);
        st8_sc1(a.ws + WS_MLB + ((size_t)(b * NSP + sp) * 32 + t2) * 8, f32x2{salpha[32 + t2], L});
      }
      __builtin_amdgcn_sched_barrier(0);
      __builtin_amdgcn_s_barrier();
      asm volatile("s_waitcnt vmcnt(0)" ::: "memory");
      if (lane == 0) {
        if (__hip_atomic_fetch_add(cnt, 1u, __ATOMIC_RELAXED, __HIP_MEMORY_SCOPE_WORKGROUP) == 7u) {
          __hip_atomic_store(reinterpret_cast<u64*>(a.ws + WS_F2 + wg * FST), tag, RLX_AGENT);
        }
      }
#pragma unroll
      for (int k = 0; k < 2; ++k) *reinterpret_cast<u32x4*>(lds + L_W8 + (16 * (w - 8) + 8 * k + (lane >> 3)) * 144 + (lane & 7) * 16) = W8[k];
    } else {
      __builtin_amdgcn_s_barrier();
      static_for<16 - EUV>([&](auto ic) __attribute__((always_inline)) {
        asm volatile("s_waitcnt vmcnt(%0)" :: "n"(WPD - 1) : "memory");
        UV_LOAD(EUV + ic.value);
        __builtin_amdgcn_sched_barrier(0);
      });
    }
  }
  {
    int tid = threadIdx.x; asm volatile("" : "+v"(tid));
    const int lane = tid & 63, j = lane & 15, g = lane >> 4;
    u16* Am2 = reinterpret_cast<u16*>(lds + L_AM2);
    float* cpart = reinterpret_cast<float*>(lds + L_CP);
    __builtin_amdgcn_sched_barrier(0);
    if constexpr (LD) {
      if (w == 8) {
        wave_wait(reinterpret_cast<const u64*>(a.ws + WS_F2) + (lane & (NBW - 1)) * (FST / 8), lane < NBW, tag);
        asm volatile("s_waitcnt vmcnt(0)" ::: "memory");
        if (lane == 0) __hip_atomic_store(cnt + 1, 1u, __ATOMIC_RELEASE, __HIP_MEMORY_SCOPE_WORKGROUP);
      } else {
        for (u32 spins = 0; spins < 64u * SPIN_CAP; ++spins) { if (__hip_atomic_load(cnt + 1, __ATOMIC_ACQUIRE, __HIP_MEMORY_SCOPE_WORKGROUP) != 0u) break; __builtin_amdgcn_s_sleep(1); }
      }
      __builtin_amdgcn_sched_barrier(0);
      const int b = (tid >> 7) & 3, c4 = tid & 127;
      u32x2 pv[NSP];
      const f32x2 ml = __builtin_bit_cast(f32x2, __builtin_amdgcn_raw_buffer_load_b64(rs, (int)WS_MLB + ((b * NSP + (j & 7)) * 32 + h3) * 8, 0, 16));
#pragma unroll
      for (int s = 0; s < NSP; ++s) pv[s] = __builtin_amdgcn_raw_buffer_load_b64(rs, (int)WS_CTXP + (((b * NSP + s) * 32 + h3) * 512 + 4 * c4) * 2, 0, 16);
      const float M = row16_max(ml[0]);
      float wt = __builtin_amdgcn_exp2f(ml[0] - M);
      wt *= 2.f / row16_sum(wt * ml[1]);
      float c0 = 0.f, c1 = 0.f, c2 = 0.f, c3 = 0.f;
#pragma unroll
      for (int s = 0; s < NSP; ++s) { const float ws_ = rdlane(wt, s); c0 = fmaf(ws_, bf_lo(pv[s][0]), c0); c1 = fmaf(ws_, bf_hi(pv[s][0]), c1); c2 = fmaf(ws_, bf_lo(pv[s][1]), c2); c3 = fmaf(ws_, bf_hi(pv[s][1]), c3); }
      const u32 h0 = pk_bf16(c0, c1), h1 = pk_bf16(c2, c3);
      *reinterpret_cast<u32x2*>(Am2 + (2 * b) * AM2_LD + 4 * c4) = u32x2{h0, h1};
      *reinterpret_cast<u32x2*>(Am2 + (2 * b + 1) * AM2_LD + 4 * c4) = u32x2{pk_bf16(c0 - bf_lo(h0), c1 - bf_hi(h0)), pk_bf16(c2 - bf_lo(h1), c3 - bf_hi(h1))};
      __syncthreads();
      __syncthreads();
      const int t2 = tid - 512;
      float s = 0.f;
#pragma unroll
      for (int ww = 0; ww < 8; ++ww) s += cpart[ww * 512 + t2];
      u16* Am = reinterpret_cast<u16*>(lds + L_AM);
      const int bb = t2 >> 7, d = t2 & 127;
      const u16 hi = to_bf16(s);
      Am[(4 * bb) * AM_LD + d] = hi; Am[(4 * bb + 1) * AM_LD + d] = to_bf16(s - bf2f(hi));
    } else {
      __syncthreads();
      f32x4 acc[8];
#pragma unroll
      for (int t = 0; t < 8; ++t) acc[t] = f32x4{0.f, 0.f, 0.f, 0.f};
#pragma unroll
      for (int k2 = 0; k2 < 2; ++k2) {
        const u32x4 af = *reinterpret_cast<const u32x4*>(Am2 + j * AM2_LD + 64 * w + 32 * k2 + 8 * g);
#pragma unroll
        for (int t = 0; t < 8; ++t) acc[t] = mfma16(af, tcol8(RW[2 * k2 + (t >> 2)], t & 3), acc[t]);
      }
      if (g < 2) {
#pragma unroll
        for (int bb = 0; bb < 2; ++bb) {
          float* p = cpart + (w * 4 + 2 * g + bb) * 128 + 8 * j;
          *reinterpret_cast<f32x4*>(p) = f32x4{acc[0][2 * bb] + acc[0][2 * bb + 1], acc[1][2 * bb] + acc[1][2 * bb + 1], acc[2][2 * bb] + acc[2][2 * bb + 1], acc[3][2 * bb] + acc[3][2 * bb + 1]};
          *reinterpret_cast<f32x4*>(p + 4) = f32x4{acc[4][2 * bb] + acc[4][2 * bb + 1], acc[5][2 * bb] + acc[5][2 * bb + 1], acc[6][2 * bb] + acc[6][2 * bb + 1], acc[7][2 * bb] + acc[7][2 * bb + 1]};
        }
      }
      __builtin_amdgcn_sched_barrier(0);
      __syncthreads();
      if (YDEF == 1 && w == 1) {
        u32 dl = tb0 + 5200u;
#pragma unroll 1
        while ((int)(rt32() - dl) < 0) {
          const u32x2 rv = __builtin_amdgcn_raw_buffer_load_b64(rs, lane < 16 ? (int)WS_RPT + lane * 4352 : OOB, 128 * (NRND - 1) * 35, 16);
          const bool ok = rv[1] == (u32)tag + (NRND - 1);
          const float cn = rdlane(row16_sum(ok ? 1.f : 0.f), 0);
          if (cn >= 8.f) { dl = tb0 + (u32)(int)(rdlane(row16_sum(ok ? (float)(int)(rv[0] - tb0) : 0.f), 0) / cn) + (u32)(due_at(NSEQ - 1) - due_at(rtab[NRND - 1]) + YOFF); break; }
          __builtin_amdgcn_s_sleep(32);
        }
        if (lane == 0) cnt[2] = (int)(dl - tb0) > 5200 ? tb0 : dl;
      }
      if (w == 0) {
#pragma unroll
        for (int s = 0; s < 4; ++s)
#pragma unroll
          for (int jj = 0; jj < 8; ++jj) RW[s][jj] = *reinterpret_cast<const u32x2*>(lds + L_W8 + (32 * s + 8 * g + jj) * 144 + 8 * j);
      }
    }
    __syncthreads();
    if (LD || w == 0) {
      f32x4 ya[4];
      wo_mfma(RW, lds, lane, ya);
      if (YDEF) { const u32 dl = YDEF == 1 ? __builtin_amdgcn_readfirstlane(cnt[2]) : tb0 + YDEF; while ((int)(rt32() - dl) < 0) __builtin_amdgcn_s_sleep(4); }
      st16_sc1(rs, WS_Y + ((8 * (LD ? w - 8 : 8) + (j >> 1)) * 256 + NAW + h3) * 128 + g * 32 + (j & 1) * 16, yrow(ya, (u32)(tag >> 32)));
    }
  }
}

__global__ void __launch_bounds__(1024, 4) k_main(Args a) {
  __shared__ __attribute__((aligned(16))) unsigned char lds[L_TOTAL];
  u32 pf;
  { u64 pc; u32 len;
    asm volatile("s_getpc_b64 %0\n.Lpf%=:\n\ts_mov_b32 %1, k_tail_mark-.Lpf%=" : "=s"(pc), "=s"(len));
    pf = __builtin_amdgcn_raw_buffer_load_b32(__builtin_amdgcn_make_buffer_rsrc(reinterpret_cast<void*>(pc), 0, len < 131072u ? len : 0u, 0x00020000), CPFM ? (threadIdx.x < 16 ? ((blockIdx.x >> 3) * 16 + threadIdx.x) * 128 : 0x7ffff000) : threadIdx.x * 128, 0, 0);
    __builtin_amdgcn_sched_barrier(0); }
  u64 tag = (hw_dispatch_id() + 1ull) * 0x9E3779B97F4A7C15ull;
  tag ^= ((u64)__builtin_amdgcn_dispatch_ptr() >> 6) * 0xC2B2AE3D27D4EB4Full;
  tag |= 1ull << 63;
  if (__builtin_expect(blockIdx.x >= NBW, 1)) part_a(a, lds, blockIdx.x - NBW, tag, pf);
  else if (NOB) { const int w = threadIdx.x >> 6, lane = threadIdx.x & 63, j = lane & 15, g = lane >> 4; const u32 tg = (u32)(tag >> 32);
    if (w < 9) st16_sc1(__builtin_amdgcn_make_buffer_rsrc(a.ws, 0, (int)WS_END, 0x00020000), WS_Y + ((8 * w + (j >> 1)) * 256 + NAW + blockIdx.x) * 128 + g * 32 + (j & 1) * 16, u32x4{0u, tg, 0u, tg}); }
  else if (__builtin_amdgcn_readfirstlane(threadIdx.x >> 6) < 8) part_b<false>(a, lds, blockIdx.x, tag, pf);
  else part_b<true>(a, lds, blockIdx.x, tag, pf);
}

extern "C" __global__ void k_tail_mark() {}

extern "C" void kernel_launch(void* const* d_in, const int* in_sizes, int n_in, void* d_out, int out_size, void* d_ws, size_t ws_size,
                              hipStream_t stream) {
  Args a;
  a.xA = (const u16*)d_in[0]; a.K = (const u16*)d_in[1]; a.V = (const u16*)d_in[2]; a.wq = (const u16*)d_in[3]; a.wo = (const u16*)d_in[4];
  a.xB = (const u16*)d_in[5]; a.C = (const u16*)d_in[6]; a.wqB = (const u16*)d_in[7]; a.wuk = (const u16*)d_in[8]; a.wuv = (const u16*)d_in[9]; a.woB = (const u16*)d_in[10];
#define WSTOP 32
  if (WSTOP && ws_size >= ((size_t)WSTOP << 20)) d_ws = (unsigned char*)d_ws + ((ws_size - ((size_t)WSTOP << 20)) & ~(size_t)4095);
  a.ws = (unsigned char*)d_ws;
  a.out = (u16*)d_out;
  k_main<<<NBW + NAW, 1024, 0, stream>>>(a);
}
```

```cpp
#include <hip/hip_runtime.h>
#include <stdint.h>
#include <utility>

typedef unsigned short u16;
typedef unsigned u32;
typedef unsigned long long u64;
typedef u32 u32x4 __attribute__((ext_vector_type(4)));
typedef u32 u32x2 __attribute__((ext_vector_type(2)));
typedef float f32x4 __attribute__((ext_vector_type(4)));
typedef float f32x2 __attribute__((ext_vector_type(2)));
typedef short s16x4 __attribute__((ext_vector_type(4)));
typedef __bf16 bf16x8 __attribute__((ext_vector_type(8)));
typedef __bf16 bf16x2 __attribute__((ext_vector_type(2)));
#define DI __device__ __forceinline__
#define RLX_AGENT __ATOMIC_RELAXED, __HIP_MEMORY_SCOPE_AGENT
extern "C" __device__ u64 hw_dispatch_id() __asm("llvm.amdgcn.dispatch.id");

template <int... Is, class F> DI void static_for_impl(std::integer_sequence<int, Is...>, F&& f) { (f(std::integral_constant<int, Is>{}), ...); }
template <int N, class F> DI void static_for(F&& f) { static_for_impl(std::make_integer_sequence<int, N>{}, f); }

constexpr int H = 32, SK = 4096, DM = 576, DC = 512;
constexpr int ACU = 7;
constexpr int NBW = 32;
constexpr int NAW = H * ACU;
constexpr int NSP = 8;
constexpr float C2 = 0.08838834764831845f * 1.4426950408889634f;

#define FST 4352
constexpr size_t WS_CTXP = 0;
constexpr size_t WS_QABS = WS_CTXP + (size_t)4 * NSP * 32 * 512 * 2;
constexpr size_t WS_MLB = WS_QABS + 4 * 32 * 512 * 2;
constexpr size_t WS_Y = WS_MLB + 4 * NSP * 32 * 2 * 4;
constexpr size_t WS_MLA = WS_Y + (size_t)72 * 256 * 128;
constexpr size_t WS_RPT = WS_MLA + 256 * 128;
constexpr size_t WS_QA = WS_RPT + 64 * 4480;
constexpr size_t WS_F1 = WS_QA + 256 * 2048, WS_F2 = WS_F1 + 512;
constexpr size_t WS_END = WS_F1 + 32 * 4352 + 1024;

constexpr int CRS = 1056;
constexpr int CBUF = 64 * CRS;
constexpr int L_BIG = 0;
constexpr int L_AM = 2 * CBUF;
constexpr int L_QS = L_AM + 16 * 272;
constexpr int L_ST = L_QS + 1024;
constexpr int L_PF = L_ST + 4096;
constexpr int L_TOTAL = L_PF + 4096;
constexpr int AM_LD = 136;
constexpr int AM2_LD = 520;
constexpr int L_AM2 = 69632, L_CP = 88064, L_W8 = 106496;

struct Args {
  const u16 *xA, *K, *V, *wq, *wo, *xB, *C, *wqB, *wuk, *wuv, *woB;
  unsigned char* ws;
  u16* out;
};

DI float bf_lo(u32 x) { return __uint_as_float(x << 16); }
DI float bf_hi(u32 x) { return __uint_as_float(x & 0xffff0000u); }
DI u32 pk_bf16(float lo, float hi) { bf16x2 v = {(__bf16)lo, (__bf16)hi}; return __builtin_bit_cast(u32, v); }
DI u16 to_bf16(float x) { return __builtin_bit_cast(u16, (__bf16)x); }
DI float bf2f(u16 x) { return __uint_as_float(((u32)x) << 16); }
DI float dot2(u32 a, u32 b, float c) { return __builtin_amdgcn_fdot2_f32_bf16(__builtin_bit_cast(bf16x2, a), __builtin_bit_cast(bf16x2, b), c, false); }
template <int CTRL> DI float dpp(float x) { return __builtin_bit_cast(float, __builtin_amdgcn_update_dpp(0, __builtin_bit_cast(int, x), CTRL, 0xf, 0xf, true)); }
DI float row16_sum(float x) { x += dpp<0x128>(x); x += dpp<0x124>(x); x += dpp<0x122>(x); x += dpp<0x121>(x); return x; }
DI float row16_max(float x) { x = fmaxf(x, dpp<0x128>(x)); x = fmaxf(x, dpp<0x124>(x)); x = fmaxf(x, dpp<0x122>(x)); x = fmaxf(x, dpp<0x121>(x)); return x; }
DI float xg_max(float x) {
  auto s = __builtin_amdgcn_permlane16_swap(__float_as_uint(x), __float_as_uint(x), false, false);
  x = fmaxf(__uint_as_float(s[0]), __uint_as_float(s[1]));
  auto t = __builtin_amdgcn_permlane32_swap(__float_as_uint(x), __float_as_uint(x), false, false);
  return fmaxf(__uint_as_float(t[0]), __uint_as_float(t[1]));
}
DI float xg_sum(float x) {
  auto s = __builtin_amdgcn_permlane16_swap(__float_as_uint(x), __float_as_uint(x), false, false);
  x = __uint_as_float(s[0]) + __uint_as_float(s[1]);
  auto t = __builtin_amdgcn_permlane32_swap(__float_as_uint(x), __float_as_uint(x), false, false);
  return __uint_as_float(t[0]) + __uint_as_float(t[1]);
}
DI float sw32(float a, float b) {
  auto s = __builtin_amdgcn_permlane32_swap(__float_as_uint(a), __float_as_uint(b), false, false);
  return __uint_as_float(s[0]) + __uint_as_float(s[1]);
}
DI float sw16(float a, float b) {
  auto s = __builtin_amdgcn_permlane16_swap(__float_as_uint(a), __float_as_uint(b), false, false);
  return __uint_as_float(s[0]) + __uint_as_float(s[1]);
}
DI f32x4 mfma16(u32x4 a, u32x4 b, f32x4 c) { return __builtin_amdgcn_mfma_f32_16x16x32_bf16(__builtin_bit_cast(bf16x8, a), __builtin_bit_cast(bf16x8, b), c, 0, 0, 0); }
DI u32x4 ld16(const u16* p) { return __builtin_nontemporal_load(reinterpret_cast<const u32x4*>(p)); }
DI u32x2 ld8(const u16* p) { return __builtin_nontemporal_load(reinterpret_cast<const u32x2*>(p)); }
DI u32x2 trread(const unsigned char* p) {
  return __builtin_bit_cast(u32x2, __builtin_amdgcn_ds_read_tr16_b64_v4i16((__attribute__((address_space(3))) s16x4*)(p)));
}
DI u32 prm(u32 hi, u32 lo, int odd) { return __builtin_amdgcn_perm(hi, lo, odd ? 0x07060302u : 0x05040100u); }

DI u32x4 tcol16(const u32x4 (&R)[8], int t) {
  u32x4 T;
#pragma unroll
  for (int q = 0; q < 4; ++q) T[q] = prm(R[2 * q + 1][t >> 1], R[2 * q][t >> 1], t & 1);
  return T;
}
DI u32x4 tcol8(const u32x2 (&R)[8], int t) {
  u32x4 T;
#pragma unroll
  for (int q = 0; q < 4; ++q) T[q] = prm(R[2 * q + 1][t >> 1], R[2 * q][t >> 1], t & 1);
  return T;
}

#define SPINLOG 22
#define PSL 4
constexpr u32 SPIN_CAP = 1u << SPINLOG;
DI void wave_wait(const u64* f, bool active, u64 tag) {
#pragma unroll 1
  for (u32 spins = 0; spins < SPIN_CAP; ++spins) {
    if (active) active = __hip_atomic_load(f, RLX_AGENT) != tag;
    if (!__any(active)) break;
    __builtin_amdgcn_s_sleep(PSL);
  }
}
DI void wg_wait64(const unsigned char* flags, u64 tag, int pw = 0) {
  if ((int)(threadIdx.x >> 6) == pw) {
    wave_wait(reinterpret_cast<const u64*>(flags) + (threadIdx.x & (NBW - 1)) * (FST / 8), (threadIdx.x & 63) < NBW, tag);
    asm volatile("s_waitcnt vmcnt(0)" ::: "memory");
  }
  __syncthreads();
}
DI void st16_sc1(__amdgpu_buffer_rsrc_t rs, size_t off, u32x4 v) { __builtin_amdgcn_raw_buffer_store_b128(v, rs, (int)off, 0, 16); }
DI void st8_sc1(unsigned char* p, f32x2 v) { __hip_atomic_store(reinterpret_cast<u64*>(p), __builtin_bit_cast(u64, v), RLX_AGENT); }

struct QPre { u32x4 R0[8]; u32x4 R1; u32x2 xa, xb; u32 x1; };
DI void qproj_load(QPre& Q, const u16* x, const u16* Wh, int w, int lane) {
  const int j = lane & 15, g = lane >> 4;
  Q.xa = u32x2{0u, 0u}; Q.xb = u32x2{0u, 0u}; Q.x1 = 0u;
  if (j < 4) { Q.xa = ld8(x + j * DM + 36 * w + 8 * g); Q.xb = ld8(x + j * DM + 36 * w + 8 * g + 4); Q.x1 = x[j * DM + 36 * w + 32 + g]; }
#pragma unroll
  for (int jj = 0; jj < 8; ++jj) Q.R0[jj] = ld16(Wh + (size_t)(36 * w + 8 * g + jj) * 4096 + 8 * j);
  Q.R1 = ld16(Wh + (size_t)(36 * w + 32 + g) * 4096 + 8 * j);
}
DI void qproj_mfma(const QPre& Q, unsigned char* lds, int w, int lane) {
  const int j = lane & 15, g = lane >> 4;
  float* qpart = reinterpret_cast<float*>(lds + L_BIG);
  const u32x4 a = {Q.xa[0], Q.xa[1], Q.xb[0], Q.xb[1]}, a1 = {Q.x1, 0u, 0u, 0u};
#pragma unroll
  for (int hf = 0; hf < 2; ++hf) {
    f32x4 acc[4];
#pragma unroll
    for (int t = 0; t < 4; ++t) acc[t] = mfma16(a, tcol16(Q.R0, 4 * hf + t), f32x4{0.f, 0.f, 0.f, 0.f});
#pragma unroll
    for (int t = 0; t < 4; ++t) { const u32x4 bb = {(t & 1) ? (Q.R1[2 * hf + (t >> 1)] >> 16) : (Q.R1[2 * hf + (t >> 1)] & 0xffffu), 0u, 0u, 0u}; acc[t] = mfma16(a1, bb, acc[t]); }
    if (g == 0) {
#pragma unroll
      for (int r = 0; r < 4; ++r) *reinterpret_cast<f32x4*>(qpart + (w * 4 + r) * 128 + 8 * j + 4 * hf) = f32x4{acc[0][r], acc[1][r], acc[2][r], acc[3][r]};
    }
    __builtin_amdgcn_sched_barrier(0);
  }
}
DI void wo_load(const u16* Wo, u32x2 (&R)[4][8], int nb, int lane, int s0, int s1) {
  const int j = lane & 15, g = lane >> 4;
#pragma unroll
  for (int s = 0; s < 4; ++s) if (s >= s0 && s < s1)
#pragma unroll
    for (int jj = 0; jj < 8; ++jj) R[s][jj] = ld8(Wo + (size_t)(32 * s + 8 * g + jj) * DM + 64 * nb + 4 * j);
}
DI void wo_mfma(const u32x2 (&R)[4][8], const unsigned char* lds, int lane, f32x4 (&acc)[4]) {
  const int j = lane & 15, g = lane >> 4;
#pragma unroll
  for (int t = 0; t < 4; ++t) acc[t] = f32x4{0.f, 0.f, 0.f, 0.f};
#pragma unroll
  for (int s = 0; s < 4; ++s) {
    const u32x4 a = *reinterpret_cast<const u32x4*>(lds + L_AM + j * 272 + (32 * s + 8 * g) * 2);
#pragma unroll
    for (int t = 0; t < 4; ++t) acc[t] = mfma16(a, tcol8(R[s], t), acc[t]);
  }
}
DI u32x4 yrow(const f32x4 (&acc)[4], u32 tg) {
  return u32x4{pk_bf16(acc[0][0] + acc[0][1], acc[1][0] + acc[1][1]), tg, pk_bf16(acc[2][0] + acc[2][1], acc[3][0] + acc[3][1]), tg};
}
DI void am_zero(unsigned char* lds, int tid) {
  if (tid < 136) { const int pr = tid / 34; *reinterpret_cast<u32x4*>(lds + L_AM + (4 * pr + 2) * 272 + (tid - 34 * pr) * 16) = u32x4{0u, 0u, 0u, 0u}; }
}
DI float rdlane(float x, int l) { return __builtin_bit_cast(float, __builtin_amdgcn_readlane(__builtin_bit_cast(int, x), l)); }

constexpr int NS = 10;
constexpr int NSEQ = 8 * NS;
#define RNG 9
#define NP 3
#define QP 4
#define SP 4
#define BPF 2
#define TPS 3
#define KD0 60
#define KDP 60
#define KDS 60
#define NOWO 0
#define NOB 0
#define GF0 12
#define GF1 99
#define WOF 1
#define RAUX 16
#define SAUX 16
#define NOACT 0
#define NOPOST 0
#define NOREAD 0
#define PFULL 0
#define PAUX 16
#define RSPR 0
#define QTRY 0
#define QAUX 3
#define QSA 16
#define LATE 0
#define EXQ 1
#define PF2S 0
#define SMO 1
#define TST 440
#define WTS0 42
#define SLN 0
#define SLK 3
#define SLT 0x7513
#define BMODE 2
#define NB 50
#define BT 160
#define BS 180
#define TF 8
#define LN 24
#define TPN 8
#define TPC 12
#define TPW 10
#define K1G 1
#define PSKIP 9999
#define D3FROM 60
#define D4FROM 99
#define ROTM 1
#define OAUX 0
#define DEP 2
constexpr int DEPTH = DEP, RING = RNG, RAMP = 2;
constexpr int issued(int i) {
  int d = RING - (i + RAMP - 1) / RAMP; if (d < DEPTH) d = DEPTH;
  if (i >= D3FROM && d < 3) d = 3;
  if (i >= D4FROM && d < 4) d = 4;
  int e = i + d; if (e < RING) e = RING; return e > NSEQ ? NSEQ : e;
}
#define PACE 1
#define YDEF 1
#define WO0 16
#define WON 36
#define TA1 535
#define TA2 440
#define NK 68
#define ORG0 130
#define SLMAX 80
#define MAXGAP 70
constexpr int cmin(int a, int b) { return a < b ? a : b; }
#define RSH 1
#define RD 4
#define BMAP 1
#define EBIG 9999
#define NJ 54
#define TA3 515
#define PTAB 510, 510, 510, 510, 510, 510, 515, 515, 515, 515, 515, 515, 520, 510, 440, 432, 432
#define PTAB1 0, 0, 0, 0, 0, 0, 0, 0, 0, 0, 530, 490, 460, 455, 440, 432, 432
constexpr int ptab[2][17] = {{PTAB}, {PTAB1}};
constexpr int due_at(int n, int m = 0) { int t = 0; for (int k = 0; k < n; ++k) t += ptab[m && k >= NB][k / 5]; return t / 10; }
constexpr int due_x(int n, int m) { return due_at(n, m) + ((m && n >= NB) ? BT : 0); }
constexpr int due_b(int n, int m) { return n % 10 == 9 ? 0 : due_x(n - n % 10, m) + (due_at(n - n % 10 + 10, m) - due_at(n - n % 10, m)) * (n % 10) / 9; }
#define CI(x) std::integral_constant<int, (x)>::value
#define RTAB 20, 30, 40, 50, 60, 70
#define GTAB 0, 12, 6, 12, 12, 12
#define BTAB 8, 12, 6, 12, 12, 0
#define MISS 40
#define DB 10
#define DTAB 22, 16, 18, 15, 10, 3, 3, 3
#define XTAB 0, 0, 2210, 0, 0, 0
#define MTAB -1, -1, 2, -1, -1, -1
#define CB 16
#define SBMAX 100
constexpr int xtab[] = {XTAB}, mtab[] = {MTAB};
constexpr int rtab[] = {RTAB}, gtab[] = {GTAB}, btab[] = {BTAB}, dtab[] = {DTAB};
constexpr int NRND = sizeof(rtab) / sizeof(int);
constexpr int rgap(int k) { return k ? rtab[k] - rtab[k - 1] : rtab[0] - RING; }
constexpr int rnd_at(int n) { for (int k = 0; k < NRND; ++k) if (rtab[k] == n) return k; return -1; }
static_assert(rtab[0] >= RING + 2 && rtab[NRND - 1] + RD + 2 < NSEQ, "");
constexpr int wo_step(int k) { return WO0 + k * WON / 16; }
#define WTS 52
#define PJ0 56
#define PJ1 63
#define PJ2 72
#define XW 13
#define XR 50
#define FDLY 8
#define FSL 2
#define CPFM 1
#define STAG 16
#define EXM(m, ...) do { if (EXQ) { __builtin_amdgcn_sched_barrier(0); asm volatile("s_mov_b64 exec, %0" :: "s"((u64)(m))); __builtin_amdgcn_sched_barrier(0); } __VA_ARGS__; \
    if (EXQ) { __builtin_amdgcn_sched_barrier(0); asm volatile("s_mov_b64 exec, -1"); __builtin_amdgcn_sched_barrier(0); } } while (0)
DI u32 rt32() { return (u32)__builtin_amdgcn_s_memrealtime(); }
constexpr int OOB = 0x7ffff000;
constexpr int L_AB = 32768;
#define PERM 12
DI int uoff(int c, int u, int x) {
  if (PERM == 0) return c * 16384 + u * 1024;
  if (PERM == 1) return ((u >> 2) << 10) | ((u & 3) << 15) | ((c & 7) << 12) | ((c >> 3) << 17);
  if (PERM == 2) return ((c & 3) << 10) | ((u & 8) << 9) | ((u & 4) << 11) | ((c & 4) << 12) | ((u & 3) << 15) | ((c >> 3) << 17);
  if (PERM == 3) return ((((u >> 2) ^ x) & 3) << 10) | ((u & 3) << 15) | ((c & 7) << 12) | ((c >> 3) << 17);
  if (PERM == 4) return ((c & 3) << 10) | ((u & 3) << 12) | ((c & 4) << 12) | ((u >> 2) << 15) | ((c >> 3) << 17);
  if (PERM == 12) return c * 16384 + ((u ^ ((c >> 1) & 3)) * 1024);
  if (PERM == 13) return c * 16384 + ((u ^ ((c >> 1) & 3) ^ ((c >> 3) & 3)) * 1024);
  if (PERM == 5) return (((u ^ x) & 3) << 10) | ((u >> 2) << 15) | ((c & 7) << 12) | ((c >> 3) << 17);
  return 0;
}
DI void part_a(const Args& a, unsigned char* lds, int idx, u64 tag, u32 pf) {
  const u64 T64 = __builtin_amdgcn_s_memrealtime();
  __builtin_amdgcn_sched_barrier(0);
  const int tid = threadIdx.x, lane = tid & 63, w = __builtin_amdgcn_readfirstlane(tid >> 6);
  const int j = lane & 15, g = lane >> 4;
  const int h = (idx & 7) * 4 + ((idx >> 3) & 3), ci = idx >> 5;
  const int rr = (((h & 3) * 5) >> 1) + (h >> 2), rot = SMO ? (ROTM == 2 ? (h >> 2) + (h & 1) * 5 : h >> 2) % 9 : (rr >= NS ? rr - NS : rr), l0 = (ci * 37) >> 4, ln = ((ci * 37 + 37) >> 4) - l0;
  const bool xtra = w < ln;
  const int bad_sl = xtra ? -1 : SMO ? NS - 1 : NS - 1 - rot;
  int vtab;
  { const int p = SMO ? (lane == NS - 1 ? lane : lane + rot >= NS - 1 ? lane + rot - (NS - 1) : lane + rot) : lane + rot >= NS ? lane + rot - NS : lane + rot;
    const bool last = p == NS - 1;
    vtab = uoff(last ? 63 : p * 7 + ci, last ? (l0 + w) & 15 : w, last ? 0 : ci) + ((last && w >= ln) ? 1 << 30 : 0) + (h << 20); }
  const bool prod = ci < 2 * NP && w < 9 / NP;
  const int r0 = ((ci >> 1) * (9 / NP) + w) * 64;
  int voff = lane * 16, wqo = ((r0 + 32 * (j >> 3) + 8 * g) * 4096 + h * 128 + (ci & 1) * 64 + 8 * (j & 7)) * 2;
  int xo = j < 8 ? ((j & 3) * DM + r0 + 32 * (j >> 2) + 8 * g) * 2 : OOB;
  const int pk = (w - 3) % 3, pn = pk ? 19 : 21;
  int pfo = (w >= 3 && w < 3 + 3 * SLN && pk < SLK && lane < pn) ? ((idx >> 3) * pn + lane) * 8192 + (idx & 7) * 1024 + ((SLT >> (4 * ((w - 3) / 3))) & 7) * 128 : OOB;
  int so0 = __builtin_amdgcn_readlane(vtab, 0);
  asm volatile("" : "+v"(vtab), "+v"(voff), "+v"(wqo), "+v"(xo), "+v"(pfo), "+s"(so0));
  __builtin_amdgcn_sched_barrier(0);
  asm volatile("" :: "s"(a.xB), "s"(a.C), "s"(a.wqB), "s"(a.wuk), "s"(a.wuv), "s"(a.woB), "s"(T64));
  u32 T0 = (u32)T64;
  const u32 toff = (u32)(w * TST) / (STAG * (SMO ? 9 : 10));
  u32* lorg = reinterpret_cast<u32*>(lds + L_ST + 2048);
  const u32 tg = (u32)(tag >> 32);
  u32 last = T0;
  bool mode = false;
  u32x2 rv = {0u, 0u}; u32 rt = 0;
  const __amdgpu_buffer_rsrc_t rs = __builtin_amdgcn_make_buffer_rsrc(a.ws, 0, (int)WS_END, 0x00020000);
  const __amdgpu_buffer_rsrc_t rk = __builtin_amdgcn_make_buffer_rsrc(const_cast<u16*>(a.K), 0, 1 << 27, 0x00020000);
  const __amdgpu_buffer_rsrc_t rvv = __builtin_amdgcn_make_buffer_rsrc(const_cast<u16*>(a.V), 0, 1 << 27, 0x00020000);
  const __amdgpu_buffer_rsrc_t rw = __builtin_amdgcn_make_buffer_rsrc(const_cast<u16*>(a.wq), 0, DM * 4096 * 2, 0x00020000);
  const __amdgpu_buffer_rsrc_t rx = __builtin_amdgcn_make_buffer_rsrc(const_cast<u16*>(a.xA), 0, 4 * DM * 2, 0x00020000);
  u32x4 R[RING];
#define A_ISSUE(i) R[(i) % RING] = __builtin_amdgcn_raw_buffer_load_b128((i) >= 4 * NS ? rvv : rk, voff, ((i) == 0 ? so0 : __builtin_amdgcn_readlane(vtab, (i) % NS)) + ((((i) / NS) & 3) << 25), 2)
#define GATE(n) do { if (PACE) { u32 due = T0 + toff + org + ((osl * cmin(n, NK)) >> 4) + (u32)(mode ? (xtra ? CI(due_x(n, 1)) : CI(due_b(n, 1))) : (xtra ? CI(due_x(n, 0)) : CI(due_b(n, 0)))); if (MAXGAP && (int)(due - (last + MAXGAP)) > 0) due = last + MAXGAP; \
    while ((int)(now - due) < 0) { __builtin_amdgcn_s_sleep(1); now = rt32(); } if (!SMO || (n) % 10 != 9 || xtra) last = now; } } while (0)
#define CTRL(n) do { if constexpr (PACE) { \
    if constexpr (rnd_at((n) - RD - 2) >= 0) { constexpr int k = rnd_at((n) - RD - 2); \
      if (k < NOACT) asm volatile("" :: "v"(rv)); \
      const u32 dt = (rv[1] == (u32)tag + k) ? rv[0] - T0 : rt; \
      const float sm = row16_sum((float)(int)dt); \
      const int o = (int)((RSH ? rdlane(sm, 0) * (1.f / 16.f) : (rdlane(sm, 0) + rdlane(sm, 16)) * (1.f / 32.f))) - (mode ? CI(due_x(rtab[k], 1)) : CI(due_x(rtab[k], 0))); \
      if (w == ((5 * k + 11) & 15) && lane == 0 && o > -300 && o < (k < NOACT ? -5000 : 2000)) { \
        int e = o - (int)org - (int)((osl * cmin(rtab[k], NK)) >> 4) - dtab[k]; if (e < 0) e = 0; \
        int sl_ = ((int)osl * gtab[k]) / 16 + ((e > EBIG ? 0 : e) * btab[k]) / rgap(k); sl_ = sl_ > SLMAX ? SLMAX : sl_; \
        if (xtab[k]) lorg[2] = (int)(rdlane(sm, 16) * (1.f / 16.f)) > xtab[k]; \
        __hip_atomic_store(reinterpret_cast<u64*>(lorg), __builtin_bit_cast(u64, u32x2{(u32)((int)org + e + ((((int)osl - sl_) * cmin(n, NK)) >> 4)), (u32)sl_}), __ATOMIC_RELAXED, __HIP_MEMORY_SCOPE_WORKGROUP); } } \
    if constexpr (rnd_at(n) >= 0) { constexpr int k = rnd_at(n); \
      u32 tot = 0; \
      if (ci == k % ACU && (h & ((1 << RSH) - 1)) == (k & ((1 << RSH) - 1))) { const u32 mine = (1u << 24) + ((rt32() - toff - T0) & 0xfffffu); \
        if (lane == 0) tot = __hip_atomic_fetch_add(lorg + 8 + k, mine, __ATOMIC_RELAXED, __HIP_MEMORY_SCOPE_WORKGROUP) + mine; } \
      if (PFULL) tot = lane < PFULL ? __builtin_amdgcn_readfirstlane(tot) : 0u; \
      u32x2 pv_ = {T0 + ((tot & 0xffffffu) >> 4), (u32)tag + k}; int po_ = (tot >> 24) == (k < NOPOST ? 99u : 16u) ? (int)WS_RPT + (h >> RSH) * 4352 : OOB; asm volatile("" : "+v"(pv_), "+v"(po_)); \
      EXM(1, __builtin_amdgcn_raw_buffer_store_b64(pv_, rs, po_, 128 * k * 35, PAUX)); } \
    if constexpr (rnd_at((n) - RD) >= 0) { constexpr int k = rnd_at((n) - RD); \
      int ro_ = (w == ((5 * k + 11) & 15) && lane < (xtab[k] ? 32 : 16)) ? (int)WS_RPT + lane * 4352 : OOB; asm volatile("" : "+v"(ro_)); \
      EXM(w == ((5 * k + 11) & 15) ? 0xffffffffu : 1u, rv = __builtin_amdgcn_raw_buffer_load_b64(rs, ro_, 128 * k * 35, RAUX)); rt = rt32() - T0 - MISS; } \
  } } while (0)
  u32x4 Wr[8], xf;
  u32 pf2 = 0, pf3 = 0, pf4 = 0;
  auto touch = [&]() __attribute__((always_inline)) {
    const int k = ci < 2 ? ci : (ci - 2) * 4 + (h & 3) + 2;
    const __attribute__((address_space(4))) u64* ap = (const __attribute__((address_space(4))) u64*)__builtin_amdgcn_kernarg_segment_ptr();
    const u64 p = ap[(0xCBA96421u >> (4 * (k & 7))) & 15u];
    const int len = k < 2 ? 1 << 27 : k == 3 ? 1 << 24 : k == 4 ? DC * 8192 : k == 6 ? (int)WS_END : k == 7 ? 9216 : DM * 8192;
    const int o = (k < 2 ? (lane << 25) + (h << 20) + lane * 4352 : (lane << 21)) + (idx & 31) * 256;
    pf3 = __builtin_amdgcn_raw_buffer_load_b32(__builtin_amdgcn_make_buffer_rsrc(reinterpret_cast<void*>(p), 0, (w == TPW && k < TPN) ? len : 0, 0x00020000), lane < 8 ? o : OOB, 0, 2);
  };
  if (!KD0) A_ISSUE(0);
  __builtin_amdgcn_sched_barrier(0);
  if (prod) {
#pragma unroll
    for (int k = 0; k < 8; ++k) Wr[k] = __builtin_amdgcn_raw_buffer_load_b128(rw, wqo, k * 8192, 2);
    xf = __builtin_amdgcn_raw_buffer_load_b128(rx, xo, 0, 2);
  }
  __builtin_amdgcn_sched_barrier(0);
#define PF2() do { const __amdgpu_buffer_rsrc_t rp_ = __builtin_amdgcn_make_buffer_rsrc(const_cast<u16*>(pk == 0 ? a.wqB : pk == 1 ? a.wuk : a.wuv), 0, pk ? DC * 8192 : DM * 8192, 0x00020000); \
    EXM(0xffffffffu, pf2 = __builtin_amdgcn_raw_buffer_load_b32(rp_, pfo, 0, 2); __builtin_amdgcn_sched_barrier(0); pf4 = __builtin_amdgcn_raw_buffer_load_b32(rp_, pfo, 0, 0)); } while (0)
#define TWAIT(d) do { while ((int)(rt32() - T0 - (d)) < 0) __builtin_amdgcn_s_sleep(1); __builtin_amdgcn_sched_barrier(0); } while (0)
  if (SLN) PF2();
  if (TPS == 0) touch();
  if (!K1G) A_ISSUE(1);
  __builtin_amdgcn_raw_buffer_store_b64(u32x2{T0, (u32)tag + 15u}, rs, (ci == 6 && (h & 1) == 0 && tid == 0) ? (int)WS_RPT + (h >> 1) * 4352 : OOB, 128 * 15 * 35, 16);
  __builtin_amdgcn_sched_barrier(0);
  if (tid < 32) lorg[tid] = tid == 0 ? ORG0 : tid == 2 ? T0 : 0u;
  __syncthreads();
  T0 = __builtin_amdgcn_readfirstlane(lorg[2]);
  if (KD0) { TWAIT(KD0 + ((w * KDS) >> 4)); A_ISSUE(0); last = rt32(); }
  __builtin_amdgcn_sched_barrier(0);
  if (prod) {
    float* qpart = reinterpret_cast<float*>(lds + L_BIG);
#pragma unroll
    for (int hf = 0; hf < 2; ++hf) {
      f32x4 d4[4];
#pragma unroll
      for (int t = 0; t < 4; ++t) d4[t] = mfma16(xf, tcol16(Wr, 4 * hf + t), f32x4{0.f, 0.f, 0.f, 0.f});
      if (g == (j >> 3)) {
#pragma unroll
        for (int r = 0; r < 4; ++r) *reinterpret_cast<f32x4*>(qpart + ((w * 2 + g) * 4 + r) * 64 + 8 * (j & 7) + 4 * hf) = f32x4{d4[0][r], d4[1][r], d4[2][r], d4[3][r]};
      }
    }
    asm volatile("" ::: "memory");
    u32 arr = 0;
    if (lane == 0) arr = __hip_atomic_fetch_add(lorg + 4, 1u, __ATOMIC_RELAXED, __HIP_MEMORY_SCOPE_WORKGROUP);
    if (__builtin_amdgcn_readfirstlane(arr) == 9 / NP - 1) {
      asm volatile("" ::: "memory");
      f32x4 s0 = {0.f, 0.f, 0.f, 0.f};
#pragma unroll
      for (int k = 0; k < 18 / NP; ++k) s0 += *reinterpret_cast<const f32x4*>(qpart + k * 256 + 4 * lane);
      const int qo = (int)WS_QA + (h * 8 + ci) * 2048 + lane * 16;
      __builtin_amdgcn_raw_buffer_store_b128(u32x4{__float_as_uint(s0[0]), tg, __float_as_uint(s0[1]), tg}, rs, qo, 0, QSA);
      __builtin_amdgcn_raw_buffer_store_b128(u32x4{__float_as_uint(s0[2]), tg, __float_as_uint(s0[3]), tg}, rs, qo + 1024, 0, QSA);
    }
  }
  static_for<RING - 2 + K1G>([&](auto tc) __attribute__((always_inline)) {
    constexpr int t = tc.value + 2 - K1G;
    u32 now = rt32();
    const u32 org = __builtin_amdgcn_readfirstlane(__hip_atomic_load(lorg, __ATOMIC_RELAXED, __HIP_MEMORY_SCOPE_WORKGROUP)), osl = 0;
    GATE(t);
    A_ISSUE(t);
    if constexpr (t == TPS) touch();
    __builtin_amdgcn_sched_barrier(0);
    if constexpr (t == SP) {
      if (w == 13) {
        const u32x2 sv = __builtin_amdgcn_raw_buffer_load_b64(rs, lane < 16 ? (int)WS_RPT + lane * 4352 : OOB, 128 * 15 * 35, SAUX);
        const bool ok = sv[1] == (u32)tag + 15u;
        const float sm = row16_sum(ok ? (float)(int)(sv[0] - T0) : 0.f), cn = row16_sum(ok ? 1.f : 0.f);
        const int o = ORG0 + (cn > 0.f ? (int)(sm / cn) : 0);
        if (lane == 0 && o > -300 && o < 1000) lorg[0] = (u32)o;
      }
    }
    if constexpr (t == QP) {
      if (w >= 14) {
        const int qo = (int)WS_QA + (h * 8 + (w & 1)) * 2048 + lane * 16;
        u32x4 v[2 * NP];
#pragma unroll 1
        for (u32 spins = 0; spins < SPIN_CAP; ++spins) {
          bool ok = true;
          if (spins < QTRY) {
#pragma unroll
            for (int k = 0; k < 2 * NP; ++k) v[k] = __builtin_amdgcn_raw_buffer_load_b128(rs, qo, (k >> 1) * 4096 + (k & 1) * 1024, QAUX);
          } else {
#pragma unroll
            for (int k = 0; k < 2 * NP; ++k) v[k] = __builtin_amdgcn_raw_buffer_load_b128(rs, qo, (k >> 1) * 4096 + (k & 1) * 1024, 16);
          }
#pragma unroll
          for (int k = 0; k < 2 * NP; ++k) ok = ok && v[k][1] == tg && v[k][3] == tg;
          if (__all(ok)) break;
          __builtin_amdgcn_s_sleep(4);
        }
        f32x4 s0 = {0.f, 0.f, 0.f, 0.f};
#pragma unroll
        for (int k = 0; k < NP; ++k) s0 += f32x4{__uint_as_float(v[2 * k][0]), __uint_as_float(v[2 * k][2]), __uint_as_float(v[2 * k + 1][0]), __uint_as_float(v[2 * k + 1][2])};
        *reinterpret_cast<u32x2*>(lds + L_QS + (g * 128 + (w & 1) * 64 + 4 * j) * 2) = u32x2{pk_bf16(s0[0], s0[1]), pk_bf16(s0[2], s0[3])};
        asm volatile("" ::: "memory");
        if (lane == 0) __hip_atomic_fetch_add(lorg + 5, 1u, __ATOMIC_RELAXED, __HIP_MEMORY_SCOPE_WORKGROUP);
      }
    }
    __builtin_amdgcn_sched_barrier(0);
  });
  while (__hip_atomic_load(lorg + 5, __ATOMIC_RELAXED, __HIP_MEMORY_SCOPE_WORKGROUP) != 2u) __builtin_amdgcn_s_sleep(1);
  asm volatile("" :: "v"(pf), "v"(pf2), "v"(pf4) : "memory");
  float* wtab = reinterpret_cast<float*>(lds + L_ST + 1024);
  u32x2 xv = {0u, 0u};
  const int xoff = (w == XW && lane < 8 * ACU) ? (int)WS_MLA + (h * 8 + (lane >> 3)) * 128 + (lane & 7) * 8 : OOB;
  float* pml = reinterpret_cast<float*>(lds + L_ST);
  u32x4 q;
  u32 pb0 = 0, pb1 = 0;
  float acc[8], S[4];
  u32x4 RW[2][8];
  u32x4 WT[4][4];
  float Y[4];
  auto project = [&](int b) __attribute__((always_inline)) {
    f32x4 d4[4];
#pragma unroll
    for (int t = 0; t < 4; ++t) d4[t] = f32x4{0.f, 0.f, 0.f, 0.f};
    const f32x4 wt = *reinterpret_cast<const f32x4*>(wtab + b * 16 + 4 * g);
#pragma unroll
    for (int s = 0; s < 4; ++s) {
      const u32x4 af = *reinterpret_cast<const u32x4*>(lds + L_AB + (b * 16 + j) * 272 + (32 * s + 8 * g) * 2);
#pragma unroll
      for (int t = 0; t < 4; ++t) d4[t] = mfma16(af, WT[s][t], d4[t]);
    }
    float y[4];
#pragma unroll
    for (int t = 0; t < 4; ++t) y[t] = fmaf(d4[t][0], wt[0], d4[t][1] * wt[1]) + fmaf(d4[t][2], wt[2], d4[t][3] * wt[3]);
    Y[b] = sw16(sw32(y[0], y[2]), sw32(y[1], y[3]));
  };
  const __amdgpu_buffer_rsrc_t rwo = __builtin_amdgcn_make_buffer_rsrc(const_cast<u16*>(a.wo + (size_t)h * 128 * DM), 0, w < 9 ? 128 * DM * 2 : 0, 0x00020000);
  int wvo = ((32 * (j >> 3) + 8 * g) * DM + 64 * w + 8 * (j & 7)) * 2;
  asm volatile("" : "+v"(wvo));
  const u64 wmask = w < 9 ? ~0ull : 1ull;
#define WO_ISSUE() static_for<16>([&](auto kc) __attribute__((always_inline)) { constexpr int k = kc.value; \
    if constexpr (wo_step(k) == i) EXM(wmask, RW[k >> 3][k & 7] = __builtin_amdgcn_raw_buffer_load_b128(rwo, wvo, (64 * (k >> 3) + (k & 7)) * DM * 2, 2)); })
#define ISSUE_STEP() do { if constexpr (issued(i + 1) > issued(i)) { GATE(issued(i)); if constexpr (gf) { if (!SMO || i >= D3FROM - 1 || issued(i) % 10 != 0 || xtra) asm volatile("" :: "v"(r)); __builtin_amdgcn_sched_barrier(0); } \
    if (WOF) WO_ISSUE(); A_ISSUE(issued(i)); if (!WOF) WO_ISSUE(); if constexpr (gf) __builtin_amdgcn_sched_barrier(0); CTRL(issued(i)); \
    if constexpr (issued(i + 1) > issued(i) + 1) { GATE(issued(i) + 1); A_ISSUE(issued(i) + 1); CTRL(issued(i) + 1); } } else { WO_ISSUE(); } } while (0)
  static_for<NSEQ>([&](auto ic) __attribute__((always_inline)) {
    constexpr int i = ic.value, bb = (i / NS) & 3, pass = i / (4 * NS), sl = i % NS;
    u32 now = 0; if (PACE) now = rt32();
    u32 org = 0, osl = 0; if (PACE) { const u32x2 ov = __builtin_bit_cast(u32x2, __hip_atomic_load(reinterpret_cast<u64*>(lorg), __ATOMIC_RELAXED, __HIP_MEMORY_SCOPE_WORKGROUP)); org = __builtin_amdgcn_readfirstlane(ov[0]); osl = __builtin_amdgcn_readfirstlane(ov[1]); }
    if constexpr (BMODE && issued(i) == NB && issued(i + 1) > NB) mode = BMODE == 1 || __builtin_amdgcn_readfirstlane(__hip_atomic_load(lorg + 2, __ATOMIC_RELAXED, __HIP_MEMORY_SCOPE_WORKGROUP)) == 1u;
    __builtin_amdgcn_sched_barrier(0);
    const u32x4 r = R[i % RING];
    constexpr bool gf = i >= GF0 && i < GF1;
    if constexpr (BMODE && issued(i) == NB && issued(i + 1) > NB) {
      u64 bp = 0; int bl = 0, co = OOB;
      if (__builtin_expect(mode, 0)) {
        constexpr int CL = (8 - TF) * 512, N0 = 4 * ((CL + LN - 1) / LN), N1 = N0 + (4608 + LN - 1) / LN;
        const int id = w * 28 + (idx >> 3), l = (id < N0 ? id >> 2 : id < N1 ? id - N0 : id - N1) * LN + lane;
        bp = ((const __attribute__((address_space(4))) u64*)__builtin_amdgcn_kernarg_segment_ptr())[id < N0 ? 6 : id < N1 ? 10 : 9];
        bl = id < N0 ? 1 << 24 : id < N1 ? DM * 8192 : DC * 8192;
        co = l >= (id < N0 ? CL : 4608) ? OOB : id < N0 ? ((idx & 6) << 21) + ((idx & 1) << 19) + ((id & 3) << 20) + (TF << 16) + l * 128 : id < N1 ? (idx & 7) * 589824 + l * 128 : (l >> 3) * 8192 + (idx & 7) * 1024 + (l & 7) * 128;
        TWAIT(org + ((osl * NB) >> 4) + CI(due_at(NB)) + ((w * BS) >> 4)); last = T0 + 100000u; now = rt32();
      }
      asm volatile("" : "+v"(co));
      const __amdgpu_buffer_rsrc_t rc = __builtin_amdgcn_make_buffer_rsrc(reinterpret_cast<void*>(bp), 0, bl, 0x00020000);
      EXM(mode ? (1ull << LN) - 1ull : 1ull, pb0 = __builtin_amdgcn_raw_buffer_load_b32(rc, co, 0, 2); __builtin_amdgcn_sched_barrier(0); pb1 = __builtin_amdgcn_raw_buffer_load_b32(rc, co, 0, 0));
    }
    if constexpr (BMODE && i == NB + 1) asm volatile("" :: "v"(pb0), "v"(pb1));
    if constexpr (gf) { ISSUE_STEP(); }
    if constexpr (i == TPC) asm volatile("" :: "v"(pf3));
    if constexpr (pass == 0) {
      if constexpr (sl == 0) { q = *reinterpret_cast<const u32x4*>(lds + L_QS + (bb * 128 + 8 * j) * 2); S[bb] = -INFINITY; }
      float d = dot2(r[0], q[0], 0.f); d = dot2(r[1], q[1], d); d = dot2(r[2], q[2], d); d = dot2(r[3], q[3], d);
      asm volatile("" : "+v"(d));
      if constexpr (!gf) { ISSUE_STEP(); }
      d = row16_sum(d);
      S[bb] = (j == sl) ? d : S[bb];
      if constexpr (sl == NS - 1) {
        const float sc = (j == bad_sl) ? -INFINITY : S[bb] * C2;
        const float m = xg_max(row16_max(sc));
        S[bb] = __builtin_amdgcn_exp2f(sc - m);
        const float l = xg_sum(row16_sum(S[bb]));
        if (lane == 0) *reinterpret_cast<f32x2*>(pml + (bb * 16 + w) * 2) = f32x2{m, l};
      }
      if constexpr (i == 4 * NS - 1) {
        asm volatile("" ::: "memory");
        u32 arr = 0;
        if (lane == 0) arr = __hip_atomic_fetch_add(lorg + 6, 1u, __ATOMIC_RELAXED, __HIP_MEMORY_SCOPE_WORKGROUP);
        arr = __builtin_amdgcn_readfirstlane(arr);
        asm volatile("" ::: "memory");
        const f32x2 ml = *reinterpret_cast<const f32x2*>(pml + lane * 2);
        const float M = row16_max(ml[0]);
        const float L = row16_sum(__builtin_amdgcn_exp2f(ml[0] - M) * ml[1]);
        __builtin_amdgcn_raw_buffer_store_b64(u32x2{__float_as_uint(j ? L : M), tg}, rs, (arr == 15u && j < 2) ? (int)WS_MLA + (h * 8 + ci) * 128 + g * 16 + j * 8 : OOB, 0, 16);
      }
    } else {
      const float p = dpp<0x150 + sl>(S[bb]);
      if constexpr (sl == 0) {
#pragma unroll
        for (int k = 0; k < 8; ++k) acc[k] = 0.f;
      }
#pragma unroll
      for (int k = 0; k < 4; ++k) { acc[2 * k] = fmaf(p, bf_lo(r[k]), acc[2 * k]); acc[2 * k + 1] = fmaf(p, bf_hi(r[k]), acc[2 * k + 1]); }
      asm volatile("" : "+v"(acc[0]), "+v"(acc[1]), "+v"(acc[2]), "+v"(acc[3]), "+v"(acc[4]), "+v"(acc[5]), "+v"(acc[6]), "+v"(acc[7]));
      if constexpr (!gf) { ISSUE_STEP(); }
      if constexpr (i == XR) xv = __builtin_amdgcn_raw_buffer_load_b64(rs, xoff, 0, 16);
      if constexpr (i == XR + 2) {
        if (w == XW) {
          if (__builtin_expect(!__all(lane >= 8 * ACU || xv[1] == tg), 0)) {
#pragma unroll 1
            for (u32 spins = 0; spins < SPIN_CAP; ++spins) {
              __builtin_amdgcn_s_sleep(8); xv = __builtin_amdgcn_raw_buffer_load_b64(rs, xoff, 0, 16);
              if (__all(lane >= 8 * ACU || xv[1] == tg)) break;
            }
          }
          const float x = __uint_as_float(xv[0]);
          const float m = lane < 8 * ACU ? dpp<0xA0>(x) : -INFINITY, l = lane < 8 * ACU ? dpp<0xF5>(x) : 0.f;
          const float M = xg_max(fmaxf(m, dpp<0x128>(m)));
          float t = __builtin_amdgcn_exp2f(m - M) * l; t = xg_sum(t + dpp<0x128>(t));
          if (lane < 8 && !(lane & 1)) *reinterpret_cast<f32x2*>(wtab + 64 + lane) = f32x2{M, 1.f / t};
          asm volatile("" ::: "memory");
          const f32x2 gm = *reinterpret_cast<const f32x2*>(wtab + 64 + 2 * g);
          wtab[lane] = __builtin_amdgcn_exp2f(pml[lane * 2] - gm[0]) * gm[1];
          asm volatile("" ::: "memory");
          if (lane == 0) __hip_atomic_store(lorg + 7, 1u, __ATOMIC_RELAXED, __HIP_MEMORY_SCOPE_WORKGROUP);
        }
      }
      if constexpr ((i >= WTS0 && i < WTS0 + 4) || (i >= WTS && i < WTS + 4)) {
        constexpr int s = i >= WTS, t = i - (s ? WTS : WTS0);
        const u32x4 lo = tcol16(RW[s], t), hi = tcol16(RW[s], t + 4);
#pragma unroll
        for (int c = 0; c < 4; ++c) {
          WT[2 * s][t][c] = __builtin_amdgcn_update_dpp(lo[c], hi[c], 0x128, 0xf, 0xc, false);
          WT[2 * s + 1][t][c] = __builtin_amdgcn_update_dpp(hi[c], lo[c], 0x128, 0xf, 0x3, false);
        }
        asm volatile("" : "+v"(WT[2 * s][t]), "+v"(WT[2 * s + 1][t]));
      }
      if constexpr (i == PJ0 || i == PJ1 || i == PJ2) {
        constexpr int b = i == PJ0 ? 0 : i == PJ1 ? 1 : 2;
        Y[b] = 0.f;
        if (w < 9) {
          while (__hip_atomic_load(lorg + 24 + b, __ATOMIC_RELAXED, __HIP_MEMORY_SCOPE_WORKGROUP) != 16u || __hip_atomic_load(lorg + 7, __ATOMIC_RELAXED, __HIP_MEMORY_SCOPE_WORKGROUP) == 0u) __builtin_amdgcn_s_sleep(1);
          asm volatile("" ::: "memory");
          project(b);
        }
        asm volatile("" : "+v"(Y[b]));
      }
      if constexpr (sl == NS - 1) {
        const float t0 = sw16(sw32(acc[0], acc[4]), sw32(acc[2], acc[6])), t1 = sw16(sw32(acc[1], acc[5]), sw32(acc[3], acc[7]));
        *reinterpret_cast<u32*>(lds + L_AB + (bb * 16 + w) * 272 + 16 * j + 4 * g) = pk_bf16(t0, t1);
        if constexpr (bb < 3) { asm volatile("" ::: "memory"); if (lane == 0) __hip_atomic_fetch_add(lorg + 24 + bb, 1u, __ATOMIC_RELAXED, __HIP_MEMORY_SCOPE_WORKGROUP); }
      }
    }
    __builtin_amdgcn_sched_barrier(0);
  });
#undef A_ISSUE
  __syncthreads();
  if (w < 9) {
    project(3);
    st16_sc1(rs, WS_Y + ((8 * w + (j & 7)) * 256 + idx) * 128 + ((j >> 3) * 4 + g) * 16, u32x4{pk_bf16(Y[0], Y[1]), tg, pk_bf16(Y[2], Y[3]), tg});
  }
  if (idx < 72) {
    __builtin_amdgcn_sched_barrier(0);
    __builtin_amdgcn_s_barrier();
    __builtin_amdgcn_s_sleep(FDLY);
    const int yo = (int)WS_Y + (idx * 256 + 16 * w) * 128 + lane * 16;
    u32x4 v0, v1;
#pragma unroll 1
    for (u32 spins = 0; spins < SPIN_CAP; ++spins) {
      v0 = __builtin_amdgcn_raw_buffer_load_b128(rs, yo, 0, 16); v1 = __builtin_amdgcn_raw_buffer_load_b128(rs, yo + 1024, 0, 16);
      if (__all(v0[1] == tg && v0[3] == tg && v1[1] == tg && v1[3] == tg)) break;
      __builtin_amdgcn_s_sleep(FSL);
    }
    float y = sw16(sw32(bf_lo(v0[0]) + bf_lo(v1[0]), bf_lo(v0[2]) + bf_lo(v1[2])), sw32(bf_hi(v0[0]) + bf_hi(v1[0]), bf_hi(v0[2]) + bf_hi(v1[2])));
    y += dpp<0x128>(y);
    float* red = reinterpret_cast<float*>(lds + L_PF);
    if (!(lane & 8)) red[((lane & 7) * 4 + g) * 20 + w] = y;
    __syncthreads();
    if (tid < 32) {
      const f32x4 r0 = *reinterpret_cast<const f32x4*>(red + tid * 20), r1 = *reinterpret_cast<const f32x4*>(red + tid * 20 + 4);
      const f32x4 r2 = *reinterpret_cast<const f32x4*>(red + tid * 20 + 8), r3 = *reinterpret_cast<const f32x4*>(red + tid * 20 + 12);
      const f32x4 r = r0 + r1 + r2;
      const __amdgpu_buffer_rsrc_t ro = __builtin_amdgcn_make_buffer_rsrc(a.out, 0, 4 * 1152 * 2, 0x00020000);
      __builtin_amdgcn_raw_buffer_store_b16(to_bf16(((r[0] + r[1]) + (r[2] + r[3])) + (r3[0] + r3[1])), ro, ((tid & 3) * 1152 + idx * 8 + (tid >> 2)) * 2, 0, OAUX);
      __builtin_amdgcn_raw_buffer_store_b16(to_bf16(r3[2] + r3[3]), ro, ((tid >> 3) * 1152 + 576 + idx * 8 + (tid & 7)) * 2, 0, OAUX);
    }
  }
}

#define CROW 1
constexpr int CW = CROW ? 1 : 8, CR = CROW ? 8 : 1;
#define IA 2
#define IB 4
#define IC 6
#define EUV 4
#define YOFF 90
#define WPD 4
#define CQ 0
#define CLAY 1
DI int ctx_row(int p, int hd) {
  if (CLAY == 1) return (int)WS_CTXP + ((p >> 3) << 18) + ((hd >> 2) << 15) + ((p & 7) << 12) + ((hd & 3) << 10);
  if (CLAY == 2) return (int)WS_CTXP + ((p >> 4) << 19) + ((hd >> 1) << 15) + (((p & 15) ^ (hd >> 4)) << 11) + ((hd & 1) << 10);
  if (CLAY == 3) return (int)WS_CTXP + ((p >> 2) << 17) + ((hd >> 3) << 15) + ((p & 3) << 13) + ((hd & 7) << 10);
  return (int)WS_CTXP + (p * 32 + hd) * 1024;
}
template <bool LD> DI void part_b(const Args& a, unsigned char* lds, int wg, u64 tag, u32 pf) {
  const int tid = threadIdx.x, lane = tid & 63, w = __builtin_amdgcn_readfirstlane(tid >> 6);
  const int j = lane & 15, g = lane >> 4;
  const __amdgpu_buffer_rsrc_t rs = __builtin_amdgcn_make_buffer_rsrc(a.ws, 0, (int)WS_END, 0x00020000);
  const u32 tb0 = rt32();
  const int h = BPF ? (wg & 7) * 4 + (wg >> 3) : wg;
  const int h3 = h ^ 2;
  const int b2 = BMAP ? (wg & 7) >> 1 : wg >> 3, sp = BMAP ? (wg >> 3) * 2 + (wg & 1) : wg & 7;
  const u16* cl = a.C + ((size_t)b2 * SK + sp * 512 + CW * (w - 8)) * DC + lane * 8;
  u32x4 X[16];
  u32x2 RW[4][8];
  u32x4 W8[2];
  u32* cnt = reinterpret_cast<u32*>(lds + L_ST + 4064);
#define B_POST(T) _Pragma("unroll") for (int k = 0; k < NRND; ++k) if (mtab[k] == (T) && xtab[k]) { u32x2 pv_ = {rt32(), (u32)tag + k}; int po_ = (w == 8 && !((wg ^ (wg >> 3)) & 1)) ? (int)WS_RPT + (16 + (wg >> 1)) * 4352 + 128 * 35 * k : OOB; \
    asm volatile("" : "+v"(pv_), "+v"(po_)); EXM(1, __builtin_amdgcn_raw_buffer_store_b64(pv_, rs, po_, 0, 16)); }
#define C_ISSUE(T) _Pragma("unroll") for (int r = 0; r < 8; ++r) X[8 * ((T) & 1) + r] = ld16(cl + (size_t)(64 * (T) + CR * r) * DC)
#define C_HALF(T, hf) _Pragma("unroll") for (int r = 4 * (hf); r < 4 * (hf) + 4; ++r) X[8 * ((T) & 1) + r] = ld16(cl + (size_t)(64 * (T) + CR * r) * DC)
#define L_PART(T, r0, r1) do { __builtin_amdgcn_sched_barrier(0); \
    if ((T) + 3 < 8) { _Pragma("unroll") for (int r = r0; r < r1; ++r) X[8 * (((T) + 3) & 1) + r] = ld16(cl + (size_t)(64 * ((T) + 3) + CR * r) * DC); } \
    if ((T) == 5 || (T) == 6) { _Pragma("unroll") for (int k = 2 * (r0); k < 2 * (r1); ++k) \
      RW[2 * (((T) - 5) & 1) + (k >> 3)][k & 7] = ld8(a.woB + ((size_t)h3 * 128 + 32 * (2 * (((T) - 5) & 1) + (k >> 3)) + 8 * g + (k & 7)) * DM + 64 * (w - 8) + 4 * j); } \
    __builtin_amdgcn_sched_barrier(0); } while (0)
#define UV_LOAD(n) do { const u32x4 v_ = ld16(a.wuv + ((size_t)(64 * w + 32 * ((n) >> 3) + 8 * g + ((n) & 7)) * H + h3) * 128 + 8 * j); \
    RW[2 * ((n) >> 3)][(n) & 7] = u32x2{v_[0], v_[1]}; RW[2 * ((n) >> 3) + 1][(n) & 7] = u32x2{v_[2], v_[3]}; } while (0)
#define C_WIN(T, r0, r1) _Pragma("unroll") for (int r = r0; r < r1; ++r) { asm volatile("s_waitcnt vmcnt(%0)" :: "n"(WPD - 1) : "memory"); X[8 * ((T) & 1) + r] = ld16(cl + (size_t)(64 * (T) + CR * r) * DC); __builtin_amdgcn_sched_barrier(0); }
  u32 sb0 = L_BIG + CW * (w - 8) * CRS + lane * 16, sb1 = sb0 + CBUF;
  asm volatile("" : "+v"(sb0), "+v"(sb1));
#define C_STAGE(T) _Pragma("unroll") for (int r = 0; r < 8; ++r) *reinterpret_cast<u32x4*>(lds + (((T) & 1) ? sb1 : sb0) + CR * r * CRS) = X[8 * ((T) & 1) + r]
  {
    const u16* Wq = a.wqB + h * 128;
    u32x4 U[8];
    QPre Q;
    qproj_load(Q, a.xB, Wq, w, lane);
#pragma unroll
    for (int i = 0; i < 8; ++i) U[i] = ld16(a.wuk + ((size_t)(32 * w + 4 * i + (j >> 2)) * H + h) * 128 + 8 * (4 * g + (j & 3)));
    __builtin_amdgcn_sched_barrier(0);
    if constexpr (!LD) am_zero(lds, tid);
    qproj_mfma(Q, lds, w, lane);
    asm volatile("" :: "v"(pf));
    __syncthreads();
    if constexpr (LD) { C_HALF(0, 0); }
    else {
      const float* qpart = reinterpret_cast<const float*>(lds + L_BIG);
      float s = 0.f;
#pragma unroll
      for (int ww = 0; ww < 16; ++ww) s += qpart[ww * 512 + tid];
      reinterpret_cast<u16*>(lds + L_QS)[tid] = to_bf16(s);
    }
    __builtin_amdgcn_sched_barrier(0);
    __syncthreads();
    const u32x4 af = *reinterpret_cast<const u32x4*>(lds + L_QS + ((j >> 2) * 128 + (4 * g + (j & 3)) * 8) * 2);
    u16* qa = reinterpret_cast<u16*>(lds + L_PF);
    float v2[2] = {0.f, 0.f};
#pragma unroll
    for (int i = 0; i < 8; ++i) {
      const f32x4 d4 = mfma16(af, U[i], f32x4{0.f, 0.f, 0.f, 0.f});
      float d = (j & 2) ? ((j & 1) ? d4[3] : d4[2]) : ((j & 1) ? d4[1] : d4[0]);
      d += dpp<0xB1>(d); d += dpp<0x4E>(d);
      v2[i >> 2] = ((j & 3) == (i & 3)) ? d : v2[i >> 2];
    }
    qa[g * 512 + 32 * w + 4 * (j & 3) + (j >> 2)] = to_bf16(v2[0]); qa[g * 512 + 32 * w + 16 + 4 * (j & 3) + (j >> 2)] = to_bf16(v2[1]);
    __builtin_amdgcn_sched_barrier(0);
    __syncthreads();
    if constexpr (!LD) {
      if (w == 0) {
#pragma unroll
        for (int bb = 0; bb < 4; ++bb) st16_sc1(rs, WS_QABS + ((bb * H + h) * 512 + lane * 8) * 2, *reinterpret_cast<const u32x4*>(qa + bb * 512 + lane * 8));
      }
      __builtin_amdgcn_sched_barrier(0);
      __builtin_amdgcn_s_barrier();
      if (w == 0) {
        asm volatile("s_waitcnt vmcnt(0)" ::: "memory");
        if (lane == 0) { __hip_atomic_store(reinterpret_cast<u64*>(a.ws + WS_F1 + wg * FST), tag, RLX_AGENT); cnt[0] = 0u; cnt[1] = 0u; }
      }
    } else {
      __builtin_amdgcn_s_barrier();
      C_WIN(0, 4, 8); C_WIN(1, 0, 8);
      C_STAGE(0);
      __builtin_amdgcn_sched_barrier(0);
      C_WIN(2, 0, 8);
    }
  }
  {
    const int b = b2;
    if constexpr (!LD) wg_wait64(a.ws + WS_F1, tag); else __syncthreads();
    const int kt = w & 3, hh = (w >> 2) & 1;
    float* stmax = reinterpret_cast<float*>(lds + L_ST);
    float* stsum = stmax + 128;
    float* salpha = stmax + 256;
    unsigned char* Pf = lds + L_PF;
    float M = -INFINITY, lsum = 0.f;
    f32x4 acc[2][2];
#pragma unroll
    for (int ct = 0; ct < 2; ++ct) { acc[ct][0] = f32x4{0.f, 0.f, 0.f, 0.f}; acc[ct][1] = f32x4{0.f, 0.f, 0.f, 0.f}; }
    const int troff = (4 * g + (j >> 2)) * CRS + (32 * w + 4 * (j & 3)) * 2;
    auto pv_step = [&](const unsigned char* cb) __attribute__((always_inline)) {
      const f32x4 al0 = *reinterpret_cast<const f32x4*>(salpha + 4 * g), al1 = *reinterpret_cast<const f32x4*>(salpha + 16 + 4 * g);
#pragma unroll
      for (int ct = 0; ct < 2; ++ct) { acc[ct][0] *= al0; acc[ct][1] *= al1; }
#pragma unroll
      for (int blk = 0; blk < 2; ++blk) {
        const u32x4 p0 = *reinterpret_cast<const u32x4*>(Pf + ((blk * 2 + 0) * 64 + lane) * 16);
        const u32x4 p1 = *reinterpret_cast<const u32x4*>(Pf + ((blk * 2 + 1) * 64 + lane) * 16);
#pragma unroll
        for (int ct = 0; ct < 2; ++ct) {
          const u32x2 lo = trread(cb + troff + (32 * blk) * CRS + 32 * ct);
          const u32x2 hi = trread(cb + troff + (32 * blk + 16) * CRS + 32 * ct);
          const u32x4 bf = {lo[0], lo[1], hi[0], hi[1]};
          acc[ct][0] = mfma16(p0, bf, acc[ct][0]); acc[ct][1] = mfma16(p1, bf, acc[ct][1]);
        }
      }
    };
    unsigned char* Qf = lds + L_BIG + CBUF;
    if constexpr (!LD) {
      u32x4 qv[4];
#pragma unroll
      for (int k = 0; k < 4; ++k) qv[k] = __builtin_amdgcn_raw_buffer_load_b128(rs, (int)WS_QABS + (b * H * 512 + (tid + 512 * k) * 8) * 2, 0, 16);
#pragma unroll
      for (int k = 0; k < 4; ++k) { const int e = tid + 512 * k, head = e >> 6, ch = e & 63;
        *reinterpret_cast<u32x4*>(Qf + ((head >> 4) * 16 + (ch >> 2)) * 1040 + ((ch & 3) * 16 + (head & 15)) * 16) = qv[k]; }
    }
    __syncthreads();
    if constexpr (!LD) {
      u32x4 QF[16];
#pragma unroll
      for (int s = 0; s < 16; ++s) QF[s] = *reinterpret_cast<const u32x4*>(Qf + (hh * 16 + s) * 1040 + lane * 16);
      auto s_tile = [&](const unsigned char* cb, bool last) __attribute__((always_inline)) {
        __syncthreads();
        f32x4 s0 = {0.f, 0.f, 0.f, 0.f}, s1 = {0.f, 0.f, 0.f, 0.f};
#pragma unroll
        for (int s = 0; s < 16; s += 2) {
          s0 = mfma16(*reinterpret_cast<const u32x4*>(cb + (16 * kt + j) * CRS + (32 * s + 8 * g) * 2), QF[s], s0);
          s1 = mfma16(*reinterpret_cast<const u32x4*>(cb + (16 * kt + j) * CRS + (32 * s + 32 + 8 * g) * 2), QF[s + 1], s1);
        }
        const f32x4 sv = (s0 + s1) * C2;
        if (last) { __builtin_amdgcn_sched_barrier(0); _Pragma("unroll") for (int n = 0; n < EUV; ++n) UV_LOAD(n); __builtin_amdgcn_sched_barrier(0); }
        const float v = xg_max(fmaxf(fmaxf(sv[0], sv[1]), fmaxf(sv[2], sv[3])));
        if (g == 0) stmax[kt * 32 + 16 * hh + j] = v;
        __syncthreads();
        const float* sm = stmax + 16 * hh + j;
        const float Mn = fmaxf(fmaxf(M, fmaxf(sm[0], sm[32])), fmaxf(sm[64], sm[96]));
        const float alpha = __builtin_amdgcn_exp2f(M - Mn);
        M = Mn;
        u32x2 pk;
        pk[0] = pk_bf16(__builtin_amdgcn_exp2f(sv[0] - Mn), __builtin_amdgcn_exp2f(sv[1] - Mn));
        pk[1] = pk_bf16(__builtin_amdgcn_exp2f(sv[2] - Mn), __builtin_amdgcn_exp2f(sv[3] - Mn));
        lsum = lsum * alpha + ((bf_lo(pk[0]) + bf_hi(pk[0])) + (bf_lo(pk[1]) + bf_hi(pk[1])));
        *reinterpret_cast<u32x2*>(Pf + (((kt >> 1) * 2 + hh) * 64 + lane) * 16 + (kt & 1) * 8) = pk;
        if (kt == 0 && g == 0) salpha[16 * hh + j] = alpha;
        __syncthreads();
        pv_step(cb);
      };
#pragma unroll 1
      for (int T = 0; T < 6; T += 2) { s_tile(lds + L_BIG, false); s_tile(lds + L_BIG + CBUF, false); }
      s_tile(lds + L_BIG, false); s_tile(lds + L_BIG + CBUF, true);
    } else {
#pragma unroll
      for (int T = 0; T < 8; ++T) {
        __syncthreads();
        B_POST(T);
        if (T + 1 < 8) { C_STAGE(T + 1); }
        L_PART(T, 0, IA);
        if (T == 7) {
#pragma unroll
          for (int k = 0; k < 2; ++k) W8[k] = ld16(a.woB + ((size_t)h3 * 128 + 16 * (w - 8) + 8 * k + (lane >> 3)) * DM + 512 + (lane & 7) * 8);
        }
        __syncthreads();
        L_PART(T, IA, IB);
        __syncthreads();
        L_PART(T, IB, IC);
        pv_step(lds + L_BIG + (T & 1) * CBUF);
        L_PART(T, IC, 8);
      }
      B_POST(8);
    }
#undef C_ISSUE
#undef C_STAGE
    __syncthreads();
    if constexpr (!LD) { lsum = xg_sum(lsum); if (g == 0) stsum[kt * 32 + 16 * hh + j] = lsum; if (kt == 0 && g == 0) salpha[32 + 16 * hh + j] = M; }
    float* tb = reinterpret_cast<float*>(lds + L_BIG);
#pragma unroll
    for (int ct = 0; ct < 2; ++ct)
#pragma unroll
      for (int h2 = 0; h2 < 2; ++h2)
#pragma unroll
        for (int r = 0; r < 4; ++r) tb[(16 * h2 + 4 * g + r) * 516 + 32 * w + 16 * ct + j] = acc[ct][h2][r];
    if constexpr (!LD) { for (int i = tid; i < 8 * AM2_LD * 2 / 16; i += 512) reinterpret_cast<u32x4*>(lds + L_AM2 + 8 * AM2_LD * 2)[i] = u32x4{0u, 0u, 0u, 0u}; }
    __syncthreads();
    if constexpr (LD) {
      const int t2 = tid - 512;
      if (CQ) { const u32 tq = (rt32() + (CQ - 1)) & ~(u32)(CQ - 1); while ((int)(rt32() - tq) < 0) __builtin_amdgcn_s_sleep(1); }
#pragma unroll
      for (int k = 0; k < 4; ++k) {
        const int e = t2 + 512 * k, head = e >> 6, c8 = e & 63;
        const f32x4 v0 = *reinterpret_cast<const f32x4*>(tb + head * 516 + 8 * c8), v1 = *reinterpret_cast<const f32x4*>(tb + head * 516 + 8 * c8 + 4);
        __builtin_amdgcn_raw_buffer_store_b128(u32x4{pk_bf16(v0[0], v0[1]), pk_bf16(v0[2], v0[3]), pk_bf16(v1[0], v1[1]), pk_bf16(v1[2], v1[3])}, rs, ctx_row(b * NSP + sp, head) + 16 * c8, 0, 16);
      }
      if (t2 < 32) {
        const float L = (stsum[t2] + stsum[32 + t2]) + (stsum[64 + t2] + stsum[96 + t2]);
        st8_sc1(a.ws + WS_MLB + ((size_t)(b * NSP + sp) * 32 + t2) * 8, f32x2{salpha[32 + t2], L});
      }
      __builtin_amdgcn_sched_barrier(0);
      __builtin_amdgcn_s_barrier();
      asm volatile("s_waitcnt vmcnt(0)" ::: "memory");
      if (lane == 0) {
        if (__hip_atomic_fetch_add(cnt, 1u, __ATOMIC_RELAXED, __HIP_MEMORY_SCOPE_WORKGROUP) == 7u) {
          __hip_atomic_store(reinterpret_cast<u64*>(a.ws + WS_F2 + wg * FST), tag, RLX_AGENT);
        }
      }
#pragma unroll
      for (int k = 0; k < 2; ++k) *reinterpret_cast<u32x4*>(lds + L_W8 + (16 * (w - 8) + 8 * k + (lane >> 3)) * 144 + (lane & 7) * 16) = W8[k];
    } else {
      __builtin_amdgcn_s_barrier();
      static_for<16 - EUV>([&](auto ic) __attribute__((always_inline)) {
        asm volatile("s_waitcnt vmcnt(%0)" :: "n"(WPD - 1) : "memory");
        UV_LOAD(EUV + ic.value);
        __builtin_amdgcn_sched_barrier(0);
      });
    }
  }
  {
    int tid = threadIdx.x; asm volatile("" : "+v"(tid));
    const int lane = tid & 63, j = lane & 15, g = lane >> 4;
    u16* Am2 = reinterpret_cast<u16*>(lds + L_AM2);
    float* cpart = reinterpret_cast<float*>(lds + L_CP);
    __builtin_amdgcn_sched_barrier(0);
    if constexpr (LD) {
      if (w == 8) {
        wave_wait(reinterpret_cast<const u64*>(a.ws + WS_F2) + (lane & (NBW - 1)) * (FST / 8), lane < NBW, tag);
        asm volatile("s_waitcnt vmcnt(0)" ::: "memory");
        if (lane == 0) __hip_atomic_store(cnt + 1, 1u, __ATOMIC_RELEASE, __HIP_MEMORY_SCOPE_WORKGROUP);
      } else {
        for (u32 spins = 0; spins < 64u * SPIN_CAP; ++spins) { if (__hip_atomic_load(cnt + 1, __ATOMIC_ACQUIRE, __HIP_MEMORY_SCOPE_WORKGROUP) != 0u) break; __builtin_amdgcn_s_sleep(1); }
      }
      __builtin_amdgcn_sched_barrier(0);
      const int b = (tid >> 7) & 3, c4 = tid & 127;
      u32x2 pv[NSP];
      const f32x2 ml = __builtin_bit_cast(f32x2, __builtin_amdgcn_raw_buffer_load_b64(rs, (int)WS_MLB + ((b * NSP + (j & 7)) * 32 + h3) * 8, 0, 16));
#pragma unroll
      for (int s = 0; s < NSP; ++s) pv[s] = __builtin_amdgcn_raw_buffer_load_b64(rs, ctx_row(b * NSP + s, h3) + 8 * c4, 0, 16);
      const float M = row16_max(ml[0]);
      float wt = __builtin_amdgcn_exp2f(ml[0] - M);
      wt *= 2.f / row16_sum(wt * ml[1]);
      float c0 = 0.f, c1 = 0.f, c2 = 0.f, c3 = 0.f;
#pragma unroll
      for (int s = 0; s < NSP; ++s) { const float ws_ = rdlane(wt, s); c0 = fmaf(ws_, bf_lo(pv[s][0]), c0); c1 = fmaf(ws_, bf_hi(pv[s][0]), c1); c2 = fmaf(ws_, bf_lo(pv[s][1]), c2); c3 = fmaf(ws_, bf_hi(pv[s][1]), c3); }
      const u32 h0 = pk_bf16(c0, c1), h1 = pk_bf16(c2, c3);
      *reinterpret_cast<u32x2*>(Am2 + (2 * b) * AM2_LD + 4 * c4) = u32x2{h0, h1};
      *reinterpret_cast<u32x2*>(Am2 + (2 * b + 1) * AM2_LD + 4 * c4) = u32x2{pk_bf16(c0 - bf_lo(h0), c1 - bf_hi(h0)), pk_bf16(c2 - bf_lo(h1), c3 - bf_hi(h1))};
      __syncthreads();
      __syncthreads();
      const int t2 = tid - 512;
      float s = 0.f;
#pragma unroll
      for (int ww = 0; ww < 8; ++ww) s += cpart[ww * 512 + t2];
      u16* Am = reinterpret_cast<u16*>(lds + L_AM);
      const int bb = t2 >> 7, d = t2 & 127;
      const u16 hi = to_bf16(s);
      Am[(4 * bb) * AM_LD + d] = hi; Am[(4 * bb + 1) * AM_LD + d] = to_bf16(s - bf2f(hi));
    } else {
      __syncthreads();
      f32x4 acc[8];
#pragma unroll
      for (int t = 0; t < 8; ++t) acc[t] = f32x4{0.f, 0.f, 0.f, 0.f};
#pragma unroll
      for (int k2 = 0; k2 < 2; ++k2) {
        const u32x4 af = *reinterpret_cast<const u32x4*>(Am2 + j * AM2_LD + 64 * w + 32 * k2 + 8 * g);
#pragma unroll
        for (int t = 0; t < 8; ++t) acc[t] = mfma16(af, tcol8(RW[2 * k2 + (t >> 2)], t & 3), acc[t]);
      }
      if (g < 2) {
#pragma unroll
        for (int bb = 0; bb < 2; ++bb) {
          float* p = cpart + (w * 4 + 2 * g + bb) * 128 + 8 * j;
          *reinterpret_cast<f32x4*>(p) = f32x4{acc[0][2 * bb] + acc[0][2 * bb + 1], acc[1][2 * bb] + acc[1][2 * bb + 1], acc[2][2 * bb] + acc[2][2 * bb + 1], acc[3][2 * bb] + acc[3][2 * bb + 1]};
          *reinterpret_cast<f32x4*>(p + 4) = f32x4{acc[4][2 * bb] + acc[4][2 * bb + 1], acc[5][2 * bb] + acc[5][2 * bb + 1], acc[6][2 * bb] + acc[6][2 * bb + 1], acc[7][2 * bb] + acc[7][2 * bb + 1]};
        }
      }
      __builtin_amdgcn_sched_barrier(0);
      __syncthreads();
      if (YDEF == 1 && w == 1) {
        u32 dl = tb0 + 5200u;
#pragma unroll 1
        while ((int)(rt32() - dl) < 0) {
          const u32x2 rv = __builtin_amdgcn_raw_buffer_load_b64(rs, lane < 16 ? (int)WS_RPT + lane * 4352 : OOB, 128 * (NRND - 1) * 35, 16);
          const bool ok = rv[1] == (u32)tag + (NRND - 1);
          const float cn = rdlane(row16_sum(ok ? 1.f : 0.f), 0);
          if (cn >= 8.f) { dl = tb0 + (u32)(int)(rdlane(row16_sum(ok ? (float)(int)(rv[0] - tb0) : 0.f), 0) / cn) + (u32)CI(due_at(NSEQ - 1) - due_at(rtab[NRND - 1]) + YOFF); break; }
          __builtin_amdgcn_s_sleep(32);
        }
        if (lane == 0) cnt[2] = (int)(dl - tb0) > 5200 ? tb0 : dl;
      }
      if (w == 0) {
#pragma unroll
        for (int s = 0; s < 4; ++s)
#pragma unroll
          for (int jj = 0; jj < 8; ++jj) RW[s][jj] = *reinterpret_cast<const u32x2*>(lds + L_W8 + (32 * s + 8 * g + jj) * 144 + 8 * j);
      }
    }
    __syncthreads();
    if (LD || w == 0) {
      f32x4 ya[4];
      wo_mfma(RW, lds, lane, ya);
      if (YDEF) { const u32 dl = YDEF == 1 ? __builtin_amdgcn_readfirstlane(cnt[2]) : tb0 + YDEF; while ((int)(rt32() - dl) < 0) __builtin_amdgcn_s_sleep(4); }
      st16_sc1(rs, WS_Y + ((8 * (LD ? w - 8 : 8) + (j >> 1)) * 256 + NAW + h3) * 128 + g * 32 + (j & 1) * 16, yrow(ya, (u32)(tag >> 32)));
    }
  }
}

__global__ void __launch_bounds__(1024, 4) k_main(Args a) {
  __shared__ __attribute__((aligned(16))) unsigned char lds[L_TOTAL];
  u32 pf;
  { u64 pc; u32 len;
    asm volatile("s_getpc_b64 %0\n.Lpf%=:\n\ts_mov_b32 %1, k_tail_mark-.Lpf%=" : "=s"(pc), "=s"(len));
    pf = __builtin_amdgcn_raw_buffer_load_b32(__builtin_amdgcn_make_buffer_rsrc(reinterpret_cast<void*>(pc), 0, len < 131072u ? len : 0u, 0x00020000), CPFM ? (threadIdx.x < 16 ? ((blockIdx.x >> 3) * 16 + threadIdx.x) * 128 : 0x7ffff000) : threadIdx.x * 128, 0, 0);
    __builtin_amdgcn_sched_barrier(0); }
  u64 tag = (hw_dispatch_id() + 1ull) * 0x9E3779B97F4A7C15ull;
  tag ^= ((u64)__builtin_amdgcn_dispatch_ptr() >> 6) * 0xC2B2AE3D27D4EB4Full;
  tag |= 1ull << 63;
  if (__builtin_expect(blockIdx.x >= NBW, 1)) part_a(a, lds, blockIdx.x - NBW, tag, pf);
  else if (NOB) { const int w = threadIdx.x >> 6, lane = threadIdx.x & 63, j = lane & 15, g = lane >> 4; const u32 tg = (u32)(tag >> 32);
    if (w < 9) st16_sc1(__builtin_amdgcn_make_buffer_rsrc(a.ws, 0, (int)WS_END, 0x00020000), WS_Y + ((8 * w + (j >> 1)) * 256 + NAW + blockIdx.x) * 128 + g * 32 + (j & 1) * 16, u32x4{0u, tg, 0u, tg}); }
  else if (__builtin_amdgcn_readfirstlane(threadIdx.x >> 6) < 8) part_b<false>(a, lds, blockIdx.x, tag, pf);
  else part_b<true>(a, lds, blockIdx.x, tag, pf);
}

extern "C" __global__ void k_tail_mark() {}

extern "C" void kernel_launch(void* const* d_in, const int* in_sizes, int n_in, void* d_out, int out_size, void* d_ws, size_t ws_size,
                              hipStream_t stream) {
  Args a;
  a.xA = (const u16*)d_in[0]; a.K = (const u16*)d_in[1]; a.V = (const u16*)d_in[2]; a.wq = (const u16*)d_in[3]; a.wo = (const u16*)d_in[4];
  a.xB = (const u16*)d_in[5]; a.C = (const u16*)d_in[6]; a.wqB = (const u16*)d_in[7]; a.wuk = (const u16*)d_in[8]; a.wuv = (const u16*)d_in[9]; a.woB = (const u16*)d_in[10];
#define WSTOP 32
  if (WSTOP && ws_size >= ((size_t)WSTOP << 20)) d_ws = (unsigned char*)d_ws + ((ws_size - ((size_t)WSTOP << 20)) & ~(size_t)4095);
  a.ws = (unsigned char*)d_ws;
  a.out = (u16*)d_out;
  k_main<<<NBW + NAW, 1024, 0, stream>>>(a);
}
```

```cpp
#include <hip/hip_runtime.h>
#include <stdint.h>
#include <utility>

typedef unsigned short u16;
typedef unsigned u32;
typedef unsigned long long u64;
typedef u32 u32x4 __attribute__((ext_vector_type(4)));
typedef u32 u32x2 __attribute__((ext_vector_type(2)));
typedef float f32x4 __attribute__((ext_vector_type(4)));
typedef float f32x2 __attribute__((ext_vector_type(2)));
typedef short s16x4 __attribute__((ext_vector_type(4)));
typedef __bf16 bf16x8 __attribute__((ext_vector_type(8)));
typedef __bf16 bf16x2 __attribute__((ext_vector_type(2)));
#define DI __device__ __forceinline__
#define RLX_AGENT __ATOMIC_RELAXED, __HIP_MEMORY_SCOPE_AGENT
extern "C" __device__ u64 hw_dispatch_id() __asm("llvm.amdgcn.dispatch.id");

template <int... Is, class F> DI void static_for_impl(std::integer_sequence<int, Is...>, F&& f) { (f(std::integral_constant<int, Is>{}), ...); }
template <int N, class F> DI void static_for(F&& f) { static_for_impl(std::make_integer_sequence<int, N>{}, f); }

constexpr int H = 32, SK = 4096, DM = 576, DC = 512;
constexpr int ACU = 7;
constexpr int NBW = 32;
constexpr int NAW = H * ACU;
constexpr int NSP = 8;
constexpr float C2 = 0.08838834764831845f * 1.4426950408889634f;

#define FST 4352
constexpr size_t WS_CTXP = 0;
constexpr size_t WS_QABS = WS_CTXP + (size_t)4 * NSP * 32 * 512 * 2;
constexpr size_t WS_MLB = WS_QABS + 4 * 32 * 512 * 2;
constexpr size_t WS_Y = WS_MLB + 4 * NSP * 32 * 2 * 4;
constexpr size_t WS_MLA = WS_Y + (size_t)72 * 256 * 128;
constexpr size_t WS_RPT = WS_MLA + 256 * 128;
constexpr size_t WS_QA = WS_RPT + 64 * 4480;
constexpr size_t WS_F1 = WS_QA + 256 * 2048, WS_F2 = WS_F1 + 512;
constexpr size_t WS_END = WS_F1 + 32 * 4352 + 1024;

constexpr int CRS = 1056;
constexpr int CBUF = 64 * CRS;
constexpr int L_BIG = 0;
constexpr int L_AM = 2 * CBUF;
constexpr int L_QS = L_AM + 16 * 272;
constexpr int L_ST = L_QS + 1024;
constexpr int L_PF = L_ST + 4096;
constexpr int L_TOTAL = L_PF + 4096;
constexpr int AM_LD = 136;
constexpr int AM2_LD = 520;
constexpr int L_AM2 = 69632, L_CP = 88064, L_W8 = 106496;

#define KPL 0
#define TMX 20
#define KT 0
#define T0L 22
struct Args {
  const u16 *xA, *K, *V, *wq, *wo, *xB, *C, *wqB, *wuk, *wuv, *woB;
  unsigned char* ws;
  u16* out;
};

DI float bf_lo(u32 x) { return __uint_as_float(x << 16); }
DI float bf_hi(u32 x) { return __uint_as_float(x & 0xffff0000u); }
DI u32 pk_bf16(float lo, float hi) { bf16x2 v = {(__bf16)lo, (__bf16)hi}; return __builtin_bit_cast(u32, v); }
DI u16 to_bf16(float x) { return __builtin_bit_cast(u16, (__bf16)x); }
DI float bf2f(u16 x) { return __uint_as_float(((u32)x) << 16); }
DI float dot2(u32 a, u32 b, float c) { return __builtin_amdgcn_fdot2_f32_bf16(__builtin_bit_cast(bf16x2, a), __builtin_bit_cast(bf16x2, b), c, false); }
template <int CTRL> DI float dpp(float x) { return __builtin_bit_cast(float, __builtin_amdgcn_update_dpp(0, __builtin_bit_cast(int, x), CTRL, 0xf, 0xf, true)); }
DI float row16_sum(float x) { x += dpp<0x128>(x); x += dpp<0x124>(x); x += dpp<0x122>(x); x += dpp<0x121>(x); return x; }
DI float row16_max(float x) { x = fmaxf(x, dpp<0x128>(x)); x = fmaxf(x, dpp<0x124>(x)); x = fmaxf(x, dpp<0x122>(x)); x = fmaxf(x, dpp<0x121>(x)); return x; }
DI float xg_max(float x) {
  auto s = __builtin_amdgcn_permlane16_swap(__float_as_uint(x), __float_as_uint(x), false, false);
  x = fmaxf(__uint_as_float(s[0]), __uint_as_float(s[1]));
  auto t = __builtin_amdgcn_permlane32_swap(__float_as_uint(x), __float_as_uint(x), false, false);
  return fmaxf(__uint_as_float(t[0]), __uint_as_float(t[1]));
}
DI float xg_sum(float x) {
  auto s = __builtin_amdgcn_permlane16_swap(__float_as_uint(x), __float_as_uint(x), false, false);
  x = __uint_as_float(s[0]) + __uint_as_float(s[1]);
  auto t = __builtin_amdgcn_permlane32_swap(__float_as_uint(x), __float_as_uint(x), false, false);
  return __uint_as_float(t[0]) + __uint_as_float(t[1]);
}
DI float sw32(float a, float b) {
  auto s = __builtin_amdgcn_permlane32_swap(__float_as_uint(a), __float_as_uint(b), false, false);
  return __uint_as_float(s[0]) + __uint_as_float(s[1]);
}
DI float sw16(float a, float b) {
  auto s = __builtin_amdgcn_permlane16_swap(__float_as_uint(a), __float_as_uint(b), false, false);
  return __uint_as_float(s[0]) + __uint_as_float(s[1]);
}
DI f32x4 mfma16(u32x4 a, u32x4 b, f32x4 c) { return __builtin_amdgcn_mfma_f32_16x16x32_bf16(__builtin_bit_cast(bf16x8, a), __builtin_bit_cast(bf16x8, b), c, 0, 0, 0); }
DI u32x4 ld16(const u16* p) { return __builtin_nontemporal_load(reinterpret_cast<const u32x4*>(p)); }
DI u32x2 ld8(const u16* p) { return __builtin_nontemporal_load(reinterpret_cast<const u32x2*>(p)); }
DI u32x2 trread(const unsigned char* p) {
  return __builtin_bit_cast(u32x2, __builtin_amdgcn_ds_read_tr16_b64_v4i16((__attribute__((address_space(3))) s16x4*)(p)));
}
DI u32 prm(u32 hi, u32 lo, int odd) { return __builtin_amdgcn_perm(hi, lo, odd ? 0x07060302u : 0x05040100u); }

DI u32x4 tcol16(const u32x4 (&R)[8], int t) {
  u32x4 T;
#pragma unroll
  for (int q = 0; q < 4; ++q) T[q] = prm(R[2 * q + 1][t >> 1], R[2 * q][t >> 1], t & 1);
  return T;
}
DI u32x4 tcol8(const u32x2 (&R)[8], int t) {
  u32x4 T;
#pragma unroll
  for (int q = 0; q < 4; ++q) T[q] = prm(R[2 * q + 1][t >> 1], R[2 * q][t >> 1], t & 1);
  return T;
}

#define SPINLOG 22
#define PSL 4
constexpr u32 SPIN_CAP = 1u << SPINLOG;
DI void wave_wait(const u64* f, bool active, u64 tag) {
#pragma unroll 1
  for (u32 spins = 0; spins < SPIN_CAP; ++spins) {
    if (active) active = __hip_atomic_load(f, RLX_AGENT) != tag;
    if (!__any(active)) break;
    __builtin_amdgcn_s_sleep(PSL);
  }
}
DI void wg_wait64(const unsigned char* flags, u64 tag, int pw = 0) {
  if ((int)(threadIdx.x >> 6) == pw) {
    wave_wait(reinterpret_cast<const u64*>(flags) + (threadIdx.x & (NBW - 1)) * (FST / 8), (threadIdx.x & 63) < NBW, tag);
    asm volatile("s_waitcnt vmcnt(0)" ::: "memory");
  }
  __syncthreads();
}
DI void st16_sc1(__amdgpu_buffer_rsrc_t rs, size_t off, u32x4 v) { __builtin_amdgcn_raw_buffer_store_b128(v, rs, (int)off, 0, 16); }
DI void st8_sc1(unsigned char* p, f32x2 v) { __hip_atomic_store(reinterpret_cast<u64*>(p), __builtin_bit_cast(u64, v), RLX_AGENT); }

struct QPre { u32x4 R0[8]; u32x4 R1; u32x2 xa, xb; u32 x1; };
DI void qproj_load(QPre& Q, const u16* x, const u16* Wh, int w, int lane) {
  const int j = lane & 15, g = lane >> 4;
  Q.xa = u32x2{0u, 0u}; Q.xb = u32x2{0u, 0u}; Q.x1 = 0u;
  if (j < 4) { Q.xa = ld8(x + j * DM + 36 * w + 8 * g); Q.xb = ld8(x + j * DM + 36 * w + 8 * g + 4); Q.x1 = x[j * DM + 36 * w + 32 + g]; }
#pragma unroll
  for (int jj = 0; jj < 8; ++jj) Q.R0[jj] = ld16(Wh + (size_t)(36 * w + 8 * g + jj) * 4096 + 8 * j);
  Q.R1 = ld16(Wh + (size_t)(36 * w + 32 + g) * 4096 + 8 * j);
}
DI void qproj_mfma(const QPre& Q, unsigned char* lds, int w, int lane) {
  const int j = lane & 15, g = lane >> 4;
  float* qpart = reinterpret_cast<float*>(lds + L_BIG);
  const u32x4 a = {Q.xa[0], Q.xa[1], Q.xb[0], Q.xb[1]}, a1 = {Q.x1, 0u, 0u, 0u};
#pragma unroll
  for (int hf = 0; hf < 2; ++hf) {
    f32x4 acc[4];
#pragma unroll
    for (int t = 0; t < 4; ++t) acc[t] = mfma16(a, tcol16(Q.R0, 4 * hf + t), f32x4{0.f, 0.f, 0.f, 0.f});
#pragma unroll
    for (int t = 0; t < 4; ++t) { const u32x4 bb = {(t & 1) ? (Q.R1[2 * hf + (t >> 1)] >> 16) : (Q.R1[2 * hf + (t >> 1)] & 0xffffu), 0u, 0u, 0u}; acc[t] = mfma16(a1, bb, acc[t]); }
    if (g == 0) {
#pragma unroll
      for (int r = 0; r < 4; ++r) *reinterpret_cast<f32x4*>(qpart + (w * 4 + r) * 128 + 8 * j + 4 * hf) = f32x4{acc[0][r], acc[1][r], acc[2][r], acc[3][r]};
    }
    __builtin_amdgcn_sched_barrier(0);
  }
}
DI void wo_load(const u16* Wo, u32x2 (&R)[4][8], int nb, int lane, int s0, int s1) {
  const int j = lane & 15, g = lane >> 4;
#pragma unroll
  for (int s = 0; s < 4; ++s) if (s >= s0 && s < s1)
#pragma unroll
    for (int jj = 0; jj < 8; ++jj) R[s][jj] = ld8(Wo + (size_t)(32 * s + 8 * g + jj) * DM + 64 * nb + 4 * j);
}
DI void wo_mfma(const u32x2 (&R)[4][8], const unsigned char* lds, int lane, f32x4 (&acc)[4]) {
  const int j = lane & 15, g = lane >> 4;
#pragma unroll
  for (int t = 0; t < 4; ++t) acc[t] = f32x4{0.f, 0.f, 0.f, 0.f};
#pragma unroll
  for (int s = 0; s < 4; ++s) {
    const u32x4 a = *reinterpret_cast<const u32x4*>(lds + L_AM + j * 272 + (32 * s + 8 * g) * 2);
#pragma unroll
    for (int t = 0; t < 4; ++t) acc[t] = mfma16(a, tcol8(R[s], t), acc[t]);
  }
}
DI u32x4 yrow(const f32x4 (&acc)[4], u32 tg) {
  return u32x4{pk_bf16(acc[0][0] + acc[0][1], acc[1][0] + acc[1][1]), tg, pk_bf16(acc[2][0] + acc[2][1], acc[3][0] + acc[3][1]), tg};
}
DI void am_zero(unsigned char* lds, int tid) {
  if (tid < 136) { const int pr = tid / 34; *reinterpret_cast<u32x4*>(lds + L_AM + (4 * pr + 2) * 272 + (tid - 34 * pr) * 16) = u32x4{0u, 0u, 0u, 0u}; }
}
DI float rdlane(float x, int l) { return __builtin_bit_cast(float, __builtin_amdgcn_readlane(__builtin_bit_cast(int, x), l)); }

constexpr int NS = 10;
constexpr int NSEQ = 8 * NS;
#define RNG 9
#define NP 3
#define QP 4
#define SP 4
#define BPF 2
#define TPS 3
#define KD0 60
#define KDP 60
#define KDS 60
#define NOWO 0
#define NOB 0
#define GF0 12
#define GF1 99
#define WOF 1
#define RAUX 16
#define SAUX 16
#define NOACT 0
#define NOPOST 0
#define NOREAD 0
#define PFULL 0
#define PAUX 16
#define RSPR 0
#define QTRY 0
#define QAUX 3
#define QSA 16
#define LATE 0
#define EXQ 1
#define PF2S 0
#define SMO 1
#define TST 440
#define WTS0 42
#define SLN 0
#define SLK 3
#define SLT 0x7513
#define BMODE 2
#define NB 50
#define BT 160
#define BS 180
#define TF 8
#define LN 24
#define TPN 8
#define TPC 12
#define TPW 10
#define K1G 1
#define PSKIP 9999
#define D3FROM 60
#define D4FROM 99
#define ROTM 1
#define OAUX 0
#define DEP 2
constexpr int DEPTH = DEP, RING = RNG, RAMP = 2;
constexpr int issued(int i) {
  int d = RING - (i + RAMP - 1) / RAMP; if (d < DEPTH) d = DEPTH;
  if (i >= D3FROM && d < 3) d = 3;
  if (i >= D4FROM && d < 4) d = 4;
  int e = i + d; if (e < RING) e = RING; return e > NSEQ ? NSEQ : e;
}
#define PACE 1
#define YDEF 1
#define WO0 16
#define WON 36
#define TA1 535
#define TA2 440
#define NK 68
#define ORG0 130
#define SLMAX 80
#define MAXGAP 70
constexpr int cmin(int a, int b) { return a < b ? a : b; }
#define RSH 1
#define RD 4
#define BMAP 1
#define EBIG 9999
#define NJ 54
#define TA3 515
#define PTAB 510, 510, 510, 510, 510, 510, 515, 515, 515, 515, 515, 515, 520, 510, 440, 432, 432
#define PTAB1 0, 0, 0, 0, 0, 0, 0, 0, 0, 0, 530, 490, 460, 455, 440, 432, 432
constexpr int ptab[2][17] = {{PTAB}, {PTAB1}};
constexpr int due_at(int n, int m = 0) { int t = 0; for (int k = 0; k < n; ++k) t += ptab[m && k >= NB][k / 5]; return t / 10; }
constexpr int due_x(int n, int m) { return due_at(n, m) + ((m && n >= NB) ? BT : 0); }
constexpr int due_b(int n, int m) { return n % 10 == 9 ? 0 : due_x(n - n % 10, m) + (due_at(n - n % 10 + 10, m) - due_at(n - n % 10, m)) * (n % 10) / 9; }
#define CI(x) std::integral_constant<int, (x)>::value
#define RTAB 20, 30, 40, 50, 60, 70
#define GTAB 0, 12, 6, 12, 12, 12
#define BTAB 8, 12, 6, 12, 12, 0
#define MISS 40
#define DB 10
#define DTAB 22, 16, 18, 15, 10, 3, 3, 3
#define XTAB 0, 0, 2210, 0, 0, 0
#define MTAB -1, -1, 2, -1, -1, -1
#define CB 16
#define SBMAX 100
constexpr int xtab[] = {XTAB}, mtab[] = {MTAB};
constexpr int rtab[] = {RTAB}, gtab[] = {GTAB}, btab[] = {BTAB}, dtab[] = {DTAB};
constexpr int NRND = sizeof(rtab) / sizeof(int);
constexpr int rgap(int k) { return k ? rtab[k] - rtab[k - 1] : rtab[0] - RING; }
constexpr int rnd_at(int n) { for (int k = 0; k < NRND; ++k) if (rtab[k] == n) return k; return -1; }
static_assert(rtab[0] >= RING + 2 && rtab[NRND - 1] + RD + 2 < NSEQ, "");
constexpr int wo_step(int k) { return WO0 + k * WON / 16; }
#define WTS 52
#define PJ0 56
#define PJ1 63
#define PJ2 72
#define XW 13
#define XR 50
#define FDLY 8
#define FSL 2
#define CPFM 1
#define STAG 16
#define EXM(m, ...) do { if (EXQ) { __builtin_amdgcn_sched_barrier(0); asm volatile("s_mov_b64 exec, %0" :: "s"((u64)(m))); __builtin_amdgcn_sched_barrier(0); } __VA_ARGS__; \
    if (EXQ) { __builtin_amdgcn_sched_barrier(0); asm volatile("s_mov_b64 exec, -1"); __builtin_amdgcn_sched_barrier(0); } } while (0)
DI u32 rt32() { return (u32)__builtin_amdgcn_s_memrealtime(); }
constexpr int OOB = 0x7ffff000;
constexpr int L_AB = 32768;
#define PERM 12
DI int uoff(int c, int u, int x) {
  if (PERM == 0) return c * 16384 + u * 1024;
  if (PERM == 1) return ((u >> 2) << 10) | ((u & 3) << 15) | ((c & 7) << 12) | ((c >> 3) << 17);
  if (PERM == 2) return ((c & 3) << 10) | ((u & 8) << 9) | ((u & 4) << 11) | ((c & 4) << 12) | ((u & 3) << 15) | ((c >> 3) << 17);
  if (PERM == 3) return ((((u >> 2) ^ x) & 3) << 10) | ((u & 3) << 15) | ((c & 7) << 12) | ((c >> 3) << 17);
  if (PERM == 4) return ((c & 3) << 10) | ((u & 3) << 12) | ((c & 4) << 12) | ((u >> 2) << 15) | ((c >> 3) << 17);
  if (PERM == 12) return c * 16384 + ((u ^ ((c >> 1) & 3)) * 1024);
  if (PERM == 13) return c * 16384 + ((u ^ ((c >> 1) & 3) ^ ((c >> 3) & 3)) * 1024);
  if (PERM == 5) return (((u ^ x) & 3) << 10) | ((u >> 2) << 15) | ((c & 7) << 12) | ((c >> 3) << 17);
  return 0;
}
DI void part_a(const Args& a, unsigned char* lds, int idx, u64 tag, u32 pf) {
  const u64 T64 = __builtin_amdgcn_s_memrealtime();
  __builtin_amdgcn_sched_barrier(0);
  const int tid = threadIdx.x, lane = tid & 63, w = __builtin_amdgcn_readfirstlane(tid >> 6);
  const int j = lane & 15, g = lane >> 4;
  const int h = (idx & 7) * 4 + ((idx >> 3) & 3), ci = idx >> 5;
  const int rr = (((h & 3) * 5) >> 1) + (h >> 2), rot = SMO ? (ROTM == 2 ? (h >> 2) + (h & 1) * 5 : h >> 2) % 9 : (rr >= NS ? rr - NS : rr), l0 = (ci * 37) >> 4, ln = ((ci * 37 + 37) >> 4) - l0;
  const bool xtra = w < ln;
  const int bad_sl = xtra ? -1 : SMO ? NS - 1 : NS - 1 - rot;
  int vtab;
  { const int p = SMO ? (lane == NS - 1 ? lane : lane + rot >= NS - 1 ? lane + rot - (NS - 1) : lane + rot) : lane + rot >= NS ? lane + rot - NS : lane + rot;
    const bool last = p == NS - 1;
    vtab = uoff(last ? 63 : p * 7 + ci, last ? (l0 + w) & 15 : w, last ? 0 : ci) + ((last && w >= ln) ? 1 << 30 : 0) + (h << 20); }
  const bool prod = ci < 2 * NP && w < 9 / NP;
  const int r0 = ((ci >> 1) * (9 / NP) + w) * 64;
  int voff = lane * 16, wqo = ((r0 + 32 * (j >> 3) + 8 * g) * 4096 + h * 128 + (ci & 1) * 64 + 8 * (j & 7)) * 2;
  int xo = j < 8 ? ((j & 3) * DM + r0 + 32 * (j >> 2) + 8 * g) * 2 : OOB;
  const int pk = (w - 3) % 3, pn = pk ? 19 : 21;
  int pfo = (w >= 3 && w < 3 + 3 * SLN && pk < SLK && lane < pn) ? ((idx >> 3) * pn + lane) * 8192 + (idx & 7) * 1024 + ((SLT >> (4 * ((w - 3) / 3))) & 7) * 128 : OOB;
  int so0 = __builtin_amdgcn_readlane(vtab, 0);
  asm volatile("" : "+v"(vtab), "+v"(voff), "+v"(wqo), "+v"(xo), "+v"(pfo), "+s"(so0));
  __builtin_amdgcn_sched_barrier(0);
  if (!KPL) asm volatile("" :: "s"(a.xB), "s"(a.C), "s"(a.wqB), "s"(a.wuk), "s"(a.wuv), "s"(a.woB), "s"(T64));
  u32 T0 = (u32)T64;
  const u32 toff = (u32)(w * TST) / (STAG * (SMO ? 9 : 10));
  u32* lorg = reinterpret_cast<u32*>(lds + L_ST + 2048);
  const u32 tg = (u32)(tag >> 32);
  u32 last = T0;
  bool mode = false;
  u32x2 rv = {0u, 0u}; u32 rt = 0;
  const __amdgpu_buffer_rsrc_t rs = __builtin_amdgcn_make_buffer_rsrc(a.ws, 0, (int)WS_END, 0x00020000);
  const __amdgpu_buffer_rsrc_t rk = __builtin_amdgcn_make_buffer_rsrc(const_cast<u16*>(a.K), 0, 1 << 27, 0x00020000);
  const __amdgpu_buffer_rsrc_t rvv = __builtin_amdgcn_make_buffer_rsrc(const_cast<u16*>(a.V), 0, 1 << 27, 0x00020000);
  const __amdgpu_buffer_rsrc_t rw = __builtin_amdgcn_make_buffer_rsrc(const_cast<u16*>(a.wq), 0, DM * 4096 * 2, 0x00020000);
  const __amdgpu_buffer_rsrc_t rx = __builtin_amdgcn_make_buffer_rsrc(const_cast<u16*>(a.xA), 0, 4 * DM * 2, 0x00020000);
  u32x4 R[RING];
#define A_ISSUE(i) R[(i) % RING] = __builtin_amdgcn_raw_buffer_load_b128((i) >= 4 * NS ? rvv : rk, voff, ((i) == 0 ? so0 : __builtin_amdgcn_readlane(vtab, (i) % NS)) + ((((i) / NS) & 3) << 25), 2)
#define GATE(n) do { if (PACE) { u32 due = T0 + toff + org + ((osl * cmin(n, NK)) >> 4) + (u32)(mode ? (xtra ? CI(due_x(n, 1)) : CI(due_b(n, 1))) : (xtra ? CI(due_x(n, 0)) : CI(due_b(n, 0)))); if (MAXGAP && (int)(due - (last + MAXGAP)) > 0) due = last + MAXGAP; \
    while ((int)(now - due) < 0) { __builtin_amdgcn_s_sleep(1); now = rt32(); } if (!SMO || (n) % 10 != 9 || xtra) last = now; } } while (0)
#define CTRL(n) do { if constexpr (PACE) { \
    if constexpr (rnd_at((n) - RD - 2) >= 0) { constexpr int k = rnd_at((n) - RD - 2); \
      if (k < NOACT) asm volatile("" :: "v"(rv)); \
      const u32 dt = (rv[1] == (u32)tag + k) ? rv[0] - T0 : rt; \
      const float sm = row16_sum((float)(int)dt); \
      const int o = (int)((RSH ? rdlane(sm, 0) * (1.f / 16.f) : (rdlane(sm, 0) + rdlane(sm, 16)) * (1.f / 32.f))) - (mode ? CI(due_x(rtab[k], 1)) : CI(due_x(rtab[k], 0))); \
      if (w == ((5 * k + 11) & 15) && lane == 0 && o > -300 && o < (k < NOACT ? -5000 : 2000)) { \
        int e = o - (int)org - (int)((osl * cmin(rtab[k], NK)) >> 4) - dtab[k]; if (e < 0) e = 0; \
        int sl_ = ((int)osl * gtab[k]) / 16 + ((e > EBIG ? 0 : e) * btab[k]) / rgap(k); sl_ = sl_ > SLMAX ? SLMAX : sl_; \
        if (xtab[k]) lorg[2] = (int)(rdlane(sm, 16) * (1.f / 16.f)) > xtab[k]; \
        __hip_atomic_store(reinterpret_cast<u64*>(lorg), __builtin_bit_cast(u64, u32x2{(u32)((int)org + e + ((((int)osl - sl_) * cmin(n, NK)) >> 4)), (u32)sl_}), __ATOMIC_RELAXED, __HIP_MEMORY_SCOPE_WORKGROUP); } } \
    if constexpr (rnd_at(n) >= 0) { constexpr int k = rnd_at(n); \
      u32 tot = 0; \
      if (ci == k % ACU && (h & ((1 << RSH) - 1)) == (k & ((1 << RSH) - 1))) { const u32 mine = (1u << 24) + ((rt32() - toff - T0) & 0xfffffu); \
        if (lane == 0) tot = __hip_atomic_fetch_add(lorg + 8 + k, mine, __ATOMIC_RELAXED, __HIP_MEMORY_SCOPE_WORKGROUP) + mine; } \
      if (PFULL) tot = lane < PFULL ? __builtin_amdgcn_readfirstlane(tot) : 0u; \
      u32x2 pv_ = {T0 + ((tot & 0xffffffu) >> 4), (u32)tag + k}; int po_ = (tot >> 24) == (k < NOPOST ? 99u : 16u) ? (int)WS_RPT + (h >> RSH) * 4352 : OOB; asm volatile("" : "+v"(pv_), "+v"(po_)); \
      EXM(1, __builtin_amdgcn_raw_buffer_store_b64(pv_, rs, po_, 128 * k * 35, PAUX)); } \
    if constexpr (rnd_at((n) - RD) >= 0) { constexpr int k = rnd_at((n) - RD); \
      int ro_ = (w == ((5 * k + 11) & 15) && lane < (xtab[k] ? 32 : 16)) ? (int)WS_RPT + lane * 4352 : OOB; asm volatile("" : "+v"(ro_)); \
      EXM(w == ((5 * k + 11) & 15) ? 0xffffffffu : 1u, rv = __builtin_amdgcn_raw_buffer_load_b64(rs, ro_, 128 * k * 35, RAUX)); rt = rt32() - T0 - MISS; } \
  } } while (0)
  u32x4 Wr[8], xf;
  u32 pf2 = 0, pf3 = 0, pf4 = 0;
  auto touch = [&]() __attribute__((always_inline)) {
    const int k = ci < 2 ? ci : (ci - 2) * 4 + (h & 3) + 2;
    const __attribute__((address_space(4))) u64* ap = (const __attribute__((address_space(4))) u64*)__builtin_amdgcn_kernarg_segment_ptr() + KPL;
    const u64 p = ap[(0xCBA96421u >> (4 * (k & 7))) & 15u];
    const int len = k < 2 ? 1 << 27 : k == 3 ? 1 << 24 : k == 4 ? DC * 8192 : k == 6 ? (int)WS_END : k == 7 ? 9216 : DM * 8192;
    const int o = (k < 2 ? (lane << 25) + (h << 20) + lane * 4352 : (lane << 21)) + (idx & 31) * 256;
    pf3 = __builtin_amdgcn_raw_buffer_load_b32(__builtin_amdgcn_make_buffer_rsrc(reinterpret_cast<void*>(p), 0, (w == TPW && k < TPN) ? len : 0, 0x00020000), lane < 8 ? o : OOB, 0, 2);
  };
  if (!KD0) A_ISSUE(0);
  __builtin_amdgcn_sched_barrier(0);
  if (prod) {
#pragma unroll
    for (int k = 0; k < 8; ++k) Wr[k] = __builtin_amdgcn_raw_buffer_load_b128(rw, wqo, k * 8192, 2);
    xf = __builtin_amdgcn_raw_buffer_load_b128(rx, xo, 0, 2);
  }
  __builtin_amdgcn_sched_barrier(0);
  if (KPL) asm volatile("" :: "s"(a.K), "s"(a.V), "s"(a.C), "s"(a.wuv), "s"(a.woB), "s"(T64));
  if (T0L) { const u32 t1 = rt32() - T0L; if (!TMX || (int)(t1 - T0) > TMX) T0 = t1; }
  __builtin_amdgcn_sched_barrier(0);
#define PF2() do { const __amdgpu_buffer_rsrc_t rp_ = __builtin_amdgcn_make_buffer_rsrc(const_cast<u16*>(pk == 0 ? a.wqB : pk == 1 ? a.wuk : a.wuv), 0, pk ? DC * 8192 : DM * 8192, 0x00020000); \
    EXM(0xffffffffu, pf2 = __builtin_amdgcn_raw_buffer_load_b32(rp_, pfo, 0, 2); __builtin_amdgcn_sched_barrier(0); pf4 = __builtin_amdgcn_raw_buffer_load_b32(rp_, pfo, 0, 0)); } while (0)
#define TWAIT(d) do { while ((int)(rt32() - T0 - (d)) < 0) __builtin_amdgcn_s_sleep(1); __builtin_amdgcn_sched_barrier(0); } while (0)
  if (SLN) PF2();
  if (KT) pf2 = __builtin_amdgcn_raw_buffer_load_b32(rk, (w == TPW && lane == 0 && (KT == 1 || ci == 6)) ? (h << 20) + ci * 4352 : OOB, 0, 2);
  if (TPS == 0) touch();
  if (!K1G) A_ISSUE(1);
  __builtin_amdgcn_raw_buffer_store_b64(u32x2{T0, (u32)tag + 15u}, rs, (ci == 6 && (h & 1) == 0 && tid == 0) ? (int)WS_RPT + (h >> 1) * 4352 : OOB, 128 * 15 * 35, 16);
  __builtin_amdgcn_sched_barrier(0);
  if (tid < 32) lorg[tid] = tid == 0 ? ORG0 : tid == 2 ? T0 : 0u;
  __syncthreads();
  T0 = __builtin_amdgcn_readfirstlane(lorg[2]);
  if (KD0) { TWAIT(KD0 + ((w * KDS) >> 4)); A_ISSUE(0); last = rt32(); }
  __builtin_amdgcn_sched_barrier(0);
  if (prod) {
    float* qpart = reinterpret_cast<float*>(lds + L_BIG);
#pragma unroll
    for (int hf = 0; hf < 2; ++hf) {
      f32x4 d4[4];
#pragma unroll
      for (int t = 0; t < 4; ++t) d4[t] = mfma16(xf, tcol16(Wr, 4 * hf + t), f32x4{0.f, 0.f, 0.f, 0.f});
      if (g == (j >> 3)) {
#pragma unroll
        for (int r = 0; r < 4; ++r) *reinterpret_cast<f32x4*>(qpart + ((w * 2 + g) * 4 + r) * 64 + 8 * (j & 7) + 4 * hf) = f32x4{d4[0][r], d4[1][r], d4[2][r], d4[3][r]};
      }
    }
    asm volatile("" ::: "memory");
    u32 arr = 0;
    if (lane == 0) arr = __hip_atomic_fetch_add(lorg + 4, 1u, __ATOMIC_RELAXED, __HIP_MEMORY_SCOPE_WORKGROUP);
    if (__builtin_amdgcn_readfirstlane(arr) == 9 / NP - 1) {
      asm volatile("" ::: "memory");
      f32x4 s0 = {0.f, 0.f, 0.f, 0.f};
#pragma unroll
      for (int k = 0; k < 18 / NP; ++k) s0 += *reinterpret_cast<const f32x4*>(qpart + k * 256 + 4 * lane);
      const int qo = (int)WS_QA + (h * 8 + ci) * 2048 + lane * 16;
      __builtin_amdgcn_raw_buffer_store_b128(u32x4{__float_as_uint(s0[0]), tg, __float_as_uint(s0[1]), tg}, rs, qo, 0, QSA);
      __builtin_amdgcn_raw_buffer_store_b128(u32x4{__float_as_uint(s0[2]), tg, __float_as_uint(s0[3]), tg}, rs, qo + 1024, 0, QSA);
    }
  }
  static_for<RING - 2 + K1G>([&](auto tc) __attribute__((always_inline)) {
    constexpr int t = tc.value + 2 - K1G;
    u32 now = rt32();
    const u32 org = __builtin_amdgcn_readfirstlane(__hip_atomic_load(lorg, __ATOMIC_RELAXED, __HIP_MEMORY_SCOPE_WORKGROUP)), osl = 0;
    GATE(t);
    A_ISSUE(t);
    if constexpr (t == TPS) touch();
    __builtin_amdgcn_sched_barrier(0);
    if constexpr (t == SP) {
      if (w == 13) {
        const u32x2 sv = __builtin_amdgcn_raw_buffer_load_b64(rs, lane < 16 ? (int)WS_RPT + lane * 4352 : OOB, 128 * 15 * 35, SAUX);
        const bool ok = sv[1] == (u32)tag + 15u;
        const float sm = row16_sum(ok ? (float)(int)(sv[0] - T0) : 0.f), cn = row16_sum(ok ? 1.f : 0.f);
        const int o = ORG0 + (cn > 0.f ? (int)(sm / cn) : 0);
        if (lane == 0 && o > -300 && o < 1000) lorg[0] = (u32)o;
      }
    }
    if constexpr (t == QP) {
      if (w >= 14) {
        const int qo = (int)WS_QA + (h * 8 + (w & 1)) * 2048 + lane * 16;
        u32x4 v[2 * NP];
#pragma unroll 1
        for (u32 spins = 0; spins < SPIN_CAP; ++spins) {
          bool ok = true;
          if (spins < QTRY) {
#pragma unroll
            for (int k = 0; k < 2 * NP; ++k) v[k] = __builtin_amdgcn_raw_buffer_load_b128(rs, qo, (k >> 1) * 4096 + (k & 1) * 1024, QAUX);
          } else {
#pragma unroll
            for (int k = 0; k < 2 * NP; ++k) v[k] = __builtin_amdgcn_raw_buffer_load_b128(rs, qo, (k >> 1) * 4096 + (k & 1) * 1024, 16);
          }
#pragma unroll
          for (int k = 0; k < 2 * NP; ++k) ok = ok && v[k][1] == tg && v[k][3] == tg;
          if (__all(ok)) break;
          __builtin_amdgcn_s_sleep(4);
        }
        f32x4 s0 = {0.f, 0.f, 0.f, 0.f};
#pragma unroll
        for (int k = 0; k < NP; ++k) s0 += f32x4{__uint_as_float(v[2 * k][0]), __uint_as_float(v[2 * k][2]), __uint_as_float(v[2 * k + 1][0]), __uint_as_float(v[2 * k + 1][2])};
        *reinterpret_cast<u32x2*>(lds + L_QS + (g * 128 + (w & 1) * 64 + 4 * j) * 2) = u32x2{pk_bf16(s0[0], s0[1]), pk_bf16(s0[2], s0[3])};
        asm volatile("" ::: "memory");
        if (lane == 0) __hip_atomic_fetch_add(lorg + 5, 1u, __ATOMIC_RELAXED, __HIP_MEMORY_SCOPE_WORKGROUP);
      }
    }
    __builtin_amdgcn_sched_barrier(0);
  });
  while (__hip_atomic_load(lorg + 5, __ATOMIC_RELAXED, __HIP_MEMORY_SCOPE_WORKGROUP) != 2u) __builtin_amdgcn_s_sleep(1);
  asm volatile("" :: "v"(pf), "v"(pf2), "v"(pf4) : "memory");
  float* wtab = reinterpret_cast<float*>(lds + L_ST + 1024);
  u32x2 xv = {0u, 0u};
  const int xoff = (w == XW && lane < 8 * ACU) ? (int)WS_MLA + (h * 8 + (lane >> 3)) * 128 + (lane & 7) * 8 : OOB;
  float* pml = reinterpret_cast<float*>(lds + L_ST);
  u32x4 q;
  u32 pb0 = 0, pb1 = 0;
  float acc[8], S[4];
  u32x4 RW[2][8];
  u32x4 WT[4][4];
  float Y[4];
  auto project = [&](int b) __attribute__((always_inline)) {
    f32x4 d4[4];
#pragma unroll
    for (int t = 0; t < 4; ++t) d4[t] = f32x4{0.f, 0.f, 0.f, 0.f};
    const f32x4 wt = *reinterpret_cast<const f32x4*>(wtab + b * 16 + 4 * g);
#pragma unroll
    for (int s = 0; s < 4; ++s) {
      const u32x4 af = *reinterpret_cast<const u32x4*>(lds + L_AB + (b * 16 + j) * 272 + (32 * s + 8 * g) * 2);
#pragma unroll
      for (int t = 0; t < 4; ++t) d4[t] = mfma16(af, WT[s][t], d4[t]);
    }
    float y[4];
#pragma unroll
    for (int t = 0; t < 4; ++t) y[t] = fmaf(d4[t][0], wt[0], d4[t][1] * wt[1]) + fmaf(d4[t][2], wt[2], d4[t][3] * wt[3]);
    Y[b] = sw16(sw32(y[0], y[2]), sw32(y[1], y[3]));
  };
  const __amdgpu_buffer_rsrc_t rwo = __builtin_amdgcn_make_buffer_rsrc(const_cast<u16*>(a.wo + (size_t)h * 128 * DM), 0, w < 9 ? 128 * DM * 2 : 0, 0x00020000);
  int wvo = ((32 * (j >> 3) + 8 * g) * DM + 64 * w + 8 * (j & 7)) * 2;
  asm volatile("" : "+v"(wvo));
  const u64 wmask = w < 9 ? ~0ull : 1ull;
#define WO_ISSUE() static_for<16>([&](auto kc) __attribute__((always_inline)) { constexpr int k = kc.value; \
    if constexpr (wo_step(k) == i) EXM(wmask, RW[k >> 3][k & 7] = __builtin_amdgcn_raw_buffer_load_b128(rwo, wvo, (64 * (k >> 3) + (k & 7)) * DM * 2, 2)); })
#define ISSUE_STEP() do { if constexpr (issued(i + 1) > issued(i)) { GATE(issued(i)); if constexpr (gf) { if (!SMO || i >= D3FROM - 1 || issued(i) % 10 != 0 || xtra) asm volatile("" :: "v"(r)); __builtin_amdgcn_sched_barrier(0); } \
    if (WOF) WO_ISSUE(); A_ISSUE(issued(i)); if (!WOF) WO_ISSUE(); if constexpr (gf) __builtin_amdgcn_sched_barrier(0); CTRL(issued(i)); \
    if constexpr (issued(i + 1) > issued(i) + 1) { GATE(issued(i) + 1); A_ISSUE(issued(i) + 1); CTRL(issued(i) + 1); } } else { WO_ISSUE(); } } while (0)
  static_for<NSEQ>([&](auto ic) __attribute__((always_inline)) {
    constexpr int i = ic.value, bb = (i / NS) & 3, pass = i / (4 * NS), sl = i % NS;
    u32 now = 0; if (PACE) now = rt32();
    u32 org = 0, osl = 0; if (PACE) { const u32x2 ov = __builtin_bit_cast(u32x2, __hip_atomic_load(reinterpret_cast<u64*>(lorg), __ATOMIC_RELAXED, __HIP_MEMORY_SCOPE_WORKGROUP)); org = __builtin_amdgcn_readfirstlane(ov[0]); osl = __builtin_amdgcn_readfirstlane(ov[1]); }
    if constexpr (BMODE && issued(i) == NB && issued(i + 1) > NB) mode = BMODE == 1 || __builtin_amdgcn_readfirstlane(__hip_atomic_load(lorg + 2, __ATOMIC_RELAXED, __HIP_MEMORY_SCOPE_WORKGROUP)) == 1u;
    __builtin_amdgcn_sched_barrier(0);
    const u32x4 r = R[i % RING];
    constexpr bool gf = i >= GF0 && i < GF1;
    if constexpr (BMODE && issued(i) == NB && issued(i + 1) > NB) {
      u64 bp = 0; int bl = 0, co = OOB;
      if (__builtin_expect(mode, 0)) {
        constexpr int CL = (8 - TF) * 512, N0 = 4 * ((CL + LN - 1) / LN), N1 = N0 + (4608 + LN - 1) / LN;
        const int id = w * 28 + (idx >> 3), l = (id < N0 ? id >> 2 : id < N1 ? id - N0 : id - N1) * LN + lane;
        bp = ((const __attribute__((address_space(4))) u64*)__builtin_amdgcn_kernarg_segment_ptr())[KPL + (id < N0 ? 6 : id < N1 ? 10 : 9)];
        bl = id < N0 ? 1 << 24 : id < N1 ? DM * 8192 : DC * 8192;
        co = l >= (id < N0 ? CL : 4608) ? OOB : id < N0 ? ((idx & 6) << 21) + ((idx & 1) << 19) + ((id & 3) << 20) + (TF << 16) + l * 128 : id < N1 ? (idx & 7) * 589824 + l * 128 : (l >> 3) * 8192 + (idx & 7) * 1024 + (l & 7) * 128;
        TWAIT(org + ((osl * NB) >> 4) + CI(due_at(NB)) + ((w * BS) >> 4)); last = T0 + 100000u; now = rt32();
      }
      asm volatile("" : "+v"(co));
      const __amdgpu_buffer_rsrc_t rc = __builtin_amdgcn_make_buffer_rsrc(reinterpret_cast<void*>(bp), 0, bl, 0x00020000);
      EXM(mode ? (1ull << LN) - 1ull : 1ull, pb0 = __builtin_amdgcn_raw_buffer_load_b32(rc, co, 0, 2); __builtin_amdgcn_sched_barrier(0); pb1 = __builtin_amdgcn_raw_buffer_load_b32(rc, co, 0, 0));
    }
    if constexpr (BMODE && i == NB + 1) asm volatile("" :: "v"(pb0), "v"(pb1));
    if constexpr (gf) { ISSUE_STEP(); }
    if constexpr (i == TPC) asm volatile("" :: "v"(pf3));
    if constexpr (pass == 0) {
      if constexpr (sl == 0) { q = *reinterpret_cast<const u32x4*>(lds + L_QS + (bb * 128 + 8 * j) * 2); S[bb] = -INFINITY; }
      float d = dot2(r[0], q[0], 0.f); d = dot2(r[1], q[1], d); d = dot2(r[2], q[2], d); d = dot2(r[3], q[3], d);
      asm volatile("" : "+v"(d));
      if constexpr (!gf) { ISSUE_STEP(); }
      d = row16_sum(d);
      S[bb] = (j == sl) ? d : S[bb];
      if constexpr (sl == NS - 1) {
        const float sc = (j == bad_sl) ? -INFINITY : S[bb] * C2;
        const float m = xg_max(row16_max(sc));
        S[bb] = __builtin_amdgcn_exp2f(sc - m);
        const float l = xg_sum(row16_sum(S[bb]));
        if (lane == 0) *reinterpret_cast<f32x2*>(pml + (bb * 16 + w) * 2) = f32x2{m, l};
      }
      if constexpr (i == 4 * NS - 1) {
        asm volatile("" ::: "memory");
        u32 arr = 0;
        if (lane == 0) arr = __hip_atomic_fetch_add(lorg + 6, 1u, __ATOMIC_RELAXED, __HIP_MEMORY_SCOPE_WORKGROUP);
        arr = __builtin_amdgcn_readfirstlane(arr);
        asm volatile("" ::: "memory");
        const f32x2 ml = *reinterpret_cast<const f32x2*>(pml + lane * 2);
        const float M = row16_max(ml[0]);
        const float L = row16_sum(__builtin_amdgcn_exp2f(ml[0] - M) * ml[1]);
        __builtin_amdgcn_raw_buffer_store_b64(u32x2{__float_as_uint(j ? L : M), tg}, rs, (arr == 15u && j < 2) ? (int)WS_MLA + (h * 8 + ci) * 128 + g * 16 + j * 8 : OOB, 0, 16);
      }
    } else {
      const float p = dpp<0x150 + sl>(S[bb]);
      if constexpr (sl == 0) {
#pragma unroll
        for (int k = 0; k < 8; ++k) acc[k] = 0.f;
      }
#pragma unroll
      for (int k = 0; k < 4; ++k) { acc[2 * k] = fmaf(p, bf_lo(r[k]), acc[2 * k]); acc[2 * k + 1] = fmaf(p, bf_hi(r[k]), acc[2 * k + 1]); }
      asm volatile("" : "+v"(acc[0]), "+v"(acc[1]), "+v"(acc[2]), "+v"(acc[3]), "+v"(acc[4]), "+v"(acc[5]), "+v"(acc[6]), "+v"(acc[7]));
      if constexpr (!gf) { ISSUE_STEP(); }
      if constexpr (i == XR) xv = __builtin_amdgcn_raw_buffer_load_b64(rs, xoff, 0, 16);
      if constexpr (i == XR + 2) {
        if (w == XW) {
          if (__builtin_expect(!__all(lane >= 8 * ACU || xv[1] == tg), 0)) {
#pragma unroll 1
            for (u32 spins = 0; spins < SPIN_CAP; ++spins) {
              __builtin_amdgcn_s_sleep(8); xv = __builtin_amdgcn_raw_buffer_load_b64(rs, xoff, 0, 16);
              if (__all(lane >= 8 * ACU || xv[1] == tg)) break;
            }
          }
          const float x = __uint_as_float(xv[0]);
          const float m = lane < 8 * ACU ? dpp<0xA0>(x) : -INFINITY, l = lane < 8 * ACU ? dpp<0xF5>(x) : 0.f;
          const float M = xg_max(fmaxf(m, dpp<0x128>(m)));
          float t = __builtin_amdgcn_exp2f(m - M) * l; t = xg_sum(t + dpp<0x128>(t));
          if (lane < 8 && !(lane & 1)) *reinterpret_cast<f32x2*>(wtab + 64 + lane) = f32x2{M, 1.f / t};
          asm volatile("" ::: "memory");
          const f32x2 gm = *reinterpret_cast<const f32x2*>(wtab + 64 + 2 * g);
          wtab[lane] = __builtin_amdgcn_exp2f(pml[lane * 2] - gm[0]) * gm[1];
          asm volatile("" ::: "memory");
          if (lane == 0) __hip_atomic_store(lorg + 7, 1u, __ATOMIC_RELAXED, __HIP_MEMORY_SCOPE_WORKGROUP);
        }
      }
      if constexpr ((i >= WTS0 && i < WTS0 + 4) || (i >= WTS && i < WTS + 4)) {
        constexpr int s = i >= WTS, t = i - (s ? WTS : WTS0);
        const u32x4 lo = tcol16(RW[s], t), hi = tcol16(RW[s], t + 4);
#pragma unroll
        for (int c = 0; c < 4; ++c) {
          WT[2 * s][t][c] = __builtin_amdgcn_update_dpp(lo[c], hi[c], 0x128, 0xf, 0xc, false);
          WT[2 * s + 1][t][c] = __builtin_amdgcn_update_dpp(hi[c], lo[c], 0x128, 0xf, 0x3, false);
        }
        asm volatile("" : "+v"(WT[2 * s][t]), "+v"(WT[2 * s + 1][t]));
      }
      if constexpr (i == PJ0 || i == PJ1 || i == PJ2) {
        constexpr int b = i == PJ0 ? 0 : i == PJ1 ? 1 : 2;
        Y[b] = 0.f;
        if (w < 9) {
          while (__hip_atomic_load(lorg + 24 + b, __ATOMIC_RELAXED, __HIP_MEMORY_SCOPE_WORKGROUP) != 16u || __hip_atomic_load(lorg + 7, __ATOMIC_RELAXED, __HIP_MEMORY_SCOPE_WORKGROUP) == 0u) __builtin_amdgcn_s_sleep(1);
          asm volatile("" ::: "memory");
          project(b);
        }
        asm volatile("" : "+v"(Y[b]));
      }
      if constexpr (sl == NS - 1) {
        const float t0 = sw16(sw32(acc[0], acc[4]), sw32(acc[2], acc[6])), t1 = sw16(sw32(acc[1], acc[5]), sw32(acc[3], acc[7]));
        *reinterpret_cast<u32*>(lds + L_AB + (bb * 16 + w) * 272 + 16 * j + 4 * g) = pk_bf16(t0, t1);
        if constexpr (bb < 3) { asm volatile("" ::: "memory"); if (lane == 0) __hip_atomic_fetch_add(lorg + 24 + bb, 1u, __ATOMIC_RELAXED, __HIP_MEMORY_SCOPE_WORKGROUP); }
      }
    }
    __builtin_amdgcn_sched_barrier(0);
  });
#undef A_ISSUE
  __syncthreads();
  if (w < 9) {
    project(3);
    st16_sc1(rs, WS_Y + ((8 * w + (j & 7)) * 256 + idx) * 128 + ((j >> 3) * 4 + g) * 16, u32x4{pk_bf16(Y[0], Y[1]), tg, pk_bf16(Y[2], Y[3]), tg});
  }
  if (idx < 72) {
    __builtin_amdgcn_sched_barrier(0);
    __builtin_amdgcn_s_barrier();
    __builtin_amdgcn_s_sleep(FDLY);
    const int yo = (int)WS_Y + (idx * 256 + 16 * w) * 128 + lane * 16;
    u32x4 v0, v1;
#pragma unroll 1
    for (u32 spins = 0; spins < SPIN_CAP; ++spins) {
      v0 = __builtin_amdgcn_raw_buffer_load_b128(rs, yo, 0, 16); v1 = __builtin_amdgcn_raw_buffer_load_b128(rs, yo + 1024, 0, 16);
      if (__all(v0[1] == tg && v0[3] == tg && v1[1] == tg && v1[3] == tg)) break;
      __builtin_amdgcn_s_sleep(FSL);
    }
    float y = sw16(sw32(bf_lo(v0[0]) + bf_lo(v1[0]), bf_lo(v0[2]) + bf_lo(v1[2])), sw32(bf_hi(v0[0]) + bf_hi(v1[0]), bf_hi(v0[2]) + bf_hi(v1[2])));
    y += dpp<0x128>(y);
    float* red = reinterpret_cast<float*>(lds + L_PF);
    if (!(lane & 8)) red[((lane & 7) * 4 + g) * 20 + w] = y;
    __syncthreads();
    if (tid < 32) {
      const f32x4 r0 = *reinterpret_cast<const f32x4*>(red + tid * 20), r1 = *reinterpret_cast<const f32x4*>(red + tid * 20 + 4);
      const f32x4 r2 = *reinterpret_cast<const f32x4*>(red + tid * 20 + 8), r3 = *reinterpret_cast<const f32x4*>(red + tid * 20 + 12);
      const f32x4 r = r0 + r1 + r2;
      const __amdgpu_buffer_rsrc_t ro = __builtin_amdgcn_make_buffer_rsrc(a.out, 0, 4 * 1152 * 2, 0x00020000);
      __builtin_amdgcn_raw_buffer_store_b16(to_bf16(((r[0] + r[1]) + (r[2] + r[3])) + (r3[0] + r3[1])), ro, ((tid & 3) * 1152 + idx * 8 + (tid >> 2)) * 2, 0, OAUX);
      __builtin_amdgcn_raw_buffer_store_b16(to_bf16(r3[2] + r3[3]), ro, ((tid >> 3) * 1152 + 576 + idx * 8 + (tid & 7)) * 2, 0, OAUX);
    }
  }
}

#define CROW 1
constexpr int CW = CROW ? 1 : 8, CR = CROW ? 8 : 1;
#define IA 2
#define IB 4
#define IC 6
#define EUV 4
#define YOFF 90
#define WPD 4
#define CQ 0
#define CLAY 1
DI int ctx_row(int p, int hd) {
  if (CLAY == 1) return (int)WS_CTXP + ((p >> 3) << 18) + ((hd >> 2) << 15) + ((p & 7) << 12) + ((hd & 3) << 10);
  if (CLAY == 2) return (int)WS_CTXP + ((p >> 4) << 19) + ((hd >> 1) << 15) + (((p & 15) ^ (hd >> 4)) << 11) + ((hd & 1) << 10);
  if (CLAY == 3) return (int)WS_CTXP + ((p >> 2) << 17) + ((hd >> 3) << 15) + ((p & 3) << 13) + ((hd & 7) << 10);
  return (int)WS_CTXP + (p * 32 + hd) * 1024;
}
template <bool LD> DI void part_b(const Args& a, unsigned char* lds, int wg, u64 tag, u32 pf) {
  const int tid = threadIdx.x, lane = tid & 63, w = __builtin_amdgcn_readfirstlane(tid >> 6);
  const int j = lane & 15, g = lane >> 4;
  __amdgpu_buffer_rsrc_t rs;
  u64 tb64 = __builtin_amdgcn_s_memrealtime(); u32 tb0;
  const int h = BPF ? (wg & 7) * 4 + (wg >> 3) : wg;
  const int h3 = h ^ 2;
  const int b2 = BMAP ? (wg & 7) >> 1 : wg >> 3, sp = BMAP ? (wg >> 3) * 2 + (wg & 1) : wg & 7;
  const u16* cl;
  u32x4 X[16];
  u32x2 RW[4][8];
  u32x4 W8[2];
  u32* cnt = reinterpret_cast<u32*>(lds + L_ST + 4064);
#define B_POST(T) _Pragma("unroll") for (int k = 0; k < NRND; ++k) if (mtab[k] == (T) && xtab[k]) { u32x2 pv_ = {rt32(), (u32)tag + k}; int po_ = (w == 8 && !((wg ^ (wg >> 3)) & 1)) ? (int)WS_RPT + (16 + (wg >> 1)) * 4352 + 128 * 35 * k : OOB; \
    asm volatile("" : "+v"(pv_), "+v"(po_)); EXM(1, __builtin_amdgcn_raw_buffer_store_b64(pv_, rs, po_, 0, 16)); }
#define C_ISSUE(T) _Pragma("unroll") for (int r = 0; r < 8; ++r) X[8 * ((T) & 1) + r] = ld16(cl + (size_t)(64 * (T) + CR * r) * DC)
#define C_HALF(T, hf) _Pragma("unroll") for (int r = 4 * (hf); r < 4 * (hf) + 4; ++r) X[8 * ((T) & 1) + r] = ld16(cl + (size_t)(64 * (T) + CR * r) * DC)
#define L_PART(T, r0, r1) do { __builtin_amdgcn_sched_barrier(0); \
    if ((T) + 3 < 8) { _Pragma("unroll") for (int r = r0; r < r1; ++r) X[8 * (((T) + 3) & 1) + r] = ld16(cl + (size_t)(64 * ((T) + 3) + CR * r) * DC); } \
    if ((T) == 5 || (T) == 6) { _Pragma("unroll") for (int k = 2 * (r0); k < 2 * (r1); ++k) \
      RW[2 * (((T) - 5) & 1) + (k >> 3)][k & 7] = ld8(a.woB + ((size_t)h3 * 128 + 32 * (2 * (((T) - 5) & 1) + (k >> 3)) + 8 * g + (k & 7)) * DM + 64 * (w - 8) + 4 * j); } \
    __builtin_amdgcn_sched_barrier(0); } while (0)
#define UV_LOAD(n) do { const u32x4 v_ = ld16(a.wuv + ((size_t)(64 * w + 32 * ((n) >> 3) + 8 * g + ((n) & 7)) * H + h3) * 128 + 8 * j); \
    RW[2 * ((n) >> 3)][(n) & 7] = u32x2{v_[0], v_[1]}; RW[2 * ((n) >> 3) + 1][(n) & 7] = u32x2{v_[2], v_[3]}; } while (0)
#define C_WIN(T, r0, r1) _Pragma("unroll") for (int r = r0; r < r1; ++r) { asm volatile("s_waitcnt vmcnt(%0)" :: "n"(WPD - 1) : "memory"); X[8 * ((T) & 1) + r] = ld16(cl + (size_t)(64 * (T) + CR * r) * DC); __builtin_amdgcn_sched_barrier(0); }
  u32 sb0 = L_BIG + CW * (w - 8) * CRS + lane * 16, sb1 = sb0 + CBUF;
  asm volatile("" : "+v"(sb0), "+v"(sb1));
#define C_STAGE(T) _Pragma("unroll") for (int r = 0; r < 8; ++r) *reinterpret_cast<u32x4*>(lds + (((T) & 1) ? sb1 : sb0) + CR * r * CRS) = X[8 * ((T) & 1) + r]
  {
    const u16* Wq = a.wqB + h * 128;
    u32x4 U[8];
    QPre Q;
    qproj_load(Q, a.xB, Wq, w, lane);
#pragma unroll
    for (int i = 0; i < 8; ++i) U[i] = ld16(a.wuk + ((size_t)(32 * w + 4 * i + (j >> 2)) * H + h) * 128 + 8 * (4 * g + (j & 3)));
    __builtin_amdgcn_sched_barrier(0);
    { unsigned char* wsp = a.ws; u64 c64 = (u64)a.C; asm volatile("" : "+s"(wsp), "+s"(c64), "+s"(tb64) : "s"(a.wuv), "s"(a.woB), "s"(a.out));
      const u16* cp = (const u16*)(const __attribute__((address_space(1))) u16*)c64; tb0 = (u32)tb64;
      rs = __builtin_amdgcn_make_buffer_rsrc(wsp, 0, (int)WS_END, 0x00020000);
      cl = cp + ((size_t)b2 * SK + sp * 512 + CW * (w - 8)) * DC + lane * 8; }
    if constexpr (!LD) am_zero(lds, tid);
    qproj_mfma(Q, lds, w, lane);
    asm volatile("" :: "v"(pf));
    __syncthreads();
    if constexpr (LD) { C_HALF(0, 0); }
    else {
      const float* qpart = reinterpret_cast<const float*>(lds + L_BIG);
      float s = 0.f;
#pragma unroll
      for (int ww = 0; ww < 16; ++ww) s += qpart[ww * 512 + tid];
      reinterpret_cast<u16*>(lds + L_QS)[tid] = to_bf16(s);
    }
    __builtin_amdgcn_sched_barrier(0);
    __syncthreads();
    const u32x4 af = *reinterpret_cast<const u32x4*>(lds + L_QS + ((j >> 2) * 128 + (4 * g + (j & 3)) * 8) * 2);
    u16* qa = reinterpret_cast<u16*>(lds + L_PF);
    float v2[2] = {0.f, 0.f};
#pragma unroll
    for (int i = 0; i < 8; ++i) {
      const f32x4 d4 = mfma16(af, U[i], f32x4{0.f, 0.f, 0.f, 0.f});
      float d = (j & 2) ? ((j & 1) ? d4[3] : d4[2]) : ((j & 1) ? d4[1] : d4[0]);
      d += dpp<0xB1>(d); d += dpp<0x4E>(d);
      v2[i >> 2] = ((j & 3) == (i & 3)) ? d : v2[i >> 2];
    }
    qa[g * 512 + 32 * w + 4 * (j & 3) + (j >> 2)] = to_bf16(v2[0]); qa[g * 512 + 32 * w + 16 + 4 * (j & 3) + (j >> 2)] = to_bf16(v2[1]);
    __builtin_amdgcn_sched_barrier(0);
    __syncthreads();
    if constexpr (!LD) {
      if (w == 0) {
#pragma unroll
        for (int bb = 0; bb < 4; ++bb) st16_sc1(rs, WS_QABS + ((bb * H + h) * 512 + lane * 8) * 2, *reinterpret_cast<const u32x4*>(qa + bb * 512 + lane * 8));
      }
      __builtin_amdgcn_sched_barrier(0);
      __builtin_amdgcn_s_barrier();
      if (w == 0) {
        asm volatile("s_waitcnt vmcnt(0)" ::: "memory");
        if (lane == 0) { __hip_atomic_store(reinterpret_cast<u64*>(a.ws + WS_F1 + wg * FST), tag, RLX_AGENT); cnt[0] = 0u; cnt[1] = 0u; }
      }
    } else {
      __builtin_amdgcn_s_barrier();
      C_WIN(0, 4, 8); C_WIN(1, 0, 8);
      C_STAGE(0);
      __builtin_amdgcn_sched_barrier(0);
      C_WIN(2, 0, 8);
    }
  }
  {
    const int b = b2;
    if constexpr (!LD) wg_wait64(a.ws + WS_F1, tag); else __syncthreads();
    const int kt = w & 3, hh = (w >> 2) & 1;
    float* stmax = reinterpret_cast<float*>(lds + L_ST);
    float* stsum = stmax + 128;
    float* salpha = stmax + 256;
    unsigned char* Pf = lds + L_PF;
    float M = -INFINITY, lsum = 0.f;
    f32x4 acc[2][2];
#pragma unroll
    for (int ct = 0; ct < 2; ++ct) { acc[ct][0] = f32x4{0.f, 0.f, 0.f, 0.f}; acc[ct][1] = f32x4{0.f, 0.f, 0.f, 0.f}; }
    const int troff = (4 * g + (j >> 2)) * CRS + (32 * w + 4 * (j & 3)) * 2;
    auto pv_step = [&](const unsigned char* cb) __attribute__((always_inline)) {
      const f32x4 al0 = *reinterpret_cast<const f32x4*>(salpha + 4 * g), al1 = *reinterpret_cast<const f32x4*>(salpha + 16 + 4 * g);
#pragma unroll
      for (int ct = 0; ct < 2; ++ct) { acc[ct][0] *= al0; acc[ct][1] *= al1; }
#pragma unroll
      for (int blk = 0; blk < 2; ++blk) {
        const u32x4 p0 = *reinterpret_cast<const u32x4*>(Pf + ((blk * 2 + 0) * 64 + lane) * 16);
        const u32x4 p1 = *reinterpret_cast<const u32x4*>(Pf + ((blk * 2 + 1) * 64 + lane) * 16);
#pragma unroll
        for (int ct = 0; ct < 2; ++ct) {
          const u32x2 lo = trread(cb + troff + (32 * blk) * CRS + 32 * ct);
          const u32x2 hi = trread(cb + troff + (32 * blk + 16) * CRS + 32 * ct);
          const u32x4 bf = {lo[0], lo[1], hi[0], hi[1]};
          acc[ct][0] = mfma16(p0, bf, acc[ct][0]); acc[ct][1] = mfma16(p1, bf, acc[ct][1]);
        }
      }
    };
    unsigned char* Qf = lds + L_BIG + CBUF;
    if constexpr (!LD) {
      u32x4 qv[4];
#pragma unroll
      for (int k = 0; k < 4; ++k) qv[k] = __builtin_amdgcn_raw_buffer_load_b128(rs, (int)WS_QABS + (b * H * 512 + (tid + 512 * k) * 8) * 2, 0, 16);
#pragma unroll
      for (int k = 0; k < 4; ++k) { const int e = tid + 512 * k, head = e >> 6, ch = e & 63;
        *reinterpret_cast<u32x4*>(Qf + ((head >> 4) * 16 + (ch >> 2)) * 1040 + ((ch & 3) * 16 + (head & 15)) * 16) = qv[k]; }
    }
    __syncthreads();
    if constexpr (!LD) {
      u32x4 QF[16];
#pragma unroll
      for (int s = 0; s < 16; ++s) QF[s] = *reinterpret_cast<const u32x4*>(Qf + (hh * 16 + s) * 1040 + lane * 16);
      auto s_tile = [&](const unsigned char* cb, bool last) __attribute__((always_inline)) {
        __syncthreads();
        f32x4 s0 = {0.f, 0.f, 0.f, 0.f}, s1 = {0.f, 0.f, 0.f, 0.f};
#pragma unroll
        for (int s = 0; s < 16; s += 2) {
          s0 = mfma16(*reinterpret_cast<const u32x4*>(cb + (16 * kt + j) * CRS + (32 * s + 8 * g) * 2), QF[s], s0);
          s1 = mfma16(*reinterpret_cast<const u32x4*>(cb + (16 * kt + j) * CRS + (32 * s + 32 + 8 * g) * 2), QF[s + 1], s1);
        }
        const f32x4 sv = (s0 + s1) * C2;
        if (last) { __builtin_amdgcn_sched_barrier(0); _Pragma("unroll") for (int n = 0; n < EUV; ++n) UV_LOAD(n); __builtin_amdgcn_sched_barrier(0); }
        const float v = xg_max(fmaxf(fmaxf(sv[0], sv[1]), fmaxf(sv[2], sv[3])));
        if (g == 0) stmax[kt * 32 + 16 * hh + j] = v;
        __syncthreads();
        const float* sm = stmax + 16 * hh + j;
        const float Mn = fmaxf(fmaxf(M, fmaxf(sm[0], sm[32])), fmaxf(sm[64], sm[96]));
        const float alpha = __builtin_amdgcn_exp2f(M - Mn);
        M = Mn;
        u32x2 pk;
        pk[0] = pk_bf16(__builtin_amdgcn_exp2f(sv[0] - Mn), __builtin_amdgcn_exp2f(sv[1] - Mn));
        pk[1] = pk_bf16(__builtin_amdgcn_exp2f(sv[2] - Mn), __builtin_amdgcn_exp2f(sv[3] - Mn));
        lsum = lsum * alpha + ((bf_lo(pk[0]) + bf_hi(pk[0])) + (bf_lo(pk[1]) + bf_hi(pk[1])));
        *reinterpret_cast<u32x2*>(Pf + (((kt >> 1) * 2 + hh) * 64 + lane) * 16 + (kt & 1) * 8) = pk;
        if (kt == 0 && g == 0) salpha[16 * hh + j] = alpha;
        __syncthreads();
        pv_step(cb);
      };
#pragma unroll 1
      for (int T = 0; T < 6; T += 2) { s_tile(lds + L_BIG, false); s_tile(lds + L_BIG + CBUF, false); }
      s_tile(lds + L_BIG, false); s_tile(lds + L_BIG + CBUF, true);
    } else {
#pragma unroll
      for (int T = 0; T < 8; ++T) {
        __syncthreads();
        B_POST(T);
        if (T + 1 < 8) { C_STAGE(T + 1); }
        L_PART(T, 0, IA);
        if (T == 7) {
#pragma unroll
          for (int k = 0; k < 2; ++k) W8[k] = ld16(a.woB + ((size_t)h3 * 128 + 16 * (w - 8) + 8 * k + (lane >> 3)) * DM + 512 + (lane & 7) * 8);
        }
        __syncthreads();
        L_PART(T, IA, IB);
        __syncthreads();
        L_PART(T, IB, IC);
        pv_step(lds + L_BIG + (T & 1) * CBUF);
        L_PART(T, IC, 8);
      }
      B_POST(8);
    }
#undef C_ISSUE
#undef C_STAGE
    __syncthreads();
    if constexpr (!LD) { lsum = xg_sum(lsum); if (g == 0) stsum[kt * 32 + 16 * hh + j] = lsum; if (kt == 0 && g == 0) salpha[32 + 16 * hh + j] = M; }
    float* tb = reinterpret_cast<float*>(lds + L_BIG);
#pragma unroll
    for (int ct = 0; ct < 2; ++ct)
#pragma unroll
      for (int h2 = 0; h2 < 2; ++h2)
#pragma unroll
        for (int r = 0; r < 4; ++r) tb[(16 * h2 + 4 * g + r) * 516 + 32 * w + 16 * ct + j] = acc[ct][h2][r];
    if constexpr (!LD) { for (int i = tid; i < 8 * AM2_LD * 2 / 16; i += 512) reinterpret_cast<u32x4*>(lds + L_AM2 + 8 * AM2_LD * 2)[i] = u32x4{0u, 0u, 0u, 0u}; }
    __syncthreads();
    if constexpr (LD) {
      const int t2 = tid - 512;
      if (CQ) { const u32 tq = (rt32() + (CQ - 1)) & ~(u32)(CQ - 1); while ((int)(rt32() - tq) < 0) __builtin_amdgcn_s_sleep(1); }
#pragma unroll
      for (int k = 0; k < 4; ++k) {
        const int e = t2 + 512 * k, head = e >> 6, c8 = e & 63;
        const f32x4 v0 = *reinterpret_cast<const f32x4*>(tb + head * 516 + 8 * c8), v1 = *reinterpret_cast<const f32x4*>(tb + head * 516 + 8 * c8 + 4);
        __builtin_amdgcn_raw_buffer_store_b128(u32x4{pk_bf16(v0[0], v0[1]), pk_bf16(v0[2], v0[3]), pk_bf16(v1[0], v1[1]), pk_bf16(v1[2], v1[3])}, rs, ctx_row(b * NSP + sp, head) + 16 * c8, 0, 16);
      }
      if (t2 < 32) {
        const float L = (stsum[t2] + stsum[32 + t2]) + (stsum[64 + t2] + stsum[96 + t2]);
        st8_sc1(a.ws + WS_MLB + ((size_t)(b * NSP + sp) * 32 + t2) * 8, f32x2{salpha[32 + t2], L});
      }
      __builtin_amdgcn_sched_barrier(0);
      __builtin_amdgcn_s_barrier();
      asm volatile("s_waitcnt vmcnt(0)" ::: "memory");
      if (lane == 0) {
        if (__hip_atomic_fetch_add(cnt, 1u, __ATOMIC_RELAXED, __HIP_MEMORY_SCOPE_WORKGROUP) == 7u) {
          __hip_atomic_store(reinterpret_cast<u64*>(a.ws + WS_F2 + wg * FST), tag, RLX_AGENT);
        }
      }
#pragma unroll
      for (int k = 0; k < 2; ++k) *reinterpret_cast<u32x4*>(lds + L_W8 + (16 * (w - 8) + 8 * k + (lane >> 3)) * 144 + (lane & 7) * 16) = W8[k];
    } else {
      __builtin_amdgcn_s_barrier();
      static_for<16 - EUV>([&](auto ic) __attribute__((always_inline)) {
        asm volatile("s_waitcnt vmcnt(%0)" :: "n"(WPD - 1) : "memory");
        UV_LOAD(EUV + ic.value);
        __builtin_amdgcn_sched_barrier(0);
      });
    }
  }
  {
    int tid = threadIdx.x; asm volatile("" : "+v"(tid));
    const int lane = tid & 63, j = lane & 15, g = lane >> 4;
    u16* Am2 = reinterpret_cast<u16*>(lds + L_AM2);
    float* cpart = reinterpret_cast<float*>(lds + L_CP);
    __builtin_amdgcn_sched_barrier(0);
    if constexpr (LD) {
      if (w == 8) {
        wave_wait(reinterpret_cast<const u64*>(a.ws + WS_F2) + (lane & (NBW - 1)) * (FST / 8), lane < NBW, tag);
        asm volatile("s_waitcnt vmcnt(0)" ::: "memory");
        if (lane == 0) __hip_atomic_store(cnt + 1, 1u, __ATOMIC_RELEASE, __HIP_MEMORY_SCOPE_WORKGROUP);
      } else {
        for (u32 spins = 0; spins < 64u * SPIN_CAP; ++spins) { if (__hip_atomic_load(cnt + 1, __ATOMIC_ACQUIRE, __HIP_MEMORY_SCOPE_WORKGROUP) != 0u) break; __builtin_amdgcn_s_sleep(1); }
      }
      __builtin_amdgcn_sched_barrier(0);
      const int b = (tid >> 7) & 3, c4 = tid & 127;
      u32x2 pv[NSP];
      const f32x2 ml = __builtin_bit_cast(f32x2, __builtin_amdgcn_raw_buffer_load_b64(rs, (int)WS_MLB + ((b * NSP + (j & 7)) * 32 + h3) * 8, 0, 16));
#pragma unroll
      for (int s = 0; s < NSP; ++s) pv[s] = __builtin_amdgcn_raw_buffer_load_b64(rs, ctx_row(b * NSP + s, h3) + 8 * c4, 0, 16);
      const float M = row16_max(ml[0]);
      float wt = __builtin_amdgcn_exp2f(ml[0] - M);
      wt *= 2.f / row16_sum(wt * ml[1]);
      float c0 = 0.f, c1 = 0.f, c2 = 0.f, c3 = 0.f;
#pragma unroll
      for (int s = 0; s < NSP; ++s) { const float ws_ = rdlane(wt, s); c0 = fmaf(ws_, bf_lo(pv[s][0]), c0); c1 = fmaf(ws_, bf_hi(pv[s][0]), c1); c2 = fmaf(ws_, bf_lo(pv[s][1]), c2); c3 = fmaf(ws_, bf_hi(pv[s][1]), c3); }
      const u32 h0 = pk_bf16(c0, c1), h1 = pk_bf16(c2, c3);
      *reinterpret_cast<u32x2*>(Am2 + (2 * b) * AM2_LD + 4 * c4) = u32x2{h0, h1};
      *reinterpret_cast<u32x2*>(Am2 + (2 * b + 1) * AM2_LD + 4 * c4) = u32x2{pk_bf16(c0 - bf_lo(h0), c1 - bf_hi(h0)), pk_bf16(c2 - bf_lo(h1), c3 - bf_hi(h1))};
      __syncthreads();
      __syncthreads();
      const int t2 = tid - 512;
      float s = 0.f;
#pragma unroll
      for (int ww = 0; ww < 8; ++ww) s += cpart[ww * 512 + t2];
      u16* Am = reinterpret_cast<u16*>(lds + L_AM);
      const int bb = t2 >> 7, d = t2 & 127;
      const u16 hi = to_bf16(s);
      Am[(4 * bb) * AM_LD + d] = hi; Am[(4 * bb + 1) * AM_LD + d] = to_bf16(s - bf2f(hi));
    } else {
      __syncthreads();
      f32x4 acc[8];
#pragma unroll
      for (int t = 0; t < 8; ++t) acc[t] = f32x4{0.f, 0.f, 0.f, 0.f};
#pragma unroll
      for (int k2 = 0; k2 < 2; ++k2) {
        const u32x4 af = *reinterpret_cast<const u32x4*>(Am2 + j * AM2_LD + 64 * w + 32 * k2 + 8 * g);
#pragma unroll
        for (int t = 0; t < 8; ++t) acc[t] = mfma16(af, tcol8(RW[2 * k2 + (t >> 2)], t & 3), acc[t]);
      }
      if (g < 2) {
#pragma unroll
        for (int bb = 0; bb < 2; ++bb) {
          float* p = cpart + (w * 4 + 2 * g + bb) * 128 + 8 * j;
          *reinterpret_cast<f32x4*>(p) = f32x4{acc[0][2 * bb] + acc[0][2 * bb + 1], acc[1][2 * bb] + acc[1][2 * bb + 1], acc[2][2 * bb] + acc[2][2 * bb + 1], acc[3][2 * bb] + acc[3][2 * bb + 1]};
          *reinterpret_cast<f32x4*>(p + 4) = f32x4{acc[4][2 * bb] + acc[4][2 * bb + 1], acc[5][2 * bb] + acc[5][2 * bb + 1], acc[6][2 * bb] + acc[6][2 * bb + 1], acc[7][2 * bb] + acc[7][2 * bb + 1]};
        }
      }
      __builtin_amdgcn_sched_barrier(0);
      __syncthreads();
      if (YDEF == 1 && w == 1) {
        u32 dl = tb0 + 5200u;
#pragma unroll 1
        while ((int)(rt32() - dl) < 0) {
          const u32x2 rv = __builtin_amdgcn_raw_buffer_load_b64(rs, lane < 16 ? (int)WS_RPT + lane * 4352 : OOB, 128 * (NRND - 1) * 35, 16);
          const bool ok = rv[1] == (u32)tag + (NRND - 1);
          const float cn = rdlane(row16_sum(ok ? 1.f : 0.f), 0);
          if (cn >= 8.f) { dl = tb0 + (u32)(int)(rdlane(row16_sum(ok ? (float)(int)(rv[0] - tb0) : 0.f), 0) / cn) + (u32)CI(due_at(NSEQ - 1) - due_at(rtab[NRND - 1]) + YOFF); break; }
          __builtin_amdgcn_s_sleep(32);
        }
        if (lane == 0) cnt[2] = (int)(dl - tb0) > 5200 ? tb0 : dl;
      }
      if (w == 0) {
#pragma unroll
        for (int s = 0; s < 4; ++s)
#pragma unroll
          for (int jj = 0; jj < 8; ++jj) RW[s][jj] = *reinterpret_cast<const u32x2*>(lds + L_W8 + (32 * s + 8 * g + jj) * 144 + 8 * j);
      }
    }
    __syncthreads();
    if (LD || w == 0) {
      f32x4 ya[4];
      wo_mfma(RW, lds, lane, ya);
      if (YDEF) { const u32 dl = YDEF == 1 ? __builtin_amdgcn_readfirstlane(cnt[2]) : tb0 + YDEF; while ((int)(rt32() - dl) < 0) __builtin_amdgcn_s_sleep(4); }
      st16_sc1(rs, WS_Y + ((8 * (LD ? w - 8 : 8) + (j >> 1)) * 256 + NAW + h3) * 128 + g * 32 + (j & 1) * 16, yrow(ya, (u32)(tag >> 32)));
    }
  }
}

__global__ void __launch_bounds__(1024, 4) k_main(Args a) {
  __shared__ __attribute__((aligned(16))) unsigned char lds[L_TOTAL];
  u32 pf;
  { u64 pc; u32 len;
    asm volatile("s_getpc_b64 %0\n.Lpf%=:\n\ts_mov_b32 %1, k_tail_mark-.Lpf%=" : "=s"(pc), "=s"(len));
    pf = __builtin_amdgcn_raw_buffer_load_b32(__builtin_amdgcn_make_buffer_rsrc(reinterpret_cast<void*>(pc), 0, len < 131072u ? len : 0u, 0x00020000), CPFM ? (threadIdx.x < 16 ? ((blockIdx.x >> 3) * 16 + threadIdx.x) * 128 : 0x7ffff000) : threadIdx.x * 128, 0, 0);
    __builtin_amdgcn_sched_barrier(0); }
  u64 tag = (hw_dispatch_id() + 1ull) * 0x9E3779B97F4A7C15ull;
  tag ^= ((u64)__builtin_amdgcn_dispatch_ptr() >> 6) * 0xC2B2AE3D27D4EB4Full;
  tag |= 1ull << 63;
  if (__builtin_expect(blockIdx.x >= NBW, 1)) part_a(a, lds, blockIdx.x - NBW, tag, pf);
  else if (NOB) { const int w = threadIdx.x >> 6, lane = threadIdx.x & 63, j = lane & 15, g = lane >> 4; const u32 tg = (u32)(tag >> 32);
    if (w < 9) st16_sc1(__builtin_amdgcn_make_buffer_rsrc(a.ws, 0, (int)WS_END, 0x00020000), WS_Y + ((8 * w + (j >> 1)) * 256 + NAW + blockIdx.x) * 128 + g * 32 + (j & 1) * 16, u32x4{0u, tg, 0u, tg}); }
  else if (__builtin_amdgcn_readfirstlane(threadIdx.x >> 6) < 8) part_b<false>(a, lds, blockIdx.x, tag, pf);
  else part_b<true>(a, lds, blockIdx.x, tag, pf);
}

extern "C" __global__ void k_tail_mark() {}

extern "C" void kernel_launch(void* const* d_in, const int* in_sizes, int n_in, void* d_out, int out_size, void* d_ws, size_t ws_size,
                              hipStream_t stream) {
  Args a;
  a.xA = (const u16*)d_in[0]; a.K = (const u16*)d_in[1]; a.V = (const u16*)d_in[2]; a.wq = (const u16*)d_in[3]; a.wo = (const u16*)d_in[4];
  a.xB = (const u16*)d_in[5]; a.C = (const u16*)d_in[6]; a.wqB = (const u16*)d_in[7]; a.wuk = (const u16*)d_in[8]; a.wuv = (const u16*)d_in[9]; a.woB = (const u16*)d_in[10];
#define WSTOP 32
  if (WSTOP && ws_size >= ((size_t)WSTOP << 20)) d_ws = (unsigned char*)d_ws + ((ws_size - ((size_t)WSTOP << 20)) & ~(size_t)4095);
  a.ws = (unsigned char*)d_ws;
  a.out = (u16*)d_out;
  k_main<<<NBW + NAW, 1024, 0, stream>>>(a);
}
```

```cpp
#include <hip/hip_runtime.h>
#include <stdint.h>
#include <utility>

typedef unsigned short u16;
typedef unsigned u32;
typedef unsigned long long u64;
typedef u32 u32x4 __attribute__((ext_vector_type(4)));
typedef u32 u32x2 __attribute__((ext_vector_type(2)));
typedef float f32x4 __attribute__((ext_vector_type(4)));
typedef float f32x2 __attribute__((ext_vector_type(2)));
typedef short s16x4 __attribute__((ext_vector_type(4)));
typedef __bf16 bf16x8 __attribute__((ext_vector_type(8)));
typedef __bf16 bf16x2 __attribute__((ext_vector_type(2)));
#define DI __device__ __forceinline__
#define RLX_AGENT __ATOMIC_RELAXED, __HIP_MEMORY_SCOPE_AGENT
extern "C" __device__ u64 hw_dispatch_id() __asm("llvm.amdgcn.dispatch.id");

template <int... Is, class F> DI void static_for_impl(std::integer_sequence<int, Is...>, F&& f) { (f(std::integral_constant<int, Is>{}), ...); }
template <int N, class F> DI void static_for(F&& f) { static_for_impl(std::make_integer_sequence<int, N>{}, f); }

constexpr int H = 32, SK = 4096, DM = 576, DC = 512;
constexpr int ACU = 7;
constexpr int NBW = 32;
constexpr int NAW = H * ACU;
constexpr int NSP = 8;
constexpr float C2 = 0.08838834764831845f * 1.4426950408889634f;

#define FST 4352
constexpr size_t WS_CTXP = 0;
constexpr size_t WS_QABS = WS_CTXP + (size_t)4 * NSP * 32 * 512 * 2;
constexpr size_t WS_MLB = WS_QABS + 4 * 32 * 512 * 2;
constexpr size_t WS_Y = WS_MLB + 4 * NSP * 32 * 2 * 4;
constexpr size_t WS_MLA = WS_Y + (size_t)72 * 256 * 128;
constexpr size_t WS_RPT = WS_MLA + 256 * 128;
constexpr size_t WS_QA = WS_RPT + 64 * 4480;
constexpr size_t WS_F1 = WS_QA + 256 * 2048, WS_F2 = WS_F1 + 512;
constexpr size_t WS_END = WS_F1 + 32 * 4352 + 1024;

constexpr int CRS = 1056;
constexpr int CBUF = 64 * CRS;
constexpr int L_BIG = 0;
constexpr int L_AM = 2 * CBUF;
constexpr int L_QS = L_AM + 16 * 272;
constexpr int L_ST = L_QS + 1024;
constexpr int L_PF = L_ST + 4096;
constexpr int L_TOTAL = L_PF + 4096;
constexpr int AM_LD = 136;
constexpr int AM2_LD = 520;
constexpr int L_AM2 = 69632, L_CP = 88064, L_W8 = 106496;

#define KPL 0
#define TMX 20
#define KT 0
#define BW 0
#define T0L 22
struct Args {
  const u16 *xA, *K, *V, *wq, *wo, *xB, *C, *wqB, *wuk, *wuv, *woB;
  unsigned char* ws;
  u16* out;
};

DI float bf_lo(u32 x) { return __uint_as_float(x << 16); }
DI float bf_hi(u32 x) { return __uint_as_float(x & 0xffff0000u); }
DI u32 pk_bf16(float lo, float hi) { bf16x2 v = {(__bf16)lo, (__bf16)hi}; return __builtin_bit_cast(u32, v); }
DI u16 to_bf16(float x) { return __builtin_bit_cast(u16, (__bf16)x); }
DI float bf2f(u16 x) { return __uint_as_float(((u32)x) << 16); }
DI float dot2(u32 a, u32 b, float c) { return __builtin_amdgcn_fdot2_f32_bf16(__builtin_bit_cast(bf16x2, a), __builtin_bit_cast(bf16x2, b), c, false); }
template <int CTRL> DI float dpp(float x) { return __builtin_bit_cast(float, __builtin_amdgcn_update_dpp(0, __builtin_bit_cast(int, x), CTRL, 0xf, 0xf, true)); }
DI float row16_sum(float x) { x += dpp<0x128>(x); x += dpp<0x124>(x); x += dpp<0x122>(x); x += dpp<0x121>(x); return x; }
DI float row16_max(float x) { x = fmaxf(x, dpp<0x128>(x)); x = fmaxf(x, dpp<0x124>(x)); x = fmaxf(x, dpp<0x122>(x)); x = fmaxf(x, dpp<0x121>(x)); return x; }
DI float xg_max(float x) {
  auto s = __builtin_amdgcn_permlane16_swap(__float_as_uint(x), __float_as_uint(x), false, false);
  x = fmaxf(__uint_as_float(s[0]), __uint_as_float(s[1]));
  auto t = __builtin_amdgcn_permlane32_swap(__float_as_uint(x), __float_as_uint(x), false, false);
  return fmaxf(__uint_as_float(t[0]), __uint_as_float(t[1]));
}
DI float xg_sum(float x) {
  auto s = __builtin_amdgcn_permlane16_swap(__float_as_uint(x), __float_as_uint(x), false, false);
  x = __uint_as_float(s[0]) + __uint_as_float(s[1]);
  auto t = __builtin_amdgcn_permlane32_swap(__float_as_uint(x), __float_as_uint(x), false, false);
  return __uint_as_float(t[0]) + __uint_as_float(t[1]);
}
DI float sw32(float a, float b) {
  auto s = __builtin_amdgcn_permlane32_swap(__float_as_uint(a), __float_as_uint(b), false, false);
  return __uint_as_float(s[0]) + __uint_as_float(s[1]);
}
DI float sw16(float a, float b) {
  auto s = __builtin_amdgcn_permlane16_swap(__float_as_uint(a), __float_as_uint(b), false, false);
  return __uint_as_float(s[0]) + __uint_as_float(s[1]);
}
DI f32x4 mfma16(u32x4 a, u32x4 b, f32x4 c) { return __builtin_amdgcn_mfma_f32_16x16x32_bf16(__builtin_bit_cast(bf16x8, a), __builtin_bit_cast(bf16x8, b), c, 0, 0, 0); }
DI u32x4 ld16(const u16* p) { return __builtin_nontemporal_load(reinterpret_cast<const u32x4*>(p)); }
DI u32x2 ld8(const u16* p) { return __builtin_nontemporal_load(reinterpret_cast<const u32x2*>(p)); }
DI u32x2 trread(const unsigned char* p) {
  return __builtin_bit_cast(u32x2, __builtin_amdgcn_ds_read_tr16_b64_v4i16((__attribute__((address_space(3))) s16x4*)(p)));
}
DI u32 prm(u32 hi, u32 lo, int odd) { return __builtin_amdgcn_perm(hi, lo, odd ? 0x07060302u : 0x05040100u); }

DI u32x4 tcol16(const u32x4 (&R)[8], int t) {
  u32x4 T;
#pragma unroll
  for (int q = 0; q < 4; ++q) T[q] = prm(R[2 * q + 1][t >> 1], R[2 * q][t >> 1], t & 1);
  return T;
}
DI u32x4 tcol8(const u32x2 (&R)[8], int t) {
  u32x4 T;
#pragma unroll
  for (int q = 0; q < 4; ++q) T[q] = prm(R[2 * q + 1][t >> 1], R[2 * q][t >> 1], t & 1);
  return T;
}

#define SPINLOG 22
#define PSL 4
constexpr u32 SPIN_CAP = 1u << SPINLOG;
DI void wave_wait(const u64* f, bool active, u64 tag) {
#pragma unroll 1
  for (u32 spins = 0; spins < SPIN_CAP; ++spins) {
    if (active) active = __hip_atomic_load(f, RLX_AGENT) != tag;
    if (!__any(active)) break;
    __builtin_amdgcn_s_sleep(PSL);
  }
}
DI void wg_wait64(const unsigned char* flags, u64 tag, int pw = 0) {
  if ((int)(threadIdx.x >> 6) == pw) {
    wave_wait(reinterpret_cast<const u64*>(flags) + (threadIdx.x & (NBW - 1)) * (FST / 8), (threadIdx.x & 63) < NBW, tag);
    asm volatile("s_waitcnt vmcnt(0)" ::: "memory");
  }
  __syncthreads();
}
DI void st16_sc1(__amdgpu_buffer_rsrc_t rs, size_t off, u32x4 v) { __builtin_amdgcn_raw_buffer_store_b128(v, rs, (int)off, 0, 16); }
DI void st8_sc1(unsigned char* p, f32x2 v) { __hip_atomic_store(reinterpret_cast<u64*>(p), __builtin_bit_cast(u64, v), RLX_AGENT); }

struct QPre { u32x4 R0[8]; u32x4 R1; u32x2 xa, xb; u32 x1; };
DI void qproj_load(QPre& Q, const u16* x, const u16* Wh, int w, int lane) {
  const int j = lane & 15, g = lane >> 4;
  Q.xa = u32x2{0u, 0u}; Q.xb = u32x2{0u, 0u}; Q.x1 = 0u;
  if (j < 4) { Q.xa = ld8(x + j * DM + 36 * w + 8 * g); Q.xb = ld8(x + j * DM + 36 * w + 8 * g + 4); Q.x1 = x[j * DM + 36 * w + 32 + g]; }
#pragma unroll
  for (int jj = 0; jj < 8; ++jj) Q.R0[jj] = ld16(Wh + (size_t)(36 * w + 8 * g + jj) * 4096 + 8 * j);
  Q.R1 = ld16(Wh + (size_t)(36 * w + 32 + g) * 4096 + 8 * j);
}
DI void qproj_mfma(const QPre& Q, unsigned char* lds, int w, int lane) {
  const int j = lane & 15, g = lane >> 4;
  float* qpart = reinterpret_cast<float*>(lds + L_BIG);
  const u32x4 a = {Q.xa[0], Q.xa[1], Q.xb[0], Q.xb[1]}, a1 = {Q.x1, 0u, 0u, 0u};
#pragma unroll
  for (int hf = 0; hf < 2; ++hf) {
    f32x4 acc[4];
#pragma unroll
    for (int t = 0; t < 4; ++t) acc[t] = mfma16(a, tcol16(Q.R0, 4 * hf + t), f32x4{0.f, 0.f, 0.f, 0.f});
#pragma unroll
    for (int t = 0; t < 4; ++t) { const u32x4 bb = {(t & 1) ? (Q.R1[2 * hf + (t >> 1)] >> 16) : (Q.R1[2 * hf + (t >> 1)] & 0xffffu), 0u, 0u, 0u}; acc[t] = mfma16(a1, bb, acc[t]); }
    if (g == 0) {
#pragma unroll
      for (int r = 0; r < 4; ++r) *reinterpret_cast<f32x4*>(qpart + (w * 4 + r) * 128 + 8 * j + 4 * hf) = f32x4{acc[0][r], acc[1][r], acc[2][r], acc[3][r]};
    }
    __builtin_amdgcn_sched_barrier(0);
  }
}
DI void wo_load(const u16* Wo, u32x2 (&R)[4][8], int nb, int lane, int s0, int s1) {
  const int j = lane & 15, g = lane >> 4;
#pragma unroll
  for (int s = 0; s < 4; ++s) if (s >= s0 && s < s1)
#pragma unroll
    for (int jj = 0; jj < 8; ++jj) R[s][jj] = ld8(Wo + (size_t)(32 * s + 8 * g + jj) * DM + 64 * nb + 4 * j);
}
DI void wo_mfma(const u32x2 (&R)[4][8], const unsigned char* lds, int lane, f32x4 (&acc)[4]) {
  const int j = lane & 15, g = lane >> 4;
#pragma unroll
  for (int t = 0; t < 4; ++t) acc[t] = f32x4{0.f, 0.f, 0.f, 0.f};
#pragma unroll
  for (int s = 0; s < 4; ++s) {
    const u32x4 a = *reinterpret_cast<const u32x4*>(lds + L_AM + j * 272 + (32 * s + 8 * g) * 2);
#pragma unroll
    for (int t = 0; t < 4; ++t) acc[t] = mfma16(a, tcol8(R[s], t), acc[t]);
  }
}
DI u32x4 yrow(const f32x4 (&acc)[4], u32 tg) {
  return u32x4{pk_bf16(acc[0][0] + acc[0][1], acc[1][0] + acc[1][1]), tg, pk_bf16(acc[2][0] + acc[2][1], acc[3][0] + acc[3][1]), tg};
}
DI void am_zero(unsigned char* lds, int tid) {
  if (tid < 136) { const int pr = tid / 34; *reinterpret_cast<u32x4*>(lds + L_AM + (4 * pr + 2) * 272 + (tid - 34 * pr) * 16) = u32x4{0u, 0u, 0u, 0u}; }
}
DI float rdlane(float x, int l) { return __builtin_bit_cast(float, __builtin_amdgcn_readlane(__builtin_bit_cast(int, x), l)); }

constexpr int NS = 10;
constexpr int NSEQ = 8 * NS;
#define RNG 9
#define NP 3
#define QP 4
#define SP 4
#define BPF 2
#define TPS 3
#define KD0 60
#define KDP 60
#define KDS 60
#define NOWO 0
#define NOB 0
#define GF0 12
#define GF1 99
#define WOF 1
#define RAUX 16
#define SAUX 16
#define NOACT 0
#define NOPOST 0
#define NOREAD 0
#define PFULL 0
#define PAUX 16
#define RSPR 0
#define QTRY 0
#define QAUX 3
#define QSA 16
#define LATE 0
#define EXQ 1
#define PF2S 0
#define SMO 1
#define TST 440
#define WTS0 42
#define SLN 0
#define SLK 3
#define SLT 0x7513
#define BMODE 2
#define NB 50
#define BT 190
#define BS 180
#define TF 8
#define LN 24
#define TPN 8
#define TPC 12
#define TPW 10
#define K1G 1
#define PSKIP 9999
#define D3FROM 60
#define D4FROM 99
#define ROTM 1
#define OAUX 0
#define DEP 2
constexpr int DEPTH = DEP, RING = RNG, RAMP = 2;
constexpr int issued(int i) {
  int d = RING - (i + RAMP - 1) / RAMP; if (d < DEPTH) d = DEPTH;
  if (i >= D3FROM && d < 3) d = 3;
  if (i >= D4FROM && d < 4) d = 4;
  int e = i + d; if (e < RING) e = RING; return e > NSEQ ? NSEQ : e;
}
#define PACE 1
#define YDEF 1
#define WO0 12
#define WON 36
#define TA1 535
#define TA2 440
#define NK 68
#define ORG0 130
#define SLMAX 80
#define MAXGAP 70
constexpr int cmin(int a, int b) { return a < b ? a : b; }
#define RSH 1
#define RD 4
#define BMAP 1
#define EBIG 9999
#define NJ 54
#define TA3 515
#define PTAB 510, 510, 510, 510, 510, 510, 515, 515, 515, 515, 515, 515, 520, 510, 440, 432, 432
#define PTAB1 0, 0, 0, 0, 0, 0, 0, 0, 0, 0, 530, 490, 460, 455, 440, 432, 432
constexpr int ptab[2][17] = {{PTAB}, {PTAB1}};
constexpr int due_at(int n, int m = 0) { int t = 0; for (int k = 0; k < n; ++k) t += ptab[m && k >= NB][k / 5]; return t / 10; }
constexpr int due_x(int n, int m) { return due_at(n, m) + ((m && n >= NB) ? BT : 0); }
constexpr int due_b(int n, int m) { return n % 10 == 9 ? 0 : due_x(n - n % 10, m) + (due_at(n - n % 10 + 10, m) - due_at(n - n % 10, m)) * (n % 10) / 9; }
#define CI(x) std::integral_constant<int, (x)>::value
#define RTAB 20, 30, 40, 50, 60, 70
#define GTAB 0, 12, 6, 12, 12, 12
#define BTAB 8, 12, 6, 12, 12, 0
#define MISS 40
#define DB 10
#define DTAB 22, 16, 18, 15, 10, 3, 3, 3
#define XTAB 0, 0, 2210, 0, 0, 0
#define MTAB -1, -1, 2, -1, -1, -1
#define CB 16
#define SBMAX 100
constexpr int xtab[] = {XTAB}, mtab[] = {MTAB};
constexpr int rtab[] = {RTAB}, gtab[] = {GTAB}, btab[] = {BTAB}, dtab[] = {DTAB};
constexpr int NRND = sizeof(rtab) / sizeof(int);
constexpr int rgap(int k) { return k ? rtab[k] - rtab[k - 1] : rtab[0] - RING; }
constexpr int rnd_at(int n) { for (int k = 0; k < NRND; ++k) if (rtab[k] == n) return k; return -1; }
static_assert(rtab[0] >= RING + 2 && rtab[NRND - 1] + RD + 2 < NSEQ, "");
constexpr int wo_step(int k) { return WO0 + k * WON / 16; }
#define WTS 52
#define PJ0 56
#define PJ1 63
#define PJ2 72
#define XW 13
#define XR 50
#define FDLY 8
#define FSL 2
#define CPFM 1
#define STAG 16
#define EXM(m, ...) do { if (EXQ) { __builtin_amdgcn_sched_barrier(0); asm volatile("s_mov_b64 exec, %0" :: "s"((u64)(m))); __builtin_amdgcn_sched_barrier(0); } __VA_ARGS__; \
    if (EXQ) { __builtin_amdgcn_sched_barrier(0); asm volatile("s_mov_b64 exec, -1"); __builtin_amdgcn_sched_barrier(0); } } while (0)
DI u32 rt32() { return (u32)__builtin_amdgcn_s_memrealtime(); }
constexpr int OOB = 0x7ffff000;
constexpr int L_AB = 32768;
#define PERM 12
DI int uoff(int c, int u, int x) {
  if (PERM == 0) return c * 16384 + u * 1024;
  if (PERM == 1) return ((u >> 2) << 10) | ((u & 3) << 15) | ((c & 7) << 12) | ((c >> 3) << 17);
  if (PERM == 2) return ((c & 3) << 10) | ((u & 8) << 9) | ((u & 4) << 11) | ((c & 4) << 12) | ((u & 3) << 15) | ((c >> 3) << 17);
  if (PERM == 3) return ((((u >> 2) ^ x) & 3) << 10) | ((u & 3) << 15) | ((c & 7) << 12) | ((c >> 3) << 17);
  if (PERM == 4) return ((c & 3) << 10) | ((u & 3) << 12) | ((c & 4) << 12) | ((u >> 2) << 15) | ((c >> 3) << 17);
  if (PERM == 12) return c * 16384 + ((u ^ ((c >> 1) & 3)) * 1024);
  if (PERM == 13) return c * 16384 + ((u ^ ((c >> 1) & 3) ^ ((c >> 3) & 3)) * 1024);
  if (PERM == 5) return (((u ^ x) & 3) << 10) | ((u >> 2) << 15) | ((c & 7) << 12) | ((c >> 3) << 17);
  return 0;
}
DI void part_a(const Args& a, unsigned char* lds, int idx, u64 tag, u32 pf) {
  const u64 T64 = __builtin_amdgcn_s_memrealtime();
  __builtin_amdgcn_sched_barrier(0);
  const int tid = threadIdx.x, lane = tid & 63, w = __builtin_amdgcn_readfirstlane(tid >> 6);
  const int j = lane & 15, g = lane >> 4;
  const int h = (idx & 7) * 4 + ((idx >> 3) & 3), ci = idx >> 5;
  const int rr = (((h & 3) * 5) >> 1) + (h >> 2), rot = SMO ? (ROTM == 2 ? (h >> 2) + (h & 1) * 5 : h >> 2) % 9 : (rr >= NS ? rr - NS : rr), l0 = (ci * 37) >> 4, ln = ((ci * 37 + 37) >> 4) - l0;
  const bool xtra = w < ln;
  const int bad_sl = xtra ? -1 : SMO ? NS - 1 : NS - 1 - rot;
  int vtab;
  { const int p = SMO ? (lane == NS - 1 ? lane : lane + rot >= NS - 1 ? lane + rot - (NS - 1) : lane + rot) : lane + rot >= NS ? lane + rot - NS : lane + rot;
    const bool last = p == NS - 1;
    vtab = uoff(last ? 63 : p * 7 + ci, last ? (l0 + w) & 15 : w, last ? 0 : ci) + ((last && w >= ln) ? 1 << 30 : 0) + (h << 20); }
  const bool prod = ci < 2 * NP && w < 9 / NP;
  const int r0 = ((ci >> 1) * (9 / NP) + w) * 64;
  int voff = lane * 16, wqo = ((r0 + 32 * (j >> 3) + 8 * g) * 4096 + h * 128 + (ci & 1) * 64 + 8 * (j & 7)) * 2;
  int xo = j < 8 ? ((j & 3) * DM + r0 + 32 * (j >> 2) + 8 * g) * 2 : OOB;
  const int pk = (w - 3) % 3, pn = pk ? 19 : 21;
  int pfo = (w >= 3 && w < 3 + 3 * SLN && pk < SLK && lane < pn) ? ((idx >> 3) * pn + lane) * 8192 + (idx & 7) * 1024 + ((SLT >> (4 * ((w - 3) / 3))) & 7) * 128 : OOB;
  int so0 = __builtin_amdgcn_readlane(vtab, 0);
  asm volatile("" : "+v"(vtab), "+v"(voff), "+v"(wqo), "+v"(xo), "+v"(pfo), "+s"(so0));
  __builtin_amdgcn_sched_barrier(0);
  if (!KPL) asm volatile("" :: "s"(a.xB), "s"(a.C), "s"(a.wqB), "s"(a.wuk), "s"(a.wuv), "s"(a.woB), "s"(T64));
  u32 T0 = (u32)T64;
  const u32 toff = (u32)(w * TST) / (STAG * (SMO ? 9 : 10));
  u32* lorg = reinterpret_cast<u32*>(lds + L_ST + 2048);
  const u32 tg = (u32)(tag >> 32);
  u32 last = T0;
  bool mode = false;
  u32x2 rv = {0u, 0u}; u32 rt = 0;
  const __amdgpu_buffer_rsrc_t rs = __builtin_amdgcn_make_buffer_rsrc(a.ws, 0, (int)WS_END, 0x00020000);
  const __amdgpu_buffer_rsrc_t rk = __builtin_amdgcn_make_buffer_rsrc(const_cast<u16*>(a.K), 0, 1 << 27, 0x00020000);
  const __amdgpu_buffer_rsrc_t rvv = __builtin_amdgcn_make_buffer_rsrc(const_cast<u16*>(a.V), 0, 1 << 27, 0x00020000);
  const __amdgpu_buffer_rsrc_t rw = __builtin_amdgcn_make_buffer_rsrc(const_cast<u16*>(a.wq), 0, DM * 4096 * 2, 0x00020000);
  const __amdgpu_buffer_rsrc_t rx = __builtin_amdgcn_make_buffer_rsrc(const_cast<u16*>(a.xA), 0, 4 * DM * 2, 0x00020000);
  u32x4 R[RING];
#define A_ISSUE(i) R[(i) % RING] = __builtin_amdgcn_raw_buffer_load_b128((i) >= 4 * NS ? rvv : rk, voff, ((i) == 0 ? so0 : __builtin_amdgcn_readlane(vtab, (i) % NS)) + ((((i) / NS) & 3) << 25), 2)
#define GATE(n) do { if (PACE) { u32 due = T0 + toff + org + ((osl * cmin(n, NK)) >> 4) + (u32)(mode ? (xtra ? CI(due_x(n, 1)) : CI(due_b(n, 1))) : (xtra ? CI(due_x(n, 0)) : CI(due_b(n, 0)))); if (MAXGAP && (int)(due - (last + MAXGAP)) > 0) due = last + MAXGAP; \
    while ((int)(now - due) < 0) { __builtin_amdgcn_s_sleep(1); now = rt32(); } if (!SMO || (n) % 10 != 9 || xtra) last = now; } } while (0)
#define CTRL(n) do { if constexpr (PACE) { \
    if constexpr (rnd_at((n) - RD - 2) >= 0) { constexpr int k = rnd_at((n) - RD - 2); \
      if (k < NOACT) asm volatile("" :: "v"(rv)); \
      const u32 dt = (rv[1] == (u32)tag + k) ? rv[0] - T0 : rt; \
      const float sm = row16_sum((float)(int)dt); \
      const int o = (int)((RSH ? rdlane(sm, 0) * (1.f / 16.f) : (rdlane(sm, 0) + rdlane(sm, 16)) * (1.f / 32.f))) - (mode ? CI(due_x(rtab[k], 1)) : CI(due_x(rtab[k], 0))); \
      if (w == ((5 * k + 11) & 15) && lane == 0 && o > -300 && o < (k < NOACT ? -5000 : 2000)) { \
        int e = o - (int)org - (int)((osl * cmin(rtab[k], NK)) >> 4) - dtab[k]; if (e < 0) e = 0; \
        int sl_ = ((int)osl * gtab[k]) / 16 + ((e > EBIG ? 0 : e) * btab[k]) / rgap(k); sl_ = sl_ > SLMAX ? SLMAX : sl_; \
        if (xtab[k]) lorg[2] = (int)(rdlane(sm, 16) * (1.f / 16.f)) > xtab[k]; \
        __hip_atomic_store(reinterpret_cast<u64*>(lorg), __builtin_bit_cast(u64, u32x2{(u32)((int)org + e + ((((int)osl - sl_) * cmin(n, NK)) >> 4)), (u32)sl_}), __ATOMIC_RELAXED, __HIP_MEMORY_SCOPE_WORKGROUP); } } \
    if constexpr (rnd_at(n) >= 0) { constexpr int k = rnd_at(n); \
      u32 tot = 0; \
      if (ci == k % ACU && (h & ((1 << RSH) - 1)) == (k & ((1 << RSH) - 1))) { const u32 mine = (1u << 24) + ((rt32() - toff - T0) & 0xfffffu); \
        if (lane == 0) tot = __hip_atomic_fetch_add(lorg + 8 + k, mine, __ATOMIC_RELAXED, __HIP_MEMORY_SCOPE_WORKGROUP) + mine; } \
      if (PFULL) tot = lane < PFULL ? __builtin_amdgcn_readfirstlane(tot) : 0u; \
      u32x2 pv_ = {T0 + ((tot & 0xffffffu) >> 4), (u32)tag + k}; int po_ = (tot >> 24) == (k < NOPOST ? 99u : 16u) ? (int)WS_RPT + (h >> RSH) * 4352 : OOB; asm volatile("" : "+v"(pv_), "+v"(po_)); \
      EXM(1, __builtin_amdgcn_raw_buffer_store_b64(pv_, rs, po_, 128 * k * 35, PAUX)); } \
    if constexpr (rnd_at((n) - RD) >= 0) { constexpr int k = rnd_at((n) - RD); \
      int ro_ = (w == ((5 * k + 11) & 15) && lane < (xtab[k] ? 32 : 16)) ? (int)WS_RPT + lane * 4352 : OOB; asm volatile("" : "+v"(ro_)); \
      EXM(w == ((5 * k + 11) & 15) ? 0xffffffffu : 1u, rv = __builtin_amdgcn_raw_buffer_load_b64(rs, ro_, 128 * k * 35, RAUX)); rt = rt32() - T0 - MISS; } \
  } } while (0)
  u32x4 Wr[8], xf;
  u32 pf2 = 0, pf3 = 0, pf4 = 0;
  auto touch = [&]() __attribute__((always_inline)) {
    const int k = ci < 2 ? ci : (ci - 2) * 4 + (h & 3) + 2;
    const __attribute__((address_space(4))) u64* ap = (const __attribute__((address_space(4))) u64*)__builtin_amdgcn_kernarg_segment_ptr() + KPL;
    const u64 p = ap[(0xCBA96421u >> (4 * (k & 7))) & 15u];
    const int len = k < 2 ? 1 << 27 : k == 3 ? 1 << 24 : k == 4 ? DC * 8192 : k == 6 ? (int)WS_END : k == 7 ? 9216 : DM * 8192;
    const int o = (k < 2 ? (lane << 25) + (h << 20) + lane * 4352 : (lane << 21)) + (idx & 31) * 256;
    pf3 = __builtin_amdgcn_raw_buffer_load_b32(__builtin_amdgcn_make_buffer_rsrc(reinterpret_cast<void*>(p), 0, (w == TPW && k < TPN) ? len : 0, 0x00020000), lane < 8 ? o : OOB, 0, 2);
  };
  if (!KD0) A_ISSUE(0);
  __builtin_amdgcn_sched_barrier(0);
  if (prod) {
#pragma unroll
    for (int k = 0; k < 8; ++k) Wr[k] = __builtin_amdgcn_raw_buffer_load_b128(rw, wqo, k * 8192, 2);
    xf = __builtin_amdgcn_raw_buffer_load_b128(rx, xo, 0, 2);
  }
  __builtin_amdgcn_sched_barrier(0);
  if (KPL) asm volatile("" :: "s"(a.K), "s"(a.V), "s"(a.C), "s"(a.wuv), "s"(a.woB), "s"(T64));
  if (T0L) { const u32 t1 = rt32() - T0L; if (!TMX || (int)(t1 - T0) > TMX) T0 = t1; }
  __builtin_amdgcn_sched_barrier(0);
#define PF2() do { const __amdgpu_buffer_rsrc_t rp_ = __builtin_amdgcn_make_buffer_rsrc(const_cast<u16*>(pk == 0 ? a.wqB : pk == 1 ? a.wuk : a.wuv), 0, pk ? DC * 8192 : DM * 8192, 0x00020000); \
    EXM(0xffffffffu, pf2 = __builtin_amdgcn_raw_buffer_load_b32(rp_, pfo, 0, 2); __builtin_amdgcn_sched_barrier(0); pf4 = __builtin_amdgcn_raw_buffer_load_b32(rp_, pfo, 0, 0)); } while (0)
#define TWAIT(d) do { while ((int)(rt32() - T0 - (d)) < 0) __builtin_amdgcn_s_sleep(1); __builtin_amdgcn_sched_barrier(0); } while (0)
  if (SLN) PF2();
  if (KT) pf2 = __builtin_amdgcn_raw_buffer_load_b32(rk, (w == TPW && lane == 0 && (KT == 1 || ci == 6)) ? (h << 20) + ci * 4352 : OOB, 0, 2);
  if (TPS == 0) touch();
  if (!K1G) A_ISSUE(1);
  __builtin_amdgcn_raw_buffer_store_b64(u32x2{T0, (u32)tag + 15u}, rs, (ci == 6 && (h & 1) == 0 && tid == 0) ? (int)WS_RPT + (h >> 1) * 4352 : OOB, 128 * 15 * 35, 16);
  __builtin_amdgcn_sched_barrier(0);
  if (tid < 32) lorg[tid] = tid == 0 ? ORG0 : tid == 2 ? T0 : 0u;
  __syncthreads();
  T0 = __builtin_amdgcn_readfirstlane(lorg[2]);
  if (KD0) { TWAIT(KD0 + ((w * KDS) >> 4)); A_ISSUE(0); last = rt32(); }
  __builtin_amdgcn_sched_barrier(0);
  if (prod) {
    float* qpart = reinterpret_cast<float*>(lds + L_BIG);
#pragma unroll
    for (int hf = 0; hf < 2; ++hf) {
      f32x4 d4[4];
#pragma unroll
      for (int t = 0; t < 4; ++t) d4[t] = mfma16(xf, tcol16(Wr, 4 * hf + t), f32x4{0.f, 0.f, 0.f, 0.f});
      if (g == (j >> 3)) {
#pragma unroll
        for (int r = 0; r < 4; ++r) *reinterpret_cast<f32x4*>(qpart + ((w * 2 + g) * 4 + r) * 64 + 8 * (j & 7) + 4 * hf) = f32x4{d4[0][r], d4[1][r], d4[2][r], d4[3][r]};
      }
    }
    asm volatile("" ::: "memory");
    u32 arr = 0;
    if (lane == 0) arr = __hip_atomic_fetch_add(lorg + 4, 1u, __ATOMIC_RELAXED, __HIP_MEMORY_SCOPE_WORKGROUP);
    if (__builtin_amdgcn_readfirstlane(arr) == 9 / NP - 1) {
      asm volatile("" ::: "memory");
      f32x4 s0 = {0.f, 0.f, 0.f, 0.f};
#pragma unroll
      for (int k = 0; k < 18 / NP; ++k) s0 += *reinterpret_cast<const f32x4*>(qpart + k * 256 + 4 * lane);
      const int qo = (int)WS_QA + (h * 8 + ci) * 2048 + lane * 16;
      __builtin_amdgcn_raw_buffer_store_b128(u32x4{__float_as_uint(s0[0]), tg, __float_as_uint(s0[1]), tg}, rs, qo, 0, QSA);
      __builtin_amdgcn_raw_buffer_store_b128(u32x4{__float_as_uint(s0[2]), tg, __float_as_uint(s0[3]), tg}, rs, qo + 1024, 0, QSA);
    }
  }
  static_for<RING - 2 + K1G>([&](auto tc) __attribute__((always_inline)) {
    constexpr int t = tc.value + 2 - K1G;
    u32 now = rt32();
    const u32 org = __builtin_amdgcn_readfirstlane(__hip_atomic_load(lorg, __ATOMIC_RELAXED, __HIP_MEMORY_SCOPE_WORKGROUP)), osl = 0;
    GATE(t);
    A_ISSUE(t);
    if constexpr (t == TPS) touch();
    __builtin_amdgcn_sched_barrier(0);
    if constexpr (t == SP) {
      if (w == 13) {
        const u32x2 sv = __builtin_amdgcn_raw_buffer_load_b64(rs, lane < 16 ? (int)WS_RPT + lane * 4352 : OOB, 128 * 15 * 35, SAUX);
        const bool ok = sv[1] == (u32)tag + 15u;
        const float sm = row16_sum(ok ? (float)(int)(sv[0] - T0) : 0.f), cn = row16_sum(ok ? 1.f : 0.f);
        const int o = ORG0 + (cn > 0.f ? (int)(sm / cn) : 0);
        if (lane == 0 && o > -300 && o < 1000) lorg[0] = (u32)o;
      }
    }
    if constexpr (t == QP) {
      if (w >= 14) {
        const int qo = (int)WS_QA + (h * 8 + (w & 1)) * 2048 + lane * 16;
        u32x4 v[2 * NP];
#pragma unroll 1
        for (u32 spins = 0; spins < SPIN_CAP; ++spins) {
          bool ok = true;
          if (spins < QTRY) {
#pragma unroll
            for (int k = 0; k < 2 * NP; ++k) v[k] = __builtin_amdgcn_raw_buffer_load_b128(rs, qo, (k >> 1) * 4096 + (k & 1) * 1024, QAUX);
          } else {
#pragma unroll
            for (int k = 0; k < 2 * NP; ++k) v[k] = __builtin_amdgcn_raw_buffer_load_b128(rs, qo, (k >> 1) * 4096 + (k & 1) * 1024, 16);
          }
#pragma unroll
          for (int k = 0; k < 2 * NP; ++k) ok = ok && v[k][1] == tg && v[k][3] == tg;
          if (__all(ok)) break;
          __builtin_amdgcn_s_sleep(4);
        }
        f32x4 s0 = {0.f, 0.f, 0.f, 0.f};
#pragma unroll
        for (int k = 0; k < NP; ++k) s0 += f32x4{__uint_as_float(v[2 * k][0]), __uint_as_float(v[2 * k][2]), __uint_as_float(v[2 * k + 1][0]), __uint_as_float(v[2 * k + 1][2])};
        *reinterpret_cast<u32x2*>(lds + L_QS + (g * 128 + (w & 1) * 64 + 4 * j) * 2) = u32x2{pk_bf16(s0[0], s0[1]), pk_bf16(s0[2], s0[3])};
        asm volatile("" ::: "memory");
        if (lane == 0) __hip_atomic_fetch_add(lorg + 5, 1u, __ATOMIC_RELAXED, __HIP_MEMORY_SCOPE_WORKGROUP);
      }
    }
    __builtin_amdgcn_sched_barrier(0);
  });
  while (__hip_atomic_load(lorg + 5, __ATOMIC_RELAXED, __HIP_MEMORY_SCOPE_WORKGROUP) != 2u) __builtin_amdgcn_s_sleep(1);
  asm volatile("" :: "v"(pf), "v"(pf2), "v"(pf4) : "memory");
  float* wtab = reinterpret_cast<float*>(lds + L_ST + 1024);
  u32x2 xv = {0u, 0u};
  const int xoff = (w == XW && lane < 8 * ACU) ? (int)WS_MLA + (h * 8 + (lane >> 3)) * 128 + (lane & 7) * 8 : OOB;
  float* pml = reinterpret_cast<float*>(lds + L_ST);
  u32x4 q;
  u32 pb0 = 0, pb1 = 0;
  float acc[8], S[4];
  u32x4 RW[2][8];
  u32x4 WT[4][4];
  float Y[4];
  auto project = [&](int b) __attribute__((always_inline)) {
    f32x4 d4[4];
#pragma unroll
    for (int t = 0; t < 4; ++t) d4[t] = f32x4{0.f, 0.f, 0.f, 0.f};
    const f32x4 wt = *reinterpret_cast<const f32x4*>(wtab + b * 16 + 4 * g);
#pragma unroll
    for (int s = 0; s < 4; ++s) {
      const u32x4 af = *reinterpret_cast<const u32x4*>(lds + L_AB + (b * 16 + j) * 272 + (32 * s + 8 * g) * 2);
#pragma unroll
      for (int t = 0; t < 4; ++t) d4[t] = mfma16(af, WT[s][t], d4[t]);
    }
    float y[4];
#pragma unroll
    for (int t = 0; t < 4; ++t) y[t] = fmaf(d4[t][0], wt[0], d4[t][1] * wt[1]) + fmaf(d4[t][2], wt[2], d4[t][3] * wt[3]);
    Y[b] = sw16(sw32(y[0], y[2]), sw32(y[1], y[3]));
  };
  const __amdgpu_buffer_rsrc_t rwo = __builtin_amdgcn_make_buffer_rsrc(const_cast<u16*>(a.wo + (size_t)h * 128 * DM), 0, w < 9 ? 128 * DM * 2 : 0, 0x00020000);
  int wvo = ((32 * (j >> 3) + 8 * g) * DM + 64 * w + 8 * (j & 7)) * 2;
  asm volatile("" : "+v"(wvo));
  const u64 wmask = w < 9 ? ~0ull : 1ull;
#define WO_ISSUE() static_for<16>([&](auto kc) __attribute__((always_inline)) { constexpr int k = kc.value; \
    if constexpr (wo_step(k) == i) EXM(wmask, RW[k >> 3][k & 7] = __builtin_amdgcn_raw_buffer_load_b128(rwo, wvo, (64 * (k >> 3) + (k & 7)) * DM * 2, 2)); })
#define ISSUE_STEP() do { if constexpr (issued(i + 1) > issued(i)) { GATE(issued(i)); if constexpr (gf) { if (!SMO || i >= D3FROM - 1 || issued(i) % 10 != 0 || xtra) asm volatile("" :: "v"(r)); __builtin_amdgcn_sched_barrier(0); } \
    if (WOF) WO_ISSUE(); A_ISSUE(issued(i)); if (!WOF) WO_ISSUE(); if constexpr (gf) __builtin_amdgcn_sched_barrier(0); CTRL(issued(i)); \
    if constexpr (issued(i + 1) > issued(i) + 1) { GATE(issued(i) + 1); A_ISSUE(issued(i) + 1); CTRL(issued(i) + 1); } } else { WO_ISSUE(); } } while (0)
  static_for<NSEQ>([&](auto ic) __attribute__((always_inline)) {
    constexpr int i = ic.value, bb = (i / NS) & 3, pass = i / (4 * NS), sl = i % NS;
    u32 now = 0; if (PACE) now = rt32();
    u32 org = 0, osl = 0; if (PACE) { const u32x2 ov = __builtin_bit_cast(u32x2, __hip_atomic_load(reinterpret_cast<u64*>(lorg), __ATOMIC_RELAXED, __HIP_MEMORY_SCOPE_WORKGROUP)); org = __builtin_amdgcn_readfirstlane(ov[0]); osl = __builtin_amdgcn_readfirstlane(ov[1]); }
    if constexpr (BMODE && issued(i) == NB && issued(i + 1) > NB) mode = BMODE == 1 || __builtin_amdgcn_readfirstlane(__hip_atomic_load(lorg + 2, __ATOMIC_RELAXED, __HIP_MEMORY_SCOPE_WORKGROUP)) == 1u;
    __builtin_amdgcn_sched_barrier(0);
    const u32x4 r = R[i % RING];
    constexpr bool gf = i >= GF0 && i < GF1;
    if constexpr (BMODE && issued(i) == NB && issued(i + 1) > NB) {
      u64 bp = 0; int bl = 0, co = OOB;
      if (__builtin_expect(mode, 0)) {
        constexpr int CL = (8 - TF) * 512, N0 = 4 * ((CL + LN - 1) / LN), N1 = N0 + (4608 + LN - 1) / LN;
        const int id = w * 28 + (idx >> 3), l = (id < N0 ? id >> 2 : id < N1 ? id - N0 : id - N1) * LN + lane;
        bp = ((const __attribute__((address_space(4))) u64*)__builtin_amdgcn_kernarg_segment_ptr())[KPL + (id < N0 ? 6 : id < N1 ? 10 : 9)];
        bl = id < N0 ? 1 << 24 : id < N1 ? DM * 8192 : DC * 8192;
        co = l >= (id < N0 ? CL : 4608) ? OOB : id < N0 ? ((idx & 6) << 21) + ((idx & 1) << 19) + ((id & 3) << 20) + (TF << 16) + l * 128 : id < N1 ? (idx & 7) * 589824 + l * 128 : (l >> 3) * 8192 + (idx & 7) * 1024 + (l & 7) * 128;
        TWAIT(org + ((osl * NB) >> 4) + CI(due_at(NB)) + ((w * BS) >> 4)); last = T0 + 100000u; now = rt32();
      }
      asm volatile("" : "+v"(co));
      const __amdgpu_buffer_rsrc_t rc = __builtin_amdgcn_make_buffer_rsrc(reinterpret_cast<void*>(bp), 0, bl, 0x00020000);
      EXM(mode ? (1ull << LN) - 1ull : 1ull, pb0 = __builtin_amdgcn_raw_buffer_load_b32(rc, co, 0, 2); __builtin_amdgcn_sched_barrier(0);
          if (BW) asm volatile("s_cmp_eq_u32 %0, 0\n\ts_cbranch_scc1 .Lbw%=\n\ts_waitcnt vmcnt(0)\n.Lbw%=:" :: "s"(bl) : "scc"); __builtin_amdgcn_sched_barrier(0);
          pb1 = __builtin_amdgcn_raw_buffer_load_b32(rc, co, 0, 0));
    }
    if constexpr (BMODE && i == NB + 1) asm volatile("" :: "v"(pb0), "v"(pb1));
    if constexpr (gf) { ISSUE_STEP(); }
    if constexpr (i == TPC) asm volatile("" :: "v"(pf3));
    if constexpr (pass == 0) {
      if constexpr (sl == 0) { q = *reinterpret_cast<const u32x4*>(lds + L_QS + (bb * 128 + 8 * j) * 2); S[bb] = -INFINITY; }
      float d = dot2(r[0], q[0], 0.f); d = dot2(r[1], q[1], d); d = dot2(r[2], q[2], d); d = dot2(r[3], q[3], d);
      asm volatile("" : "+v"(d));
      if constexpr (!gf) { ISSUE_STEP(); }
      d = row16_sum(d);
      S[bb] = (j == sl) ? d : S[bb];
      if constexpr (sl == NS - 1) {
        const float sc = (j == bad_sl) ? -INFINITY : S[bb] * C2;
        const float m = xg_max(row16_max(sc));
        S[bb] = __builtin_amdgcn_exp2f(sc - m);
        const float l = xg_sum(row16_sum(S[bb]));
        if (lane == 0) *reinterpret_cast<f32x2*>(pml + (bb * 16 + w) * 2) = f32x2{m, l};
      }
      if constexpr (i == 4 * NS - 1) {
        asm volatile("" ::: "memory");
        u32 arr = 0;
        if (lane == 0) arr = __hip_atomic_fetch_add(lorg + 6, 1u, __ATOMIC_RELAXED, __HIP_MEMORY_SCOPE_WORKGROUP);
        arr = __builtin_amdgcn_readfirstlane(arr);
        asm volatile("" ::: "memory");
        const f32x2 ml = *reinterpret_cast<const f32x2*>(pml + lane * 2);
        const float M = row16_max(ml[0]);
        const float L = row16_sum(__builtin_amdgcn_exp2f(ml[0] - M) * ml[1]);
        __builtin_amdgcn_raw_buffer_store_b64(u32x2{__float_as_uint(j ? L : M), tg}, rs, (arr == 15u && j < 2) ? (int)WS_MLA + (h * 8 + ci) * 128 + g * 16 + j * 8 : OOB, 0, 16);
      }
    } else {
      const float p = dpp<0x150 + sl>(S[bb]);
      if constexpr (sl == 0) {
#pragma unroll
        for (int k = 0; k < 8; ++k) acc[k] = 0.f;
      }
#pragma unroll
      for (int k = 0; k < 4; ++k) { acc[2 * k] = fmaf(p, bf_lo(r[k]), acc[2 * k]); acc[2 * k + 1] = fmaf(p, bf_hi(r[k]), acc[2 * k + 1]); }
      asm volatile("" : "+v"(acc[0]), "+v"(acc[1]), "+v"(acc[2]), "+v"(acc[3]), "+v"(acc[4]), "+v"(acc[5]), "+v"(acc[6]), "+v"(acc[7]));
      if constexpr (!gf) { ISSUE_STEP(); }
      if constexpr (i == XR) xv = __builtin_amdgcn_raw_buffer_load_b64(rs, xoff, 0, 16);
      if constexpr (i == XR + 2) {
        if (w == XW) {
          if (__builtin_expect(!__all(lane >= 8 * ACU || xv[1] == tg), 0)) {
#pragma unroll 1
            for (u32 spins = 0; spins < SPIN_CAP; ++spins) {
              __builtin_amdgcn_s_sleep(8); xv = __builtin_amdgcn_raw_buffer_load_b64(rs, xoff, 0, 16);
              if (__all(lane >= 8 * ACU || xv[1] == tg)) break;
            }
          }
          const float x = __uint_as_float(xv[0]);
          const float m = lane < 8 * ACU ? dpp<0xA0>(x) : -INFINITY, l = lane < 8 * ACU ? dpp<0xF5>(x) : 0.f;
          const float M = xg_max(fmaxf(m, dpp<0x128>(m)));
          float t = __builtin_amdgcn_exp2f(m - M) * l; t = xg_sum(t + dpp<0x128>(t));
          if (lane < 8 && !(lane & 1)) *reinterpret_cast<f32x2*>(wtab + 64 + lane) = f32x2{M, 1.f / t};
          asm volatile("" ::: "memory");
          const f32x2 gm = *reinterpret_cast<const f32x2*>(wtab + 64 + 2 * g);
          wtab[lane] = __builtin_amdgcn_exp2f(pml[lane * 2] - gm[0]) * gm[1];
          asm volatile("" ::: "memory");
          if (lane == 0) __hip_atomic_store(lorg + 7, 1u, __ATOMIC_RELAXED, __HIP_MEMORY_SCOPE_WORKGROUP);
        }
      }
      if constexpr ((i >= WTS0 && i < WTS0 + 4) || (i >= WTS && i < WTS + 4)) {
        constexpr int s = i >= WTS, t = i - (s ? WTS : WTS0);
        const u32x4 lo = tcol16(RW[s], t), hi = tcol16(RW[s], t + 4);
#pragma unroll
        for (int c = 0; c < 4; ++c) {
          WT[2 * s][t][c] = __builtin_amdgcn_update_dpp(lo[c], hi[c], 0x128, 0xf, 0xc, false);
          WT[2 * s + 1][t][c] = __builtin_amdgcn_update_dpp(hi[c], lo[c], 0x128, 0xf, 0x3, false);
        }
        asm volatile("" : "+v"(WT[2 * s][t]), "+v"(WT[2 * s + 1][t]));
      }
      if constexpr (i == PJ0 || i == PJ1 || i == PJ2) {
        constexpr int b = i == PJ0 ? 0 : i == PJ1 ? 1 : 2;
        Y[b] = 0.f;
        if (w < 9) {
          while (__hip_atomic_load(lorg + 24 + b, __ATOMIC_RELAXED, __HIP_MEMORY_SCOPE_WORKGROUP) != 16u || __hip_atomic_load(lorg + 7, __ATOMIC_RELAXED, __HIP_MEMORY_SCOPE_WORKGROUP) == 0u) __builtin_amdgcn_s_sleep(1);
          asm volatile("" ::: "memory");
          project(b);
        }
        asm volatile("" : "+v"(Y[b]));
      }
      if constexpr (sl == NS - 1) {
        const float t0 = sw16(sw32(acc[0], acc[4]), sw32(acc[2], acc[6])), t1 = sw16(sw32(acc[1], acc[5]), sw32(acc[3], acc[7]));
        *reinterpret_cast<u32*>(lds + L_AB + (bb * 16 + w) * 272 + 16 * j + 4 * g) = pk_bf16(t0, t1);
        if constexpr (bb < 3) { asm volatile("" ::: "memory"); if (lane == 0) __hip_atomic_fetch_add(lorg + 24 + bb, 1u, __ATOMIC_RELAXED, __HIP_MEMORY_SCOPE_WORKGROUP); }
      }
    }
    __builtin_amdgcn_sched_barrier(0);
  });
#undef A_ISSUE
  __syncthreads();
  if (w < 9) {
    project(3);
    st16_sc1(rs, WS_Y + ((8 * w + (j & 7)) * 256 + idx) * 128 + ((j >> 3) * 4 + g) * 16, u32x4{pk_bf16(Y[0], Y[1]), tg, pk_bf16(Y[2], Y[3]), tg});
  }
  if (idx < 72) {
    __builtin_amdgcn_sched_barrier(0);
    __builtin_amdgcn_s_barrier();
    __builtin_amdgcn_s_sleep(FDLY);
    const int yo = (int)WS_Y + (idx * 256 + 16 * w) * 128 + lane * 16;
    u32x4 v0, v1;
#pragma unroll 1
    for (u32 spins = 0; spins < SPIN_CAP; ++spins) {
      v0 = __builtin_amdgcn_raw_buffer_load_b128(rs, yo, 0, 16); v1 = __builtin_amdgcn_raw_buffer_load_b128(rs, yo + 1024, 0, 16);
      if (__all(v0[1] == tg && v0[3] == tg && v1[1] == tg && v1[3] == tg)) break;
      __builtin_amdgcn_s_sleep(FSL);
    }
    float y = sw16(sw32(bf_lo(v0[0]) + bf_lo(v1[0]), bf_lo(v0[2]) + bf_lo(v1[2])), sw32(bf_hi(v0[0]) + bf_hi(v1[0]), bf_hi(v0[2]) + bf_hi(v1[2])));
    y += dpp<0x128>(y);
    float* red = reinterpret_cast<float*>(lds + L_PF);
    if (!(lane & 8)) red[((lane & 7) * 4 + g) * 20 + w] = y;
    __syncthreads();
    if (tid < 32) {
      const f32x4 r0 = *reinterpret_cast<const f32x4*>(red + tid * 20), r1 = *reinterpret_cast<const f32x4*>(red + tid * 20 + 4);
      const f32x4 r2 = *reinterpret_cast<const f32x4*>(red + tid * 20 + 8), r3 = *reinterpret_cast<const f32x4*>(red + tid * 20 + 12);
      const f32x4 r = r0 + r1 + r2;
      const __amdgpu_buffer_rsrc_t ro = __builtin_amdgcn_make_buffer_rsrc(a.out, 0, 4 * 1152 * 2, 0x00020000);
      __builtin_amdgcn_raw_buffer_store_b16(to_bf16(((r[0] + r[1]) + (r[2] + r[3])) + (r3[0] + r3[1])), ro, ((tid & 3) * 1152 + idx * 8 + (tid >> 2)) * 2, 0, OAUX);
      __builtin_amdgcn_raw_buffer_store_b16(to_bf16(r3[2] + r3[3]), ro, ((tid >> 3) * 1152 + 576 + idx * 8 + (tid & 7)) * 2, 0, OAUX);
    }
  }
}

#define CROW 1
constexpr int CW = CROW ? 1 : 8, CR = CROW ? 8 : 1;
#define IA 2
#define IB 4
#define IC 6
#define EUV 4
#define YOFF 90
#define WPD 4
#define CQ 0
#define CLAY 1
DI int ctx_row(int p, int hd) {
  if (CLAY == 1) return (int)WS_CTXP + ((p >> 3) << 18) + ((hd >> 2) << 15) + ((p & 7) << 12) + ((hd & 3) << 10);
  if (CLAY == 2) return (int)WS_CTXP + ((p >> 4) << 19) + ((hd >> 1) << 15) + (((p & 15) ^ (hd >> 4)) << 11) + ((hd & 1) << 10);
  if (CLAY == 3) return (int)WS_CTXP + ((p >> 2) << 17) + ((hd >> 3) << 15) + ((p & 3) << 13) + ((hd & 7) << 10);
  return (int)WS_CTXP + (p * 32 + hd) * 1024;
}
template <bool LD> DI void part_b(const Args& a, unsigned char* lds, int wg, u64 tag, u32 pf) {
  const int tid = threadIdx.x, lane = tid & 63, w = __builtin_amdgcn_readfirstlane(tid >> 6);
  const int j = lane & 15, g = lane >> 4;
  __amdgpu_buffer_rsrc_t rs;
  u64 tb64 = __builtin_amdgcn_s_memrealtime(); u32 tb0;
  const int h = BPF ? (wg & 7) * 4 + (wg >> 3) : wg;
  const int h3 = h ^ 2;
  const int b2 = BMAP ? (wg & 7) >> 1 : wg >> 3, sp = BMAP ? (wg >> 3) * 2 + (wg & 1) : wg & 7;
  const u16* cl;
  u32x4 X[16];
  u32x2 RW[4][8];
  u32x4 W8[2];
  u32* cnt = reinterpret_cast<u32*>(lds + L_ST + 4064);
#define B_POST(T) _Pragma("unroll") for (int k = 0; k < NRND; ++k) if (mtab[k] == (T) && xtab[k]) { u32x2 pv_ = {rt32(), (u32)tag + k}; int po_ = (w == 8 && !((wg ^ (wg >> 3)) & 1)) ? (int)WS_RPT + (16 + (wg >> 1)) * 4352 + 128 * 35 * k : OOB; \
    asm volatile("" : "+v"(pv_), "+v"(po_)); EXM(1, __builtin_amdgcn_raw_buffer_store_b64(pv_, rs, po_, 0, 16)); }
#define C_ISSUE(T) _Pragma("unroll") for (int r = 0; r < 8; ++r) X[8 * ((T) & 1) + r] = ld16(cl + (size_t)(64 * (T) + CR * r) * DC)
#define C_HALF(T, hf) _Pragma("unroll") for (int r = 4 * (hf); r < 4 * (hf) + 4; ++r) X[8 * ((T) & 1) + r] = ld16(cl + (size_t)(64 * (T) + CR * r) * DC)
#define L_PART(T, r0, r1) do { __builtin_amdgcn_sched_barrier(0); \
    if ((T) + 3 < 8) { _Pragma("unroll") for (int r = r0; r < r1; ++r) X[8 * (((T) + 3) & 1) + r] = ld16(cl + (size_t)(64 * ((T) + 3) + CR * r) * DC); } \
    if ((T) == 5 || (T) == 6) { _Pragma("unroll") for (int k = 2 * (r0); k < 2 * (r1); ++k) \
      RW[2 * (((T) - 5) & 1) + (k >> 3)][k & 7] = ld8(a.woB + ((size_t)h3 * 128 + 32 * (2 * (((T) - 5) & 1) + (k >> 3)) + 8 * g + (k & 7)) * DM + 64 * (w - 8) + 4 * j); } \
    __builtin_amdgcn_sched_barrier(0); } while (0)
#define UV_LOAD(n) do { const u32x4 v_ = ld16(a.wuv + ((size_t)(64 * w + 32 * ((n) >> 3) + 8 * g + ((n) & 7)) * H + h3) * 128 + 8 * j); \
    RW[2 * ((n) >> 3)][(n) & 7] = u32x2{v_[0], v_[1]}; RW[2 * ((n) >> 3) + 1][(n) & 7] = u32x2{v_[2], v_[3]}; } while (0)
#define C_WIN(T, r0, r1) _Pragma("unroll") for (int r = r0; r < r1; ++r) { asm volatile("s_waitcnt vmcnt(%0)" :: "n"(WPD - 1) : "memory"); X[8 * ((T) & 1) + r] = ld16(cl + (size_t)(64 * (T) + CR * r) * DC); __builtin_amdgcn_sched_barrier(0); }
  u32 sb0 = L_BIG + CW * (w - 8) * CRS + lane * 16, sb1 = sb0 + CBUF;
  asm volatile("" : "+v"(sb0), "+v"(sb1));
#define C_STAGE(T) _Pragma("unroll") for (int r = 0; r < 8; ++r) *reinterpret_cast<u32x4*>(lds + (((T) & 1) ? sb1 : sb0) + CR * r * CRS) = X[8 * ((T) & 1) + r]
  {
    const u16* Wq = a.wqB + h * 128;
    u32x4 U[8];
    QPre Q;
    qproj_load(Q, a.xB, Wq, w, lane);
#pragma unroll
    for (int i = 0; i < 8; ++i) U[i] = ld16(a.wuk + ((size_t)(32 * w + 4 * i + (j >> 2)) * H + h) * 128 + 8 * (4 * g + (j & 3)));
    __builtin_amdgcn_sched_barrier(0);
    { unsigned char* wsp = a.ws; u64 c64 = (u64)a.C; asm volatile("" : "+s"(wsp), "+s"(c64), "+s"(tb64) : "s"(a.wuv), "s"(a.woB), "s"(a.out));
      const u16* cp = (const u16*)(const __attribute__((address_space(1))) u16*)c64; tb0 = (u32)tb64;
      rs = __builtin_amdgcn_make_buffer_rsrc(wsp, 0, (int)WS_END, 0x00020000);
      cl = cp + ((size_t)b2 * SK + sp * 512 + CW * (w - 8)) * DC + lane * 8; }
    if constexpr (!LD) am_zero(lds, tid);
    qproj_mfma(Q, lds, w, lane);
    asm volatile("" :: "v"(pf));
    __syncthreads();
    if constexpr (LD) { C_HALF(0, 0); }
    else {
      const float* qpart = reinterpret_cast<const float*>(lds + L_BIG);
      float s = 0.f;
#pragma unroll
      for (int ww = 0; ww < 16; ++ww) s += qpart[ww * 512 + tid];
      reinterpret_cast<u16*>(lds + L_QS)[tid] = to_bf16(s);
    }
    __builtin_amdgcn_sched_barrier(0);
    __syncthreads();
    const u32x4 af = *reinterpret_cast<const u32x4*>(lds + L_QS + ((j >> 2) * 128 + (4 * g + (j & 3)) * 8) * 2);
    u16* qa = reinterpret_cast<u16*>(lds + L_PF);
    float v2[2] = {0.f, 0.f};
#pragma unroll
    for (int i = 0; i < 8; ++i) {
      const f32x4 d4 = mfma16(af, U[i], f32x4{0.f, 0.f, 0.f, 0.f});
      float d = (j & 2) ? ((j & 1) ? d4[3] : d4[2]) : ((j & 1) ? d4[1] : d4[0]);
      d += dpp<0xB1>(d); d += dpp<0x4E>(d);
      v2[i >> 2] = ((j & 3) == (i & 3)) ? d : v2[i >> 2];
    }
    qa[g * 512 + 32 * w + 4 * (j & 3) + (j >> 2)] = to_bf16(v2[0]); qa[g * 512 + 32 * w + 16 + 4 * (j & 3) + (j >> 2)] = to_bf16(v2[1]);
    __builtin_amdgcn_sched_barrier(0);
    __syncthreads();
    if constexpr (!LD) {
      if (w == 0) {
#pragma unroll
        for (int bb = 0; bb < 4; ++bb) st16_sc1(rs, WS_QABS + ((bb * H + h) * 512 + lane * 8) * 2, *reinterpret_cast<const u32x4*>(qa + bb * 512 + lane * 8));
      }
      __builtin_amdgcn_sched_barrier(0);
      __builtin_amdgcn_s_barrier();
      if (w == 0) {
        asm volatile("s_waitcnt vmcnt(0)" ::: "memory");
        if (lane == 0) { __hip_atomic_store(reinterpret_cast<u64*>(a.ws + WS_F1 + wg * FST), tag, RLX_AGENT); cnt[0] = 0u; cnt[1] = 0u; }
      }
    } else {
      __builtin_amdgcn_s_barrier();
      C_WIN(0, 4, 8); C_WIN(1, 0, 8);
      C_STAGE(0);
      __builtin_amdgcn_sched_barrier(0);
      C_WIN(2, 0, 8);
    }
  }
  {
    const int b = b2;
    if constexpr (!LD) wg_wait64(a.ws + WS_F1, tag); else __syncthreads();
    const int kt = w & 3, hh = (w >> 2) & 1;
    float* stmax = reinterpret_cast<float*>(lds + L_ST);
    float* stsum = stmax + 128;
    float* salpha = stmax + 256;
    unsigned char* Pf = lds + L_PF;
    float M = -INFINITY, lsum = 0.f;
    f32x4 acc[2][2];
#pragma unroll
    for (int ct = 0; ct < 2; ++ct) { acc[ct][0] = f32x4{0.f, 0.f, 0.f, 0.f}; acc[ct][1] = f32x4{0.f, 0.f, 0.f, 0.f}; }
    const int troff = (4 * g + (j >> 2)) * CRS + (32 * w + 4 * (j & 3)) * 2;
    auto pv_step = [&](const unsigned char* cb) __attribute__((always_inline)) {
      const f32x4 al0 = *reinterpret_cast<const f32x4*>(salpha + 4 * g), al1 = *reinterpret_cast<const f32x4*>(salpha + 16 + 4 * g);
#pragma unroll
      for (int ct = 0; ct < 2; ++ct) { acc[ct][0] *= al0; acc[ct][1] *= al1; }
#pragma unroll
      for (int blk = 0; blk < 2; ++blk) {
        const u32x4 p0 = *reinterpret_cast<const u32x4*>(Pf + ((blk * 2 + 0) * 64 + lane) * 16);
        const u32x4 p1 = *reinterpret_cast<const u32x4*>(Pf + ((blk * 2 + 1) * 64 + lane) * 16);
#pragma unroll
        for (int ct = 0; ct < 2; ++ct) {
          const u32x2 lo = trread(cb + troff + (32 * blk) * CRS + 32 * ct);
          const u32x2 hi = trread(cb + troff + (32 * blk + 16) * CRS + 32 * ct);
          const u32x4 bf = {lo[0], lo[1], hi[0], hi[1]};
          acc[ct][0] = mfma16(p0, bf, acc[ct][0]); acc[ct][1] = mfma16(p1, bf, acc[ct][1]);
        }
      }
    };
    unsigned char* Qf = lds + L_BIG + CBUF;
    if constexpr (!LD) {
      u32x4 qv[4];
#pragma unroll
      for (int k = 0; k < 4; ++k) qv[k] = __builtin_amdgcn_raw_buffer_load_b128(rs, (int)WS_QABS + (b * H * 512 + (tid + 512 * k) * 8) * 2, 0, 16);
#pragma unroll
      for (int k = 0; k < 4; ++k) { const int e = tid + 512 * k, head = e >> 6, ch = e & 63;
        *reinterpret_cast<u32x4*>(Qf + ((head >> 4) * 16 + (ch >> 2)) * 1040 + ((ch & 3) * 16 + (head & 15)) * 16) = qv[k]; }
    }
    __syncthreads();
    if constexpr (!LD) {
      u32x4 QF[16];
#pragma unroll
      for (int s = 0; s < 16; ++s) QF[s] = *reinterpret_cast<const u32x4*>(Qf + (hh * 16 + s) * 1040 + lane * 16);
      auto s_tile = [&](const unsigned char* cb, bool last) __attribute__((always_inline)) {
        __syncthreads();
        f32x4 s0 = {0.f, 0.f, 0.f, 0.f}, s1 = {0.f, 0.f, 0.f, 0.f};
#pragma unroll
        for (int s = 0; s < 16; s += 2) {
          s0 = mfma16(*reinterpret_cast<const u32x4*>(cb + (16 * kt + j) * CRS + (32 * s + 8 * g) * 2), QF[s], s0);
          s1 = mfma16(*reinterpret_cast<const u32x4*>(cb + (16 * kt + j) * CRS + (32 * s + 32 + 8 * g) * 2), QF[s + 1], s1);
        }
        const f32x4 sv = (s0 + s1) * C2;
        if (last) { __builtin_amdgcn_sched_barrier(0); _Pragma("unroll") for (int n = 0; n < EUV; ++n) UV_LOAD(n); __builtin_amdgcn_sched_barrier(0); }
        const float v = xg_max(fmaxf(fmaxf(sv[0], sv[1]), fmaxf(sv[2], sv[3])));
        if (g == 0) stmax[kt * 32 + 16 * hh + j] = v;
        __syncthreads();
        const float* sm = stmax + 16 * hh + j;
        const float Mn = fmaxf(fmaxf(M, fmaxf(sm[0], sm[32])), fmaxf(sm[64], sm[96]));
        const float alpha = __builtin_amdgcn_exp2f(M - Mn);
        M = Mn;
        u32x2 pk;
        pk[0] = pk_bf16(__builtin_amdgcn_exp2f(sv[0] - Mn), __builtin_amdgcn_exp2f(sv[1] - Mn));
        pk[1] = pk_bf16(__builtin_amdgcn_exp2f(sv[2] - Mn), __builtin_amdgcn_exp2f(sv[3] - Mn));
        lsum = lsum * alpha + ((bf_lo(pk[0]) + bf_hi(pk[0])) + (bf_lo(pk[1]) + bf_hi(pk[1])));
        *reinterpret_cast<u32x2*>(Pf + (((kt >> 1) * 2 + hh) * 64 + lane) * 16 + (kt & 1) * 8) = pk;
        if (kt == 0 && g == 0) salpha[16 * hh + j] = alpha;
        __syncthreads();
        pv_step(cb);
      };
#pragma unroll 1
      for (int T = 0; T < 6; T += 2) { s_tile(lds + L_BIG, false); s_tile(lds + L_BIG + CBUF, false); }
      s_tile(lds + L_BIG, false); s_tile(lds + L_BIG + CBUF, true);
    } else {
#pragma unroll
      for (int T = 0; T < 8; ++T) {
        __syncthreads();
        B_POST(T);
        if (T + 1 < 8) { C_STAGE(T + 1); }
        L_PART(T, 0, IA);
        if (T == 7) {
#pragma unroll
          for (int k = 0; k < 2; ++k) W8[k] = ld16(a.woB + ((size_t)h3 * 128 + 16 * (w - 8) + 8 * k + (lane >> 3)) * DM + 512 + (lane & 7) * 8);
        }
        __syncthreads();
        L_PART(T, IA, IB);
        __syncthreads();
        L_PART(T, IB, IC);
        pv_step(lds + L_BIG + (T & 1) * CBUF);
        L_PART(T, IC, 8);
      }
      B_POST(8);
    }
#undef C_ISSUE
#undef C_STAGE
    __syncthreads();
    if constexpr (!LD) { lsum = xg_sum(lsum); if (g == 0) stsum[kt * 32 + 16 * hh + j] = lsum; if (kt == 0 && g == 0) salpha[32 + 16 * hh + j] = M; }
    float* tb = reinterpret_cast<float*>(lds + L_BIG);
#pragma unroll
    for (int ct = 0; ct < 2; ++ct)
#pragma unroll
      for (int h2 = 0; h2 < 2; ++h2)
#pragma unroll
        for (int r = 0; r < 4; ++r) tb[(16 * h2 + 4 * g + r) * 516 + 32 * w + 16 * ct + j] = acc[ct][h2][r];
    if constexpr (!LD) { for (int i = tid; i < 8 * AM2_LD * 2 / 16; i += 512) reinterpret_cast<u32x4*>(lds + L_AM2 + 8 * AM2_LD * 2)[i] = u32x4{0u, 0u, 0u, 0u}; }
    __syncthreads();
    if constexpr (LD) {
      const int t2 = tid - 512;
      if (CQ) { const u32 tq = (rt32() + (CQ - 1)) & ~(u32)(CQ - 1); while ((int)(rt32() - tq) < 0) __builtin_amdgcn_s_sleep(1); }
#pragma unroll
      for (int k = 0; k < 4; ++k) {
        const int e = t2 + 512 * k, head = e >> 6, c8 = e & 63;
        const f32x4 v0 = *reinterpret_cast<const f32x4*>(tb + head * 516 + 8 * c8), v1 = *reinterpret_cast<const f32x4*>(tb + head * 516 + 8 * c8 + 4);
        __builtin_amdgcn_raw_buffer_store_b128(u32x4{pk_bf16(v0[0], v0[1]), pk_bf16(v0[2], v0[3]), pk_bf16(v1[0], v1[1]), pk_bf16(v1[2], v1[3])}, rs, ctx_row(b * NSP + sp, head) + 16 * c8, 0, 16);
      }
      if (t2 < 32) {
        const float L = (stsum[t2] + stsum[32 + t2]) + (stsum[64 + t2] + stsum[96 + t2]);
        st8_sc1(a.ws + WS_MLB + ((size_t)(b * NSP + sp) * 32 + t2) * 8, f32x2{salpha[32 + t2], L});
      }
      __builtin_amdgcn_sched_barrier(0);
      __builtin_amdgcn_s_barrier();
      asm volatile("s_waitcnt vmcnt(0)" ::: "memory");
      if (lane == 0) {
        if (__hip_atomic_fetch_add(cnt, 1u, __ATOMIC_RELAXED, __HIP_MEMORY_SCOPE_WORKGROUP) == 7u) {
          __hip_atomic_store(reinterpret_cast<u64*>(a.ws + WS_F2 + wg * FST), tag, RLX_AGENT);
        }
      }
#pragma unroll
      for (int k = 0; k < 2; ++k) *reinterpret_cast<u32x4*>(lds + L_W8 + (16 * (w - 8) + 8 * k + (lane >> 3)) * 144 + (lane & 7) * 16) = W8[k];
    } else {
      __builtin_amdgcn_s_barrier();
      static_for<16 - EUV>([&](auto ic) __attribute__((always_inline)) {
        asm volatile("s_waitcnt vmcnt(%0)" :: "n"(WPD - 1) : "memory");
        UV_LOAD(EUV + ic.value);
        __builtin_amdgcn_sched_barrier(0);
      });
    }
  }
  {
    int tid = threadIdx.x; asm volatile("" : "+v"(tid));
    const int lane = tid & 63, j = lane & 15, g = lane >> 4;
    u16* Am2 = reinterpret_cast<u16*>(lds + L_AM2);
    float* cpart = reinterpret_cast<float*>(lds + L_CP);
    __builtin_amdgcn_sched_barrier(0);
    if constexpr (LD) {
      if (w == 8) {
        wave_wait(reinterpret_cast<const u64*>(a.ws + WS_F2) + (lane & (NBW - 1)) * (FST / 8), lane < NBW, tag);
        asm volatile("s_waitcnt vmcnt(0)" ::: "memory");
        if (lane == 0) __hip_atomic_store(cnt + 1, 1u, __ATOMIC_RELEASE, __HIP_MEMORY_SCOPE_WORKGROUP);
      } else {
        for (u32 spins = 0; spins < 64u * SPIN_CAP; ++spins) { if (__hip_atomic_load(cnt + 1, __ATOMIC_ACQUIRE, __HIP_MEMORY_SCOPE_WORKGROUP) != 0u) break; __builtin_amdgcn_s_sleep(1); }
      }
      __builtin_amdgcn_sched_barrier(0);
      const int b = (tid >> 7) & 3, c4 = tid & 127;
      u32x2 pv[NSP];
      const f32x2 ml = __builtin_bit_cast(f32x2, __builtin_amdgcn_raw_buffer_load_b64(rs, (int)WS_MLB + ((b * NSP + (j & 7)) * 32 + h3) * 8, 0, 16));
#pragma unroll
      for (int s = 0; s < NSP; ++s) pv[s] = __builtin_amdgcn_raw_buffer_load_b64(rs, ctx_row(b * NSP + s, h3) + 8 * c4, 0, 16);
      const float M = row16_max(ml[0]);
      float wt = __builtin_amdgcn_exp2f(ml[0] - M);
      wt *= 2.f / row16_sum(wt * ml[1]);
      float c0 = 0.f, c1 = 0.f, c2 = 0.f, c3 = 0.f;
#pragma unroll
      for (int s = 0; s < NSP; ++s) { const float ws_ = rdlane(wt, s); c0 = fmaf(ws_, bf_lo(pv[s][0]), c0); c1 = fmaf(ws_, bf_hi(pv[s][0]), c1); c2 = fmaf(ws_, bf_lo(pv[s][1]), c2); c3 = fmaf(ws_, bf_hi(pv[s][1]), c3); }
      const u32 h0 = pk_bf16(c0, c1), h1 = pk_bf16(c2, c3);
      *reinterpret_cast<u32x2*>(Am2 + (2 * b) * AM2_LD + 4 * c4) = u32x2{h0, h1};
      *reinterpret_cast<u32x2*>(Am2 + (2 * b + 1) * AM2_LD + 4 * c4) = u32x2{pk_bf16(c0 - bf_lo(h0), c1 - bf_hi(h0)), pk_bf16(c2 - bf_lo(h1), c3 - bf_hi(h1))};
      __syncthreads();
      __syncthreads();
      const int t2 = tid - 512;
      float s = 0.f;
#pragma unroll
      for (int ww = 0; ww < 8; ++ww) s += cpart[ww * 512 + t2];
      u16* Am = reinterpret_cast<u16*>(lds + L_AM);
      const int bb = t2 >> 7, d = t2 & 127;
      const u16 hi = to_bf16(s);
      Am[(4 * bb) * AM_LD + d] = hi; Am[(4 * bb + 1) * AM_LD + d] = to_bf16(s - bf2f(hi));
    } else {
      __syncthreads();
      f32x4 acc[8];
#pragma unroll
      for (int t = 0; t < 8; ++t) acc[t] = f32x4{0.f, 0.f, 0.f, 0.f};
#pragma unroll
      for (int k2 = 0; k2 < 2; ++k2) {
        const u32x4 af = *reinterpret_cast<const u32x4*>(Am2 + j * AM2_LD + 64 * w + 32 * k2 + 8 * g);
#pragma unroll
        for (int t = 0; t < 8; ++t) acc[t] = mfma16(af, tcol8(RW[2 * k2 + (t >> 2)], t & 3), acc[t]);
      }
      if (g < 2) {
#pragma unroll
        for (int bb = 0; bb < 2; ++bb) {
          float* p = cpart + (w * 4 + 2 * g + bb) * 128 + 8 * j;
          *reinterpret_cast<f32x4*>(p) = f32x4{acc[0][2 * bb] + acc[0][2 * bb + 1], acc[1][2 * bb] + acc[1][2 * bb + 1], acc[2][2 * bb] + acc[2][2 * bb + 1], acc[3][2 * bb] + acc[3][2 * bb + 1]};
          *reinterpret_cast<f32x4*>(p + 4) = f32x4{acc[4][2 * bb] + acc[4][2 * bb + 1], acc[5][2 * bb] + acc[5][2 * bb + 1], acc[6][2 * bb] + acc[6][2 * bb + 1], acc[7][2 * bb] + acc[7][2 * bb + 1]};
        }
      }
      __builtin_amdgcn_sched_barrier(0);
      __syncthreads();
      if (YDEF == 1 && w == 1) {
        u32 dl = tb0 + 5200u;
#pragma unroll 1
        while ((int)(rt32() - dl) < 0) {
          const u32x2 rv = __builtin_amdgcn_raw_buffer_load_b64(rs, lane < 16 ? (int)WS_RPT + lane * 4352 : OOB, 128 * (NRND - 1) * 35, 16);
          const bool ok = rv[1] == (u32)tag + (NRND - 1);
          const float cn = rdlane(row16_sum(ok ? 1.f : 0.f), 0);
          if (cn >= 8.f) { dl = tb0 + (u32)(int)(rdlane(row16_sum(ok ? (float)(int)(rv[0] - tb0) : 0.f), 0) / cn) + (u32)CI(due_at(NSEQ - 1) - due_at(rtab[NRND - 1]) + YOFF); break; }
          __builtin_amdgcn_s_sleep(32);
        }
        if (lane == 0) cnt[2] = (int)(dl - tb0) > 5200 ? tb0 : dl;
      }
      if (w == 0) {
#pragma unroll
        for (int s = 0; s < 4; ++s)
#pragma unroll
          for (int jj = 0; jj < 8; ++jj) RW[s][jj] = *reinterpret_cast<const u32x2*>(lds + L_W8 + (32 * s + 8 * g + jj) * 144 + 8 * j);
      }
    }
    __syncthreads();
    if (LD || w == 0) {
      f32x4 ya[4];
      wo_mfma(RW, lds, lane, ya);
      if (YDEF) { const u32 dl = YDEF == 1 ? __builtin_amdgcn_readfirstlane(cnt[2]) : tb0 + YDEF; while ((int)(rt32() - dl) < 0) __builtin_amdgcn_s_sleep(4); }
      st16_sc1(rs, WS_Y + ((8 * (LD ? w - 8 : 8) + (j >> 1)) * 256 + NAW + h3) * 128 + g * 32 + (j & 1) * 16, yrow(ya, (u32)(tag >> 32)));
    }
  }
}

__global__ void __launch_bounds__(1024, 4) k_main(Args a) {
  __shared__ __attribute__((aligned(16))) unsigned char lds[L_TOTAL];
  u32 pf;
  { u64 pc; u32 len;
    asm volatile("s_getpc_b64 %0\n.Lpf%=:\n\ts_mov_b32 %1, k_tail_mark-.Lpf%=" : "=s"(pc), "=s"(len));
    pf = __builtin_amdgcn_raw_buffer_load_b32(__builtin_amdgcn_make_buffer_rsrc(reinterpret_cast<void*>(pc), 0, len < 131072u ? len : 0u, 0x00020000), CPFM ? (threadIdx.x < 16 ? ((blockIdx.x >> 3) * 16 + threadIdx.x) * 128 : 0x7ffff000) : threadIdx.x * 128, 0, 0);
    __builtin_amdgcn_sched_barrier(0); }
  u64 tag = (hw_dispatch_id() + 1ull) * 0x9E3779B97F4A7C15ull;
  tag ^= ((u64)__builtin_amdgcn_dispatch_ptr() >> 6) * 0xC2B2AE3D27D4EB4Full;
  tag |= 1ull << 63;
  if (__builtin_expect(blockIdx.x >= NBW, 1)) part_a(a, lds, blockIdx.x - NBW, tag, pf);
  else if (NOB) { const int w = threadIdx.x >> 6, lane = threadIdx.x & 63, j = lane & 15, g = lane >> 4; const u32 tg = (u32)(tag >> 32);
    if (w < 9) st16_sc1(__builtin_amdgcn_make_buffer_rsrc(a.ws, 0, (int)WS_END, 0x00020000), WS_Y + ((8 * w + (j >> 1)) * 256 + NAW + blockIdx.x) * 128 + g * 32 + (j & 1) * 16, u32x4{0u, tg, 0u, tg}); }
  else if (__builtin_amdgcn_readfirstlane(threadIdx.x >> 6) < 8) part_b<false>(a, lds, blockIdx.x, tag, pf);
  else part_b<true>(a, lds, blockIdx.x, tag, pf);
}

extern "C" __global__ void k_tail_mark() {}

extern "C" void kernel_launch(void* const* d_in, const int* in_sizes, int n_in, void* d_out, int out_size, void* d_ws, size_t ws_size,
                              hipStream_t stream) {
  Args a;
  a.xA = (const u16*)d_in[0]; a.K = (const u16*)d_in[1]; a.V = (const u16*)d_in[2]; a.wq = (const u16*)d_in[3]; a.wo = (const u16*)d_in[4];
  a.xB = (const u16*)d_in[5]; a.C = (const u16*)d_in[6]; a.wqB = (const u16*)d_in[7]; a.wuk = (const u16*)d_in[8]; a.wuv = (const u16*)d_in[9]; a.woB = (const u16*)d_in[10];
#define WSTOP 32
  if (WSTOP && ws_size >= ((size_t)WSTOP << 20)) d_ws = (unsigned char*)d_ws + ((ws_size - ((size_t)WSTOP << 20)) & ~(size_t)4095);
  a.ws = (unsigned char*)d_ws;
  a.out = (u16*)d_out;
  k_main<<<NBW + NAW, 1024, 0, stream>>>(a);
}
```

```cpp
#include <hip/hip_runtime.h>
#include <stdint.h>
#include <utility>

typedef unsigned short u16;
typedef unsigned u32;
typedef unsigned long long u64;
typedef u32 u32x4 __attribute__((ext_vector_type(4)));
typedef u32 u32x2 __attribute__((ext_vector_type(2)));
typedef float f32x4 __attribute__((ext_vector_type(4)));
typedef float f32x2 __attribute__((ext_vector_type(2)));
typedef short s16x4 __attribute__((ext_vector_type(4)));
typedef __bf16 bf16x8 __attribute__((ext_vector_type(8)));
typedef __bf16 bf16x2 __attribute__((ext_vector_type(2)));
#define DI __device__ __forceinline__
#define RLX_AGENT __ATOMIC_RELAXED, __HIP_MEMORY_SCOPE_AGENT
extern "C" __device__ u64 hw_dispatch_id() __asm("llvm.amdgcn.dispatch.id");

template <int... Is, class F> DI void static_for_impl(std::integer_sequence<int, Is...>, F&& f) { (f(std::integral_constant<int, Is>{}), ...); }
template <int N, class F> DI void static_for(F&& f) { static_for_impl(std::make_integer_sequence<int, N>{}, f); }

constexpr int H = 32, SK = 4096, DM = 576, DC = 512;
constexpr int ACU = 7;
constexpr int NBW = 32;
constexpr int NAW = H * ACU;
constexpr int NSP = 8;
constexpr float C2 = 0.08838834764831845f * 1.4426950408889634f;

#define FST 4352
constexpr size_t WS_CTXP = 0;
constexpr size_t WS_QABS = WS_CTXP + (size_t)4 * NSP * 32 * 512 * 2;
constexpr size_t WS_MLB = WS_QABS + 4 * 32 * 512 * 2;
constexpr size_t WS_Y = WS_MLB + 4 * NSP * 32 * 2 * 4;
constexpr size_t WS_MLA = WS_Y + (size_t)72 * 256 * 128;
constexpr size_t WS_RPT = WS_MLA + 256 * 128;
constexpr size_t WS_QA = WS_RPT + 64 * 4480;
constexpr size_t WS_F1 = WS_QA + 256 * 2048, WS_F2 = WS_F1 + 512;
constexpr size_t WS_END = WS_F1 + 32 * 4352 + 1024;

constexpr int CRS = 1056;
constexpr int CBUF = 64 * CRS;
constexpr int L_BIG = 0;
constexpr int L_AM = 2 * CBUF;
constexpr int L_QS = L_AM + 16 * 272;
constexpr int L_ST = L_QS + 1024;
constexpr int L_PF = L_ST + 4096;
constexpr int L_TOTAL = L_PF + 4096;
constexpr int AM_LD = 136;
constexpr int AM2_LD = 520;
constexpr int L_AM2 = 69632, L_CP = 88064, L_W8 = 106496;

#define TMX 20
#define MGS 2
#define T0L 22
struct Args {
  const u16 *xA, *K, *V, *wq, *wo, *xB, *C, *wqB, *wuk, *wuv, *woB;
  unsigned char* ws;
  u16* out;
};

DI float bf_lo(u32 x) { return __uint_as_float(x << 16); }
DI float bf_hi(u32 x) { return __uint_as_float(x & 0xffff0000u); }
DI u32 pk_bf16(float lo, float hi) { bf16x2 v = {(__bf16)lo, (__bf16)hi}; return __builtin_bit_cast(u32, v); }
DI u16 to_bf16(float x) { return __builtin_bit_cast(u16, (__bf16)x); }
DI float bf2f(u16 x) { return __uint_as_float(((u32)x) << 16); }
DI float dot2(u32 a, u32 b, float c) { return __builtin_amdgcn_fdot2_f32_bf16(__builtin_bit_cast(bf16x2, a), __builtin_bit_cast(bf16x2, b), c, false); }
template <int CTRL> DI float dpp(float x) { return __builtin_bit_cast(float, __builtin_amdgcn_update_dpp(0, __builtin_bit_cast(int, x), CTRL, 0xf, 0xf, true)); }
DI float row16_sum(float x) { x += dpp<0x128>(x); x += dpp<0x124>(x); x += dpp<0x122>(x); x += dpp<0x121>(x); return x; }
DI float row16_max(float x) { x = fmaxf(x, dpp<0x128>(x)); x = fmaxf(x, dpp<0x124>(x)); x = fmaxf(x, dpp<0x122>(x)); x = fmaxf(x, dpp<0x121>(x)); return x; }
DI float xg_max(float x) {
  auto s = __builtin_amdgcn_permlane16_swap(__float_as_uint(x), __float_as_uint(x), false, false);
  x = fmaxf(__uint_as_float(s[0]), __uint_as_float(s[1]));
  auto t = __builtin_amdgcn_permlane32_swap(__float_as_uint(x), __float_as_uint(x), false, false);
  return fmaxf(__uint_as_float(t[0]), __uint_as_float(t[1]));
}
DI float xg_sum(float x) {
  auto s = __builtin_amdgcn_permlane16_swap(__float_as_uint(x), __float_as_uint(x), false, false);
  x = __uint_as_float(s[0]) + __uint_as_float(s[1]);
  auto t = __builtin_amdgcn_permlane32_swap(__float_as_uint(x), __float_as_uint(x), false, false);
  return __uint_as_float(t[0]) + __uint_as_float(t[1]);
}
DI float sw32(float a, float b) {
  auto s = __builtin_amdgcn_permlane32_swap(__float_as_uint(a), __float_as_uint(b), false, false);
  return __uint_as_float(s[0]) + __uint_as_float(s[1]);
}
DI float sw16(float a, float b) {
  auto s = __builtin_amdgcn_permlane16_swap(__float_as_uint(a), __float_as_uint(b), false, false);
  return __uint_as_float(s[0]) + __uint_as_float(s[1]);
}
DI f32x4 mfma16(u32x4 a, u32x4 b, f32x4 c) { return __builtin_amdgcn_mfma_f32_16x16x32_bf16(__builtin_bit_cast(bf16x8, a), __builtin_bit_cast(bf16x8, b), c, 0, 0, 0); }
DI u32x4 ld16(const u16* p) { return __builtin_nontemporal_load(reinterpret_cast<const u32x4*>(p)); }
DI u32x2 ld8(const u16* p) { return __builtin_nontemporal_load(reinterpret_cast<const u32x2*>(p)); }
DI u32x2 trread(const unsigned char* p) {
  return __builtin_bit_cast(u32x2, __builtin_amdgcn_ds_read_tr16_b64_v4i16((__attribute__((address_space(3))) s16x4*)(p)));
}
DI u32 prm(u32 hi, u32 lo, int odd) { return __builtin_amdgcn_perm(hi, lo, odd ? 0x07060302u : 0x05040100u); }

DI u32x4 tcol16(const u32x4 (&R)[8], int t) {
  u32x4 T;
#pragma unroll
  for (int q = 0; q < 4; ++q) T[q] = prm(R[2 * q + 1][t >> 1], R[2 * q][t >> 1], t & 1);
  return T;
}
DI u32x4 tcol8(const u32x2 (&R)[8], int t) {
  u32x4 T;
#pragma unroll
  for (int q = 0; q < 4; ++q) T[q] = prm(R[2 * q + 1][t >> 1], R[2 * q][t >> 1], t & 1);
  return T;
}

constexpr u32 SPIN_CAP = 1u << 22;
DI void wave_wait(const u64* f, bool active, u64 tag) {
#pragma unroll 1
  for (u32 spins = 0; spins < SPIN_CAP; ++spins) {
    if (active) active = __hip_atomic_load(f, RLX_AGENT) != tag;
    if (!__any(active)) break;
    __builtin_amdgcn_s_sleep(4);
  }
}
DI void wg_wait64(const unsigned char* flags, u64 tag, int pw = 0) {
  if ((int)(threadIdx.x >> 6) == pw) {
    wave_wait(reinterpret_cast<const u64*>(flags) + (threadIdx.x & (NBW - 1)) * (FST / 8), (threadIdx.x & 63) < NBW, tag);
    asm volatile("s_waitcnt vmcnt(0)" ::: "memory");
  }
  __syncthreads();
}
DI void st16_sc1(__amdgpu_buffer_rsrc_t rs, size_t off, u32x4 v) { __builtin_amdgcn_raw_buffer_store_b128(v, rs, (int)off, 0, 16); }
DI void st8_sc1(unsigned char* p, f32x2 v) { __hip_atomic_store(reinterpret_cast<u64*>(p), __builtin_bit_cast(u64, v), RLX_AGENT); }

struct QPre { u32x4 R0[8]; u32x4 R1; u32x2 xa, xb; u32 x1; };
DI void qproj_load(QPre& Q, const u16* x, const u16* Wh, int w, int lane) {
  const int j = lane & 15, g = lane >> 4;
  Q.xa = u32x2{0u, 0u}; Q.xb = u32x2{0u, 0u}; Q.x1 = 0u;
  if (j < 4) { Q.xa = ld8(x + j * DM + 36 * w + 8 * g); Q.xb = ld8(x + j * DM + 36 * w + 8 * g + 4); Q.x1 = x[j * DM + 36 * w + 32 + g]; }
#pragma unroll
  for (int jj = 0; jj < 8; ++jj) Q.R0[jj] = ld16(Wh + (size_t)(36 * w + 8 * g + jj) * 4096 + 8 * j);
  Q.R1 = ld16(Wh + (size_t)(36 * w + 32 + g) * 4096 + 8 * j);
}
DI void qproj_mfma(const QPre& Q, unsigned char* lds, int w, int lane) {
  const int j = lane & 15, g = lane >> 4;
  float* qpart = reinterpret_cast<float*>(lds + L_BIG);
  const u32x4 a = {Q.xa[0], Q.xa[1], Q.xb[0], Q.xb[1]}, a1 = {Q.x1, 0u, 0u, 0u};
#pragma unroll
  for (int hf = 0; hf < 2; ++hf) {
    f32x4 acc[4];
#pragma unroll
    for (int t = 0; t < 4; ++t) acc[t] = mfma16(a, tcol16(Q.R0, 4 * hf + t), f32x4{0.f, 0.f, 0.f, 0.f});
#pragma unroll
    for (int t = 0; t < 4; ++t) { const u32x4 bb = {(t & 1) ? (Q.R1[2 * hf + (t >> 1)] >> 16) : (Q.R1[2 * hf + (t >> 1)] & 0xffffu), 0u, 0u, 0u}; acc[t] = mfma16(a1, bb, acc[t]); }
    if (g == 0) {
#pragma unroll
      for (int r = 0; r < 4; ++r) *reinterpret_cast<f32x4*>(qpart + (w * 4 + r) * 128 + 8 * j + 4 * hf) = f32x4{acc[0][r], acc[1][r], acc[2][r], acc[3][r]};
    }
    __builtin_amdgcn_sched_barrier(0);
  }
}
DI void wo_load(const u16* Wo, u32x2 (&R)[4][8], int nb, int lane, int s0, int s1) {
  const int j = lane & 15, g = lane >> 4;
#pragma unroll
  for (int s = 0; s < 4; ++s) if (s >= s0 && s < s1)
#pragma unroll
    for (int jj = 0; jj < 8; ++jj) R[s][jj] = ld8(Wo + (size_t)(32 * s + 8 * g + jj) * DM + 64 * nb + 4 * j);
}
DI void wo_mfma(const u32x2 (&R)[4][8], const unsigned char* lds, int lane, f32x4 (&acc)[4]) {
  const int j = lane & 15, g = lane >> 4;
#pragma unroll
  for (int t = 0; t < 4; ++t) acc[t] = f32x4{0.f, 0.f, 0.f, 0.f};
#pragma unroll
  for (int s = 0; s < 4; ++s) {
    const u32x4 a = *reinterpret_cast<const u32x4*>(lds + L_AM + j * 272 + (32 * s + 8 * g) * 2);
#pragma unroll
    for (int t = 0; t < 4; ++t) acc[t] = mfma16(a, tcol8(R[s], t), acc[t]);
  }
}
DI u32x4 yrow(const f32x4 (&acc)[4], u32 tg) {
  return u32x4{pk_bf16(acc[0][0] + acc[0][1], acc[1][0] + acc[1][1]), tg, pk_bf16(acc[2][0] + acc[2][1], acc[3][0] + acc[3][1]), tg};
}
DI void am_zero(unsigned char* lds, int tid) {
  if (tid < 136) { const int pr = tid / 34; *reinterpret_cast<u32x4*>(lds + L_AM + (4 * pr + 2) * 272 + (tid - 34 * pr) * 16) = u32x4{0u, 0u, 0u, 0u}; }
}
DI float rdlane(float x, int l) { return __builtin_bit_cast(float, __builtin_amdgcn_readlane(__builtin_bit_cast(int, x), l)); }

constexpr int NS = 10;
constexpr int NSEQ = 8 * NS;
#define RNG 9
#define NP 3
#define QP 4
#define SP 4
#define TPS 3
#define KD0 60
#define KDS 60
#define GF0 12
#define TST 440
#define WTS0 42
#define NB 50
#define BT 190
#define BS 180
#define TF 8
#define LN 24
#define TPN 8
#define TPC 12
#define TPW 10
#define D3FROM 60
#define DEP 2
constexpr int DEPTH = DEP, RING = RNG, RAMP = 2;
constexpr int issued(int i) {
  int d = RING - (i + RAMP - 1) / RAMP; if (d < DEPTH) d = DEPTH;
  if (i >= D3FROM && d < 3) d = 3;
  int e = i + d; if (e < RING) e = RING; return e > NSEQ ? NSEQ : e;
}
#define PACE 1
#define WO0 12
#define WON 36
#define NK 68
#define ORG0 130
#define SLMAX 80
#define MAXGAP 60
constexpr int cmin(int a, int b) { return a < b ? a : b; }
#define RSH 1
#define RD 4
#define PTAB 510, 510, 510, 510, 510, 510, 515, 515, 515, 515, 515, 515, 520, 510, 440, 432, 432
#define PTAB1 0, 0, 0, 0, 0, 0, 0, 0, 0, 0, 510, 480, 455, 450, 440, 432, 432
constexpr int ptab[2][17] = {{PTAB}, {PTAB1}};
constexpr int due_at(int n, int m = 0) { int t = 0; for (int k = 0; k < n; ++k) t += ptab[m && k >= NB][k / 5]; return t / 10; }
constexpr int due_x(int n, int m) { return due_at(n, m) + ((m && n >= NB) ? BT : 0); }
constexpr int due_b(int n, int m) { return n % 10 == 9 ? 0 : due_x(n - n % 10, m) + (due_at(n - n % 10 + 10, m) - due_at(n - n % 10, m)) * (n % 10) / 9; }
#define CI(x) std::integral_constant<int, (x)>::value
#define RTAB 20, 30, 40, 50, 60, 70
#define GTAB 0, 12, 6, 12, 12, 12
#define BTAB 8, 12, 6, 12, 12, 0
#define MISS 40
#define DTAB 22, 16, 18, 15, 10, 3, 3, 3
#define XTAB 0, 0, 2210, 0, 0, 0
#define MTAB -1, -1, 2, -1, -1, -1
constexpr int xtab[] = {XTAB}, mtab[] = {MTAB};
constexpr int rtab[] = {RTAB}, gtab[] = {GTAB}, btab[] = {BTAB}, dtab[] = {DTAB};
constexpr int NRND = sizeof(rtab) / sizeof(int);
constexpr int rgap(int k) { return k ? rtab[k] - rtab[k - 1] : rtab[0] - RING; }
constexpr int rnd_at(int n) { for (int k = 0; k < NRND; ++k) if (rtab[k] == n) return k; return -1; }
static_assert(rtab[0] >= RING + 2 && rtab[NRND - 1] + RD + 2 < NSEQ, "");
constexpr int wo_step(int k) { return WO0 + k * WON / 16; }
#define WTS 52
#define PJ0 56
#define PJ1 63
#define PJ2 72
#define XW 13
#define XR 50
#define FDLY 8
#define FSL 2
#define EXM(m, ...) do { __builtin_amdgcn_sched_barrier(0); asm volatile("s_mov_b64 exec, %0" :: "s"((u64)(m))); __builtin_amdgcn_sched_barrier(0); __VA_ARGS__; \
    __builtin_amdgcn_sched_barrier(0); asm volatile("s_mov_b64 exec, -1"); __builtin_amdgcn_sched_barrier(0); } while (0)
DI u32 rt32() { return (u32)__builtin_amdgcn_s_memrealtime(); }
constexpr int OOB = 0x7ffff000;
constexpr int L_AB = 32768;
DI int uoff(int c, int u) {
  return c * 16384 + ((u ^ ((c >> 1) & 3)) * 1024);
}
DI void part_a(const Args& a, unsigned char* lds, int idx, u64 tag, u32 pf) {
  const u64 T64 = __builtin_amdgcn_s_memrealtime();
  __builtin_amdgcn_sched_barrier(0);
  const int tid = threadIdx.x, lane = tid & 63, w = __builtin_amdgcn_readfirstlane(tid >> 6);
  const int j = lane & 15, g = lane >> 4;
  const int h = (idx & 7) * 4 + ((idx >> 3) & 3), ci = idx >> 5;
  const int rot = (h >> 2) % 9, l0 = (ci * 37) >> 4, ln = ((ci * 37 + 37) >> 4) - l0;
  const bool xtra = w < ln;
  const int bad_sl = xtra ? -1 : NS - 1;
  int vtab;
  { const int p = lane == NS - 1 ? lane : lane + rot >= NS - 1 ? lane + rot - (NS - 1) : lane + rot;
    const bool last = p == NS - 1;
    vtab = uoff(last ? 63 : p * 7 + ci, last ? (l0 + w) & 15 : w) + ((last && w >= ln) ? 1 << 30 : 0) + (h << 20); }
  const bool prod = ci < 2 * NP && w < 9 / NP;
  const int r0 = ((ci >> 1) * (9 / NP) + w) * 64;
  int voff = lane * 16, wqo = ((r0 + 32 * (j >> 3) + 8 * g) * 4096 + h * 128 + (ci & 1) * 64 + 8 * (j & 7)) * 2;
  int xo = j < 8 ? ((j & 3) * DM + r0 + 32 * (j >> 2) + 8 * g) * 2 : OOB;
  int so0 = __builtin_amdgcn_readlane(vtab, 0);
  asm volatile("" : "+v"(vtab), "+v"(voff), "+v"(wqo), "+v"(xo), "+s"(so0));
  __builtin_amdgcn_sched_barrier(0);
  asm volatile("" :: "s"(a.xB), "s"(a.C), "s"(a.wqB), "s"(a.wuk), "s"(a.wuv), "s"(a.woB), "s"(T64));
  u32 T0 = (u32)T64;
  const u32 toff = (u32)(w * TST) / (16 * 9);
  u32* lorg = reinterpret_cast<u32*>(lds + L_ST + 2048);
  const u32 tg = (u32)(tag >> 32);
  u32 last = T0;
  bool mode = false;
  u32x2 rv = {0u, 0u}; u32 rt = 0;
  const __amdgpu_buffer_rsrc_t rs = __builtin_amdgcn_make_buffer_rsrc(a.ws, 0, (int)WS_END, 0x00020000);
  const __amdgpu_buffer_rsrc_t rk = __builtin_amdgcn_make_buffer_rsrc(const_cast<u16*>(a.K), 0, 1 << 27, 0x00020000);
  const __amdgpu_buffer_rsrc_t rvv = __builtin_amdgcn_make_buffer_rsrc(const_cast<u16*>(a.V), 0, 1 << 27, 0x00020000);
  const __amdgpu_buffer_rsrc_t rw = __builtin_amdgcn_make_buffer_rsrc(const_cast<u16*>(a.wq), 0, DM * 4096 * 2, 0x00020000);
  const __amdgpu_buffer_rsrc_t rx = __builtin_amdgcn_make_buffer_rsrc(const_cast<u16*>(a.xA), 0, 4 * DM * 2, 0x00020000);
  u32x4 R[RING];
#define A_ISSUE(i) R[(i) % RING] = __builtin_amdgcn_raw_buffer_load_b128((i) >= 4 * NS ? rvv : rk, voff, ((i) == 0 ? so0 : __builtin_amdgcn_readlane(vtab, (i) % NS)) + ((((i) / NS) & 3) << 25), 2)
#define GATE(n) do { if (PACE) { u32 due = T0 + toff + org + ((osl * cmin(n, NK)) >> 4) + (u32)(mode ? (xtra ? CI(due_x(n, 1)) : CI(due_b(n, 1))) : (xtra ? CI(due_x(n, 0)) : CI(due_b(n, 0)))); { const u32 lim_ = last + MAXGAP + (osl >> MGS); if ((int)(due - lim_) > 0) due = lim_; } \
    while ((int)(now - due) < 0) { __builtin_amdgcn_s_sleep(1); now = rt32(); } if ((n) % 10 != 9 || xtra) last = now; } } while (0)
#define CTRL(n) do { if constexpr (PACE) { \
    if constexpr (rnd_at((n) - RD - 2) >= 0) { constexpr int k = rnd_at((n) - RD - 2); \
      const u32 dt = (rv[1] == (u32)tag + k) ? rv[0] - T0 : rt; \
      const float sm = row16_sum((float)(int)dt); \
      const int o = (int)((RSH ? rdlane(sm, 0) * (1.f / 16.f) : (rdlane(sm, 0) + rdlane(sm, 16)) * (1.f / 32.f))) - (mode ? CI(due_x(rtab[k], 1)) : CI(due_x(rtab[k], 0))); \
      if (w == ((5 * k + 11) & 15) && lane == 0 && o > -300 && o < 2000) { \
        int e = o - (int)org - (int)((osl * cmin(rtab[k], NK)) >> 4) - dtab[k]; if (e < 0) e = 0; \
        int sl_ = ((int)osl * gtab[k]) / 16 + (e * btab[k]) / rgap(k); sl_ = sl_ > SLMAX ? SLMAX : sl_; \
        if (xtab[k]) lorg[2] = (int)(rdlane(sm, 16) * (1.f / 16.f)) > xtab[k]; \
        __hip_atomic_store(reinterpret_cast<u64*>(lorg), __builtin_bit_cast(u64, u32x2{(u32)((int)org + e + ((((int)osl - sl_) * cmin(n, NK)) >> 4)), (u32)sl_}), __ATOMIC_RELAXED, __HIP_MEMORY_SCOPE_WORKGROUP); } } \
    if constexpr (rnd_at(n) >= 0) { constexpr int k = rnd_at(n); \
      u32 tot = 0; \
      if (ci == k % ACU && (h & ((1 << RSH) - 1)) == (k & ((1 << RSH) - 1))) { const u32 mine = (1u << 24) + ((rt32() - toff - T0) & 0xfffffu); \
        if (lane == 0) tot = __hip_atomic_fetch_add(lorg + 8 + k, mine, __ATOMIC_RELAXED, __HIP_MEMORY_SCOPE_WORKGROUP) + mine; } \
      u32x2 pv_ = {T0 + ((tot & 0xffffffu) >> 4), (u32)tag + k}; int po_ = (tot >> 24) == 16u ? (int)WS_RPT + (h >> RSH) * 4352 : OOB; asm volatile("" : "+v"(pv_), "+v"(po_)); \
      EXM(1, __builtin_amdgcn_raw_buffer_store_b64(pv_, rs, po_, 128 * k * 35, 16)); } \
    if constexpr (rnd_at((n) - RD) >= 0) { constexpr int k = rnd_at((n) - RD); \
      int ro_ = (w == ((5 * k + 11) & 15) && lane < (xtab[k] ? 32 : 16)) ? (int)WS_RPT + lane * 4352 : OOB; asm volatile("" : "+v"(ro_)); \
      EXM(w == ((5 * k + 11) & 15) ? 0xffffffffu : 1u, rv = __builtin_amdgcn_raw_buffer_load_b64(rs, ro_, 128 * k * 35, 16)); rt = rt32() - T0 - MISS; } \
  } } while (0)
  u32x4 Wr[8], xf;
  u32 pf3 = 0;
  auto touch = [&]() __attribute__((always_inline)) {
    const int k = ci < 2 ? ci : (ci - 2) * 4 + (h & 3) + 2;
    const __attribute__((address_space(4))) u64* ap = (const __attribute__((address_space(4))) u64*)__builtin_amdgcn_kernarg_segment_ptr();
    const u64 p = ap[(0xCBA96421u >> (4 * (k & 7))) & 15u];
    const int len = k < 2 ? 1 << 27 : k == 3 ? 1 << 24 : k == 4 ? DC * 8192 : k == 6 ? (int)WS_END : k == 7 ? 9216 : DM * 8192;
    const int o = (k < 2 ? (lane << 25) + (h << 20) + lane * 4352 : (lane << 21)) + (idx & 31) * 256;
    pf3 = __builtin_amdgcn_raw_buffer_load_b32(__builtin_amdgcn_make_buffer_rsrc(reinterpret_cast<void*>(p), 0, (w == TPW && k < TPN) ? len : 0, 0x00020000), lane < 8 ? o : OOB, 0, 2);
  };
  __builtin_amdgcn_sched_barrier(0);
  if (prod) {
#pragma unroll
    for (int k = 0; k < 8; ++k) Wr[k] = __builtin_amdgcn_raw_buffer_load_b128(rw, wqo, k * 8192, 2);
    xf = __builtin_amdgcn_raw_buffer_load_b128(rx, xo, 0, 2);
  }
  __builtin_amdgcn_sched_barrier(0);
  { const u32 t1 = rt32() - T0L; if ((int)(t1 - T0) > TMX) T0 = t1; }
  __builtin_amdgcn_sched_barrier(0);
#define TWAIT(d) do { while ((int)(rt32() - T0 - (d)) < 0) __builtin_amdgcn_s_sleep(1); __builtin_amdgcn_sched_barrier(0); } while (0)
  __builtin_amdgcn_raw_buffer_store_b64(u32x2{T0, (u32)tag + 15u}, rs, (ci == 6 && (h & 1) == 0 && tid == 0) ? (int)WS_RPT + (h >> 1) * 4352 : OOB, 128 * 15 * 35, 16);
  __builtin_amdgcn_sched_barrier(0);
  if (tid < 32) lorg[tid] = tid == 0 ? ORG0 : tid == 2 ? T0 : 0u;
  __syncthreads();
  T0 = __builtin_amdgcn_readfirstlane(lorg[2]);
  TWAIT(KD0 + ((w * KDS) >> 4)); A_ISSUE(0); last = rt32();
  __builtin_amdgcn_sched_barrier(0);
  if (prod) {
    float* qpart = reinterpret_cast<float*>(lds + L_BIG);
#pragma unroll
    for (int hf = 0; hf < 2; ++hf) {
      f32x4 d4[4];
#pragma unroll
      for (int t = 0; t < 4; ++t) d4[t] = mfma16(xf, tcol16(Wr, 4 * hf + t), f32x4{0.f, 0.f, 0.f, 0.f});
      if (g == (j >> 3)) {
#pragma unroll
        for (int r = 0; r < 4; ++r) *reinterpret_cast<f32x4*>(qpart + ((w * 2 + g) * 4 + r) * 64 + 8 * (j & 7) + 4 * hf) = f32x4{d4[0][r], d4[1][r], d4[2][r], d4[3][r]};
      }
    }
    asm volatile("" ::: "memory");
    u32 arr = 0;
    if (lane == 0) arr = __hip_atomic_fetch_add(lorg + 4, 1u, __ATOMIC_RELAXED, __HIP_MEMORY_SCOPE_WORKGROUP);
    if (__builtin_amdgcn_readfirstlane(arr) == 9 / NP - 1) {
      asm volatile("" ::: "memory");
      f32x4 s0 = {0.f, 0.f, 0.f, 0.f};
#pragma unroll
      for (int k = 0; k < 18 / NP; ++k) s0 += *reinterpret_cast<const f32x4*>(qpart + k * 256 + 4 * lane);
      const int qo = (int)WS_QA + (h * 8 + ci) * 2048 + lane * 16;
      __builtin_amdgcn_raw_buffer_store_b128(u32x4{__float_as_uint(s0[0]), tg, __float_as_uint(s0[1]), tg}, rs, qo, 0, 16);
      __builtin_amdgcn_raw_buffer_store_b128(u32x4{__float_as_uint(s0[2]), tg, __float_as_uint(s0[3]), tg}, rs, qo + 1024, 0, 16);
    }
  }
  static_for<RING - 1>([&](auto tc) __attribute__((always_inline)) {
    constexpr int t = tc.value + 1;
    u32 now = rt32();
    const u32 org = __builtin_amdgcn_readfirstlane(__hip_atomic_load(lorg, __ATOMIC_RELAXED, __HIP_MEMORY_SCOPE_WORKGROUP)), osl = 0;
    GATE(t);
    A_ISSUE(t);
    if constexpr (t == TPS) touch();
    __builtin_amdgcn_sched_barrier(0);
    if constexpr (t == SP) {
      if (w == 13) {
        const u32x2 sv = __builtin_amdgcn_raw_buffer_load_b64(rs, lane < 16 ? (int)WS_RPT + lane * 4352 : OOB, 128 * 15 * 35, 16);
        const bool ok = sv[1] == (u32)tag + 15u;
        const float sm = row16_sum(ok ? (float)(int)(sv[0] - T0) : 0.f), cn = row16_sum(ok ? 1.f : 0.f);
        const int o = ORG0 + (cn > 0.f ? (int)(sm / cn) : 0);
        if (lane == 0 && o > -300 && o < 1000) lorg[0] = (u32)o;
      }
    }
    if constexpr (t == QP) {
      if (w >= 14) {
        const int qo = (int)WS_QA + (h * 8 + (w & 1)) * 2048 + lane * 16;
        u32x4 v[2 * NP];
#pragma unroll 1
        for (u32 spins = 0; spins < SPIN_CAP; ++spins) {
          bool ok = true;
#pragma unroll
            for (int k = 0; k < 2 * NP; ++k) v[k] = __builtin_amdgcn_raw_buffer_load_b128(rs, qo, (k >> 1) * 4096 + (k & 1) * 1024, 16);
#pragma unroll
          for (int k = 0; k < 2 * NP; ++k) ok = ok && v[k][1] == tg && v[k][3] == tg;
          if (__all(ok)) break;
          __builtin_amdgcn_s_sleep(4);
        }
        f32x4 s0 = {0.f, 0.f, 0.f, 0.f};
#pragma unroll
        for (int k = 0; k < NP; ++k) s0 += f32x4{__uint_as_float(v[2 * k][0]), __uint_as_float(v[2 * k][2]), __uint_as_float(v[2 * k + 1][0]), __uint_as_float(v[2 * k + 1][2])};
        *reinterpret_cast<u32x2*>(lds + L_QS + (g * 128 + (w & 1) * 64 + 4 * j) * 2) = u32x2{pk_bf16(s0[0], s0[1]), pk_bf16(s0[2], s0[3])};
        asm volatile("" ::: "memory");
        if (lane == 0) __hip_atomic_fetch_add(lorg + 5, 1u, __ATOMIC_RELAXED, __HIP_MEMORY_SCOPE_WORKGROUP);
      }
    }
    __builtin_amdgcn_sched_barrier(0);
  });
  while (__hip_atomic_load(lorg + 5, __ATOMIC_RELAXED, __HIP_MEMORY_SCOPE_WORKGROUP) != 2u) __builtin_amdgcn_s_sleep(1);
  asm volatile("" :: "v"(pf) : "memory");
  float* wtab = reinterpret_cast<float*>(lds + L_ST + 1024);
  u32x2 xv = {0u, 0u};
  const int xoff = (w == XW && lane < 8 * ACU) ? (int)WS_MLA + (h * 8 + (lane >> 3)) * 128 + (lane & 7) * 8 : OOB;
  float* pml = reinterpret_cast<float*>(lds + L_ST);
  u32x4 q;
  u32 pb0 = 0, pb1 = 0;
  float acc[8], S[4];
  u32x4 RW[2][8];
  u32x4 WT[4][4];
  float Y[4];
  auto project = [&](int b) __attribute__((always_inline)) {
    f32x4 d4[4];
#pragma unroll
    for (int t = 0; t < 4; ++t) d4[t] = f32x4{0.f, 0.f, 0.f, 0.f};
    const f32x4 wt = *reinterpret_cast<const f32x4*>(wtab + b * 16 + 4 * g);
#pragma unroll
    for (int s = 0; s < 4; ++s) {
      const u32x4 af = *reinterpret_cast<const u32x4*>(lds + L_AB + (b * 16 + j) * 272 + (32 * s + 8 * g) * 2);
#pragma unroll
      for (int t = 0; t < 4; ++t) d4[t] = mfma16(af, WT[s][t], d4[t]);
    }
    float y[4];
#pragma unroll
    for (int t = 0; t < 4; ++t) y[t] = fmaf(d4[t][0], wt[0], d4[t][1] * wt[1]) + fmaf(d4[t][2], wt[2], d4[t][3] * wt[3]);
    Y[b] = sw16(sw32(y[0], y[2]), sw32(y[1], y[3]));
  };
  const __amdgpu_buffer_rsrc_t rwo = __builtin_amdgcn_make_buffer_rsrc(const_cast<u16*>(a.wo + (size_t)h * 128 * DM), 0, w < 9 ? 128 * DM * 2 : 0, 0x00020000);
  int wvo = ((32 * (j >> 3) + 8 * g) * DM + 64 * w + 8 * (j & 7)) * 2;
  asm volatile("" : "+v"(wvo));
  const u64 wmask = w < 9 ? ~0ull : 1ull;
#define WO_ISSUE() static_for<16>([&](auto kc) __attribute__((always_inline)) { constexpr int k = kc.value; \
    if constexpr (wo_step(k) == i) EXM(wmask, RW[k >> 3][k & 7] = __builtin_amdgcn_raw_buffer_load_b128(rwo, wvo, (64 * (k >> 3) + (k & 7)) * DM * 2, 2)); })
#define ISSUE_STEP() do { if constexpr (issued(i + 1) > issued(i)) { GATE(issued(i)); if constexpr (gf) { if (i >= D3FROM - 1 || issued(i) % 10 != 0 || xtra) asm volatile("" :: "v"(r)); __builtin_amdgcn_sched_barrier(0); } \
    WO_ISSUE(); A_ISSUE(issued(i)); if constexpr (gf) __builtin_amdgcn_sched_barrier(0); CTRL(issued(i)); \
    if constexpr (issued(i + 1) > issued(i) + 1) { GATE(issued(i) + 1); A_ISSUE(issued(i) + 1); CTRL(issued(i) + 1); } } else { WO_ISSUE(); } } while (0)
  static_for<NSEQ>([&](auto ic) __attribute__((always_inline)) {
    constexpr int i = ic.value, bb = (i / NS) & 3, pass = i / (4 * NS), sl = i % NS;
    u32 now = 0; if (PACE) now = rt32();
    u32 org = 0, osl = 0; if (PACE) { const u32x2 ov = __builtin_bit_cast(u32x2, __hip_atomic_load(reinterpret_cast<u64*>(lorg), __ATOMIC_RELAXED, __HIP_MEMORY_SCOPE_WORKGROUP)); org = __builtin_amdgcn_readfirstlane(ov[0]); osl = __builtin_amdgcn_readfirstlane(ov[1]); }
    if constexpr (issued(i) == NB && issued(i + 1) > NB) mode = __builtin_amdgcn_readfirstlane(__hip_atomic_load(lorg + 2, __ATOMIC_RELAXED, __HIP_MEMORY_SCOPE_WORKGROUP)) == 1u;
    __builtin_amdgcn_sched_barrier(0);
    const u32x4 r = R[i % RING];
    constexpr bool gf = i >= GF0;
    if constexpr (issued(i) == NB && issued(i + 1) > NB) {
      u64 bp = 0; int bl = 0, co = OOB;
      if (__builtin_expect(mode, 0)) {
        constexpr int CL = (8 - TF) * 512, N0 = 4 * ((CL + LN - 1) / LN), N1 = N0 + (4608 + LN - 1) / LN;
        const int id = w * 28 + (idx >> 3), l = (id < N0 ? id >> 2 : id < N1 ? id - N0 : id - N1) * LN + lane;
        bp = ((const __attribute__((address_space(4))) u64*)__builtin_amdgcn_kernarg_segment_ptr())[id < N0 ? 6 : id < N1 ? 10 : 9];
        bl = id < N0 ? 1 << 24 : id < N1 ? DM * 8192 : DC * 8192;
        co = l >= (id < N0 ? CL : 4608) ? OOB : id < N0 ? ((idx & 6) << 21) + ((idx & 1) << 19) + ((id & 3) << 20) + (TF << 16) + l * 128 : id < N1 ? (idx & 7) * 589824 + l * 128 : (l >> 3) * 8192 + (idx & 7) * 1024 + (l & 7) * 128;
        TWAIT(org + ((osl * NB) >> 4) + CI(due_at(NB)) + ((w * BS) >> 4)); last = T0 + 100000u; now = rt32();
      }
      asm volatile("" : "+v"(co));
      const __amdgpu_buffer_rsrc_t rc = __builtin_amdgcn_make_buffer_rsrc(reinterpret_cast<void*>(bp), 0, bl, 0x00020000);
      EXM(mode ? (1ull << LN) - 1ull : 1ull, pb0 = __builtin_amdgcn_raw_buffer_load_b32(rc, co, 0, 2); __builtin_amdgcn_sched_barrier(0);
          pb1 = __builtin_amdgcn_raw_buffer_load_b32(rc, co, 0, 0));
    }
    if constexpr (i == NB + 1) asm volatile("" :: "v"(pb0), "v"(pb1));
    if constexpr (gf) { ISSUE_STEP(); }
    if constexpr (i == TPC) asm volatile("" :: "v"(pf3));
    if constexpr (pass == 0) {
      if constexpr (sl == 0) { q = *reinterpret_cast<const u32x4*>(lds + L_QS + (bb * 128 + 8 * j) * 2); S[bb] = -INFINITY; }
      float d = dot2(r[0], q[0], 0.f); d = dot2(r[1], q[1], d); d = dot2(r[2], q[2], d); d = dot2(r[3], q[3], d);
      asm volatile("" : "+v"(d));
      if constexpr (!gf) { ISSUE_STEP(); }
      d = row16_sum(d);
      S[bb] = (j == sl) ? d : S[bb];
      if constexpr (sl == NS - 1) {
        const float sc = (j == bad_sl) ? -INFINITY : S[bb] * C2;
        const float m = xg_max(row16_max(sc));
        S[bb] = __builtin_amdgcn_exp2f(sc - m);
        const float l = xg_sum(row16_sum(S[bb]));
        if (lane == 0) *reinterpret_cast<f32x2*>(pml + (bb * 16 + w) * 2) = f32x2{m, l};
      }
      if constexpr (i == 4 * NS - 1) {
        asm volatile("" ::: "memory");
        u32 arr = 0;
        if (lane == 0) arr = __hip_atomic_fetch_add(lorg + 6, 1u, __ATOMIC_RELAXED, __HIP_MEMORY_SCOPE_WORKGROUP);
        arr = __builtin_amdgcn_readfirstlane(arr);
        asm volatile("" ::: "memory");
        const f32x2 ml = *reinterpret_cast<const f32x2*>(pml + lane * 2);
        const float M = row16_max(ml[0]);
        const float L = row16_sum(__builtin_amdgcn_exp2f(ml[0] - M) * ml[1]);
        __builtin_amdgcn_raw_buffer_store_b64(u32x2{__float_as_uint(j ? L : M), tg}, rs, (arr == 15u && j < 2) ? (int)WS_MLA + (h * 8 + ci) * 128 + g * 16 + j * 8 : OOB, 0, 16);
      }
    } else {
      const float p = dpp<0x150 + sl>(S[bb]);
      if constexpr (sl == 0) {
#pragma unroll
        for (int k = 0; k < 8; ++k) acc[k] = 0.f;
      }
#pragma unroll
      for (int k = 0; k < 4; ++k) { acc[2 * k] = fmaf(p, bf_lo(r[k]), acc[2 * k]); acc[2 * k + 1] = fmaf(p, bf_hi(r[k]), acc[2 * k + 1]); }
      asm volatile("" : "+v"(acc[0]), "+v"(acc[1]), "+v"(acc[2]), "+v"(acc[3]), "+v"(acc[4]), "+v"(acc[5]), "+v"(acc[6]), "+v"(acc[7]));
      if constexpr (!gf) { ISSUE_STEP(); }
      if constexpr (i == XR) xv = __builtin_amdgcn_raw_buffer_load_b64(rs, xoff, 0, 16);
      if constexpr (i == XR + 2) {
        if (w == XW) {
          if (__builtin_expect(!__all(lane >= 8 * ACU || xv[1] == tg), 0)) {
#pragma unroll 1
            for (u32 spins = 0; spins < SPIN_CAP; ++spins) {
              __builtin_amdgcn_s_sleep(8); xv = __builtin_amdgcn_raw_buffer_load_b64(rs, xoff, 0, 16);
              if (__all(lane >= 8 * ACU || xv[1] == tg)) break;
            }
          }
          const float x = __uint_as_float(xv[0]);
          const float m = lane < 8 * ACU ? dpp<0xA0>(x) : -INFINITY, l = lane < 8 * ACU ? dpp<0xF5>(x) : 0.f;
          const float M = xg_max(fmaxf(m, dpp<0x128>(m)));
          float t = __builtin_amdgcn_exp2f(m - M) * l; t = xg_sum(t + dpp<0x128>(t));
          if (lane < 8 && !(lane & 1)) *reinterpret_cast<f32x2*>(wtab + 64 + lane) = f32x2{M, 1.f / t};
          asm volatile("" ::: "memory");
          const f32x2 gm = *reinterpret_cast<const f32x2*>(wtab + 64 + 2 * g);
          wtab[lane] = __builtin_amdgcn_exp2f(pml[lane * 2] - gm[0]) * gm[1];
          asm volatile("" ::: "memory");
          if (lane == 0) __hip_atomic_store(lorg + 7, 1u, __ATOMIC_RELAXED, __HIP_MEMORY_SCOPE_WORKGROUP);
        }
      }
      if constexpr ((i >= WTS0 && i < WTS0 + 4) || (i >= WTS && i < WTS + 4)) {
        constexpr int s = i >= WTS, t = i - (s ? WTS : WTS0);
        const u32x4 lo = tcol16(RW[s], t), hi = tcol16(RW[s], t + 4);
#pragma unroll
        for (int c = 0; c < 4; ++c) {
          WT[2 * s][t][c] = __builtin_amdgcn_update_dpp(lo[c], hi[c], 0x128, 0xf, 0xc, false);
          WT[2 * s + 1][t][c] = __builtin_amdgcn_update_dpp(hi[c], lo[c], 0x128, 0xf, 0x3, false);
        }
        asm volatile("" : "+v"(WT[2 * s][t]), "+v"(WT[2 * s + 1][t]));
      }
      if constexpr (i == PJ0 || i == PJ1 || i == PJ2) {
        constexpr int b = i == PJ0 ? 0 : i == PJ1 ? 1 : 2;
        Y[b] = 0.f;
        if (w < 9) {
          while (__hip_atomic_load(lorg + 24 + b, __ATOMIC_RELAXED, __HIP_MEMORY_SCOPE_WORKGROUP) != 16u || __hip_atomic_load(lorg + 7, __ATOMIC_RELAXED, __HIP_MEMORY_SCOPE_WORKGROUP) == 0u) __builtin_amdgcn_s_sleep(1);
          asm volatile("" ::: "memory");
          project(b);
        }
        asm volatile("" : "+v"(Y[b]));
      }
      if constexpr (sl == NS - 1) {
        const float t0 = sw16(sw32(acc[0], acc[4]), sw32(acc[2], acc[6])), t1 = sw16(sw32(acc[1], acc[5]), sw32(acc[3], acc[7]));
        *reinterpret_cast<u32*>(lds + L_AB + (bb * 16 + w) * 272 + 16 * j + 4 * g) = pk_bf16(t0, t1);
        if constexpr (bb < 3) { asm volatile("" ::: "memory"); if (lane == 0) __hip_atomic_fetch_add(lorg + 24 + bb, 1u, __ATOMIC_RELAXED, __HIP_MEMORY_SCOPE_WORKGROUP); }
      }
    }
    __builtin_amdgcn_sched_barrier(0);
  });
#undef A_ISSUE
  __syncthreads();
  if (w < 9) {
    project(3);
    st16_sc1(rs, WS_Y + ((8 * w + (j & 7)) * 256 + idx) * 128 + ((j >> 3) * 4 + g) * 16, u32x4{pk_bf16(Y[0], Y[1]), tg, pk_bf16(Y[2], Y[3]), tg});
  }
  if (idx < 72) {
    __builtin_amdgcn_sched_barrier(0);
    __builtin_amdgcn_s_barrier();
    __builtin_amdgcn_s_sleep(FDLY);
    const int yo = (int)WS_Y + (idx * 256 + 16 * w) * 128 + lane * 16;
    u32x4 v0, v1;
#pragma unroll 1
    for (u32 spins = 0; spins < SPIN_CAP; ++spins) {
      v0 = __builtin_amdgcn_raw_buffer_load_b128(rs, yo, 0, 16); v1 = __builtin_amdgcn_raw_buffer_load_b128(rs, yo + 1024, 0, 16);
      if (__all(v0[1] == tg && v0[3] == tg && v1[1] == tg && v1[3] == tg)) break;
      __builtin_amdgcn_s_sleep(FSL);
    }
    float y = sw16(sw32(bf_lo(v0[0]) + bf_lo(v1[0]), bf_lo(v0[2]) + bf_lo(v1[2])), sw32(bf_hi(v0[0]) + bf_hi(v1[0]), bf_hi(v0[2]) + bf_hi(v1[2])));
    y += dpp<0x128>(y);
    float* red = reinterpret_cast<float*>(lds + L_PF);
    if (!(lane & 8)) red[((lane & 7) * 4 + g) * 20 + w] = y;
    __syncthreads();
    if (tid < 32) {
      const f32x4 r0 = *reinterpret_cast<const f32x4*>(red + tid * 20), r1 = *reinterpret_cast<const f32x4*>(red + tid * 20 + 4);
      const f32x4 r2 = *reinterpret_cast<const f32x4*>(red + tid * 20 + 8), r3 = *reinterpret_cast<const f32x4*>(red + tid * 20 + 12);
      const f32x4 r = r0 + r1 + r2;
      const __amdgpu_buffer_rsrc_t ro = __builtin_amdgcn_make_buffer_rsrc(a.out, 0, 4 * 1152 * 2, 0x00020000);
      __builtin_amdgcn_raw_buffer_store_b16(to_bf16(((r[0] + r[1]) + (r[2] + r[3])) + (r3[0] + r3[1])), ro, ((tid & 3) * 1152 + idx * 8 + (tid >> 2)) * 2, 0, 0);
      __builtin_amdgcn_raw_buffer_store_b16(to_bf16(r3[2] + r3[3]), ro, ((tid >> 3) * 1152 + 576 + idx * 8 + (tid & 7)) * 2, 0, 0);
    }
  }
}

constexpr int CW = 1, CR = 8;
#define IA 2
#define IB 4
#define IC 6
#define EUV 4
#define YOFF 90
#define WPD 4
DI int ctx_row(int p, int hd) {
  return (int)WS_CTXP + ((p >> 3) << 18) + ((hd >> 2) << 15) + ((p & 7) << 12) + ((hd & 3) << 10);
}
template <bool LD> DI void part_b(const Args& a, unsigned char* lds, int wg, u64 tag, u32 pf) {
  const int tid = threadIdx.x, lane = tid & 63, w = __builtin_amdgcn_readfirstlane(tid >> 6);
  const int j = lane & 15, g = lane >> 4;
  __amdgpu_buffer_rsrc_t rs;
  u64 tb64 = __builtin_amdgcn_s_memrealtime(); u32 tb0;
  const int h = (wg & 7) * 4 + (wg >> 3);
  const int h3 = h ^ 2;
  const int b2 = (wg & 7) >> 1, sp = (wg >> 3) * 2 + (wg & 1);
  const u16* cl;
  u32x4 X[16];
  u32x2 RW[4][8];
  u32x4 W8[2];
  u32* cnt = reinterpret_cast<u32*>(lds + L_ST + 4064);
#define B_POST(T) _Pragma("unroll") for (int k = 0; k < NRND; ++k) if (mtab[k] == (T) && xtab[k]) { u32x2 pv_ = {rt32(), (u32)tag + k}; int po_ = (w == 8 && !((wg ^ (wg >> 3)) & 1)) ? (int)WS_RPT + (16 + (wg >> 1)) * 4352 + 128 * 35 * k : OOB; \
    asm volatile("" : "+v"(pv_), "+v"(po_)); EXM(1, __builtin_amdgcn_raw_buffer_store_b64(pv_, rs, po_, 0, 16)); }
#define C_ISSUE(T) _Pragma("unroll") for (int r = 0; r < 8; ++r) X[8 * ((T) & 1) + r] = ld16(cl + (size_t)(64 * (T) + CR * r) * DC)
#define C_HALF(T, hf) _Pragma("unroll") for (int r = 4 * (hf); r < 4 * (hf) + 4; ++r) X[8 * ((T) & 1) + r] = ld16(cl + (size_t)(64 * (T) + CR * r) * DC)
#define L_PART(T, r0, r1) do { __builtin_amdgcn_sched_barrier(0); \
    if ((T) + 3 < 8) { _Pragma("unroll") for (int r = r0; r < r1; ++r) X[8 * (((T) + 3) & 1) + r] = ld16(cl + (size_t)(64 * ((T) + 3) + CR * r) * DC); } \
    if ((T) == 5 || (T) == 6) { _Pragma("unroll") for (int k = 2 * (r0); k < 2 * (r1); ++k) \
      RW[2 * (((T) - 5) & 1) + (k >> 3)][k & 7] = ld8(a.woB + ((size_t)h3 * 128 + 32 * (2 * (((T) - 5) & 1) + (k >> 3)) + 8 * g + (k & 7)) * DM + 64 * (w - 8) + 4 * j); } \
    __builtin_amdgcn_sched_barrier(0); } while (0)
#define UV_LOAD(n) do { const u32x4 v_ = ld16(a.wuv + ((size_t)(64 * w + 32 * ((n) >> 3) + 8 * g + ((n) & 7)) * H + h3) * 128 + 8 * j); \
    RW[2 * ((n) >> 3)][(n) & 7] = u32x2{v_[0], v_[1]}; RW[2 * ((n) >> 3) + 1][(n) & 7] = u32x2{v_[2], v_[3]}; } while (0)
#define C_WIN(T, r0, r1) _Pragma("unroll") for (int r = r0; r < r1; ++r) { asm volatile("s_waitcnt vmcnt(%0)" :: "n"(WPD - 1) : "memory"); X[8 * ((T) & 1) + r] = ld16(cl + (size_t)(64 * (T) + CR * r) * DC); __builtin_amdgcn_sched_barrier(0); }
  u32 sb0 = L_BIG + CW * (w - 8) * CRS + lane * 16, sb1 = sb0 + CBUF;
  asm volatile("" : "+v"(sb0), "+v"(sb1));
#define C_STAGE(T) _Pragma("unroll") for (int r = 0; r < 8; ++r) *reinterpret_cast<u32x4*>(lds + (((T) & 1) ? sb1 : sb0) + CR * r * CRS) = X[8 * ((T) & 1) + r]
  {
    const u16* Wq = a.wqB + h * 128;
    u32x4 U[8];
    QPre Q;
    qproj_load(Q, a.xB, Wq, w, lane);
#pragma unroll
    for (int i = 0; i < 8; ++i) U[i] = ld16(a.wuk + ((size_t)(32 * w + 4 * i + (j >> 2)) * H + h) * 128 + 8 * (4 * g + (j & 3)));
    __builtin_amdgcn_sched_barrier(0);
    { unsigned char* wsp = a.ws; u64 c64 = (u64)a.C; asm volatile("" : "+s"(wsp), "+s"(c64), "+s"(tb64) : "s"(a.wuv), "s"(a.woB), "s"(a.out));
      const u16* cp = (const u16*)(const __attribute__((address_space(1))) u16*)c64; tb0 = (u32)tb64;
      rs = __builtin_amdgcn_make_buffer_rsrc(wsp, 0, (int)WS_END, 0x00020000);
      cl = cp + ((size_t)b2 * SK + sp * 512 + CW * (w - 8)) * DC + lane * 8; }
    if constexpr (!LD) am_zero(lds, tid);
    qproj_mfma(Q, lds, w, lane);
    asm volatile("" :: "v"(pf));
    __syncthreads();
    if constexpr (LD) { C_HALF(0, 0); }
    else {
      const float* qpart = reinterpret_cast<const float*>(lds + L_BIG);
      float s = 0.f;
#pragma unroll
      for (int ww = 0; ww < 16; ++ww) s += qpart[ww * 512 + tid];
      reinterpret_cast<u16*>(lds + L_QS)[tid] = to_bf16(s);
    }
    __builtin_amdgcn_sched_barrier(0);
    __syncthreads();
    const u32x4 af = *reinterpret_cast<const u32x4*>(lds + L_QS + ((j >> 2) * 128 + (4 * g + (j & 3)) * 8) * 2);
    u16* qa = reinterpret_cast<u16*>(lds + L_PF);
    float v2[2] = {0.f, 0.f};
#pragma unroll
    for (int i = 0; i < 8; ++i) {
      const f32x4 d4 = mfma16(af, U[i], f32x4{0.f, 0.f, 0.f, 0.f});
      float d = (j & 2) ? ((j & 1) ? d4[3] : d4[2]) : ((j & 1) ? d4[1] : d4[0]);
      d += dpp<0xB1>(d); d += dpp<0x4E>(d);
      v2[i >> 2] = ((j & 3) == (i & 3)) ? d : v2[i >> 2];
    }
    qa[g * 512 + 32 * w + 4 * (j & 3) + (j >> 2)] = to_bf16(v2[0]); qa[g * 512 + 32 * w + 16 + 4 * (j & 3) + (j >> 2)] = to_bf16(v2[1]);
    __builtin_amdgcn_sched_barrier(0);
    __syncthreads();
    if constexpr (!LD) {
      if (w == 0) {
#pragma unroll
        for (int bb = 0; bb < 4; ++bb) st16_sc1(rs, WS_QABS + ((bb * H + h) * 512 + lane * 8) * 2, *reinterpret_cast<const u32x4*>(qa + bb * 512 + lane * 8));
      }
      __builtin_amdgcn_sched_barrier(0);
      __builtin_amdgcn_s_barrier();
      if (w == 0) {
        asm volatile("s_waitcnt vmcnt(0)" ::: "memory");
        if (lane == 0) { __hip_atomic_store(reinterpret_cast<u64*>(a.ws + WS_F1 + wg * FST), tag, RLX_AGENT); cnt[0] = 0u; cnt[1] = 0u; }
      }
    } else {
      __builtin_amdgcn_s_barrier();
      C_WIN(0, 4, 8); C_WIN(1, 0, 8);
      C_STAGE(0);
      __builtin_amdgcn_sched_barrier(0);
      C_WIN(2, 0, 8);
    }
  }
  {
    const int b = b2;
    if constexpr (!LD) wg_wait64(a.ws + WS_F1, tag); else __syncthreads();
    const int kt = w & 3, hh = (w >> 2) & 1;
    float* stmax = reinterpret_cast<float*>(lds + L_ST);
    float* stsum = stmax + 128;
    float* salpha = stmax + 256;
    unsigned char* Pf = lds + L_PF;
    float M = -INFINITY, lsum = 0.f;
    f32x4 acc[2][2];
#pragma unroll
    for (int ct = 0; ct < 2; ++ct) { acc[ct][0] = f32x4{0.f, 0.f, 0.f, 0.f}; acc[ct][1] = f32x4{0.f, 0.f, 0.f, 0.f}; }
    const int troff = (4 * g + (j >> 2)) * CRS + (32 * w + 4 * (j & 3)) * 2;
    auto pv_step = [&](const unsigned char* cb) __attribute__((always_inline)) {
      const f32x4 al0 = *reinterpret_cast<const f32x4*>(salpha + 4 * g), al1 = *reinterpret_cast<const f32x4*>(salpha + 16 + 4 * g);
#pragma unroll
      for (int ct = 0; ct < 2; ++ct) { acc[ct][0] *= al0; acc[ct][1] *= al1; }
#pragma unroll
      for (int blk = 0; blk < 2; ++blk) {
        const u32x4 p0 = *reinterpret_cast<const u32x4*>(Pf + ((blk * 2 + 0) * 64 + lane) * 16);
        const u32x4 p1 = *reinterpret_cast<const u32x4*>(Pf + ((blk * 2 + 1) * 64 + lane) * 16);
#pragma unroll
        for (int ct = 0; ct < 2; ++ct) {
          const u32x2 lo = trread(cb + troff + (32 * blk) * CRS + 32 * ct);
          const u32x2 hi = trread(cb + troff + (32 * blk + 16) * CRS + 32 * ct);
          const u32x4 bf = {lo[0], lo[1], hi[0], hi[1]};
          acc[ct][0] = mfma16(p0, bf, acc[ct][0]); acc[ct][1] = mfma16(p1, bf, acc[ct][1]);
        }
      }
    };
    unsigned char* Qf = lds + L_BIG + CBUF;
    if constexpr (!LD) {
      u32x4 qv[4];
#pragma unroll
      for (int k = 0; k < 4; ++k) qv[k] = __builtin_amdgcn_raw_buffer_load_b128(rs, (int)WS_QABS + (b * H * 512 + (tid + 512 * k) * 8) * 2, 0, 16);
#pragma unroll
      for (int k = 0; k < 4; ++k) { const int e = tid + 512 * k, head = e >> 6, ch = e & 63;
        *reinterpret_cast<u32x4*>(Qf + ((head >> 4) * 16 + (ch >> 2)) * 1040 + ((ch & 3) * 16 + (head & 15)) * 16) = qv[k]; }
    }
    __syncthreads();
    if constexpr (!LD) {
      u32x4 QF[16];
#pragma unroll
      for (int s = 0; s < 16; ++s) QF[s] = *reinterpret_cast<const u32x4*>(Qf + (hh * 16 + s) * 1040 + lane * 16);
      auto s_tile = [&](const unsigned char* cb, bool last) __attribute__((always_inline)) {
        __syncthreads();
        f32x4 s0 = {0.f, 0.f, 0.f, 0.f}, s1 = {0.f, 0.f, 0.f, 0.f};
#pragma unroll
        for (int s = 0; s < 16; s += 2) {
          s0 = mfma16(*reinterpret_cast<const u32x4*>(cb + (16 * kt + j) * CRS + (32 * s + 8 * g) * 2), QF[s], s0);
          s1 = mfma16(*reinterpret_cast<const u32x4*>(cb + (16 * kt + j) * CRS + (32 * s + 32 + 8 * g) * 2), QF[s + 1], s1);
        }
        const f32x4 sv = (s0 + s1) * C2;
        if (last) { __builtin_amdgcn_sched_barrier(0); _Pragma("unroll") for (int n = 0; n < EUV; ++n) UV_LOAD(n); __builtin_amdgcn_sched_barrier(0); }
        const float v = xg_max(fmaxf(fmaxf(sv[0], sv[1]), fmaxf(sv[2], sv[3])));
        if (g == 0) stmax[kt * 32 + 16 * hh + j] = v;
        __syncthreads();
        const float* sm = stmax + 16 * hh + j;
        const float Mn = fmaxf(fmaxf(M, fmaxf(sm[0], sm[32])), fmaxf(sm[64], sm[96]));
        const float alpha = __builtin_amdgcn_exp2f(M - Mn);
        M = Mn;
        u32x2 pk;
        pk[0] = pk_bf16(__builtin_amdgcn_exp2f(sv[0] - Mn), __builtin_amdgcn_exp2f(sv[1] - Mn));
        pk[1] = pk_bf16(__builtin_amdgcn_exp2f(sv[2] - Mn), __builtin_amdgcn_exp2f(sv[3] - Mn));
        lsum = lsum * alpha + ((bf_lo(pk[0]) + bf_hi(pk[0])) + (bf_lo(pk[1]) + bf_hi(pk[1])));
        *reinterpret_cast<u32x2*>(Pf + (((kt >> 1) * 2 + hh) * 64 + lane) * 16 + (kt & 1) * 8) = pk;
        if (kt == 0 && g == 0) salpha[16 * hh + j] = alpha;
        __syncthreads();
        pv_step(cb);
      };
#pragma unroll 1
      for (int T = 0; T < 6; T += 2) { s_tile(lds + L_BIG, false); s_tile(lds + L_BIG + CBUF, false); }
      s_tile(lds + L_BIG, false); s_tile(lds + L_BIG + CBUF, true);
    } else {
#pragma unroll
      for (int T = 0; T < 8; ++T) {
        __syncthreads();
        B_POST(T);
        if (T + 1 < 8) { C_STAGE(T + 1); }
        L_PART(T, 0, IA);
        if (T == 7) {
#pragma unroll
          for (int k = 0; k < 2; ++k) W8[k] = ld16(a.woB + ((size_t)h3 * 128 + 16 * (w - 8) + 8 * k + (lane >> 3)) * DM + 512 + (lane & 7) * 8);
        }
        __syncthreads();
        L_PART(T, IA, IB);
        __syncthreads();
        L_PART(T, IB, IC);
        pv_step(lds + L_BIG + (T & 1) * CBUF);
        L_PART(T, IC, 8);
      }
      B_POST(8);
    }
#undef C_ISSUE
#undef C_STAGE
    __syncthreads();
    if constexpr (!LD) { lsum = xg_sum(lsum); if (g == 0) stsum[kt * 32 + 16 * hh + j] = lsum; if (kt == 0 && g == 0) salpha[32 + 16 * hh + j] = M; }
    float* tb = reinterpret_cast<float*>(lds + L_BIG);
#pragma unroll
    for (int ct = 0; ct < 2; ++ct)
#pragma unroll
      for (int h2 = 0; h2 < 2; ++h2)
#pragma unroll
        for (int r = 0; r < 4; ++r) tb[(16 * h2 + 4 * g + r) * 516 + 32 * w + 16 * ct + j] = acc[ct][h2][r];
    if constexpr (!LD) { for (int i = tid; i < 8 * AM2_LD * 2 / 16; i += 512) reinterpret_cast<u32x4*>(lds + L_AM2 + 8 * AM2_LD * 2)[i] = u32x4{0u, 0u, 0u, 0u}; }
    __syncthreads();
    if constexpr (LD) {
      const int t2 = tid - 512;
#pragma unroll
      for (int k = 0; k < 4; ++k) {
        const int e = t2 + 512 * k, head = e >> 6, c8 = e & 63;
        const f32x4 v0 = *reinterpret_cast<const f32x4*>(tb + head * 516 + 8 * c8), v1 = *reinterpret_cast<const f32x4*>(tb + head * 516 + 8 * c8 + 4);
        __builtin_amdgcn_raw_buffer_store_b128(u32x4{pk_bf16(v0[0], v0[1]), pk_bf16(v0[2], v0[3]), pk_bf16(v1[0], v1[1]), pk_bf16(v1[2], v1[3])}, rs, ctx_row(b * NSP + sp, head) + 16 * c8, 0, 16);
      }
      if (t2 < 32) {
        const float L = (stsum[t2] + stsum[32 + t2]) + (stsum[64 + t2] + stsum[96 + t2]);
        st8_sc1(a.ws + WS_MLB + ((size_t)(b * NSP + sp) * 32 + t2) * 8, f32x2{salpha[32 + t2], L});
      }
      __builtin_amdgcn_sched_barrier(0);
      __builtin_amdgcn_s_barrier();
      asm volatile("s_waitcnt vmcnt(0)" ::: "memory");
      if (lane == 0) {
        if (__hip_atomic_fetch_add(cnt, 1u, __ATOMIC_RELAXED, __HIP_MEMORY_SCOPE_WORKGROUP) == 7u) {
          __hip_atomic_store(reinterpret_cast<u64*>(a.ws + WS_F2 + wg * FST), tag, RLX_AGENT);
        }
      }
#pragma unroll
      for (int k = 0; k < 2; ++k) *reinterpret_cast<u32x4*>(lds + L_W8 + (16 * (w - 8) + 8 * k + (lane >> 3)) * 144 + (lane & 7) * 16) = W8[k];
    } else {
      __builtin_amdgcn_s_barrier();
      static_for<16 - EUV>([&](auto ic) __attribute__((always_inline)) {
        asm volatile("s_waitcnt vmcnt(%0)" :: "n"(WPD - 1) : "memory");
        UV_LOAD(EUV + ic.value);
        __builtin_amdgcn_sched_barrier(0);
      });
    }
  }
  {
    int tid = threadIdx.x; asm volatile("" : "+v"(tid));
    const int lane = tid & 63, j = lane & 15, g = lane >> 4;
    u16* Am2 = reinterpret_cast<u16*>(lds + L_AM2);
    float* cpart = reinterpret_cast<float*>(lds + L_CP);
    __builtin_amdgcn_sched_barrier(0);
    if constexpr (LD) {
      if (w == 8) {
        wave_wait(reinterpret_cast<const u64*>(a.ws + WS_F2) + (lane & (NBW - 1)) * (FST / 8), lane < NBW, tag);
        asm volatile("s_waitcnt vmcnt(0)" ::: "memory");
        if (lane == 0) __hip_atomic_store(cnt + 1, 1u, __ATOMIC_RELEASE, __HIP_MEMORY_SCOPE_WORKGROUP);
      } else {
        for (u32 spins = 0; spins < 64u * SPIN_CAP; ++spins) { if (__hip_atomic_load(cnt + 1, __ATOMIC_ACQUIRE, __HIP_MEMORY_SCOPE_WORKGROUP) != 0u) break; __builtin_amdgcn_s_sleep(1); }
      }
      __builtin_amdgcn_sched_barrier(0);
      const int b = (tid >> 7) & 3, c4 = tid & 127;
      u32x2 pv[NSP];
      const f32x2 ml = __builtin_bit_cast(f32x2, __builtin_amdgcn_raw_buffer_load_b64(rs, (int)WS_MLB + ((b * NSP + (j & 7)) * 32 + h3) * 8, 0, 16));
#pragma unroll
      for (int s = 0; s < NSP; ++s) pv[s] = __builtin_amdgcn_raw_buffer_load_b64(rs, ctx_row(b * NSP + s, h3) + 8 * c4, 0, 16);
      const float M = row16_max(ml[0]);
      float wt = __builtin_amdgcn_exp2f(ml[0] - M);
      wt *= 2.f / row16_sum(wt * ml[1]);
      float c0 = 0.f, c1 = 0.f, c2 = 0.f, c3 = 0.f;
#pragma unroll
      for (int s = 0; s < NSP; ++s) { const float ws_ = rdlane(wt, s); c0 = fmaf(ws_, bf_lo(pv[s][0]), c0); c1 = fmaf(ws_, bf_hi(pv[s][0]), c1); c2 = fmaf(ws_, bf_lo(pv[s][1]), c2); c3 = fmaf(ws_, bf_hi(pv[s][1]), c3); }
      const u32 h0 = pk_bf16(c0, c1), h1 = pk_bf16(c2, c3);
      *reinterpret_cast<u32x2*>(Am2 + (2 * b) * AM2_LD + 4 * c4) = u32x2{h0, h1};
      *reinterpret_cast<u32x2*>(Am2 + (2 * b + 1) * AM2_LD + 4 * c4) = u32x2{pk_bf16(c0 - bf_lo(h0), c1 - bf_hi(h0)), pk_bf16(c2 - bf_lo(h1), c3 - bf_hi(h1))};
      __syncthreads();
      __syncthreads();
      const int t2 = tid - 512;
      float s = 0.f;
#pragma unroll
      for (int ww = 0; ww < 8; ++ww) s += cpart[ww * 512 + t2];
      u16* Am = reinterpret_cast<u16*>(lds + L_AM);
      const int bb = t2 >> 7, d = t2 & 127;
      const u16 hi = to_bf16(s);
      Am[(4 * bb) * AM_LD + d] = hi; Am[(4 * bb + 1) * AM_LD + d] = to_bf16(s - bf2f(hi));
    } else {
      __syncthreads();
      f32x4 acc[8];
#pragma unroll
      for (int t = 0; t < 8; ++t) acc[t] = f32x4{0.f, 0.f, 0.f, 0.f};
#pragma unroll
      for (int k2 = 0; k2 < 2; ++k2) {
        const u32x4 af = *reinterpret_cast<const u32x4*>(Am2 + j * AM2_LD + 64 * w + 32 * k2 + 8 * g);
#pragma unroll
        for (int t = 0; t < 8; ++t) acc[t] = mfma16(af, tcol8(RW[2 * k2 + (t >> 2)], t & 3), acc[t]);
      }
      if (g < 2) {
#pragma unroll
        for (int bb = 0; bb < 2; ++bb) {
          float* p = cpart + (w * 4 + 2 * g + bb) * 128 + 8 * j;
          *reinterpret_cast<f32x4*>(p) = f32x4{acc[0][2 * bb] + acc[0][2 * bb + 1], acc[1][2 * bb] + acc[1][2 * bb + 1], acc[2][2 * bb] + acc[2][2 * bb + 1], acc[3][2 * bb] + acc[3][2 * bb + 1]};
          *reinterpret_cast<f32x4*>(p + 4) = f32x4{acc[4][2 * bb] + acc[4][2 * bb + 1], acc[5][2 * bb] + acc[5][2 * bb + 1], acc[6][2 * bb] + acc[6][2 * bb + 1], acc[7][2 * bb] + acc[7][2 * bb + 1]};
        }
      }
      __builtin_amdgcn_sched_barrier(0);
      __syncthreads();
      if (w == 1) {
        u32 dl = tb0 + 5200u;
#pragma unroll 1
        while ((int)(rt32() - dl) < 0) {
          const u32x2 rv = __builtin_amdgcn_raw_buffer_load_b64(rs, lane < 16 ? (int)WS_RPT + lane * 4352 : OOB, 128 * (NRND - 1) * 35, 16);
          const bool ok = rv[1] == (u32)tag + (NRND - 1);
          const float cn = rdlane(row16_sum(ok ? 1.f : 0.f), 0);
          if (cn >= 8.f) { dl = tb0 + (u32)(int)(rdlane(row16_sum(ok ? (float)(int)(rv[0] - tb0) : 0.f), 0) / cn) + (u32)CI(due_at(NSEQ - 1) - due_at(rtab[NRND - 1]) + YOFF); break; }
          __builtin_amdgcn_s_sleep(32);
        }
        if (lane == 0) cnt[2] = (int)(dl - tb0) > 5200 ? tb0 : dl;
      }
      if (w == 0) {
#pragma unroll
        for (int s = 0; s < 4; ++s)
#pragma unroll
          for (int jj = 0; jj < 8; ++jj) RW[s][jj] = *reinterpret_cast<const u32x2*>(lds + L_W8 + (32 * s + 8 * g + jj) * 144 + 8 * j);
      }
    }
    __syncthreads();
    if (LD || w == 0) {
      f32x4 ya[4];
      wo_mfma(RW, lds, lane, ya);
      { const u32 dl = __builtin_amdgcn_readfirstlane(cnt[2]); while ((int)(rt32() - dl) < 0) __builtin_amdgcn_s_sleep(4); }
      st16_sc1(rs, WS_Y + ((8 * (LD ? w - 8 : 8) + (j >> 1)) * 256 + NAW + h3) * 128 + g * 32 + (j & 1) * 16, yrow(ya, (u32)(tag >> 32)));
    }
  }
}

__global__ void __launch_bounds__(1024, 4) k_main(Args a) {
  __shared__ __attribute__((aligned(16))) unsigned char lds[L_TOTAL];
  u32 pf;
  { u64 pc; u32 len;
    asm volatile("s_getpc_b64 %0\n.Lpf%=:\n\ts_mov_b32 %1, k_tail_mark-.Lpf%=" : "=s"(pc), "=s"(len));
    pf = __builtin_amdgcn_raw_buffer_load_b32(__builtin_amdgcn_make_buffer_rsrc(reinterpret_cast<void*>(pc), 0, len < 131072u ? len : 0u, 0x00020000), threadIdx.x < 16 ? ((blockIdx.x >> 3) * 16 + threadIdx.x) * 128 : 0x7ffff000, 0, 0);
    __builtin_amdgcn_sched_barrier(0); }
  u64 tag = (hw_dispatch_id() + 1ull) * 0x9E3779B97F4A7C15ull;
  tag ^= ((u64)__builtin_amdgcn_dispatch_ptr() >> 6) * 0xC2B2AE3D27D4EB4Full;
  tag |= 1ull << 63;
  if (__builtin_expect(blockIdx.x >= NBW, 1)) part_a(a, lds, blockIdx.x - NBW, tag, pf);
  else if (__builtin_amdgcn_readfirstlane(threadIdx.x >> 6) < 8) part_b<false>(a, lds, blockIdx.x, tag, pf);
  else part_b<true>(a, lds, blockIdx.x, tag, pf);
}

extern "C" __global__ void k_tail_mark() {}

extern "C" void kernel_launch(void* const* d_in, const int* in_sizes, int n_in, void* d_out, int out_size, void* d_ws, size_t ws_size,
                              hipStream_t stream) {
  Args a;
  a.xA = (const u16*)d_in[0]; a.K = (const u16*)d_in[1]; a.V = (const u16*)d_in[2]; a.wq = (const u16*)d_in[3]; a.wo = (const u16*)d_in[4];
  a.xB = (const u16*)d_in[5]; a.C = (const u16*)d_in[6]; a.wqB = (const u16*)d_in[7]; a.wuk = (const u16*)d_in[8]; a.wuv = (const u16*)d_in[9]; a.woB = (const u16*)d_in[10];
#define WSTOP 32
  if (WSTOP && ws_size >= ((size_t)WSTOP << 20)) d_ws = (unsigned char*)d_ws + ((ws_size - ((size_t)WSTOP << 20)) & ~(size_t)4095);
  a.ws = (unsigned char*)d_ws;
  a.out = (u16*)d_out;
  k_main<<<NBW + NAW, 1024, 0, stream>>>(a);
}
```
